```python
import torch

def _softmax(x, axis=-1):
    return torch.softmax(x, dim=axis)
def _gelu(x):
    return torch.nn.functional.gelu(x, approximate="tanh")
def _silu(x):
    return torch.nn.functional.silu(x)
def _sigmoid(x):
    return torch.sigmoid(x)
def _layernorm(x, axis=-1, eps=1e-5):
    m = x.mean(dim=axis, keepdim=True)
    v = x.var(dim=axis, keepdim=True, unbiased=False)
    return (x - m) / torch.sqrt(v + eps)
def _scatter_add(tgt, idx, src, axis=0):
    return tgt.clone().index_add_(axis, idx, src)
def _segment_sum(src, seg, n):
    out = torch.zeros((n,) + src.shape[1:], dtype=src.dtype)
    return out.index_add_(0, seg, src)
def _pad(x, pw):
    flat = tuple(v for pair in reversed(pw) for v in pair)
    return torch.nn.functional.pad(x, flat)

kernel_name = 'standalone__deep_cross_xdeepfm__recsys'

def setup_inputs():

    _w = (lambda a: (lambda t: t.to(torch.bfloat16) if t.is_floating_point() else t)(torch.as_tensor(a)))
    B, F, D, L = 1024, 26, 32, 3
    V = min(100000, 100000)
    Din = F * D
    return {
        "idx": _w((lambda lo, hi, *s: torch.randint(lo, hi, s))(0, V, B, F).to(torch.int64)),
        "table": _w((lambda *s: torch.randn(*s, dtype=torch.bfloat16) * 1.6)(V, D)),
        "ws_cin": _w((lambda *s: torch.randn(*s, dtype=torch.bfloat16) * 0.1)(L, F, F, F) * (1.0 / F)),
        "w_cin_out": _w((lambda *s: torch.randn(*s, dtype=torch.bfloat16) * 0.1)(L * F) * ((L * F) ** -0.5)),
        "w_dnn1": _w((lambda *s: torch.randn(*s, dtype=torch.bfloat16) * 1.6)(Din, Din) * (Din ** -0.5)),
        "b_dnn1": _w((lambda *s: torch.randn(*s, dtype=torch.bfloat16) * 12.8)(Din) * 0.1),
        "w_dnn2": _w((lambda *s: torch.randn(*s, dtype=torch.bfloat16) * 0.05)(Din) * (Din ** -0.5)),
        "b_out": _w((lambda *s: torch.randn(*s, dtype=torch.bfloat16) * 1.6)(1)),
    }

def reference(idx, table, ws_cin, w_cin_out, w_dnn1, b_dnn1, w_dnn2, b_out):
    x0 = table[idx]
    B = x0.shape[0]
    L = ws_cin.shape[0]
    xk = x0
    pooled = []
    for l in range(L):
        xk = torch.einsum('bid,bjd,hij->bhd', xk, x0, ws_cin[l])
        pooled.append(xk.sum(axis=-1))
    p_plus = torch.stack(pooled, axis=1).reshape(B, -1)
    logit_cin = p_plus @ w_cin_out
    h = x0.reshape(B, -1)
    h = _gelu(h @ w_dnn1 + b_dnn1)
    logit_dnn = h @ w_dnn2
    return logit_cin + logit_dnn + b_out
```

```cpp
#include <hip/hip_runtime.h>
#include <hip/hip_bf16.h>

__device__ __forceinline__ float bf2f(unsigned short x) { return __uint_as_float(((unsigned)x) << 16); }
__device__ __forceinline__ float rbf(float x) { return __bfloat162float(__float2bfloat16(x)); }

__global__ void __launch_bounds__(256) naive_kernel(const int* idx, const unsigned short* table, const unsigned short* ws_cin,
                             const unsigned short* w_cin_out, const unsigned short* w1, const unsigned short* b1,
                             const unsigned short* w2, const unsigned short* b_out, __hip_bfloat16* out) {
    __shared__ float x0[26 * 32];
    __shared__ float xa[26 * 32];
    __shared__ float xb[26 * 32];
    __shared__ float red[256];
    int b = blockIdx.x, t = threadIdx.x;
    for (int e = t; e < 832; e += 256) {
        int f = e >> 5, d = e & 31;
        float v = bf2f(table[(size_t)idx[b * 26 + f] * 32 + d]);
        x0[e] = v; xa[e] = v;
    }
    __syncthreads();
    float acc = 0.f;
    float* cur = xa; float* nxt = xb;
    for (int l = 0; l < 3; ++l) {
        for (int e = t; e < 832; e += 256) {
            int h = e >> 5, d = e & 31;
            float s = 0.f;
            for (int i = 0; i < 26; ++i)
                for (int j = 0; j < 26; ++j)
                    s += bf2f(ws_cin[((l * 26 + h) * 26 + i) * 26 + j]) * (cur[i * 32 + d] * x0[j * 32 + d]);
            nxt[e] = s;
            acc += s * bf2f(w_cin_out[l * 26 + h]);
        }
        __syncthreads();
        float* tmp = cur; cur = nxt; nxt = tmp;
    }
    for (int n = t; n < 832; n += 256) {
        float s = 0.f;
        for (int k = 0; k < 832; ++k) s += x0[k] * bf2f(w1[k * 832 + n]);
        s += bf2f(b1[n]);
        float g = 0.5f * s * (1.f + tanhf(0.7978845608028654f * (s + 0.044715f * s * s * s)));
        acc += g * bf2f(w2[n]);
    }
    red[t] = acc;
    __syncthreads();
    for (int s = 128; s > 0; s >>= 1) { if (t < s) red[t] += red[t + s]; __syncthreads(); }
    if (t == 0) out[b] = __float2bfloat16(red[0] + bf2f(b_out[0]));
}

extern "C" void kernel_launch(void* const* d_in, const int* in_sizes, int n_in,
                              void* d_out, int out_size, void* d_ws, size_t ws_size,
                              hipStream_t stream) {
    naive_kernel<<<1024, 256, 0, stream>>>((const int*)d_in[0], (const unsigned short*)d_in[1], (const unsigned short*)d_in[2],
        (const unsigned short*)d_in[3], (const unsigned short*)d_in[4], (const unsigned short*)d_in[5],
        (const unsigned short*)d_in[6], (const unsigned short*)d_in[7], (__hip_bfloat16*)d_out);
}
```

```cpp
#include <hip/hip_runtime.h>
#include <stdint.h>

#define DI __device__ __forceinline__
#define GAS __attribute__((address_space(1)))
#define LAS __attribute__((address_space(3)))

typedef short    s16x4 __attribute__((ext_vector_type(4)));
typedef short    s16x8 __attribute__((ext_vector_type(8)));
typedef float    f32x2 __attribute__((ext_vector_type(2)));
typedef float    f32x4 __attribute__((ext_vector_type(4)));
typedef float    f32x16 __attribute__((ext_vector_type(16)));
typedef unsigned u32x2 __attribute__((ext_vector_type(2)));
typedef unsigned u32x4 __attribute__((ext_vector_type(4)));
typedef __bf16   bf16x2 __attribute__((ext_vector_type(2)));
typedef GAS unsigned gu32;
typedef GAS unsigned long long gu64;
typedef LAS unsigned char lds8;

#define FUSED 1
#define MAPPING 1

constexpr int DIN = 832;
constexpr unsigned RSRC3 = 0x00020000u;
constexpr int AUX_SC1 = 16;
constexpr unsigned SPIN_CAP = 1u << 20;

constexpr unsigned WS_EP = 0;
constexpr unsigned WS_FL = 4096;
constexpr unsigned WS_PART = 8192;
constexpr unsigned WS_A = 8192 + 16 * 32 * 64 * 8;
constexpr unsigned A_BYTES = 46 * 832;

constexpr unsigned L_W = 0;
constexpr unsigned L_A = 832 * 128;
constexpr unsigned L_X = L_A + A_BYTES;
constexpr unsigned L_F = L_X + 16384;
constexpr unsigned L_END = L_F + 256;
constexpr unsigned LP_VT = 0;
constexpr unsigned LP_T0 = 2048;
constexpr unsigned LP_T1 = 2048 + 3072;

struct AB { int a, b; };
constexpr AB slot_ab(int t) {
    if (t < 156) { int a = 2 * (t / 12); return {a, (a + t % 12) % 26}; }
    if (t < 170) { int u = t - 156; int a = 2 * (u / 2); return {a, (a + 12 + (u & 1)) % 26}; }
    if (t < 176) { int a = 2 * (7 + t - 170); return {a, (a + 12) % 26}; }
    if (t < 189) return {2 * (t - 176), 26};
    if (t == 189) return {26, 26};
    return {27, 27};
}
constexpr unsigned T_ZERO = 27, T_CONST = 55;
struct PrepTab { unsigned v[24 * 2 * 8]; };
constexpr PrepTab make_tab() {
    PrepTab tb{};
    for (int s = 0; s < 24; ++s) for (int h = 0; h < 2; ++h) for (int e = 0; e < 8; ++e) {
        int t = 8 * s + e; AB ab = slot_ab(t);
        unsigned o1 = T_ZERO, o2 = T_ZERO;
        if (ab.a == 27) {}
        else if (ab.a == 26) { if (h == 0) o1 = T_CONST; }
        else if (ab.b == 26) { o1 = ((ab.a + h) % 26) * 28 + 26; }
        else {
            int u = (ab.a + h) % 26, v = (ab.b + h) % 26;
            bool dup = (t == 169 && h == 1);
            if (dup) {}
            else if (u == v) o1 = u * 28 + u;
            else { o1 = u * 28 + v; o2 = v * 28 + u; }
        }
        tb.v[(s * 2 + h) * 8 + e] = o1 | (o2 << 16);
    }
    return tb;
}
static_assert(slot_ab(169).a == 12 && slot_ab(169).b == 25, "dup slot");
__device__ const PrepTab g_tab = make_tab();

struct Params {
    const int* idx; const unsigned short* table; const unsigned short* wcin; const unsigned short* wout;
    const unsigned short* w1; const unsigned short* b1; const unsigned short* w2; const unsigned short* bout;
    unsigned short* out; unsigned char* ws;
};

DI float bf_lo(unsigned v) { return __uint_as_float(v << 16); }
DI float bf_hi(unsigned v) { return __uint_as_float(v & 0xffff0000u); }
DI unsigned pk_bf16(float lo, float hi) { f32x2 v = {lo, hi}; return __builtin_bit_cast(unsigned, __builtin_convertvector(v, bf16x2)); }
DI f32x4 mfma16(s16x8 a, s16x8 b, f32x4 c) { return __builtin_amdgcn_mfma_f32_16x16x32_bf16(a, b, c, 0, 0, 0); }
DI f32x16 mfma32(s16x8 a, s16x8 b, f32x16 c) { return __builtin_amdgcn_mfma_f32_32x32x16_bf16(a, b, c, 0, 0, 0); }
DI s16x8 as_frag(u32x4 v) { return __builtin_bit_cast(s16x8, v); }
template <int CTRL, int ROWMASK> DI float dpp_add(float v) {
    return v + __int_as_float(__builtin_amdgcn_update_dpp(0, __float_as_int(v), CTRL, ROWMASK, 0xf, true));
}
DI float wave_sum_to_lane63(float v) {
    v = dpp_add<0x111, 0xf>(v); v = dpp_add<0x112, 0xf>(v); v = dpp_add<0x114, 0xf>(v); v = dpp_add<0x118, 0xf>(v);
    v = dpp_add<0x142, 0xa>(v); v = dpp_add<0x143, 0xc>(v);
    return v;
}
DI float sum_lane_groups(float v) {
    auto a = __builtin_amdgcn_permlane16_swap(__float_as_uint(v), __float_as_uint(v), false, false);
    v = __uint_as_float(a[0]) + __uint_as_float(a[1]);
    auto b = __builtin_amdgcn_permlane32_swap(__float_as_uint(v), __float_as_uint(v), false, false);
    return __uint_as_float(b[0]) + __uint_as_float(b[1]);
}
DI float gelu_tanh(float x) {
    const float k1 = -2.0f * 0.7978845608028654f * 1.4426950408889634f;
    const float k2 = k1 * 0.044715f;
    float arg = x * __builtin_fmaf(x * x, k2, k1);
    float e = __builtin_amdgcn_exp2f(arg);
    return x * __builtin_amdgcn_rcpf(1.0f + e);
}
DI void dma16(unsigned lds_addr, unsigned voff, __amdgpu_buffer_rsrc_t srd, unsigned soff) {
    asm volatile("s_mov_b32 m0, %0\n\ts_nop 0\n\tbuffer_load_dwordx4 %1, %2, %3 offen lds"
                 :: "s"(lds_addr), "v"(voff), "s"(srd), "s"(soff) : "memory");
}
DI void st_granule(gu64* g, unsigned tag, unsigned val) {
    __hip_atomic_store(g, ((unsigned long long)tag << 32) | val, __ATOMIC_RELAXED, __HIP_MEMORY_SCOPE_AGENT);
}
DI unsigned ld_agent(const gu32* p) { return __hip_atomic_load(p, __ATOMIC_RELAXED, __HIP_MEMORY_SCOPE_AGENT); }
DI unsigned lds_ld(lds8* l, unsigned off) { return __hip_atomic_load((LAS unsigned*)(l + off), __ATOMIC_RELAXED, __HIP_MEMORY_SCOPE_WORKGROUP); }
DI void lds_st(lds8* l, unsigned off, unsigned v) { __hip_atomic_store((LAS unsigned*)(l + off), v, __ATOMIC_RELAXED, __HIP_MEMORY_SCOPE_WORKGROUP); }
#define WAIT_LGKM() asm volatile("s_waitcnt lgkmcnt(0)" ::: "memory")
#define WAIT_VM0() asm volatile("s_waitcnt vmcnt(0)" ::: "memory")
#define CBAR() asm volatile("" ::: "memory")

DI void dnn_role(const Params& P, lds8* lds, int w, int lane, int g, int ntile, unsigned tag) {
    const int mh = w & 1, kh = w >> 1;
    const int r = lane & 15, gq = lane >> 4;
    const unsigned ldsb = (unsigned)(size_t)lds;
    const int ncol = ntile * 64 + kh * 32 + 4 * gq;
    const u32x2 bia0 = *(const GAS u32x2*)(P.b1 + ncol), bia1 = *(const GAS u32x2*)(P.b1 + ncol + 16);
    const u32x2 wv0 = *(const GAS u32x2*)(P.w2 + ncol), wv1 = *(const GAS u32x2*)(P.w2 + ncol + 16);
    u32x4 ia[2], ib[2], ic[2]; unsigned id[2];
    {
        const unsigned o0 = ((64 * g + 32 * mh + r) * 26 + 13 * kh) * 4, o1 = o0 + 16 * 26 * 4;
        asm volatile("global_load_dwordx4 %0, %8, %10\n\tglobal_load_dwordx4 %1, %8, %10 offset:16\n\tglobal_load_dwordx4 %2, %8, %10 offset:32\n\tglobal_load_dword %3, %8, %10 offset:48\n\t"
                     "global_load_dwordx4 %4, %9, %10\n\tglobal_load_dwordx4 %5, %9, %10 offset:16\n\tglobal_load_dwordx4 %6, %9, %10 offset:32\n\tglobal_load_dword %7, %9, %10 offset:48"
                     : "=&v"(ia[0]), "=&v"(ib[0]), "=&v"(ic[0]), "=&v"(id[0]), "=&v"(ia[1]), "=&v"(ib[1]), "=&v"(ic[1]), "=&v"(id[1])
                     : "v"(o0), "v"(o1), "s"(P.idx) : "memory");
    }
    {
        const __amdgpu_buffer_rsrc_t srdW = __builtin_amdgcn_make_buffer_rsrc((void*)P.w1, 0, DIN * DIN * 2, RSRC3);
        const unsigned sw0 = (((lane >> 4) & 1)) << 1, sw1 = (((lane >> 4) & 1) | 2) << 1;
        const unsigned v0 = (lane >> 3) * 1664 + (((lane & 7) ^ sw0) << 4), v1 = (lane >> 3) * 1664 + (((lane & 7) ^ sw1) << 4);
        const unsigned row0 = 416 * kh + 208 * mh;
        const unsigned sbase = row0 * 1664 + ntile * 128, lbase = ldsb + L_W + row0 * 128;
#pragma unroll
        for (int jj = 0; jj < 26; ++jj) dma16(lbase + jj * 1024, (jj & 1) ? v1 : v0, srdW, sbase + jj * 13312);
    }
    asm volatile("s_waitcnt vmcnt(26)" : "+v"(ia[0]), "+v"(ib[0]), "+v"(ic[0]), "+v"(id[0]), "+v"(ia[1]), "+v"(ib[1]), "+v"(ic[1]), "+v"(id[1]) :: "memory");
    u32x4 xf[2][13];
    {
        const GAS unsigned char* tb = (const GAS unsigned char*)P.table;
#pragma unroll
        for (int s = 0; s < 13; ++s) {
#pragma unroll
            for (int mt = 0; mt < 2; ++mt) {
                const unsigned ix = s < 4 ? ia[mt][s & 3] : s < 8 ? ib[mt][s & 3] : s < 12 ? ic[mt][s & 3] : id[mt];
                xf[mt][s] = *(const GAS u32x4*)(tb + (ix * 64u + gq * 16u));
            }
        }
    }
    asm volatile("s_waitcnt vmcnt(26)" ::: "memory");
    __builtin_amdgcn_s_barrier();
    f32x4 acc[2][4];
#pragma unroll
    for (int mt = 0; mt < 2; ++mt) {
        acc[mt][0] = (f32x4){bf_lo(bia0[0]), bf_hi(bia0[0]), bf_lo(bia0[1]), bf_hi(bia0[1])};
        acc[mt][1] = (f32x4){bf_lo(bia1[0]), bf_hi(bia1[0]), bf_lo(bia1[1]), bf_hi(bia1[1])};
        acc[mt][2] = (f32x4){0.f, 0.f, 0.f, 0.f};
        acc[mt][3] = (f32x4){0.f, 0.f, 0.f, 0.f};
    }
    {
        const int q = (lane & 15) >> 2, pp = lane & 3;
        const unsigned swz = ((q >> 1) | ((gq & 1) << 1)) << 1;
        unsigned ad[4];
#pragma unroll
        for (int j = 0; j < 4; ++j) {
            const unsigned c = 2 * (j ^ (2 * kh)) + (pp >> 1);
            ad[j] = L_W + (416 * kh + 8 * gq + q) * 128 + ((c ^ swz) << 4) + (pp & 1) * 8;
        }
#pragma unroll
        for (int s = 0; s < 13; ++s) {
#pragma unroll
            for (int j = 0; j < 4; ++j) {
                const s16x4 lo = __builtin_amdgcn_ds_read_tr16_b64_v4i16((LAS s16x4*)(lds + ad[j] + s * 4096));
                const s16x4 hi = __builtin_amdgcn_ds_read_tr16_b64_v4i16((LAS s16x4*)(lds + ad[j] + s * 4096 + 512));
                const s16x8 wf = __builtin_shufflevector(lo, hi, 0, 1, 2, 3, 4, 5, 6, 7);
                acc[0][j] = mfma16(wf, as_frag(xf[0][s]), acc[0][j]);
                acc[1][j] = mfma16(wf, as_frag(xf[1][s]), acc[1][j]);
            }
        }
    }
    {
        lds8* xw = lds + L_X + w * 4096 + lane * 16;
        *(LAS f32x4*)(xw) = acc[0][2]; *(LAS f32x4*)(xw + 1024) = acc[0][3]; *(LAS f32x4*)(xw + 2048) = acc[1][2]; *(LAS f32x4*)(xw + 3072) = acc[1][3];
        WAIT_LGKM();
        if (lane == 0) lds_st(lds, L_F + 4 * w, 1);
        for (unsigned sp = 0; lds_ld(lds, L_F + 4 * (w ^ 2)) == 0 && sp < SPIN_CAP; ++sp) __builtin_amdgcn_s_sleep(0);
        CBAR();
        const lds8* xr = lds + L_X + (w ^ 2) * 4096 + lane * 16;
        acc[0][0] += *(const LAS f32x4*)(xr); acc[0][1] += *(const LAS f32x4*)(xr + 1024);
        acc[1][0] += *(const LAS f32x4*)(xr + 2048); acc[1][1] += *(const LAS f32x4*)(xr + 3072);
    }
    const float w20[4] = {bf_lo(wv0[0]), bf_hi(wv0[0]), bf_lo(wv0[1]), bf_hi(wv0[1])};
    const float w21[4] = {bf_lo(wv1[0]), bf_hi(wv1[0]), bf_lo(wv1[1]), bf_hi(wv1[1])};
    float part[2];
#pragma unroll
    for (int mt = 0; mt < 2; ++mt) {
        float s = 0.f;
#pragma unroll
        for (int e = 0; e < 4; ++e) { s = __builtin_fmaf(gelu_tanh(acc[mt][0][e]), w20[e], s); s = __builtin_fmaf(gelu_tanh(acc[mt][1][e]), w21[e], s); }
        part[mt] = sum_lane_groups(s);
    }
    if (lane < 32) {
        const float v = gq ? part[1] : part[0];
        st_granule((gu64*)(P.ws + WS_PART) + ((g * 32 + 2 * ntile + kh) * 64 + 32 * mh + lane), tag, __float_as_uint(v));
    }
}

DI void prep_role(const Params& P, lds8* lds, int w, int lane, int p, unsigned tagv) {
    const int tid = w * 64 + lane;
    const int n = lane & 15, gq = lane >> 4;
    const __amdgpu_buffer_rsrc_t srdC = __builtin_amdgcn_make_buffer_rsrc((void*)P.wcin, 0, 3 * 17576 * 2, RSRC3);
    u32x2 l3[3][8];
#pragma unroll
    for (int i = 0; i < 3; ++i) {
        const int T = w + 4 * i;
#pragma unroll
        for (int e = 0; e < 8; ++e)
            l3[i][e] = __builtin_amdgcn_raw_buffer_load_b64(srdC, (T < 11) ? (unsigned)((52 + 8 * gq + e) * 1352 + (64 * T + 4 * n) * 2) : 0xfffffff0u, 0, 0);
    }
    unsigned l2[8];
#pragma unroll
    for (int e = 0; e < 8; ++e) l2[e] = __builtin_amdgcn_raw_buffer_load_b32(srdC, (unsigned)(35152 + (8 * gq + e) * 1352 + p * 52 + n * 4), 0, 0);
    u32x2 l1 = (u32x2){0, 0};
    if (tid < 169) l1 = *(const GAS u32x2*)((const GAS unsigned char*)P.wcin + p * 1352 + tid * 8);
    u32x4 wo3;
#pragma unroll
    for (int j = 0; j < 4; ++j) {
        const int h0 = 8 * gq + 2 * j;
        const unsigned lo = h0 < 26 ? P.wout[52 + h0] : 0, hi = h0 + 1 < 26 ? P.wout[52 + h0 + 1] : 0;
        wo3[j] = lo | (hi << 16);
    }
    LAS unsigned short* vt = (LAS unsigned short*)(lds + LP_VT);
    LAS float* t0 = (LAS float*)(lds + LP_T0);
    LAS float* t1 = (LAS float*)(lds + LP_T1);
    if (tid < 27 * 6) vt[(tid / 6) * 32 + 26 + tid % 6] = 0;
    if (tid < 26) vt[26 * 32 + tid] = P.wout[26 + tid];
    if (tid == 0) { t0[T_ZERO] = 0.f; t1[T_ZERO] = 0.f; t1[T_CONST] = bf_lo(P.wout[p]); }
    if (tid < 169) {
#pragma unroll
        for (int e = 0; e < 4; ++e) {
            const int c = tid * 4 + e;
            t0[(c / 26) * 28 + c % 26] = (e & 1) ? bf_hi(l1[e >> 1]) : bf_lo(l1[e >> 1]);
        }
    }
#pragma unroll
    for (int i = 0; i < 3; ++i) {
        const int T = w + 4 * i;
        float res[4];
#pragma unroll
        for (int t = 0; t < 4; ++t) {
            u32x4 bfr;
#pragma unroll
            for (int j = 0; j < 4; ++j)
                bfr[j] = __builtin_amdgcn_perm(l3[i][2 * j + 1][t >> 1], l3[i][2 * j][t >> 1], (t & 1) ? 0x07060302u : 0x05040100u);
            const f32x4 d = mfma16(as_frag(wo3), as_frag(bfr), (f32x4){0.f, 0.f, 0.f, 0.f});
            res[t] = d[0];
        }
        const float v = gq == 0 ? res[0] : gq == 1 ? res[1] : gq == 2 ? res[2] : res[3];
        const int c = 64 * T + 4 * n + gq;
        if (T < 11 && c < 676) vt[(c % 26) * 32 + c / 26] = (unsigned short)(pk_bf16(v, v) & 0xffffu);
    }
    WAIT_LGKM();
    __builtin_amdgcn_s_barrier();
    if (w != 0) return;
    {
        u32x4 a0, a1;
#pragma unroll
        for (int j = 0; j < 4; ++j) {
            a0[j] = __builtin_amdgcn_perm(l2[2 * j + 1], l2[2 * j], 0x05040100u);
            a1[j] = __builtin_amdgcn_perm(l2[2 * j + 1], l2[2 * j], 0x07060302u);
        }
#pragma unroll
        for (int u = 0; u < 2; ++u) {
            const u32x4 bf = *(const LAS u32x4*)(lds + LP_VT + (16 * u + n) * 64 + gq * 16);
            const f32x4 d0 = mfma16(as_frag(a0), as_frag(bf), (f32x4){0.f, 0.f, 0.f, 0.f});
            const f32x4 d1 = mfma16(as_frag(a1), as_frag(bf), (f32x4){0.f, 0.f, 0.f, 0.f});
            const int jp = 16 * u + n;
#pragma unroll
            for (int e = 0; e < 4; ++e) {
                const int q0 = 2 * (4 * gq + e);
                if (q0 < 26 && jp < 27) { t1[q0 * 28 + jp] = d0[e]; t1[(q0 + 1) * 28 + jp] = d1[e]; }
            }
        }
    }
    WAIT_LGKM();
    const __amdgpu_buffer_rsrc_t srdA = __builtin_amdgcn_make_buffer_rsrc((void*)(P.ws + WS_A), 0, A_BYTES, RSRC3);
#pragma unroll
    for (int rnd = 0; rnd < 2; ++rnd) {
        const int ch = rnd * 64 + lane;
        if (ch < 92) {
            const int fr = ch >> 1, h = ch & 1, s = fr < 22 ? fr : fr - 22;
            const GAS u32x4* tp = (const GAS u32x4*)&g_tab.v[(s * 2 + h) * 8];
            const u32x4 ta = tp[0], tb = tp[1];
            const LAS float* t = fr < 22 ? t0 : t1;
            u32x4 o;
#pragma unroll
            for (int j = 0; j < 4; ++j) {
                const unsigned e0 = j < 2 ? ta[2 * j] : tb[2 * j - 4], e1 = j < 2 ? ta[2 * j + 1] : tb[2 * j - 3];
                o[j] = pk_bf16(t[e0 & 0xffff] + t[e0 >> 16], t[e1 & 0xffff] + t[e1 >> 16]);
            }
            __builtin_amdgcn_raw_buffer_store_b128(o, srdA, (unsigned)(fr * 832 + (26 * h + p) * 16), 0, AUX_SC1);
        }
    }
    WAIT_VM0();
    if (lane < 16) __hip_atomic_store((gu32*)(P.ws + WS_FL) + lane * 32 + p, tagv, __ATOMIC_RELAXED, __HIP_MEMORY_SCOPE_AGENT);
}

DI void cin_role(const Params& P, lds8* lds, int cw, int lane, int g, int m, unsigned tag) {
    const int d = lane & 31, h = lane >> 5;
    const int b = 64 * g + 4 * m + cw;
    f32x2 x[13];
    {
        const GAS unsigned* ip = (const GAS unsigned*)P.idx + b * 26;
        unsigned ix[26];
#pragma unroll
        for (int c = 0; c < 6; ++c) {
            const u32x4 v = *(const GAS u32x4*)(ip + h + 4 * c);
            ix[4 * c] = v[0]; ix[4 * c + 1] = v[1]; ix[4 * c + 2] = v[2]; ix[4 * c + 3] = v[3];
        }
        ix[24] = ip[24 + h];
        ix[25] = ip[h ? 0 : 25];
        const GAS unsigned char* tb = (const GAS unsigned char*)P.table;
        unsigned short xs[26];
#pragma unroll
        for (int a = 0; a < 26; ++a) xs[a] = *(const GAS unsigned short*)(tb + (ix[a] * 64u + d * 2u));
        CBAR();
        __builtin_amdgcn_s_barrier();
#pragma unroll
        for (int a = 0; a < 13; ++a) x[a] = (f32x2){bf_lo(xs[2 * a]), bf_lo(xs[2 * a + 1])};
    }
    u32x4 z[24];
#pragma unroll
    for (int s = 0; s < 24; ++s) {
#pragma unroll
        for (int j = 0; j < 4; ++j) {
            const AB p0 = slot_ab(8 * s + 2 * j), p1 = slot_ab(8 * s + 2 * j + 1);
            const float a0 = p0.a < 26 ? x[p0.a >> 1][p0.a & 1] : (p0.a == 26 ? 1.f : 0.f), b0 = p0.b < 26 ? x[p0.b >> 1][p0.b & 1] : 1.f;
            const float a1 = p1.a < 26 ? x[p1.a >> 1][p1.a & 1] : (p1.a == 26 ? 1.f : 0.f), b1 = p1.b < 26 ? x[p1.b >> 1][p1.b & 1] : 1.f;
            z[s][j] = pk_bf16(a0 * b0, a1 * b1);
        }
    }
    {
        const __amdgpu_buffer_rsrc_t srdA = __builtin_amdgcn_make_buffer_rsrc((void*)(P.ws + WS_A), 0, A_BYTES, RSRC3);
        if (cw == 0) {
            const gu32* fl = (const gu32*)(P.ws + WS_FL) + g * 32 + (lane < 26 ? lane : 0);
            for (unsigned sp = 0; sp < SPIN_CAP; ++sp) { if (__all(ld_agent(fl) == tag)) break; __builtin_amdgcn_s_sleep(1); }
            if (lane == 0) lds_st(lds, L_F + 16, 1);
        } else {
            for (unsigned sp = 0; lds_ld(lds, L_F + 16) == 0 && sp < SPIN_CAP; ++sp) __builtin_amdgcn_s_sleep(1);
        }
        CBAR();
        u32x4 av[10];
#pragma unroll
        for (int i = 0; i < 10; ++i) av[i] = __builtin_amdgcn_raw_buffer_load_b128(srdA, (unsigned)((i * 256 + cw * 64 + lane) * 16), 0, AUX_SC1);
#pragma unroll
        for (int i = 0; i < 10; ++i) {
            const unsigned o = (i * 256 + cw * 64 + lane) * 16;
            if (i < 9 || o < A_BYTES) *(LAS u32x4*)(lds + L_A + o) = av[i];
        }
        WAIT_LGKM();
        if (lane == 0) __hip_atomic_fetch_add((LAS unsigned*)(lds + L_F + 20), 1u, __ATOMIC_RELAXED, __HIP_MEMORY_SCOPE_WORKGROUP);
        for (unsigned sp = 0; lds_ld(lds, L_F + 20) < 4 && sp < SPIN_CAP; ++sp) __builtin_amdgcn_s_sleep(0);
        CBAR();
    }
    f32x16 acc0, acc1;
#pragma unroll
    for (int i = 0; i < 16; ++i) { acc0[i] = 0.f; acc1[i] = 0.f; }
    {
        const lds8* ab = lds + L_A + (26 * h + d) * 16;
#pragma unroll
        for (int s = 0; s < 24; ++s) {
            const u32x4 a1 = *(const LAS u32x4*)(ab + (22 + s) * 832);
            acc1 = mfma32(as_frag(a1), as_frag(z[s]), acc1);
            if (s < 22) {
                const u32x4 a0 = *(const LAS u32x4*)(ab + s * 832);
                acc0 = mfma32(as_frag(a0), as_frag(z[s]), acc0);
            }
        }
    }
    float sa = 0.f;
#pragma unroll
    for (int i = 0; i < 12; ++i) sa = __builtin_fmaf(acc0[i], acc1[i], sa);
    const float sb = __builtin_fmaf(acc0[12], acc1[12], acc0[13] * acc1[13]);
    float tot = h == 0 ? sa + sb : sa;
    const gu64* pp = (const gu64*)(P.ws + WS_PART) + ((g * 32 + (lane < 26 ? lane : 0)) * 64 + 4 * m + cw);
    unsigned long long pv = __hip_atomic_load(pp, __ATOMIC_RELAXED, __HIP_MEMORY_SCOPE_AGENT);
    for (unsigned sp = 0; sp < SPIN_CAP; ++sp) {
        if (__all((unsigned)(pv >> 32) == tag)) break;
        pv = __hip_atomic_load(pp, __ATOMIC_RELAXED, __HIP_MEMORY_SCOPE_AGENT);
    }
    if (lane < 26) tot += __uint_as_float((unsigned)pv);
    tot = wave_sum_to_lane63(tot);
    if (lane == 63) {
        const float o = tot + bf_lo(P.bout[0]);
        P.out[b] = (unsigned short)(pk_bf16(o, o) & 0xffffu);
    }
    if (m == 0 && cw == 0) {
        const gu64* ap = (const gu64*)(P.ws + WS_PART) + ((g * 32 + 26 + (lane < 3 ? lane : 0)) * 64);
        for (unsigned sp = 0; sp < SPIN_CAP; ++sp) {
            const unsigned long long av = __hip_atomic_load(ap, __ATOMIC_RELAXED, __HIP_MEMORY_SCOPE_AGENT);
            if (__all((unsigned)(av >> 32) == tag)) break;
        }
        if (lane == 0) __hip_atomic_store((gu32*)(P.ws + WS_EP) + g, tag, __ATOMIC_RELAXED, __HIP_MEMORY_SCOPE_AGENT);
    }
}

DI void decode_block(int bid, int& g, int& m, int& ntile, int& k) {
    const int x = bid & 7, j = bid >> 3;
    g = 2 * x + (j >> 4); m = j & 15;
    ntile = m < 13 ? m : -1; k = m - 13;
}

__global__ void __launch_bounds__(512, 2) xdfm_kernel(Params P) {
    __shared__ __attribute__((aligned(16))) unsigned char smem[L_END];
    lds8* lds = (lds8*)smem;
    const int w = __builtin_amdgcn_readfirstlane(threadIdx.x >> 6), lane = threadIdx.x & 63;
    int g, m, ntile, k;
    decode_block(blockIdx.x, g, m, ntile, k);
    const unsigned tag = ld_agent((const gu32*)(P.ws + WS_EP) + g) + 1u;
    if (w < 4) {
        if (w == 0 && lane < 8) lds_st(lds, L_F + 4 * lane, 0);
        if (ntile >= 0) { dnn_role(P, lds, w, lane, g, ntile, tag); }
        else {
            const int p = g * 3 + k;
            if (p < 26) {
                const unsigned tagv = ld_agent((const gu32*)(P.ws + WS_EP) + (lane & 15)) + 1u;
                prep_role(P, lds, w, lane, p, tagv);
            } else { WAIT_LGKM(); WAIT_VM0(); __builtin_amdgcn_s_barrier(); }
            if (w == 0 && lane == 0) st_granule((gu64*)(P.ws + WS_PART) + ((g * 32 + 26 + k) * 64), tag, 0u);
        }
    } else {
        cin_role(P, lds, w - 4, lane, g, m, tag);
    }
}

extern "C" void kernel_launch(void* const* d_in, const int* in_sizes, int n_in,
                              void* d_out, int out_size, void* d_ws, size_t ws_size,
                              hipStream_t stream) {
    Params P;
    P.idx = (const int*)d_in[0]; P.table = (const unsigned short*)d_in[1]; P.wcin = (const unsigned short*)d_in[2];
    P.wout = (const unsigned short*)d_in[3]; P.w1 = (const unsigned short*)d_in[4]; P.b1 = (const unsigned short*)d_in[5];
    P.w2 = (const unsigned short*)d_in[6]; P.bout = (const unsigned short*)d_in[7];
    P.out = (unsigned short*)d_out; P.ws = (unsigned char*)d_ws;
    xdfm_kernel<<<256, 512, 0, stream>>>(P);
}
```

```cpp
#include <hip/hip_runtime.h>
#include <stdint.h>

#define DI __device__ __forceinline__
#define GAS __attribute__((address_space(1)))
#define LAS __attribute__((address_space(3)))

typedef short    s16x4 __attribute__((ext_vector_type(4)));
typedef short    s16x8 __attribute__((ext_vector_type(8)));
typedef float    f32x2 __attribute__((ext_vector_type(2)));
typedef float    f32x4 __attribute__((ext_vector_type(4)));
typedef float    f32x16 __attribute__((ext_vector_type(16)));
typedef unsigned u32x2 __attribute__((ext_vector_type(2)));
typedef unsigned u32x4 __attribute__((ext_vector_type(4)));
typedef __bf16   bf16x2 __attribute__((ext_vector_type(2)));
typedef GAS unsigned gu32;
typedef GAS unsigned long long gu64;
typedef LAS unsigned char lds8;

#define MAPPING 1

constexpr int DIN = 832, NBATCH = 1024, NVOC = 100000;
#define A_AFTER_X 1
#define W_EARLY 8
#define XD 6
#define PKMUL 0
#define A_DMA 1
constexpr unsigned RSRC3 = 0x00020000u;
constexpr int AUX_SC1 = 16;
constexpr unsigned SPIN_CAP = 1u << 20;

constexpr unsigned WS_EP = 0;
constexpr unsigned WS_FL = 4096;
constexpr unsigned WS_PART = 8192;
constexpr unsigned WS_A = 8192 + 16 * 32 * 64 * 8;
constexpr unsigned A_BYTES = 46 * 832;

constexpr unsigned L_W = 0;
constexpr unsigned L_A = 832 * 128;
constexpr unsigned L_X = L_A + A_BYTES;
constexpr unsigned L_F = L_X + 16384;
constexpr unsigned L_END = L_F + 256;
constexpr unsigned LP_VT = 0;
constexpr unsigned LP_T0 = 2048;
constexpr unsigned LP_T1 = 2048 + 3072;

struct AB { int a, b; };
constexpr AB slot_ab(int t) {
    if (t < 156) { int a = 2 * (t / 12); return {a, (a + t % 12) % 26}; }
    if (t < 170) { int u = t - 156; int a = 2 * (u / 2); return {a, (a + 12 + (u & 1)) % 26}; }
    if (t < 176) { int a = 2 * (7 + t - 170); return {a, (a + 12) % 26}; }
    if (t < 189) return {2 * (t - 176), 26};
    if (t == 189) return {26, 26};
    return {27, 27};
}
constexpr unsigned T_ZERO = 27, T_CONST = 55;
struct PrepTab { unsigned v[24 * 2 * 8]; };
constexpr PrepTab make_tab() {
    PrepTab tb{};
    for (int s = 0; s < 24; ++s) for (int h = 0; h < 2; ++h) for (int e = 0; e < 8; ++e) {
        int t = 8 * s + e; AB ab = slot_ab(t);
        unsigned o1 = T_ZERO, o2 = T_ZERO;
        if (ab.a == 27) {}
        else if (ab.a == 26) { if (h == 0) o1 = T_CONST; }
        else if (ab.b == 26) { o1 = ((ab.a + h) % 26) * 28 + 26; }
        else {
            int u = (ab.a + h) % 26, v = (ab.b + h) % 26;
            bool dup = (t == 169 && h == 1);
            if (dup) {}
            else if (u == v) o1 = u * 28 + u;
            else { o1 = u * 28 + v; o2 = v * 28 + u; }
        }
        tb.v[(s * 2 + h) * 8 + e] = o1 | (o2 << 16);
    }
    return tb;
}
static_assert(slot_ab(169).a == 12 && slot_ab(169).b == 25, "dup slot");
__device__ const PrepTab g_tab = make_tab();

struct Params {
    const int* idx; const unsigned short* table; const unsigned short* wcin; const unsigned short* wout;
    const unsigned short* w1; const unsigned short* b1; const unsigned short* w2; const unsigned short* bout;
    unsigned short* out; unsigned char* ws;
};

DI float bf_lo(unsigned v) { return __uint_as_float(v << 16); }
DI float bf_hi(unsigned v) { return __uint_as_float(v & 0xffff0000u); }
DI unsigned pk_bf16(float lo, float hi) { f32x2 v = {lo, hi}; return __builtin_bit_cast(unsigned, __builtin_convertvector(v, bf16x2)); }
DI f32x4 mfma16(s16x8 a, s16x8 b, f32x4 c) { return __builtin_amdgcn_mfma_f32_16x16x32_bf16(a, b, c, 0, 0, 0); }
DI f32x16 mfma32(s16x8 a, s16x8 b, f32x16 c) { return __builtin_amdgcn_mfma_f32_32x32x16_bf16(a, b, c, 0, 0, 0); }
DI s16x8 as_frag(u32x4 v) { return __builtin_bit_cast(s16x8, v); }
template <int CTRL, int ROWMASK> DI float dpp_add(float v) {
    return v + __int_as_float(__builtin_amdgcn_update_dpp(0, __float_as_int(v), CTRL, ROWMASK, 0xf, true));
}
DI float wave_sum_to_lane63(float v) {
    v = dpp_add<0x111, 0xf>(v); v = dpp_add<0x112, 0xf>(v); v = dpp_add<0x114, 0xf>(v); v = dpp_add<0x118, 0xf>(v);
    v = dpp_add<0x142, 0xa>(v); v = dpp_add<0x143, 0xc>(v);
    return v;
}
DI float sum_lane_groups(float v) {
    auto a = __builtin_amdgcn_permlane16_swap(__float_as_uint(v), __float_as_uint(v), false, false);
    v = __uint_as_float(a[0]) + __uint_as_float(a[1]);
    auto b = __builtin_amdgcn_permlane32_swap(__float_as_uint(v), __float_as_uint(v), false, false);
    return __uint_as_float(b[0]) + __uint_as_float(b[1]);
}
DI float gelu_tanh(float x) {
    const float k1 = -2.0f * 0.7978845608028654f * 1.4426950408889634f;
    const float k2 = k1 * 0.044715f;
    float arg = x * __builtin_fmaf(x * x, k2, k1);
    float e = __builtin_amdgcn_exp2f(arg);
    return x * __builtin_amdgcn_rcpf(1.0f + e);
}
DI void dma16(unsigned lds_addr, unsigned voff, __amdgpu_buffer_rsrc_t srd, unsigned soff) {
    asm volatile("s_mov_b32 m0, %0\n\ts_nop 0\n\tbuffer_load_dwordx4 %1, %2, %3 offen lds"
                 :: "s"(lds_addr), "v"(voff), "s"(srd), "s"(soff) : "memory");
}
DI void dma16_sc1(unsigned lds_addr, unsigned voff, __amdgpu_buffer_rsrc_t srd, unsigned soff) {
    asm volatile("s_mov_b32 m0, %0\n\ts_nop 0\n\tbuffer_load_dwordx4 %1, %2, %3 offen sc1 lds"
                 :: "s"(lds_addr), "v"(voff), "s"(srd), "s"(soff) : "memory");
}
DI void st_granule(gu64* g, unsigned tag, unsigned val) {
    __hip_atomic_store(g, ((unsigned long long)tag << 32) | val, __ATOMIC_RELAXED, __HIP_MEMORY_SCOPE_AGENT);
}
DI unsigned ld_agent(const gu32* p) { return __hip_atomic_load(p, __ATOMIC_RELAXED, __HIP_MEMORY_SCOPE_AGENT); }
DI unsigned lds_ld(lds8* l, unsigned off) { return __hip_atomic_load((LAS unsigned*)(l + off), __ATOMIC_RELAXED, __HIP_MEMORY_SCOPE_WORKGROUP); }
DI void lds_st(lds8* l, unsigned off, unsigned v) { __hip_atomic_store((LAS unsigned*)(l + off), v, __ATOMIC_RELAXED, __HIP_MEMORY_SCOPE_WORKGROUP); }
DI void lds_add(lds8* l, unsigned off, unsigned v) { __hip_atomic_fetch_add((LAS unsigned*)(l + off), v, __ATOMIC_RELAXED, __HIP_MEMORY_SCOPE_WORKGROUP); }
#define WAIT_LGKM() asm volatile("s_waitcnt lgkmcnt(0)" ::: "memory")
#define WAIT_VM0() asm volatile("s_waitcnt vmcnt(0)" ::: "memory")
#define CBAR() asm volatile("" ::: "memory")
#define STAMP(i)
#define STAMP_ARG
#define STAMP_PASS
#define STAMP_FLUSH(ws, w, lane)

template <int K> DI unsigned quad_bcast(unsigned v) { return __builtin_amdgcn_update_dpp(0u, v, K * 0x55, 0xf, 0xf, true); }
DI void dnn_role(const Params& P, lds8* lds, int w, int lane, int g, int ntile STAMP_ARG) {
    const int mh = w & 1, kh = w >> 1;
    const int gq = lane >> 4;
    const unsigned ldsb = (unsigned)(size_t)lds;
    u32x4 i0, i1; unsigned ep; u32x2 bia0, bia1, wv0, wv1;
    {
        const __amdgpu_buffer_rsrc_t srdI = __builtin_amdgcn_make_buffer_rsrc((void*)P.idx, 0, NBATCH * 26 * 4, RSRC3);
        const unsigned vo = ((64 * g + 32 * mh + (lane >> 2)) * 26 + 13 * kh + 4 * (lane & 3)) * 4;
        const unsigned nb = (ntile * 64 + kh * 32 + 4 * gq) * 2;
        asm volatile("buffer_load_dwordx4 %0, %7, %8, 0 offen\n\tbuffer_load_dwordx4 %1, %7, %8, 0 offen offset:1664\n\t"
                     "global_load_dword %2, %9, %10 sc1\n\t"
                     "global_load_dwordx2 %3, %11, %12\n\tglobal_load_dwordx2 %4, %11, %12 offset:32\n\t"
                     "global_load_dwordx2 %5, %11, %13\n\tglobal_load_dwordx2 %6, %11, %13 offset:32"
                     : "=&v"(i0), "=&v"(i1), "=&v"(ep), "=&v"(bia0), "=&v"(bia1), "=&v"(wv0), "=&v"(wv1)
                     : "v"(vo), "s"(srdI), "v"(g * 4), "s"(P.ws + WS_EP), "v"(nb), "s"(P.b1), "s"(P.w2) : "memory");
    }
    const __amdgpu_buffer_rsrc_t srdW = __builtin_amdgcn_make_buffer_rsrc((void*)P.w1, 0, DIN * DIN * 2, RSRC3);
    const unsigned sw0 = (((lane >> 4) & 1)) << 1, sw1 = (((lane >> 4) & 1) | 2) << 1;
    const unsigned v0 = (lane >> 3) * 1664 + (((lane & 7) ^ sw0) << 4), v1 = (lane >> 3) * 1664 + (((lane & 7) ^ sw1) << 4);
    const unsigned row0 = 416 * kh + 208 * mh;
    const unsigned sbase = row0 * 1664 + ntile * 128, lbase = ldsb + L_W + row0 * 128;
#pragma unroll
    for (int jj = 0; jj < W_EARLY; ++jj) dma16(lbase + jj * 1024, (jj & 1) ? v1 : v0, srdW, sbase + jj * 13312);
    STAMP(1);
    asm volatile("s_waitcnt vmcnt(%7) lgkmcnt(0)" : "+v"(i0), "+v"(i1), "+v"(ep), "+v"(bia0), "+v"(bia1), "+v"(wv0), "+v"(wv1) : "i"(W_EARLY) : "memory");
    __builtin_amdgcn_s_barrier();
    STAMP(2);
#pragma unroll
    for (int jj = W_EARLY; jj < 26; ++jj) dma16(lbase + jj * 1024, (jj & 1) ? v1 : v0, srdW, sbase + jj * 13312);
    u32x4 xl[2][13];
    const GAS unsigned char* tb = (const GAS unsigned char*)P.table;
    const unsigned c16 = (((lane & 3) ^ ((0u - (unsigned)gq) & 3u)) << 4);
    const u32x4 t0 = i0 << 6, t1 = i1 << 6;
    auto xload = [&](int s) {
        const unsigned a0 = t0[s & 3], a1 = t1[s & 3];
        const unsigned o0 = (s < 4 ? quad_bcast<0>(a0) : s < 8 ? quad_bcast<1>(a0) : s < 12 ? quad_bcast<2>(a0) : quad_bcast<3>(a0)) + c16;
        const unsigned o1 = (s < 4 ? quad_bcast<0>(a1) : s < 8 ? quad_bcast<1>(a1) : s < 12 ? quad_bcast<2>(a1) : quad_bcast<3>(a1)) + c16;
        xl[0][s] = *(const GAS u32x4*)(tb + o0);
        xl[1][s] = *(const GAS u32x4*)(tb + o1);
    };
#pragma unroll
    for (int s = 0; s < XD; ++s) xload(s);
    STAMP(3);
    asm volatile("s_waitcnt vmcnt(%0)" :: "i"(2 * XD) : "memory");
    if (lane == 0) lds_add(lds, L_F + 32 + 4 * kh, 1);
    for (unsigned sp = 0; lds_ld(lds, L_F + 32 + 4 * kh) < 2 && sp < SPIN_CAP; ++sp) __builtin_amdgcn_s_sleep(0);
    CBAR();
    STAMP(4);
    f32x4 acc[2][4];
#pragma unroll
    for (int mt = 0; mt < 2; ++mt) {
        acc[mt][0] = (f32x4){bf_lo(bia0[0]), bf_hi(bia0[0]), bf_lo(bia0[1]), bf_hi(bia0[1])};
        acc[mt][1] = (f32x4){bf_lo(bia1[0]), bf_hi(bia1[0]), bf_lo(bia1[1]), bf_hi(bia1[1])};
        acc[mt][2] = (f32x4){0.f, 0.f, 0.f, 0.f};
        acc[mt][3] = (f32x4){0.f, 0.f, 0.f, 0.f};
    }
    {
        const int q = (lane & 15) >> 2, pp = lane & 3;
        const unsigned swz = ((q >> 1) | ((gq & 1) << 1)) << 1;
        unsigned ad[4];
#pragma unroll
        for (int j = 0; j < 4; ++j) {
            const unsigned c = 2 * (j ^ (2 * kh)) + (pp >> 1);
            ad[j] = L_W + (416 * kh + 8 * gq + q) * 128 + ((c ^ swz) << 4) + (pp & 1) * 8;
        }
        lds8* stw = lds + L_X + w * 4096 + lane * 16;
        const lds8* str = lds + L_X + w * 4096 + (lane & 15) * 64 + ((gq ^ ((0u - (unsigned)q) & 3u)) << 4);
        u32x4 xf[3][2];
        s16x8 wf[2][4];
        auto stage = [&](int s) {
            *(LAS u32x4*)(stw + (s & 1) * 2048) = xl[0][s];
            *(LAS u32x4*)(stw + (s & 1) * 2048 + 1024) = xl[1][s];
            xf[s % 3][0] = *(const LAS u32x4*)(str + (s & 1) * 2048);
            xf[s % 3][1] = *(const LAS u32x4*)(str + (s & 1) * 2048 + 1024);
        };
        auto wread = [&](int s) {
#pragma unroll
            for (int j = 0; j < 4; ++j) {
                const s16x4 lo = __builtin_amdgcn_ds_read_tr16_b64_v4i16((LAS s16x4*)(lds + ad[j] + s * 4096));
                const s16x4 hi = __builtin_amdgcn_ds_read_tr16_b64_v4i16((LAS s16x4*)(lds + ad[j] + s * 4096 + 512));
                wf[s & 1][j] = __builtin_shufflevector(lo, hi, 0, 1, 2, 3, 4, 5, 6, 7);
            }
        };
        wread(0);
        stage(0);
        stage(1);
        __builtin_amdgcn_sched_barrier(0);
#pragma unroll
        for (int s = 0; s < 13; ++s) {
            if (s + XD < 13) xload(s + XD);
            if (s + XD == 12 || (XD >= 13 && s == 0)) { CBAR(); if (lane == 0) lds_add(lds, L_F + 40, 1); }
            __builtin_amdgcn_sched_barrier(0);
            if (s + 1 < 13) wread(s + 1);
#pragma unroll
            for (int j = 0; j < 4; ++j) {
                acc[0][j] = mfma16(wf[s & 1][j], as_frag(xf[s % 3][0]), acc[0][j]);
                acc[1][j] = mfma16(wf[s & 1][j], as_frag(xf[s % 3][1]), acc[1][j]);
            }
            if (s + 1 < 13) {
#pragma unroll
                for (int j = 0; j < 8; ++j) { __builtin_amdgcn_sched_group_barrier(0x100, 1, 0); __builtin_amdgcn_sched_group_barrier(0x8, 1, 0); }
            }
            __builtin_amdgcn_sched_barrier(0);
            if (s + 2 < 13) stage(s + 2);
            __builtin_amdgcn_sched_barrier(0);
        }
    }
    STAMP(6);
    {
        lds8* xw = lds + L_X + w * 4096 + lane * 16;
        *(LAS f32x4*)(xw) = acc[0][2]; *(LAS f32x4*)(xw + 1024) = acc[0][3]; *(LAS f32x4*)(xw + 2048) = acc[1][2]; *(LAS f32x4*)(xw + 3072) = acc[1][3];
        CBAR();
        if (lane == 0) lds_st(lds, L_F + 4 * w, 1);
        for (unsigned sp = 0; lds_ld(lds, L_F + 4 * (w ^ 2)) == 0 && sp < SPIN_CAP; ++sp) __builtin_amdgcn_s_sleep(0);
        CBAR();
        const lds8* xr = lds + L_X + (w ^ 2) * 4096 + lane * 16;
        acc[0][0] += *(const LAS f32x4*)(xr); acc[0][1] += *(const LAS f32x4*)(xr + 1024);
        acc[1][0] += *(const LAS f32x4*)(xr + 2048); acc[1][1] += *(const LAS f32x4*)(xr + 3072);
    }
    STAMP(7);
    const float w20[4] = {bf_lo(wv0[0]), bf_hi(wv0[0]), bf_lo(wv0[1]), bf_hi(wv0[1])};
    const float w21[4] = {bf_lo(wv1[0]), bf_hi(wv1[0]), bf_lo(wv1[1]), bf_hi(wv1[1])};
    float part[2];
#pragma unroll
    for (int mt = 0; mt < 2; ++mt) {
        float s = 0.f;
#pragma unroll
        for (int e = 0; e < 4; ++e) { s = __builtin_fmaf(gelu_tanh(acc[mt][0][e]), w20[e], s); s = __builtin_fmaf(gelu_tanh(acc[mt][1][e]), w21[e], s); }
        part[mt] = sum_lane_groups(s);
    }
    STAMP(8);
    if (lane < 32) {
        const float v = gq ? part[1] : part[0];
        st_granule((gu64*)(P.ws + WS_PART) + ((g * 32 + 2 * ntile + kh) * 64 + 32 * mh + lane), ep + 1u, __float_as_uint(v));
    }
}

constexpr unsigned LP_W3 = 16384;
constexpr unsigned LP_W2 = 12288;
DI void prep_role(const Params& P, lds8* lds, int w, int lane, int p STAMP_ARG) {
    const int tid = w * 64 + lane;
    const int n = lane & 15, gq = lane >> 4;
    const unsigned ldsb = (unsigned)(size_t)lds;
    const __amdgpu_buffer_rsrc_t srdC = __builtin_amdgcn_make_buffer_rsrc((void*)P.wcin, 0, 3 * 17576 * 2, RSRC3);
    const __amdgpu_buffer_rsrc_t srdO = __builtin_amdgcn_make_buffer_rsrc((void*)P.wout, 0, 78 * 2, RSRC3);
    const unsigned epv = ld_agent((const gu32*)(P.ws + WS_EP) + (lane & 15));
    const u32x4 wo3 = __builtin_amdgcn_raw_buffer_load_b128(srdO, (unsigned)(104 + 16 * gq), 0, 0);
    const u32x2 l1 = __builtin_amdgcn_raw_buffer_load_b64(srdC, (unsigned)(p * 1352 + (tid < 169 ? tid : 168) * 8), 0, 0);
    const unsigned o2 = __builtin_amdgcn_raw_buffer_load_b16(srdO, (unsigned)(2 * (26 + (lane < 26 ? lane : 0))), 0, 0);
    const unsigned o1 = __builtin_amdgcn_raw_buffer_load_b16(srdO, (unsigned)(2 * p), 0, 0);
    const int ch = lane < (w == 0 ? 48 : 44) ? lane : 0;
    const int cs = ch >> 1, chh = ch & 1;
    const GAS u32x4* tp = (const GAS u32x4*)&g_tab.v[(cs * 2 + chh) * 8];
    u32x4 ta = tp[0], tb = tp[1];
    unsigned tagv = epv;
    u32x4 wo3v = wo3; u32x2 l1v = l1; unsigned o2v = o2, o1v = o1;
    CBAR();
#pragma unroll
    for (int i = 0; i < 10; ++i) {
        const int id = 64 * i + lane, row = id / 23, cc = id % 23;
        dma16(ldsb + LP_W3 + w * 10240 + i * 1024, (unsigned)((52 + row) * 1352 + 352 * w + cc * 16), srdC, 0);
    }
    if (w == 0) {
#pragma unroll
        for (int i = 0; i < 2; ++i)
            dma16(ldsb + LP_W2 + i * 1024, (unsigned)(35152 + (16 * i + (lane >> 2)) * 1352 + p * 52 + (lane & 3) * 16), srdC, 0);
    }
    STAMP(1);
    asm volatile("s_waitcnt vmcnt(10)" : "+v"(tagv), "+v"(wo3v), "+v"(l1v), "+v"(o2v), "+v"(o1v), "+v"(ta), "+v"(tb) :: "memory");
    LAS unsigned short* vt = (LAS unsigned short*)(lds + LP_VT);
    LAS float* t0 = (LAS float*)(lds + LP_T0);
    LAS float* t1 = (LAS float*)(lds + LP_T1);
    constexpr unsigned VT_DUMMY = 31 * 32 + 31, T_DUMMY = 83;
    vt[tid < 162 ? (tid / 6) * 32 + 26 + tid % 6 : VT_DUMMY] = 0;
    if (w == 3) { vt[lane < 26 ? 26 * 32 + lane : VT_DUMMY] = (unsigned short)o2v; t0[T_ZERO] = 0.f; t1[T_ZERO] = 0.f; t1[T_CONST] = bf_lo(o1v); }
#pragma unroll
    for (int e = 0; e < 4; ++e) {
        const int c = tid * 4 + e;
        t0[tid < 169 ? (c / 26) * 28 + c % 26 : T_DUMMY] = (e & 1) ? bf_hi(l1v[e >> 1]) : bf_lo(l1v[e >> 1]);
    }
    WAIT_LGKM();
    __builtin_amdgcn_s_barrier();
    WAIT_VM0();
    STAMP(2);
    {
        const int q = (lane & 15) >> 2, pp = lane & 3;
        int r0 = 8 * gq + q, r1 = r0 + 4;
        r0 = r0 < 26 ? r0 : 25; r1 = r1 < 26 ? r1 : 25;
        const lds8* b0 = lds + LP_W3 + w * 10240 + r0 * 368 + pp * 8;
        const lds8* b1 = lds + LP_W3 + w * 10240 + r1 * 368 + pp * 8;
        float res[11];
#pragma unroll
        for (int t = 0; t < 11; ++t) {
            const s16x4 lo = __builtin_amdgcn_ds_read_tr16_b64_v4i16((LAS s16x4*)(b0 + t * 32));
            const s16x4 hi = __builtin_amdgcn_ds_read_tr16_b64_v4i16((LAS s16x4*)(b1 + t * 32));
            const f32x4 d = mfma16(as_frag(wo3v), __builtin_shufflevector(lo, hi, 0, 1, 2, 3, 4, 5, 6, 7), (f32x4){0.f, 0.f, 0.f, 0.f});
            res[t] = d[0];
        }
#pragma unroll
        for (int i = 0; i < 3; ++i) {
            const float r3 = i < 2 ? res[4 * i + 3] : res[10];
            const float v = gq == 0 ? res[4 * i] : gq == 1 ? res[4 * i + 1] : gq == 2 ? res[4 * i + 2] : r3;
            const int t = 4 * i + gq;
            const int c = 176 * w + 16 * t + n;
            vt[(t < 11 && c < 676) ? (c % 26) * 32 + c / 26 : VT_DUMMY] = (unsigned short)(pk_bf16(v, v) & 0xffffu);
        }
    }
    CBAR();
    if (lane == 0) lds_add(lds, L_F + 28, 1);
    if (w > 1) return;
    STAMP(3);
    for (unsigned sp = 0; lds_ld(lds, L_F + 28) < 4 && sp < SPIN_CAP; ++sp) __builtin_amdgcn_s_sleep(0);
    CBAR();
    if (w == 0) {
        const int q = (lane & 15) >> 2, pp = lane & 3;
        int r0 = 8 * gq + q, r1 = r0 + 4;
        r0 = r0 < 26 ? r0 : 25; r1 = r1 < 26 ? r1 : 25;
        s16x8 a[2];
#pragma unroll
        for (int mt = 0; mt < 2; ++mt) {
            const s16x4 lo = __builtin_amdgcn_ds_read_tr16_b64_v4i16((LAS s16x4*)(lds + LP_W2 + r0 * 64 + mt * 32 + pp * 8));
            const s16x4 hi = __builtin_amdgcn_ds_read_tr16_b64_v4i16((LAS s16x4*)(lds + LP_W2 + r1 * 64 + mt * 32 + pp * 8));
            a[mt] = __builtin_shufflevector(lo, hi, 0, 1, 2, 3, 4, 5, 6, 7);
        }
#pragma unroll
        for (int u = 0; u < 2; ++u) {
            const u32x4 bf = *(const LAS u32x4*)(lds + LP_VT + (16 * u + n) * 64 + gq * 16);
            const int jp = 16 * u + n;
#pragma unroll
            for (int mt = 0; mt < 2; ++mt) {
                const f32x4 d = mfma16(a[mt], as_frag(bf), (f32x4){0.f, 0.f, 0.f, 0.f});
#pragma unroll
                for (int e = 0; e < 4; ++e) {
                    const int q0 = 16 * mt + 4 * gq + e;
                    t1[(q0 < 26 && jp < 27) ? q0 * 28 + jp : T_DUMMY] = d[e];
                }
            }
        }
    }
    STAMP(4);
    const __amdgpu_buffer_rsrc_t srdA = __builtin_amdgcn_make_buffer_rsrc((void*)(P.ws + WS_A), 0, A_BYTES, RSRC3);
    {
        const LAS float* t = w == 0 ? t1 : t0;
        u32x4 o;
#pragma unroll
        for (int j = 0; j < 4; ++j) {
            const unsigned e0 = j < 2 ? ta[2 * j] : tb[2 * j - 4], e1 = j < 2 ? ta[2 * j + 1] : tb[2 * j - 3];
            o[j] = pk_bf16(t[e0 & 0xffff] + t[e0 >> 16], t[e1 & 0xffff] + t[e1 >> 16]);
        }
        const int fr = w == 0 ? 22 + cs : cs;
        const bool act = lane < (w == 0 ? 48 : 44);
        __builtin_amdgcn_raw_buffer_store_b128(o, srdA, act ? (unsigned)(fr * 832 + (26 * chh + p) * 16) : 0x7ffffff0u, 0, AUX_SC1);
    }
    STAMP(5);
    WAIT_VM0();
    STAMP(6);
    if (w == 1) { if (lane == 0) lds_st(lds, L_F + 24, 1); return; }
    for (unsigned sp = 0; lds_ld(lds, L_F + 24) == 0 && sp < SPIN_CAP; ++sp) __builtin_amdgcn_s_sleep(0);
    if (lane < 16) __hip_atomic_store((gu32*)(P.ws + WS_FL) + lane * 32 + p, tagv + 1u, __ATOMIC_RELAXED, __HIP_MEMORY_SCOPE_AGENT);
}

constexpr unsigned A_PER = A_BYTES / 4;
DI void cin_role(const Params& P, lds8* lds, int cw, int lane, int g, int m, bool has_dnn STAMP_ARG) {
    const int d = lane & 31, h = lane >> 5;
    const int b = 64 * g + 4 * m + cw;
    const unsigned ldsb = (unsigned)(size_t)lds;
    __builtin_amdgcn_s_setprio(3);
    const unsigned ep = ld_agent((const gu32*)(P.ws + WS_EP) + g);
    {
        const GAS unsigned* ip = (const GAS unsigned*)P.idx + b * 26;
        const int q = lane >> 2;
        const unsigned j0 = ip[q], j1 = ip[q < 10 ? 16 + q : 25];
        const __amdgpu_buffer_rsrc_t srdT = __builtin_amdgcn_make_buffer_rsrc((void*)P.table, 0, NVOC * 64, RSRC3);
        const unsigned lb = ldsb + L_A + cw * A_PER;
        STAMP(1);
        dma16(lb, j0 * 64 + (lane & 3) * 16, srdT, 0);
        dma16(lb + 1024, j1 * 64 + (lane & 3) * 16, srdT, 0);
    }
    STAMP(2);
    __builtin_amdgcn_s_setprio(0);
    CBAR();
    __builtin_amdgcn_s_barrier();
    unsigned epv = ep;
    asm volatile("s_waitcnt vmcnt(0)" : "+v"(epv) :: "memory");
    STAMP(3);
    float x[26];
    {
        const lds8* xb = lds + L_A + cw * A_PER + h * 64 + d * 2;
        const lds8* xl = lds + L_A + cw * A_PER + (h ? 0 : 25 * 64) + d * 2;
#pragma unroll
        for (int a = 0; a < 13; ++a) {
            const unsigned lo = *(const LAS unsigned short*)(xb + 2 * a * 64);
            const unsigned hi = a < 12 ? *(const LAS unsigned short*)(xb + (2 * a + 1) * 64) : *(const LAS unsigned short*)xl;
            x[2 * a] = bf_lo(lo); x[2 * a + 1] = bf_lo(hi);
        }
    }
    u32x4 z[24];
#pragma unroll
    for (int s = 0; s < 22; ++s) {
        float fa[8], fb[8];
#pragma unroll
        for (int e = 0; e < 8; ++e) { const AB pe = slot_ab(8 * s + e); fa[e] = x[pe.a]; fb[e] = x[pe.b]; }
        float t0, t1, t2, t3, t4, t5;
        unsigned o0, o1, o2, o3;
        asm("v_mul_f32 %4, %10, %11\n\tv_mul_f32 %5, %12, %13\n\tv_mul_f32 %6, %14, %15\n\tv_mul_f32 %7, %16, %17\n\t"
            "v_cvt_pk_bf16_f32 %0, %4, %5\n\tv_mul_f32 %8, %18, %19\n\tv_mul_f32 %9, %20, %21\n\tv_cvt_pk_bf16_f32 %1, %6, %7\n\t"
            "v_mul_f32 %4, %22, %23\n\tv_mul_f32 %5, %24, %25\n\tv_cvt_pk_bf16_f32 %2, %8, %9\n\tv_cvt_pk_bf16_f32 %3, %4, %5"
            : "=&v"(o0), "=&v"(o1), "=&v"(o2), "=v"(o3), "=&v"(t0), "=&v"(t1), "=&v"(t2), "=&v"(t3), "=&v"(t4), "=&v"(t5)
            : "v"(fa[0]), "v"(fb[0]), "v"(fa[1]), "v"(fb[1]), "v"(fa[2]), "v"(fb[2]), "v"(fa[3]), "v"(fb[3]),
              "v"(fa[4]), "v"(fb[4]), "v"(fa[5]), "v"(fb[5]), "v"(fa[6]), "v"(fb[6]), "v"(fa[7]), "v"(fb[7]));
        z[s] = (u32x4){o0, o1, o2, o3};
    }
#pragma unroll
    for (int s = 22; s < 24; ++s) {
#pragma unroll
        for (int j = 0; j < 4; ++j) {
            const AB p0 = slot_ab(8 * s + 2 * j), p1 = slot_ab(8 * s + 2 * j + 1);
            const float a0 = p0.a < 26 ? x[p0.a] : (p0.a == 26 ? 1.f : 0.f);
            const float a1 = p1.a < 26 ? x[p1.a] : (p1.a == 26 ? 1.f : 0.f);
            z[s][j] = pk_bf16(a0, a1);
        }
    }
#pragma unroll
    for (int s = 0; s < 24; ++s) asm volatile("" : "+v"(z[s]));
    STAMP(4);
    const unsigned tag = epv + 1u;
    {
        const __amdgpu_buffer_rsrc_t srdA = __builtin_amdgcn_make_buffer_rsrc((void*)(P.ws + WS_A), 0, A_BYTES, RSRC3);
        if (cw == 0) {
            const gu32* fl = (const gu32*)(P.ws + WS_FL) + g * 32 + (lane < 26 ? lane : 0);
            for (unsigned sp = 0; sp < SPIN_CAP; ++sp) { if (__all(ld_agent(fl) == tag)) break; __builtin_amdgcn_s_sleep(1); }
            if (lane == 0) lds_st(lds, L_F + 16, 1);
        } else {
            for (unsigned sp = 0; lds_ld(lds, L_F + 16) == 0 && sp < SPIN_CAP; ++sp) __builtin_amdgcn_s_sleep(1);
        }
        if (has_dnn) for (unsigned sp = 0; lds_ld(lds, L_F + 40) < 4 && sp < SPIN_CAP; ++sp) __builtin_amdgcn_s_sleep(1);
        CBAR();
        STAMP(5);
        const unsigned lb = ldsb + L_A + cw * A_PER;
#pragma unroll
        for (int i = 0; i < 9; ++i) dma16_sc1(lb + i * 1024, lane * 16, srdA, cw * A_PER + i * 1024);
        if (lane < 22) dma16_sc1(lb + 9 * 1024, lane * 16, srdA, cw * A_PER + 9 * 1024);
        WAIT_VM0();
        if (lane == 0) lds_add(lds, L_F + 20, 1);
        for (unsigned sp = 0; lds_ld(lds, L_F + 20) < 4 && sp < SPIN_CAP; ++sp) __builtin_amdgcn_s_sleep(0);
        CBAR();
    }
    STAMP(6);
    const gu64* pp = (const gu64*)(P.ws + WS_PART) + ((g * 32 + (lane < 26 ? lane : 0)) * 64 + 4 * m + cw);
    unsigned long long pv = 0;
    f32x16 acc0, acc1;
#pragma unroll
    for (int i = 0; i < 16; ++i) { acc0[i] = 0.f; acc1[i] = 0.f; }
    {
        unsigned abo = L_A + (26 * h + d) * 16;
        asm volatile("" : "+v"(abo));
        const lds8* ab = lds + abo;
        constexpr int PF = 6;
        u32x4 af[46];
        auto frag_of = [](int t) { return t < 44 ? ((t & 1) ? (t >> 1) : 22 + (t >> 1)) : 22 + (t - 22); };
#pragma unroll
        for (int t = 0; t < PF; ++t) af[t] = *(const LAS u32x4*)(ab + frag_of(t) * 832);
        __builtin_amdgcn_sched_barrier(0);
#pragma unroll
        for (int t = 0; t < 46; ++t) {
            if (t + PF < 46) af[t + PF] = *(const LAS u32x4*)(ab + frag_of(t + PF) * 832);
            if (t < 44) {
                if (t & 1) acc0 = mfma32(as_frag(af[t]), as_frag(z[t >> 1]), acc0);
                else acc1 = mfma32(as_frag(af[t]), as_frag(z[t >> 1]), acc1);
            } else acc1 = mfma32(as_frag(af[t]), as_frag(z[t - 22]), acc1);
            if (t == 30) pv = __hip_atomic_load(pp, __ATOMIC_RELAXED, __HIP_MEMORY_SCOPE_AGENT);
            __builtin_amdgcn_sched_barrier(0);
        }
    }
    float sa = 0.f;
#pragma unroll
    for (int i = 0; i < 12; ++i) sa = __builtin_fmaf(acc0[i], acc1[i], sa);
    const float sb = __builtin_fmaf(acc0[12], acc1[12], acc0[13] * acc1[13]);
    float tot = h == 0 ? sa + sb : sa;
    asm volatile("" : "+v"(tot));
    STAMP(7);
    for (unsigned sp = 0; sp < SPIN_CAP; ++sp) {
        if (__all((unsigned)(pv >> 32) == tag)) break;
        pv = __hip_atomic_load(pp, __ATOMIC_RELAXED, __HIP_MEMORY_SCOPE_AGENT);
    }
    STAMP(8);
    if (lane < 26) tot += __uint_as_float((unsigned)pv);
    tot = wave_sum_to_lane63(tot);
    if (lane == 63) {
        const float o = tot + bf_lo(P.bout[0]);
        P.out[b] = (unsigned short)(pk_bf16(o, o) & 0xffffu);
    }
    if (m == 0 && cw == 0) {
        const gu64* ap = (const gu64*)(P.ws + WS_PART) + ((g * 32 + 26 + (lane < 3 ? lane : 0)) * 64);
        for (unsigned sp = 0; sp < SPIN_CAP; ++sp) {
            const unsigned long long av = __hip_atomic_load(ap, __ATOMIC_RELAXED, __HIP_MEMORY_SCOPE_AGENT);
            if (__all((unsigned)(av >> 32) == tag)) break;
        }
        if (lane == 0) __hip_atomic_store((gu32*)(P.ws + WS_EP) + g, tag, __ATOMIC_RELAXED, __HIP_MEMORY_SCOPE_AGENT);
    }
}

DI void decode_block(int bid, int& g, int& m, int& ntile, int& k) {
    const int x = bid & 7, j = bid >> 3;
    g = 2 * x + (j >> 4); m = j & 15;
    ntile = m < 13 ? m : -1; k = m - 13;
}

__global__ void __launch_bounds__(512, 2) xdfm_kernel(Params P) {
    __shared__ __attribute__((aligned(16))) unsigned char smem[L_END];
    lds8* lds = (lds8*)smem;
    STAMP(0);
    asm volatile("" :: "s"(P.idx), "s"(P.table), "s"(P.wcin), "s"(P.wout), "s"(P.w1), "s"(P.b1), "s"(P.w2), "s"(P.bout), "s"(P.out), "s"(P.ws));
    const int w = __builtin_amdgcn_readfirstlane(threadIdx.x >> 6), lane = threadIdx.x & 63;
    int g, m, ntile, k;
    decode_block(blockIdx.x, g, m, ntile, k);
    if (w < 4) {
        if (w == 0 && lane < 16) lds_st(lds, L_F + 4 * lane, 0);
        if (ntile >= 0) { dnn_role(P, lds, w, lane, g, ntile STAMP_PASS); }
        else {
            const int p = g * 3 + k;
            unsigned ep = ld_agent((const gu32*)(P.ws + WS_EP) + g);
            if (p < 26) prep_role(P, lds, w, lane, p STAMP_PASS);
            else { asm volatile("s_waitcnt vmcnt(0) lgkmcnt(0)" : "+v"(ep) :: "memory"); __builtin_amdgcn_s_barrier(); }
            if (w == 0 && lane == 0) st_granule((gu64*)(P.ws + WS_PART) + ((g * 32 + 26 + k) * 64), ep + 1u, 0u);
        }
    } else {
        cin_role(P, lds, w - 4, lane, g, m, ntile >= 0 STAMP_PASS);
    }
    STAMP(11);
    STAMP_FLUSH(P.ws, w, lane);
}

extern "C" void kernel_launch(void* const* d_in, const int* in_sizes, int n_in,
                              void* d_out, int out_size, void* d_ws, size_t ws_size,
                              hipStream_t stream) {
    Params P;
    P.idx = (const int*)d_in[0]; P.table = (const unsigned short*)d_in[1]; P.wcin = (const unsigned short*)d_in[2];
    P.wout = (const unsigned short*)d_in[3]; P.w1 = (const unsigned short*)d_in[4]; P.b1 = (const unsigned short*)d_in[5];
    P.w2 = (const unsigned short*)d_in[6]; P.bout = (const unsigned short*)d_in[7];
    P.out = (unsigned short*)d_out; P.ws = (unsigned char*)d_ws;
    xdfm_kernel<<<256, 512, 0, stream>>>(P);
}
```

```cpp
#define AMP_BLK 8
#define AMP_W 0
#define AMP_J 1
#define AMP_K 4
#include <hip/hip_runtime.h>
#include <stdint.h>

#define DI __device__ __forceinline__
#define GAS __attribute__((address_space(1)))
#define LAS __attribute__((address_space(3)))

typedef short    s16x4 __attribute__((ext_vector_type(4)));
typedef short    s16x8 __attribute__((ext_vector_type(8)));
typedef float    f32x2 __attribute__((ext_vector_type(2)));
typedef float    f32x4 __attribute__((ext_vector_type(4)));
typedef float    f32x16 __attribute__((ext_vector_type(16)));
typedef unsigned u32x2 __attribute__((ext_vector_type(2)));
typedef unsigned u32x4 __attribute__((ext_vector_type(4)));
typedef __bf16   bf16x2 __attribute__((ext_vector_type(2)));
typedef GAS unsigned gu32;
typedef GAS unsigned long long gu64;
typedef LAS unsigned char lds8;

#define MAPPING 1

constexpr int DIN = 832, NBATCH = 1024, NVOC = 100000;
#define A_AFTER_X 1
#define W_EARLY 8
#define XD 6
#define PKMUL 0
#define A_DMA 1
constexpr unsigned RSRC3 = 0x00020000u;
constexpr int AUX_SC1 = 16;
constexpr unsigned SPIN_CAP = 1u << 20;

constexpr unsigned WS_EP = 0;
constexpr unsigned WS_FL = 4096;
constexpr unsigned WS_PART = 8192;
constexpr unsigned WS_A = 8192 + 16 * 32 * 64 * 8;
constexpr unsigned A_BYTES = 46 * 832;

constexpr unsigned L_W = 0;
constexpr unsigned L_A = 832 * 128;
constexpr unsigned L_X = L_A + A_BYTES;
constexpr unsigned L_F = L_X + 16384;
constexpr unsigned L_END = L_F + 256;
constexpr unsigned LP_VT = 0;
constexpr unsigned LP_T0 = 2048;
constexpr unsigned LP_T1 = 2048 + 3072;

struct AB { int a, b; };
constexpr AB slot_ab(int t) {
    if (t < 156) { int a = 2 * (t / 12); return {a, (a + t % 12) % 26}; }
    if (t < 170) { int u = t - 156; int a = 2 * (u / 2); return {a, (a + 12 + (u & 1)) % 26}; }
    if (t < 176) { int a = 2 * (7 + t - 170); return {a, (a + 12) % 26}; }
    if (t < 189) return {2 * (t - 176), 26};
    if (t == 189) return {26, 26};
    return {27, 27};
}
constexpr unsigned T_ZERO = 27, T_CONST = 55;
struct PrepTab { unsigned v[24 * 2 * 8]; };
constexpr PrepTab make_tab() {
    PrepTab tb{};
    for (int s = 0; s < 24; ++s) for (int h = 0; h < 2; ++h) for (int e = 0; e < 8; ++e) {
        int t = 8 * s + e; AB ab = slot_ab(t);
        unsigned o1 = T_ZERO, o2 = T_ZERO;
        if (ab.a == 27) {}
        else if (ab.a == 26) { if (h == 0) o1 = T_CONST; }
        else if (ab.b == 26) { o1 = ((ab.a + h) % 26) * 28 + 26; }
        else {
            int u = (ab.a + h) % 26, v = (ab.b + h) % 26;
            bool dup = (t == 169 && h == 1);
            if (dup) {}
            else if (u == v) o1 = u * 28 + u;
            else { o1 = u * 28 + v; o2 = v * 28 + u; }
        }
        tb.v[(s * 2 + h) * 8 + e] = o1 | (o2 << 16);
    }
    return tb;
}
static_assert(slot_ab(169).a == 12 && slot_ab(169).b == 25, "dup slot");
__device__ const PrepTab g_tab = make_tab();

struct Params {
    const int* idx; const unsigned short* table; const unsigned short* wcin; const unsigned short* wout;
    const unsigned short* w1; const unsigned short* b1; const unsigned short* w2; const unsigned short* bout;
    unsigned short* out; unsigned char* ws;
};

DI float bf_lo(unsigned v) { return __uint_as_float(v << 16); }
DI float bf_hi(unsigned v) { return __uint_as_float(v & 0xffff0000u); }
DI unsigned pk_bf16(float lo, float hi) { f32x2 v = {lo, hi}; return __builtin_bit_cast(unsigned, __builtin_convertvector(v, bf16x2)); }
DI f32x4 mfma16(s16x8 a, s16x8 b, f32x4 c) { return __builtin_amdgcn_mfma_f32_16x16x32_bf16(a, b, c, 0, 0, 0); }
DI f32x16 mfma32(s16x8 a, s16x8 b, f32x16 c) { return __builtin_amdgcn_mfma_f32_32x32x16_bf16(a, b, c, 0, 0, 0); }
DI s16x8 as_frag(u32x4 v) { return __builtin_bit_cast(s16x8, v); }
template <int CTRL, int ROWMASK> DI float dpp_add(float v) {
    return v + __int_as_float(__builtin_amdgcn_update_dpp(0, __float_as_int(v), CTRL, ROWMASK, 0xf, true));
}
DI float wave_sum_to_lane63(float v) {
    v = dpp_add<0x111, 0xf>(v); v = dpp_add<0x112, 0xf>(v); v = dpp_add<0x114, 0xf>(v); v = dpp_add<0x118, 0xf>(v);
    v = dpp_add<0x142, 0xa>(v); v = dpp_add<0x143, 0xc>(v);
    return v;
}
DI float sum_lane_groups(float v) {
    auto a = __builtin_amdgcn_permlane16_swap(__float_as_uint(v), __float_as_uint(v), false, false);
    v = __uint_as_float(a[0]) + __uint_as_float(a[1]);
    auto b = __builtin_amdgcn_permlane32_swap(__float_as_uint(v), __float_as_uint(v), false, false);
    return __uint_as_float(b[0]) + __uint_as_float(b[1]);
}
DI float gelu_tanh(float x) {
    const float k1 = -2.0f * 0.7978845608028654f * 1.4426950408889634f;
    const float k2 = k1 * 0.044715f;
    float arg = x * __builtin_fmaf(x * x, k2, k1);
    float e = __builtin_amdgcn_exp2f(arg);
    return x * __builtin_amdgcn_rcpf(1.0f + e);
}
DI void dma16(unsigned lds_addr, unsigned voff, __amdgpu_buffer_rsrc_t srd, unsigned soff) {
    asm volatile("s_mov_b32 m0, %0\n\ts_nop 0\n\tbuffer_load_dwordx4 %1, %2, %3 offen lds"
                 :: "s"(lds_addr), "v"(voff), "s"(srd), "s"(soff) : "memory");
}
DI void dma16_sc1(unsigned lds_addr, unsigned voff, __amdgpu_buffer_rsrc_t srd, unsigned soff) {
    asm volatile("s_mov_b32 m0, %0\n\ts_nop 0\n\tbuffer_load_dwordx4 %1, %2, %3 offen sc1 lds"
                 :: "s"(lds_addr), "v"(voff), "s"(srd), "s"(soff) : "memory");
}
DI void st_granule(gu64* g, unsigned tag, unsigned val) {
    __hip_atomic_store(g, ((unsigned long long)tag << 32) | val, __ATOMIC_RELAXED, __HIP_MEMORY_SCOPE_AGENT);
}
DI unsigned ld_agent(const gu32* p) { return __hip_atomic_load(p, __ATOMIC_RELAXED, __HIP_MEMORY_SCOPE_AGENT); }
DI unsigned lds_ld(lds8* l, unsigned off) { return __hip_atomic_load((LAS unsigned*)(l + off), __ATOMIC_RELAXED, __HIP_MEMORY_SCOPE_WORKGROUP); }
DI void lds_st(lds8* l, unsigned off, unsigned v) { __hip_atomic_store((LAS unsigned*)(l + off), v, __ATOMIC_RELAXED, __HIP_MEMORY_SCOPE_WORKGROUP); }
DI void lds_add(lds8* l, unsigned off, unsigned v) { __hip_atomic_fetch_add((LAS unsigned*)(l + off), v, __ATOMIC_RELAXED, __HIP_MEMORY_SCOPE_WORKGROUP); }
#define WAIT_LGKM() asm volatile("s_waitcnt lgkmcnt(0)" ::: "memory")
#define WAIT_VM0() asm volatile("s_waitcnt vmcnt(0)" ::: "memory")
#define CBAR() asm volatile("" ::: "memory")
struct Stamps { unsigned long long t0, t1; };
#define STAMP(i) do { if ((i) == 0) { asm volatile("s_memrealtime %0\n\ts_waitcnt lgkmcnt(0)" : "=s"(st.t0) :: "memory"); } else if ((i) == AMP_J) { asm volatile("s_memrealtime %0\n\ts_waitcnt lgkmcnt(0)" : "=s"(st.t1) :: "memory"); } } while (0)
#define STAMP_ARG , Stamps& st
#define STAMP_PASS , st
#define STAMP_FLUSH(ws, w, lane) do { if (blockIdx.x == AMP_BLK && (w) == AMP_W) { const unsigned long long d_ = (st.t1 - st.t0) * AMP_K; const unsigned long long e_ = __builtin_amdgcn_s_memrealtime(); while (__builtin_amdgcn_s_memrealtime() - e_ < d_) __builtin_amdgcn_s_sleep(4); } } while (0)

template <int K> DI unsigned quad_bcast(unsigned v) { return __builtin_amdgcn_update_dpp(0u, v, K * 0x55, 0xf, 0xf, true); }
DI void dnn_role(const Params& P, lds8* lds, int w, int lane, int g, int ntile STAMP_ARG) {
    const int mh = w & 1, kh = w >> 1;
    const int gq = lane >> 4;
    const unsigned ldsb = (unsigned)(size_t)lds;
    u32x4 i0, i1; unsigned ep; u32x2 bia0, bia1, wv0, wv1;
    {
        const __amdgpu_buffer_rsrc_t srdI = __builtin_amdgcn_make_buffer_rsrc((void*)P.idx, 0, NBATCH * 26 * 4, RSRC3);
        const unsigned vo = ((64 * g + 32 * mh + (lane >> 2)) * 26 + 13 * kh + 4 * (lane & 3)) * 4;
        const unsigned nb = (ntile * 64 + kh * 32 + 4 * gq) * 2;
        asm volatile("buffer_load_dwordx4 %0, %7, %8, 0 offen\n\tbuffer_load_dwordx4 %1, %7, %8, 0 offen offset:1664\n\t"
                     "global_load_dword %2, %9, %10 sc1\n\t"
                     "global_load_dwordx2 %3, %11, %12\n\tglobal_load_dwordx2 %4, %11, %12 offset:32\n\t"
                     "global_load_dwordx2 %5, %11, %13\n\tglobal_load_dwordx2 %6, %11, %13 offset:32"
                     : "=&v"(i0), "=&v"(i1), "=&v"(ep), "=&v"(bia0), "=&v"(bia1), "=&v"(wv0), "=&v"(wv1)
                     : "v"(vo), "s"(srdI), "v"(g * 4), "s"(P.ws + WS_EP), "v"(nb), "s"(P.b1), "s"(P.w2) : "memory");
    }
    const __amdgpu_buffer_rsrc_t srdW = __builtin_amdgcn_make_buffer_rsrc((void*)P.w1, 0, DIN * DIN * 2, RSRC3);
    const unsigned sw0 = (((lane >> 4) & 1)) << 1, sw1 = (((lane >> 4) & 1) | 2) << 1;
    const unsigned v0 = (lane >> 3) * 1664 + (((lane & 7) ^ sw0) << 4), v1 = (lane >> 3) * 1664 + (((lane & 7) ^ sw1) << 4);
    const unsigned row0 = 416 * kh + 208 * mh;
    const unsigned sbase = row0 * 1664 + ntile * 128, lbase = ldsb + L_W + row0 * 128;
#pragma unroll
    for (int jj = 0; jj < W_EARLY; ++jj) dma16(lbase + jj * 1024, (jj & 1) ? v1 : v0, srdW, sbase + jj * 13312);
    STAMP(1);
    asm volatile("s_waitcnt vmcnt(%7) lgkmcnt(0)" : "+v"(i0), "+v"(i1), "+v"(ep), "+v"(bia0), "+v"(bia1), "+v"(wv0), "+v"(wv1) : "i"(W_EARLY) : "memory");
    __builtin_amdgcn_s_barrier();
    STAMP(2);
#pragma unroll
    for (int jj = W_EARLY; jj < 26; ++jj) dma16(lbase + jj * 1024, (jj & 1) ? v1 : v0, srdW, sbase + jj * 13312);
    u32x4 xl[2][13];
    const GAS unsigned char* tb = (const GAS unsigned char*)P.table;
    const unsigned c16 = (((lane & 3) ^ ((0u - (unsigned)gq) & 3u)) << 4);
    const u32x4 t0 = i0 << 6, t1 = i1 << 6;
    auto xload = [&](int s) {
        const unsigned a0 = t0[s & 3], a1 = t1[s & 3];
        const unsigned o0 = (s < 4 ? quad_bcast<0>(a0) : s < 8 ? quad_bcast<1>(a0) : s < 12 ? quad_bcast<2>(a0) : quad_bcast<3>(a0)) + c16;
        const unsigned o1 = (s < 4 ? quad_bcast<0>(a1) : s < 8 ? quad_bcast<1>(a1) : s < 12 ? quad_bcast<2>(a1) : quad_bcast<3>(a1)) + c16;
        xl[0][s] = *(const GAS u32x4*)(tb + o0);
        xl[1][s] = *(const GAS u32x4*)(tb + o1);
    };
#pragma unroll
    for (int s = 0; s < XD; ++s) xload(s);
    STAMP(3);
    asm volatile("s_waitcnt vmcnt(%0)" :: "i"(2 * XD) : "memory");
    if (lane == 0) lds_add(lds, L_F + 32 + 4 * kh, 1);
    for (unsigned sp = 0; lds_ld(lds, L_F + 32 + 4 * kh) < 2 && sp < SPIN_CAP; ++sp) __builtin_amdgcn_s_sleep(0);
    CBAR();
    STAMP(4);
    f32x4 acc[2][4];
#pragma unroll
    for (int mt = 0; mt < 2; ++mt) {
        acc[mt][0] = (f32x4){bf_lo(bia0[0]), bf_hi(bia0[0]), bf_lo(bia0[1]), bf_hi(bia0[1])};
        acc[mt][1] = (f32x4){bf_lo(bia1[0]), bf_hi(bia1[0]), bf_lo(bia1[1]), bf_hi(bia1[1])};
        acc[mt][2] = (f32x4){0.f, 0.f, 0.f, 0.f};
        acc[mt][3] = (f32x4){0.f, 0.f, 0.f, 0.f};
    }
    {
        const int q = (lane & 15) >> 2, pp = lane & 3;
        const unsigned swz = ((q >> 1) | ((gq & 1) << 1)) << 1;
        unsigned ad[4];
#pragma unroll
        for (int j = 0; j < 4; ++j) {
            const unsigned c = 2 * (j ^ (2 * kh)) + (pp >> 1);
            ad[j] = L_W + (416 * kh + 8 * gq + q) * 128 + ((c ^ swz) << 4) + (pp & 1) * 8;
        }
        lds8* stw = lds + L_X + w * 4096 + lane * 16;
        const lds8* str = lds + L_X + w * 4096 + (lane & 15) * 64 + ((gq ^ ((0u - (unsigned)q) & 3u)) << 4);
        u32x4 xf[3][2];
        s16x8 wf[2][4];
        auto stage = [&](int s) {
            *(LAS u32x4*)(stw + (s & 1) * 2048) = xl[0][s];
            *(LAS u32x4*)(stw + (s & 1) * 2048 + 1024) = xl[1][s];
            xf[s % 3][0] = *(const LAS u32x4*)(str + (s & 1) * 2048);
            xf[s % 3][1] = *(const LAS u32x4*)(str + (s & 1) * 2048 + 1024);
        };
        auto wread = [&](int s) {
#pragma unroll
            for (int j = 0; j < 4; ++j) {
                const s16x4 lo = __builtin_amdgcn_ds_read_tr16_b64_v4i16((LAS s16x4*)(lds + ad[j] + s * 4096));
                const s16x4 hi = __builtin_amdgcn_ds_read_tr16_b64_v4i16((LAS s16x4*)(lds + ad[j] + s * 4096 + 512));
                wf[s & 1][j] = __builtin_shufflevector(lo, hi, 0, 1, 2, 3, 4, 5, 6, 7);
            }
        };
        wread(0);
        stage(0);
        stage(1);
        __builtin_amdgcn_sched_barrier(0);
#pragma unroll
        for (int s = 0; s < 13; ++s) {
            if (s + XD < 13) xload(s + XD);
            if (s + XD == 12 || (XD >= 13 && s == 0)) { CBAR(); if (lane == 0) lds_add(lds, L_F + 40, 1); }
            __builtin_amdgcn_sched_barrier(0);
            if (s + 1 < 13) wread(s + 1);
#pragma unroll
            for (int j = 0; j < 4; ++j) {
                acc[0][j] = mfma16(wf[s & 1][j], as_frag(xf[s % 3][0]), acc[0][j]);
                acc[1][j] = mfma16(wf[s & 1][j], as_frag(xf[s % 3][1]), acc[1][j]);
            }
            if (s + 1 < 13) {
#pragma unroll
                for (int j = 0; j < 8; ++j) { __builtin_amdgcn_sched_group_barrier(0x100, 1, 0); __builtin_amdgcn_sched_group_barrier(0x8, 1, 0); }
            }
            __builtin_amdgcn_sched_barrier(0);
            if (s + 2 < 13) stage(s + 2);
            __builtin_amdgcn_sched_barrier(0);
        }
    }
    STAMP(6);
    {
        lds8* xw = lds + L_X + w * 4096 + lane * 16;
        *(LAS f32x4*)(xw) = acc[0][2]; *(LAS f32x4*)(xw + 1024) = acc[0][3]; *(LAS f32x4*)(xw + 2048) = acc[1][2]; *(LAS f32x4*)(xw + 3072) = acc[1][3];
        CBAR();
        if (lane == 0) lds_st(lds, L_F + 4 * w, 1);
        for (unsigned sp = 0; lds_ld(lds, L_F + 4 * (w ^ 2)) == 0 && sp < SPIN_CAP; ++sp) __builtin_amdgcn_s_sleep(0);
        CBAR();
        const lds8* xr = lds + L_X + (w ^ 2) * 4096 + lane * 16;
        acc[0][0] += *(const LAS f32x4*)(xr); acc[0][1] += *(const LAS f32x4*)(xr + 1024);
        acc[1][0] += *(const LAS f32x4*)(xr + 2048); acc[1][1] += *(const LAS f32x4*)(xr + 3072);
    }
    STAMP(7);
    const float w20[4] = {bf_lo(wv0[0]), bf_hi(wv0[0]), bf_lo(wv0[1]), bf_hi(wv0[1])};
    const float w21[4] = {bf_lo(wv1[0]), bf_hi(wv1[0]), bf_lo(wv1[1]), bf_hi(wv1[1])};
    float part[2];
#pragma unroll
    for (int mt = 0; mt < 2; ++mt) {
        float s = 0.f;
#pragma unroll
        for (int e = 0; e < 4; ++e) { s = __builtin_fmaf(gelu_tanh(acc[mt][0][e]), w20[e], s); s = __builtin_fmaf(gelu_tanh(acc[mt][1][e]), w21[e], s); }
        part[mt] = sum_lane_groups(s);
    }
    STAMP(8);
    if (lane < 32) {
        const float v = gq ? part[1] : part[0];
        st_granule((gu64*)(P.ws + WS_PART) + ((g * 32 + 2 * ntile + kh) * 64 + 32 * mh + lane), ep + 1u, __float_as_uint(v));
    }
}

constexpr unsigned LP_W3 = 16384;
constexpr unsigned LP_W2 = 12288;
DI void prep_role(const Params& P, lds8* lds, int w, int lane, int p STAMP_ARG) {
    const int tid = w * 64 + lane;
    const int n = lane & 15, gq = lane >> 4;
    const unsigned ldsb = (unsigned)(size_t)lds;
    const __amdgpu_buffer_rsrc_t srdC = __builtin_amdgcn_make_buffer_rsrc((void*)P.wcin, 0, 3 * 17576 * 2, RSRC3);
    const __amdgpu_buffer_rsrc_t srdO = __builtin_amdgcn_make_buffer_rsrc((void*)P.wout, 0, 78 * 2, RSRC3);
    const unsigned epv = ld_agent((const gu32*)(P.ws + WS_EP) + (lane & 15));
    const u32x4 wo3 = __builtin_amdgcn_raw_buffer_load_b128(srdO, (unsigned)(104 + 16 * gq), 0, 0);
    const u32x2 l1 = __builtin_amdgcn_raw_buffer_load_b64(srdC, (unsigned)(p * 1352 + (tid < 169 ? tid : 168) * 8), 0, 0);
    const unsigned o2 = __builtin_amdgcn_raw_buffer_load_b16(srdO, (unsigned)(2 * (26 + (lane < 26 ? lane : 0))), 0, 0);
    const unsigned o1 = __builtin_amdgcn_raw_buffer_load_b16(srdO, (unsigned)(2 * p), 0, 0);
    const int ch = lane < (w == 0 ? 48 : 44) ? lane : 0;
    const int cs = ch >> 1, chh = ch & 1;
    const GAS u32x4* tp = (const GAS u32x4*)&g_tab.v[(cs * 2 + chh) * 8];
    u32x4 ta = tp[0], tb = tp[1];
    unsigned tagv = epv;
    u32x4 wo3v = wo3; u32x2 l1v = l1; unsigned o2v = o2, o1v = o1;
    CBAR();
#pragma unroll
    for (int i = 0; i < 10; ++i) {
        const int id = 64 * i + lane, row = id / 23, cc = id % 23;
        dma16(ldsb + LP_W3 + w * 10240 + i * 1024, (unsigned)((52 + row) * 1352 + 352 * w + cc * 16), srdC, 0);
    }
    if (w == 0) {
#pragma unroll
        for (int i = 0; i < 2; ++i)
            dma16(ldsb + LP_W2 + i * 1024, (unsigned)(35152 + (16 * i + (lane >> 2)) * 1352 + p * 52 + (lane & 3) * 16), srdC, 0);
    }
    STAMP(1);
    asm volatile("s_waitcnt vmcnt(10)" : "+v"(tagv), "+v"(wo3v), "+v"(l1v), "+v"(o2v), "+v"(o1v), "+v"(ta), "+v"(tb) :: "memory");
    LAS unsigned short* vt = (LAS unsigned short*)(lds + LP_VT);
    LAS float* t0 = (LAS float*)(lds + LP_T0);
    LAS float* t1 = (LAS float*)(lds + LP_T1);
    constexpr unsigned VT_DUMMY = 31 * 32 + 31, T_DUMMY = 83;
    vt[tid < 162 ? (tid / 6) * 32 + 26 + tid % 6 : VT_DUMMY] = 0;
    if (w == 3) { vt[lane < 26 ? 26 * 32 + lane : VT_DUMMY] = (unsigned short)o2v; t0[T_ZERO] = 0.f; t1[T_ZERO] = 0.f; t1[T_CONST] = bf_lo(o1v); }
#pragma unroll
    for (int e = 0; e < 4; ++e) {
        const int c = tid * 4 + e;
        t0[tid < 169 ? (c / 26) * 28 + c % 26 : T_DUMMY] = (e & 1) ? bf_hi(l1v[e >> 1]) : bf_lo(l1v[e >> 1]);
    }
    WAIT_LGKM();
    __builtin_amdgcn_s_barrier();
    WAIT_VM0();
    STAMP(2);
    {
        const int q = (lane & 15) >> 2, pp = lane & 3;
        int r0 = 8 * gq + q, r1 = r0 + 4;
        r0 = r0 < 26 ? r0 : 25; r1 = r1 < 26 ? r1 : 25;
        const lds8* b0 = lds + LP_W3 + w * 10240 + r0 * 368 + pp * 8;
        const lds8* b1 = lds + LP_W3 + w * 10240 + r1 * 368 + pp * 8;
        float res[11];
#pragma unroll
        for (int t = 0; t < 11; ++t) {
            const s16x4 lo = __builtin_amdgcn_ds_read_tr16_b64_v4i16((LAS s16x4*)(b0 + t * 32));
            const s16x4 hi = __builtin_amdgcn_ds_read_tr16_b64_v4i16((LAS s16x4*)(b1 + t * 32));
            const f32x4 d = mfma16(as_frag(wo3v), __builtin_shufflevector(lo, hi, 0, 1, 2, 3, 4, 5, 6, 7), (f32x4){0.f, 0.f, 0.f, 0.f});
            res[t] = d[0];
        }
#pragma unroll
        for (int i = 0; i < 3; ++i) {
            const float r3 = i < 2 ? res[4 * i + 3] : res[10];
            const float v = gq == 0 ? res[4 * i] : gq == 1 ? res[4 * i + 1] : gq == 2 ? res[4 * i + 2] : r3;
            const int t = 4 * i + gq;
            const int c = 176 * w + 16 * t + n;
            vt[(t < 11 && c < 676) ? (c % 26) * 32 + c / 26 : VT_DUMMY] = (unsigned short)(pk_bf16(v, v) & 0xffffu);
        }
    }
    CBAR();
    if (lane == 0) lds_add(lds, L_F + 28, 1);
    if (w > 1) return;
    STAMP(3);
    for (unsigned sp = 0; lds_ld(lds, L_F + 28) < 4 && sp < SPIN_CAP; ++sp) __builtin_amdgcn_s_sleep(0);
    CBAR();
    if (w == 0) {
        const int q = (lane & 15) >> 2, pp = lane & 3;
        int r0 = 8 * gq + q, r1 = r0 + 4;
        r0 = r0 < 26 ? r0 : 25; r1 = r1 < 26 ? r1 : 25;
        s16x8 a[2];
#pragma unroll
        for (int mt = 0; mt < 2; ++mt) {
            const s16x4 lo = __builtin_amdgcn_ds_read_tr16_b64_v4i16((LAS s16x4*)(lds + LP_W2 + r0 * 64 + mt * 32 + pp * 8));
            const s16x4 hi = __builtin_amdgcn_ds_read_tr16_b64_v4i16((LAS s16x4*)(lds + LP_W2 + r1 * 64 + mt * 32 + pp * 8));
            a[mt] = __builtin_shufflevector(lo, hi, 0, 1, 2, 3, 4, 5, 6, 7);
        }
#pragma unroll
        for (int u = 0; u < 2; ++u) {
            const u32x4 bf = *(const LAS u32x4*)(lds + LP_VT + (16 * u + n) * 64 + gq * 16);
            const int jp = 16 * u + n;
#pragma unroll
            for (int mt = 0; mt < 2; ++mt) {
                const f32x4 d = mfma16(a[mt], as_frag(bf), (f32x4){0.f, 0.f, 0.f, 0.f});
#pragma unroll
                for (int e = 0; e < 4; ++e) {
                    const int q0 = 16 * mt + 4 * gq + e;
                    t1[(q0 < 26 && jp < 27) ? q0 * 28 + jp : T_DUMMY] = d[e];
                }
            }
        }
    }
    STAMP(4);
    const __amdgpu_buffer_rsrc_t srdA = __builtin_amdgcn_make_buffer_rsrc((void*)(P.ws + WS_A), 0, A_BYTES, RSRC3);
    {
        const LAS float* t = w == 0 ? t1 : t0;
        u32x4 o;
#pragma unroll
        for (int j = 0; j < 4; ++j) {
            const unsigned e0 = j < 2 ? ta[2 * j] : tb[2 * j - 4], e1 = j < 2 ? ta[2 * j + 1] : tb[2 * j - 3];
            o[j] = pk_bf16(t[e0 & 0xffff] + t[e0 >> 16], t[e1 & 0xffff] + t[e1 >> 16]);
        }
        const int fr = w == 0 ? 22 + cs : cs;
        const bool act = lane < (w == 0 ? 48 : 44);
        __builtin_amdgcn_raw_buffer_store_b128(o, srdA, act ? (unsigned)(fr * 832 + (26 * chh + p) * 16) : 0x7ffffff0u, 0, AUX_SC1);
    }
    STAMP(5);
    WAIT_VM0();
    STAMP(6);
    if (w == 1) { if (lane == 0) lds_st(lds, L_F + 24, 1); return; }
    for (unsigned sp = 0; lds_ld(lds, L_F + 24) == 0 && sp < SPIN_CAP; ++sp) __builtin_amdgcn_s_sleep(0);
    if (lane < 16) __hip_atomic_store((gu32*)(P.ws + WS_FL) + lane * 32 + p, tagv + 1u, __ATOMIC_RELAXED, __HIP_MEMORY_SCOPE_AGENT);
}

constexpr unsigned A_PER = A_BYTES / 4;
DI void cin_role(const Params& P, lds8* lds, int cw, int lane, int g, int m, bool has_dnn STAMP_ARG) {
    const int d = lane & 31, h = lane >> 5;
    const int b = 64 * g + 4 * m + cw;
    const unsigned ldsb = (unsigned)(size_t)lds;
    __builtin_amdgcn_s_setprio(3);
    const unsigned ep = ld_agent((const gu32*)(P.ws + WS_EP) + g);
    {
        const GAS unsigned* ip = (const GAS unsigned*)P.idx + b * 26;
        const int q = lane >> 2;
        const unsigned j0 = ip[q], j1 = ip[q < 10 ? 16 + q : 25];
        const __amdgpu_buffer_rsrc_t srdT = __builtin_amdgcn_make_buffer_rsrc((void*)P.table, 0, NVOC * 64, RSRC3);
        const unsigned lb = ldsb + L_A + cw * A_PER;
        STAMP(1);
        dma16(lb, j0 * 64 + (lane & 3) * 16, srdT, 0);
        dma16(lb + 1024, j1 * 64 + (lane & 3) * 16, srdT, 0);
    }
    STAMP(2);
    __builtin_amdgcn_s_setprio(0);
    CBAR();
    __builtin_amdgcn_s_barrier();
    unsigned epv = ep;
    asm volatile("s_waitcnt vmcnt(0)" : "+v"(epv) :: "memory");
    STAMP(3);
    float x[26];
    {
        const lds8* xb = lds + L_A + cw * A_PER + h * 64 + d * 2;
        const lds8* xl = lds + L_A + cw * A_PER + (h ? 0 : 25 * 64) + d * 2;
#pragma unroll
        for (int a = 0; a < 13; ++a) {
            const unsigned lo = *(const LAS unsigned short*)(xb + 2 * a * 64);
            const unsigned hi = a < 12 ? *(const LAS unsigned short*)(xb + (2 * a + 1) * 64) : *(const LAS unsigned short*)xl;
            x[2 * a] = bf_lo(lo); x[2 * a + 1] = bf_lo(hi);
        }
    }
    u32x4 z[24];
#pragma unroll
    for (int s = 0; s < 22; ++s) {
        float fa[8], fb[8];
#pragma unroll
        for (int e = 0; e < 8; ++e) { const AB pe = slot_ab(8 * s + e); fa[e] = x[pe.a]; fb[e] = x[pe.b]; }
        float t0, t1, t2, t3, t4, t5;
        unsigned o0, o1, o2, o3;
        asm("v_mul_f32 %4, %10, %11\n\tv_mul_f32 %5, %12, %13\n\tv_mul_f32 %6, %14, %15\n\tv_mul_f32 %7, %16, %17\n\t"
            "v_cvt_pk_bf16_f32 %0, %4, %5\n\tv_mul_f32 %8, %18, %19\n\tv_mul_f32 %9, %20, %21\n\tv_cvt_pk_bf16_f32 %1, %6, %7\n\t"
            "v_mul_f32 %4, %22, %23\n\tv_mul_f32 %5, %24, %25\n\tv_cvt_pk_bf16_f32 %2, %8, %9\n\tv_cvt_pk_bf16_f32 %3, %4, %5"
            : "=&v"(o0), "=&v"(o1), "=&v"(o2), "=v"(o3), "=&v"(t0), "=&v"(t1), "=&v"(t2), "=&v"(t3), "=&v"(t4), "=&v"(t5)
            : "v"(fa[0]), "v"(fb[0]), "v"(fa[1]), "v"(fb[1]), "v"(fa[2]), "v"(fb[2]), "v"(fa[3]), "v"(fb[3]),
              "v"(fa[4]), "v"(fb[4]), "v"(fa[5]), "v"(fb[5]), "v"(fa[6]), "v"(fb[6]), "v"(fa[7]), "v"(fb[7]));
        z[s] = (u32x4){o0, o1, o2, o3};
    }
#pragma unroll
    for (int s = 22; s < 24; ++s) {
#pragma unroll
        for (int j = 0; j < 4; ++j) {
            const AB p0 = slot_ab(8 * s + 2 * j), p1 = slot_ab(8 * s + 2 * j + 1);
            const float a0 = p0.a < 26 ? x[p0.a] : (p0.a == 26 ? 1.f : 0.f);
            const float a1 = p1.a < 26 ? x[p1.a] : (p1.a == 26 ? 1.f : 0.f);
            z[s][j] = pk_bf16(a0, a1);
        }
    }
#pragma unroll
    for (int s = 0; s < 24; ++s) asm volatile("" : "+v"(z[s]));
    STAMP(4);
    const unsigned tag = epv + 1u;
    {
        const __amdgpu_buffer_rsrc_t srdA = __builtin_amdgcn_make_buffer_rsrc((void*)(P.ws + WS_A), 0, A_BYTES, RSRC3);
        if (cw == 0) {
            const gu32* fl = (const gu32*)(P.ws + WS_FL) + g * 32 + (lane < 26 ? lane : 0);
            for (unsigned sp = 0; sp < SPIN_CAP; ++sp) { if (__all(ld_agent(fl) == tag)) break; __builtin_amdgcn_s_sleep(1); }
            if (lane == 0) lds_st(lds, L_F + 16, 1);
        } else {
            for (unsigned sp = 0; lds_ld(lds, L_F + 16) == 0 && sp < SPIN_CAP; ++sp) __builtin_amdgcn_s_sleep(1);
        }
        if (has_dnn) for (unsigned sp = 0; lds_ld(lds, L_F + 40) < 4 && sp < SPIN_CAP; ++sp) __builtin_amdgcn_s_sleep(1);
        CBAR();
        STAMP(5);
        const unsigned lb = ldsb + L_A + cw * A_PER;
#pragma unroll
        for (int i = 0; i < 9; ++i) dma16_sc1(lb + i * 1024, lane * 16, srdA, cw * A_PER + i * 1024);
        if (lane < 22) dma16_sc1(lb + 9 * 1024, lane * 16, srdA, cw * A_PER + 9 * 1024);
        WAIT_VM0();
        if (lane == 0) lds_add(lds, L_F + 20, 1);
        for (unsigned sp = 0; lds_ld(lds, L_F + 20) < 4 && sp < SPIN_CAP; ++sp) __builtin_amdgcn_s_sleep(0);
        CBAR();
    }
    STAMP(6);
    const gu64* pp = (const gu64*)(P.ws + WS_PART) + ((g * 32 + (lane < 26 ? lane : 0)) * 64 + 4 * m + cw);
    unsigned long long pv = 0;
    f32x16 acc0, acc1;
#pragma unroll
    for (int i = 0; i < 16; ++i) { acc0[i] = 0.f; acc1[i] = 0.f; }
    {
        unsigned abo = L_A + (26 * h + d) * 16;
        asm volatile("" : "+v"(abo));
        const lds8* ab = lds + abo;
        constexpr int PF = 6;
        u32x4 af[46];
        auto frag_of = [](int t) { return t < 44 ? ((t & 1) ? (t >> 1) : 22 + (t >> 1)) : 22 + (t - 22); };
#pragma unroll
        for (int t = 0; t < PF; ++t) af[t] = *(const LAS u32x4*)(ab + frag_of(t) * 832);
        __builtin_amdgcn_sched_barrier(0);
#pragma unroll
        for (int t = 0; t < 46; ++t) {
            if (t + PF < 46) af[t + PF] = *(const LAS u32x4*)(ab + frag_of(t + PF) * 832);
            if (t < 44) {
                if (t & 1) acc0 = mfma32(as_frag(af[t]), as_frag(z[t >> 1]), acc0);
                else acc1 = mfma32(as_frag(af[t]), as_frag(z[t >> 1]), acc1);
            } else acc1 = mfma32(as_frag(af[t]), as_frag(z[t - 22]), acc1);
            if (t == 30) pv = __hip_atomic_load(pp, __ATOMIC_RELAXED, __HIP_MEMORY_SCOPE_AGENT);
            __builtin_amdgcn_sched_barrier(0);
        }
    }
    float sa = 0.f;
#pragma unroll
    for (int i = 0; i < 12; ++i) sa = __builtin_fmaf(acc0[i], acc1[i], sa);
    const float sb = __builtin_fmaf(acc0[12], acc1[12], acc0[13] * acc1[13]);
    float tot = h == 0 ? sa + sb : sa;
    asm volatile("" : "+v"(tot));
    STAMP(7);
    for (unsigned sp = 0; sp < SPIN_CAP; ++sp) {
        if (__all((unsigned)(pv >> 32) == tag)) break;
        pv = __hip_atomic_load(pp, __ATOMIC_RELAXED, __HIP_MEMORY_SCOPE_AGENT);
    }
    STAMP(8);
    if (lane < 26) tot += __uint_as_float((unsigned)pv);
    tot = wave_sum_to_lane63(tot);
    if (lane == 63) {
        const float o = tot + bf_lo(P.bout[0]);
        P.out[b] = (unsigned short)(pk_bf16(o, o) & 0xffffu);
    }
    if (m == 0 && cw == 0) {
        const gu64* ap = (const gu64*)(P.ws + WS_PART) + ((g * 32 + 26 + (lane < 3 ? lane : 0)) * 64);
        for (unsigned sp = 0; sp < SPIN_CAP; ++sp) {
            const unsigned long long av = __hip_atomic_load(ap, __ATOMIC_RELAXED, __HIP_MEMORY_SCOPE_AGENT);
            if (__all((unsigned)(av >> 32) == tag)) break;
        }
        if (lane == 0) __hip_atomic_store((gu32*)(P.ws + WS_EP) + g, tag, __ATOMIC_RELAXED, __HIP_MEMORY_SCOPE_AGENT);
    }
}

DI void decode_block(int bid, int& g, int& m, int& ntile, int& k) {
    const int x = bid & 7, j = bid >> 3;
    g = 2 * x + (j >> 4); m = j & 15;
    ntile = m < 13 ? m : -1; k = m - 13;
}

__global__ void __launch_bounds__(512, 2) xdfm_kernel(Params P) {
    __shared__ __attribute__((aligned(16))) unsigned char smem[L_END];
    lds8* lds = (lds8*)smem;
    Stamps st; st.t0 = 0; st.t1 = 0;
    STAMP(0);
    asm volatile("" :: "s"(P.idx), "s"(P.table), "s"(P.wcin), "s"(P.wout), "s"(P.w1), "s"(P.b1), "s"(P.w2), "s"(P.bout), "s"(P.out), "s"(P.ws));
    const int w = __builtin_amdgcn_readfirstlane(threadIdx.x >> 6), lane = threadIdx.x & 63;
    int g, m, ntile, k;
    decode_block(blockIdx.x, g, m, ntile, k);
    if (w < 4) {
        if (w == 0 && lane < 16) lds_st(lds, L_F + 4 * lane, 0);
        if (ntile >= 0) { dnn_role(P, lds, w, lane, g, ntile STAMP_PASS); }
        else {
            const int p = g * 3 + k;
            unsigned ep = ld_agent((const gu32*)(P.ws + WS_EP) + g);
            if (p < 26) prep_role(P, lds, w, lane, p STAMP_PASS);
            else { asm volatile("s_waitcnt vmcnt(0) lgkmcnt(0)" : "+v"(ep) :: "memory"); __builtin_amdgcn_s_barrier(); }
            if (w == 0 && lane == 0) st_granule((gu64*)(P.ws + WS_PART) + ((g * 32 + 26 + k) * 64), ep + 1u, 0u);
        }
    } else {
        cin_role(P, lds, w - 4, lane, g, m, ntile >= 0 STAMP_PASS);
    }
    STAMP(11);
    STAMP_FLUSH(P.ws, w, lane);
}

extern "C" void kernel_launch(void* const* d_in, const int* in_sizes, int n_in,
                              void* d_out, int out_size, void* d_ws, size_t ws_size,
                              hipStream_t stream) {
    Params P;
    P.idx = (const int*)d_in[0]; P.table = (const unsigned short*)d_in[1]; P.wcin = (const unsigned short*)d_in[2];
    P.wout = (const unsigned short*)d_in[3]; P.w1 = (const unsigned short*)d_in[4]; P.b1 = (const unsigned short*)d_in[5];
    P.w2 = (const unsigned short*)d_in[6]; P.bout = (const unsigned short*)d_in[7];
    P.out = (unsigned short*)d_out; P.ws = (unsigned char*)d_ws;
    xdfm_kernel<<<256, 512, 0, stream>>>(P);
}
```

```cpp
#include <hip/hip_runtime.h>
#include <stdint.h>

#define DI __device__ __forceinline__
#define GAS __attribute__((address_space(1)))
#define LAS __attribute__((address_space(3)))

typedef short    s16x4 __attribute__((ext_vector_type(4)));
typedef short    s16x8 __attribute__((ext_vector_type(8)));
typedef float    f32x2 __attribute__((ext_vector_type(2)));
typedef float    f32x4 __attribute__((ext_vector_type(4)));
typedef float    f32x16 __attribute__((ext_vector_type(16)));
typedef unsigned u32x2 __attribute__((ext_vector_type(2)));
typedef unsigned u32x4 __attribute__((ext_vector_type(4)));
typedef __bf16   bf16x2 __attribute__((ext_vector_type(2)));
typedef GAS unsigned gu32;
typedef GAS unsigned long long gu64;
typedef LAS unsigned char lds8;

#define MAPPING 1

constexpr int DIN = 832, NBATCH = 1024, NVOC = 100000;
#define A_AFTER_X 1
#define W_EARLY 13
#define A_PF 1
#define CODE_PF 1
#define WC_PF 1
#define POLL_STEP 10
#define PKMUL 0
#define A_DMA 1
constexpr unsigned RSRC3 = 0x00020000u;
constexpr int AUX_SC1 = 16;
constexpr unsigned SPIN_CAP = 1u << 20;

constexpr unsigned WS_EP = 0;
constexpr unsigned WS_FL = 4096;
constexpr unsigned WS_PART = 8192;
constexpr unsigned WS_A = 8192 + 16 * 32 * 64 * 8;
constexpr unsigned A_BYTES = 46 * 832;

#define XD 6
constexpr unsigned L_W = 0;
constexpr unsigned L_A = 832 * 128;
constexpr unsigned L_X0 = L_A + 4 * XD * 2048;
constexpr unsigned L_F = L_X0 + 4 * 1664;
constexpr unsigned L_DUMMY = L_F + 256;
constexpr unsigned L_EP = L_DUMMY + 256;
constexpr unsigned L_BI = L_EP + 256;
constexpr unsigned L_W2 = L_BI + 256;
constexpr unsigned L_END = L_W2 + 256;
static_assert(4 * XD * 2048 >= A_BYTES && L_END <= 163840, "LDS map");
constexpr unsigned LP_VT = 0;
constexpr unsigned LP_T0 = 2048;
constexpr unsigned LP_T1 = 2048 + 3072;

struct AB { int a, b; };
constexpr AB slot_ab(int t) {
    if (t < 156) { int a = 2 * (t / 12); return {a, (a + t % 12) % 26}; }
    if (t < 170) { int u = t - 156; int a = 2 * (u / 2); return {a, (a + 12 + (u & 1)) % 26}; }
    if (t < 176) { int a = 2 * (7 + t - 170); return {a, (a + 12) % 26}; }
    if (t < 189) return {2 * (t - 176), 26};
    if (t == 189) return {26, 26};
    return {27, 27};
}
constexpr unsigned T_ZERO = 27, T_CONST = 55;
struct PrepTab { unsigned v[24 * 2 * 8]; };
constexpr PrepTab make_tab() {
    PrepTab tb{};
    for (int s = 0; s < 24; ++s) for (int h = 0; h < 2; ++h) for (int e = 0; e < 8; ++e) {
        int t = 8 * s + e; AB ab = slot_ab(t);
        unsigned o1 = T_ZERO, o2 = T_ZERO;
        if (ab.a == 27) {}
        else if (ab.a == 26) { if (h == 0) o1 = T_CONST; }
        else if (ab.b == 26) { o1 = ((ab.a + h) % 26) * 28 + 26; }
        else {
            int u = (ab.a + h) % 26, v = (ab.b + h) % 26;
            bool dup = (t == 169 && h == 1);
            if (dup) {}
            else if (u == v) o1 = u * 28 + u;
            else { o1 = u * 28 + v; o2 = v * 28 + u; }
        }
        tb.v[(s * 2 + h) * 8 + e] = o1 | (o2 << 16);
    }
    return tb;
}
static_assert(slot_ab(169).a == 12 && slot_ab(169).b == 25, "dup slot");
__device__ const PrepTab g_tab = make_tab();

struct Params {
    const int* idx; const unsigned short* table; const unsigned short* wcin; const unsigned short* wout;
    const unsigned short* w1; const unsigned short* b1; const unsigned short* w2; const unsigned short* bout;
    unsigned short* out; unsigned char* ws;
};

DI unsigned sel(bool c, unsigned a, unsigned b) { unsigned r = c ? a : b; return r; }
DI float bf_lo(unsigned v) { return __uint_as_float(v << 16); }
DI float bf_hi(unsigned v) { return __uint_as_float(v & 0xffff0000u); }
DI unsigned pk_bf16(float lo, float hi) { f32x2 v = {lo, hi}; return __builtin_bit_cast(unsigned, __builtin_convertvector(v, bf16x2)); }
DI f32x4 mfma16(s16x8 a, s16x8 b, f32x4 c) { return __builtin_amdgcn_mfma_f32_16x16x32_bf16(a, b, c, 0, 0, 0); }
DI f32x16 mfma32(s16x8 a, s16x8 b, f32x16 c) { return __builtin_amdgcn_mfma_f32_32x32x16_bf16(a, b, c, 0, 0, 0); }
DI s16x8 as_frag(u32x4 v) { return __builtin_bit_cast(s16x8, v); }
template <int CTRL, int ROWMASK> DI float dpp_add(float v) {
    return v + __int_as_float(__builtin_amdgcn_update_dpp(0, __float_as_int(v), CTRL, ROWMASK, 0xf, true));
}
DI float wave_sum_to_lane63(float v) {
    v = dpp_add<0x111, 0xf>(v); v = dpp_add<0x112, 0xf>(v); v = dpp_add<0x114, 0xf>(v); v = dpp_add<0x118, 0xf>(v);
    v = dpp_add<0x142, 0xa>(v); v = dpp_add<0x143, 0xc>(v);
    return v;
}
DI float sum_lane_groups(float v) {
    auto a = __builtin_amdgcn_permlane16_swap(__float_as_uint(v), __float_as_uint(v), false, false);
    v = __uint_as_float(a[0]) + __uint_as_float(a[1]);
    auto b = __builtin_amdgcn_permlane32_swap(__float_as_uint(v), __float_as_uint(v), false, false);
    return __uint_as_float(b[0]) + __uint_as_float(b[1]);
}
DI float gelu_tanh(float x) {
    const float k1 = -2.0f * 0.7978845608028654f * 1.4426950408889634f;
    const float k2 = k1 * 0.044715f;
    float arg = x * __builtin_fmaf(x * x, k2, k1);
    float e = __builtin_amdgcn_exp2f(arg);
    return x * __builtin_amdgcn_rcpf(1.0f + e);
}
DI void dma16(unsigned lds_addr, unsigned voff, __amdgpu_buffer_rsrc_t srd, unsigned soff) {
    asm volatile("s_mov_b32 m0, %0\n\ts_nop 0\n\tbuffer_load_dwordx4 %1, %2, %3 offen lds"
                 :: "s"(lds_addr), "v"(voff), "s"(srd), "s"(soff) : "memory");
}
DI void dma16_sc1(unsigned lds_addr, unsigned voff, __amdgpu_buffer_rsrc_t srd, unsigned soff) {
    asm volatile("s_mov_b32 m0, %0\n\ts_nop 0\n\tbuffer_load_dwordx4 %1, %2, %3 offen sc1 lds"
                 :: "s"(lds_addr), "v"(voff), "s"(srd), "s"(soff) : "memory");
}
DI void dma4_sc1(unsigned lds_addr, unsigned voff, __amdgpu_buffer_rsrc_t srd, unsigned soff) {
    asm volatile("s_mov_b32 m0, %0\n\ts_nop 0\n\tbuffer_load_dword %1, %2, %3 offen sc1 lds"
                 :: "s"(lds_addr), "v"(voff), "s"(srd), "s"(soff) : "memory");
}
DI void dma4(unsigned lds_addr, unsigned voff, __amdgpu_buffer_rsrc_t srd, unsigned soff) {
    asm volatile("s_mov_b32 m0, %0\n\ts_nop 0\n\tbuffer_load_dword %1, %2, %3 offen lds"
                 :: "s"(lds_addr), "v"(voff), "s"(srd), "s"(soff) : "memory");
}
DI void st_granule(gu64* g, unsigned tag, unsigned val) {
    __hip_atomic_store(g, ((unsigned long long)tag << 32) | val, __ATOMIC_RELAXED, __HIP_MEMORY_SCOPE_AGENT);
}
DI unsigned ld_agent(const gu32* p) { return __hip_atomic_load(p, __ATOMIC_RELAXED, __HIP_MEMORY_SCOPE_AGENT); }
DI unsigned lds_ld(lds8* l, unsigned off) { return __hip_atomic_load((LAS unsigned*)(l + off), __ATOMIC_RELAXED, __HIP_MEMORY_SCOPE_WORKGROUP); }
DI void lds_st(lds8* l, unsigned off, unsigned v) { __hip_atomic_store((LAS unsigned*)(l + off), v, __ATOMIC_RELAXED, __HIP_MEMORY_SCOPE_WORKGROUP); }
DI void lds_add(lds8* l, unsigned off, unsigned v) { __hip_atomic_fetch_add((LAS unsigned*)(l + off), v, __ATOMIC_RELAXED, __HIP_MEMORY_SCOPE_WORKGROUP); }
#define WAIT_LGKM() asm volatile("s_waitcnt lgkmcnt(0)" ::: "memory")
#define WAIT_VM0() asm volatile("s_waitcnt vmcnt(0)" ::: "memory")
#define CBAR() asm volatile("" ::: "memory")
#define STAMP(i)
#define STAMP_ARG
#define STAMP_PASS
#define STAMP_FLUSH(ws, w, lane)

template <int K> DI unsigned quad_bcast(unsigned v) { return __builtin_amdgcn_update_dpp(0u, v, K * 0x55, 0xf, 0xf, true); }
template <int N> DI void wait_vm() { asm volatile("s_waitcnt vmcnt(%0)" :: "i"(N) : "memory"); }
DI void dma_w4(unsigned lds_addr, unsigned voff, __amdgpu_buffer_rsrc_t srd, unsigned soff) {
    unsigned t;
    asm volatile("s_mov_b32 m0, %1\n\ts_add_u32 %0, %4, 12288\n\tbuffer_load_dwordx4 %2, %3, %4 offen lds\n\t"
                 "buffer_load_dwordx4 %2, %3, %0 offen offset:1024 lds\n\ts_add_u32 %0, %0, 12288\n\t"
                 "buffer_load_dwordx4 %2, %3, %0 offen offset:2048 lds\n\ts_add_u32 %0, %0, 12288\n\t"
                 "buffer_load_dwordx4 %2, %3, %0 offen offset:3072 lds"
                 : "=&s"(t) : "s"(lds_addr), "v"(voff), "s"(srd), "s"(soff) : "memory", "scc");
}
DI void dnn_role(const Params& P, lds8* lds, int w, int lane, int g, int ntile STAMP_ARG) {
    const int mh = w & 1, kh = w >> 1;
    const int h = lane >> 5;
    const unsigned ldsb = (unsigned)(size_t)lds;
    const unsigned ring = ldsb + L_A + w * (XD * 2048);
    {
        const __amdgpu_buffer_rsrc_t srdI = __builtin_amdgcn_make_buffer_rsrc((void*)P.idx, 0, NBATCH * 26 * 4, RSRC3);
        const unsigned vo = ((64 * g + 32 * mh + (lane >> 2)) * 26 + 13 * kh + 4 * (lane & 3)) * 4;
        dma16(ring, vo, srdI, 0);
        dma16(ring + 1024, vo, srdI, 1664);
        if (w == 1) dma4(ldsb + L_BI, ntile * 128 + lane * 4, __builtin_amdgcn_make_buffer_rsrc((void*)P.b1, 0, DIN * 2, RSRC3), 0);
        if (w == 2) dma4(ldsb + L_W2, ntile * 128 + lane * 4, __builtin_amdgcn_make_buffer_rsrc((void*)P.w2, 0, DIN * 2, RSRC3), 0);
    }
    const __amdgpu_buffer_rsrc_t srdW = __builtin_amdgcn_make_buffer_rsrc((void*)P.w1, 0, DIN * DIN * 2, RSRC3);
    const unsigned vw = (lane >> 3) * 1664 + (((lane & 7) ^ (((lane >> 4) & 1) << 2)) << 4);
    const unsigned rowA = 416 * kh + 208 * mh + 104 * (g & 1), rowB = 416 * kh + 208 * mh + 104 * ((g & 1) ^ 1);
    auto wdma13 = [&](unsigned row) {
        const unsigned sb = row * 1664 + ntile * 128, lb = ldsb + L_W + row * 128;
        dma_w4(lb, vw, srdW, sb);
        dma_w4(lb + 4096, vw, srdW, sb + 4 * 13312);
        dma_w4(lb + 8192, vw, srdW, sb + 8 * 13312);
        dma16(lb + 12288, vw, srdW, sb + 12 * 13312);
    };
    wdma13(rowA);
    STAMP(1);
    asm volatile("s_waitcnt vmcnt(13) lgkmcnt(0)" ::: "memory");
    __builtin_amdgcn_s_barrier();
    STAMP(2);
    const u32x4 i0 = *(const LAS u32x4*)(lds + L_A + w * (XD * 2048) + lane * 16);
    const u32x4 i1 = *(const LAS u32x4*)(lds + L_A + w * (XD * 2048) + 1024 + lane * 16);
    const unsigned ep = *(const LAS unsigned*)(lds + L_EP + g * 4);
    wdma13(rowB);
    const __amdgpu_buffer_rsrc_t srdT = __builtin_amdgcn_make_buffer_rsrc((void*)P.table, 0, NVOC * 64, RSRC3);
    const unsigned c16 = (((lane & 3) ^ ((lane >> 4) & 3)) << 4);
    u32x4 t0 = i0 << 6, t1 = i1 << 6;
    asm volatile("" : "+v"(t0), "+v"(t1));
    auto xload = [&](int s) {
        const unsigned a0 = t0[s & 3], a1 = t1[s & 3];
        const unsigned o0 = (s < 4 ? quad_bcast<0>(a0) : s < 8 ? quad_bcast<1>(a0) : s < 12 ? quad_bcast<2>(a0) : quad_bcast<3>(a0)) + c16;
        const unsigned o1 = (s < 4 ? quad_bcast<0>(a1) : s < 8 ? quad_bcast<1>(a1) : s < 12 ? quad_bcast<2>(a1) : quad_bcast<3>(a1)) + c16;
        dma16(ring + (s % XD) * 2048, o0, srdT, 0);
        dma16(ring + (s % XD) * 2048 + 1024, o1, srdT, 0);
    };
#pragma unroll
    for (int s = 0; s < XD; ++s) xload(s);
    STAMP(3);
    wait_vm<2 * XD>();
    if (lane == 0) lds_add(lds, L_F + 32 + 4 * kh, 1);
    for (unsigned sp = 0; lds_ld(lds, L_F + 32 + 4 * kh) < 2 && sp < SPIN_CAP; ++sp) __builtin_amdgcn_s_sleep(0);
    CBAR();
    STAMP(4);
    f32x16 acc[2];
    u32x2 bia[4], wv[4];
#pragma unroll
    for (int r4 = 0; r4 < 4; ++r4) {
        bia[r4] = *(const LAS u32x2*)(lds + L_BI + kh * 64 + h * 8 + r4 * 16);
        wv[r4] = *(const LAS u32x2*)(lds + L_W2 + kh * 64 + h * 8 + r4 * 16);
    }
#pragma unroll
    for (int r4 = 0; r4 < 4; ++r4) {
        acc[0][4 * r4] = bf_lo(bia[r4][0]); acc[0][4 * r4 + 1] = bf_hi(bia[r4][0]); acc[0][4 * r4 + 2] = bf_lo(bia[r4][1]); acc[0][4 * r4 + 3] = bf_hi(bia[r4][1]);
        acc[1][4 * r4] = 0.f; acc[1][4 * r4 + 1] = 0.f; acc[1][4 * r4 + 2] = 0.f; acc[1][4 * r4 + 3] = 0.f;
    }
    {
        const int blk = (lane >> 4) & 1, q = (lane & 15) >> 2, pp = lane & 3, n = lane & 31;
        unsigned ad[2];
#pragma unroll
        for (int j = 0; j < 2; ++j) {
            const unsigned c = 4 * (j ^ kh) + 2 * blk + (pp >> 1);
            ad[j] = L_W + (416 * kh + 8 * h + q) * 128 + ((c ^ (((q >> 1) & 1) << 2)) << 4) + (pp & 1) * 8;
        }
        const unsigned xb = L_A + w * (XD * 2048) + n * 64;
        const unsigned xa0 = xb + ((h ^ ((n >> 2) & 3)) << 4), xa1 = xb + (((2 + h) ^ ((n >> 2) & 3)) << 4);
        u32x4 xf[3][2];
        s16x8 wf[2][4];
        auto xread = [&](int s) {
            xf[s % 3][0] = *(const LAS u32x4*)(lds + xa0 + (s % XD) * 2048);
            xf[s % 3][1] = *(const LAS u32x4*)(lds + xa1 + (s % XD) * 2048);
        };
        auto wread = [&](int s) {
#pragma unroll
            for (int a = 0; a < 2; ++a)
#pragma unroll
                for (int j = 0; j < 2; ++j) {
                    const s16x4 lo = __builtin_amdgcn_ds_read_tr16_b64_v4i16((LAS s16x4*)(lds + ad[j] + s * 4096 + a * 2048));
                    const s16x4 hi = __builtin_amdgcn_ds_read_tr16_b64_v4i16((LAS s16x4*)(lds + ad[j] + s * 4096 + a * 2048 + 512));
                    wf[s & 1][2 * a + j] = __builtin_shufflevector(lo, hi, 0, 1, 2, 3, 4, 5, 6, 7);
                }
        };
        wread(0);
        wait_vm<2 * (XD - 1)>();
        xread(0);
        wait_vm<2 * (XD - 2)>();
        xread(1);
        __builtin_amdgcn_sched_barrier(0);
#pragma unroll
        for (int s = 0; s < 13; ++s) {
            if (s + 1 < 13) wread(s + 1);
#pragma unroll
            for (int a = 0; a < 2; ++a)
#pragma unroll
                for (int j = 0; j < 2; ++j) acc[j] = mfma32(wf[s & 1][2 * a + j], as_frag(xf[s % 3][a]), acc[j]);
            if (s + 1 < 13) {
#pragma unroll
                for (int j = 0; j < 4; ++j) { __builtin_amdgcn_sched_group_barrier(0x100, 2, 0); __builtin_amdgcn_sched_group_barrier(0x8, 1, 0); }
            }
            __builtin_amdgcn_sched_barrier(0);
            if (s + XD < 13) xload(s + XD);
            if (s == POLL_STEP && w == 0 && lane == 0) lds_st(lds, L_F + 52, 1);
            if (s + 2 < 13) {
                constexpr int LASTI = 12;
                const int last = s + XD < LASTI ? s + XD : LASTI;
                const int younger = last - (s + 2);
                if (younger >= 4) wait_vm<8>(); else if (younger == 3) wait_vm<6>(); else if (younger == 2) wait_vm<4>(); else if (younger == 1) wait_vm<2>(); else wait_vm<0>();
                xread(s + 2);
                if (s + 2 == 12) { CBAR(); if (lane == 0) lds_add(lds, L_F + 56, 1); }
            }
            __builtin_amdgcn_sched_barrier(0);
        }
    }
    WAIT_LGKM();
    if (lane == 0) lds_add(lds, L_F + 40, 1);
    STAMP(6);
    for (unsigned sp = 0; lds_ld(lds, L_F + 40) < 4 && sp < SPIN_CAP; ++sp) __builtin_amdgcn_s_sleep(0);
    CBAR();
    {
        lds8* xw = lds + L_W + w * 4096 + lane * 16;
#pragma unroll
        for (int r4 = 0; r4 < 4; ++r4) *(LAS f32x4*)(xw + r4 * 1024) = (f32x4){acc[1][4 * r4], acc[1][4 * r4 + 1], acc[1][4 * r4 + 2], acc[1][4 * r4 + 3]};
        CBAR();
        if (lane == 0) lds_st(lds, L_F + 4 * w, 1);
        for (unsigned sp = 0; lds_ld(lds, L_F + 4 * (w ^ 2)) == 0 && sp < SPIN_CAP; ++sp) __builtin_amdgcn_s_sleep(0);
        CBAR();
        const lds8* xr = lds + L_W + (w ^ 2) * 4096 + lane * 16;
#pragma unroll
        for (int r4 = 0; r4 < 4; ++r4) {
            const f32x4 o = *(const LAS f32x4*)(xr + r4 * 1024);
            acc[0][4 * r4] += o[0]; acc[0][4 * r4 + 1] += o[1]; acc[0][4 * r4 + 2] += o[2]; acc[0][4 * r4 + 3] += o[3];
        }
    }
    STAMP(7);
    float s0 = 0.f, s1 = 0.f;
#pragma unroll
    for (int r4 = 0; r4 < 4; ++r4) {
        s0 = __builtin_fmaf(gelu_tanh(acc[0][4 * r4]), bf_lo(wv[r4][0]), s0);
        s1 = __builtin_fmaf(gelu_tanh(acc[0][4 * r4 + 1]), bf_hi(wv[r4][0]), s1);
        s0 = __builtin_fmaf(gelu_tanh(acc[0][4 * r4 + 2]), bf_lo(wv[r4][1]), s0);
        s1 = __builtin_fmaf(gelu_tanh(acc[0][4 * r4 + 3]), bf_hi(wv[r4][1]), s1);
    }
    s0 += s1;
    const auto sw = __builtin_amdgcn_permlane32_swap(__float_as_uint(s0), __float_as_uint(s0), false, false);
    const float part = __uint_as_float(sw[0]) + __uint_as_float(sw[1]);
    STAMP(8);
    if (lane < 32) st_granule((gu64*)(P.ws + WS_PART) + ((g * 32 + 2 * ntile + kh) * 64 + 32 * mh + lane), ep + 1u, __float_as_uint(part));
}

constexpr unsigned LP_W2 = 12288;
constexpr unsigned LP_WO = 14336;
constexpr unsigned LP_W3 = 16384;
constexpr unsigned w3_slot(int j) { return j * 1280 + (j & 1) * 64 + ((j >> 2) & 1) * 128; }
DI void dma16_add(unsigned lds_base, unsigned lds_off, unsigned voff, __amdgpu_buffer_rsrc_t srd, unsigned sbase, unsigned soff) {
    unsigned t;
    asm volatile("s_add_u32 m0, %1, %2\n\ts_add_u32 %0, %5, %6\n\tbuffer_load_dwordx4 %3, %4, %0 offen lds"
                 : "=&s"(t) : "s"(lds_base), "i"(lds_off), "v"(voff), "s"(srd), "s"(sbase), "i"(soff) : "memory", "scc");
}
DI void prep_role(const Params& P, lds8* lds, int w, int lane, int p STAMP_ARG) {
    const int tid = w * 64 + lane;
    const int n = lane & 15, gq = lane >> 4;
    const unsigned ldsb = (unsigned)(size_t)lds;
    const __amdgpu_buffer_rsrc_t srdC = __builtin_amdgcn_make_buffer_rsrc((void*)P.wcin, 0, 3 * 17576 * 2, RSRC3);
    dma4(ldsb + LP_WO + w * 256, lane * 4, __builtin_amdgcn_make_buffer_rsrc((void*)P.wout, 0, 78 * 2, RSRC3), 0);
    {
        const unsigned par = lane >> 5, pos = lane & 31;
        const unsigned vo = pos < 24 ? par * 1352 + ((pos ^ (par << 1)) << 4) : 0x7fffff00u;
        const unsigned lb = ldsb + LP_W3 + w * 16640, sb = 52 * 1352 + 352 * w;
        dma16_add(lb, w3_slot(0), vo, srdC, sb, 0 * 2704);  dma16_add(lb, w3_slot(1), vo, srdC, sb, 1 * 2704);
        dma16_add(lb, w3_slot(2), vo, srdC, sb, 2 * 2704);  dma16_add(lb, w3_slot(3), vo, srdC, sb, 3 * 2704);
        dma16_add(lb, w3_slot(4), vo, srdC, sb, 4 * 2704);  dma16_add(lb, w3_slot(5), vo, srdC, sb, 5 * 2704);
        dma16_add(lb, w3_slot(6), vo, srdC, sb, 6 * 2704);  dma16_add(lb, w3_slot(7), vo, srdC, sb, 7 * 2704);
        dma16_add(lb, w3_slot(8), vo, srdC, sb, 8 * 2704);  dma16_add(lb, w3_slot(9), vo, srdC, sb, 9 * 2704);
        dma16_add(lb, w3_slot(10), vo, srdC, sb, 10 * 2704); dma16_add(lb, w3_slot(11), vo, srdC, sb, 11 * 2704);
        dma16_add(lb, w3_slot(12), vo, srdC, sb, 12 * 2704);
    }
    if (w == 0) {
        const unsigned vo = 35152 + (lane >> 2) * 1352 + p * 52 + (lane & 3) * 16;
        dma16(ldsb + LP_W2, vo, srdC, 0);
        dma16(ldsb + LP_W2 + 1024, vo, srdC, 16 * 1352);
    }
    const u32x2 l1 = __builtin_amdgcn_raw_buffer_load_b64(srdC, (unsigned)(p * 1352 + (tid < 169 ? tid : 168) * 8), 0, 0);
    const int nact = w == 0 ? 48 : 44;
    const int ch = lane < nact ? lane : 0;
    const int cs = ch >> 1, chh = ch & 1;
    const GAS u32x4* tp = (const GAS u32x4*)&g_tab.v[(cs * 2 + chh) * 8];
    const u32x4 ta = tp[0], tb = tp[1];
    STAMP(1);
    LAS unsigned short* vt = (LAS unsigned short*)(lds + LP_VT);
    LAS float* t0 = (LAS float*)(lds + LP_T0);
    LAS float* t1 = (LAS float*)(lds + LP_T1);
    constexpr unsigned VT_DUMMY = 31 * 32 + 31, T_DUMMY = 83;
    unsigned av[3], a1[4], az, ac[16];
#pragma unroll
    for (int i = 0; i < 3; ++i) {
        const unsigned t = 4 * i + gq, c = 176 * w + 16 * t + n;
        const unsigned qd = (c * 0x4ec5u) >> 19;
        const unsigned ad = LP_VT + 64 * c - 1662 * qd;
        av[i] = sel(t < 11 && c < 676, ad, LP_VT + 2 * VT_DUMMY);
    }
#pragma unroll
    for (int e = 0; e < 4; ++e) {
        const unsigned c = tid * 4 + e, qd = (c * 0x9d9u) >> 16;
        a1[e] = sel(tid < 169, LP_T0 + 4 * c + 8 * qd, LP_T0 + 4 * T_DUMMY);
    }
    { const unsigned qd = (tid * 171u) >> 10; az = sel(tid < 162, LP_VT + 2 * (qd * 26 + 26 + tid), LP_VT + 2 * VT_DUMMY); }
    const int q = (lane & 15) >> 2, pp = lane & 3;
    int r0 = 8 * gq + q, r1 = r0 + 4;
    r0 = r0 < 26 ? r0 : 25; r1 = r1 < 26 ? r1 : 25;
    unsigned b0e, b0o, b1e, b1o;
    {
        const unsigned wb = LP_W3 + w * 16640 + pp * 8;
        const unsigned j0 = r0 >> 1, j1 = r1 >> 1;
        const unsigned s0 = wb + j0 * 1280 + (j0 & 1) * 64 + ((j0 >> 2) & 1) * 128 + (r0 & 1) * 512;
        const unsigned s1 = wb + j1 * 1280 + (j1 & 1) * 64 + ((j1 >> 2) & 1) * 128 + (r1 & 1) * 512;
        b0e = s0 + (r0 & 1) * 32; b0o = s0 - (r0 & 1) * 32; b1e = s1 + (r1 & 1) * 32; b1o = s1 - (r1 & 1) * 32;
    }
#pragma unroll
    for (int mt = 0; mt < 2; ++mt)
#pragma unroll
        for (int u = 0; u < 2; ++u)
#pragma unroll
            for (int e = 0; e < 4; ++e) {
                const unsigned q0 = 16 * mt + 4 * gq + e, jp = 16 * u + n;
                ac[8 * u + 4 * mt + e] = sel(q0 < 26 && jp < 27, LP_T1 + 4 * (q0 * 28 + jp), LP_T1 + 4 * T_DUMMY);
            }
    unsigned a20 = LP_W2 + r0 * 64 + pp * 8, a21 = LP_W2 + r1 * 64 + pp * 8, abv = LP_VT + n * 64 + gq * 16;
    asm volatile("" : "+v"(av[0]), "+v"(av[1]), "+v"(av[2]), "+v"(a1[0]), "+v"(a1[1]), "+v"(a1[2]), "+v"(a1[3]), "+v"(b0e), "+v"(b0o), "+v"(b1e), "+v"(b1o),
                 "+v"(az), "+v"(a20), "+v"(a21), "+v"(abv));
#pragma unroll
    for (int e = 0; e < 16; ++e) asm volatile("" : "+v"(ac[e]));
    *(LAS unsigned short*)(lds + az) = 0;
    asm volatile("s_waitcnt vmcnt(14) lgkmcnt(0)" ::: "memory");
    __builtin_amdgcn_s_barrier();
    WAIT_VM0();
    STAMP(2);
    {
        const u32x4 wo3 = *(const LAS u32x4*)(lds + LP_WO + w * 256 + 104 + 16 * gq);
        s16x8 bfr[11];
#pragma unroll
        for (int t = 0; t < 11; ++t) {
            const s16x4 lo = __builtin_amdgcn_ds_read_tr16_b64_v4i16((LAS s16x4*)(lds + ((t & 1) ? b0o : b0e) + t * 32));
            const s16x4 hi = __builtin_amdgcn_ds_read_tr16_b64_v4i16((LAS s16x4*)(lds + ((t & 1) ? b1o : b1e) + t * 32));
            bfr[t] = __builtin_shufflevector(lo, hi, 0, 1, 2, 3, 4, 5, 6, 7);
        }
#pragma unroll
        for (int e = 0; e < 4; ++e) *(LAS float*)(lds + a1[e]) = (e & 1) ? bf_hi(l1[e >> 1]) : bf_lo(l1[e >> 1]);
        if (w == 3) {
            const unsigned o2 = *(const LAS unsigned short*)(lds + LP_WO + 768 + 52 + (lane < 26 ? lane : 0) * 2);
            const unsigned o1 = *(const LAS unsigned short*)(lds + LP_WO + 768 + p * 2);
            vt[lane < 26 ? 26 * 32 + lane : VT_DUMMY] = (unsigned short)o2; t0[T_ZERO] = 0.f; t1[T_ZERO] = 0.f; t1[T_CONST] = bf_lo(o1);
        }
        float res[11];
#pragma unroll
        for (int t = 0; t < 11; ++t) res[t] = mfma16(as_frag(wo3), bfr[t], (f32x4){0.f, 0.f, 0.f, 0.f})[0];
#pragma unroll
        for (int i = 0; i < 3; ++i) {
            const float r3 = i < 2 ? res[4 * i + 3] : res[10];
            const float v = gq == 0 ? res[4 * i] : gq == 1 ? res[4 * i + 1] : gq == 2 ? res[4 * i + 2] : r3;
            *(LAS unsigned short*)(lds + av[i]) = (unsigned short)(pk_bf16(v, v) & 0xffffu);
        }
    }
    CBAR();
    if (lane == 0) lds_add(lds, L_F + 28, 1);
    if (w > 1) return;
    STAMP(3);
    s16x8 a2[2];
    if (w == 0) {
#pragma unroll
        for (int mt = 0; mt < 2; ++mt) {
            const s16x4 lo = __builtin_amdgcn_ds_read_tr16_b64_v4i16((LAS s16x4*)(lds + a20 + mt * 32));
            const s16x4 hi = __builtin_amdgcn_ds_read_tr16_b64_v4i16((LAS s16x4*)(lds + a21 + mt * 32));
            a2[mt] = __builtin_shufflevector(lo, hi, 0, 1, 2, 3, 4, 5, 6, 7);
        }
    }
    unsigned pa[16];
    {
        const unsigned tbase = w == 0 ? LP_T1 : LP_T0;
#pragma unroll
        for (int e = 0; e < 8; ++e) {
            const unsigned x = e < 4 ? ta[e] : tb[e - 4];
            pa[2 * e] = tbase + 4 * (x & 0xffff); pa[2 * e + 1] = tbase + 4 * (x >> 16);
        }
#pragma unroll
        for (int e = 0; e < 16; ++e) asm volatile("" : "+v"(pa[e]));
    }
    for (unsigned sp = 0; lds_ld(lds, L_F + 28) < 4 && sp < SPIN_CAP; ++sp) __builtin_amdgcn_s_sleep(0);
    CBAR();
    if (w == 0) {
        const u32x4 bf0 = *(const LAS u32x4*)(lds + abv), bf1 = *(const LAS u32x4*)(lds + abv + 1024);
        const f32x4 z4 = {0.f, 0.f, 0.f, 0.f};
        const f32x4 d00 = mfma16(a2[0], as_frag(bf0), z4), d01 = mfma16(a2[1], as_frag(bf0), z4);
        const f32x4 d10 = mfma16(a2[0], as_frag(bf1), z4), d11 = mfma16(a2[1], as_frag(bf1), z4);
#pragma unroll
        for (int e = 0; e < 4; ++e) {
            *(LAS float*)(lds + ac[e]) = d00[e]; *(LAS float*)(lds + ac[4 + e]) = d01[e];
            *(LAS float*)(lds + ac[8 + e]) = d10[e]; *(LAS float*)(lds + ac[12 + e]) = d11[e];
        }
    }
    STAMP(4);
    const __amdgpu_buffer_rsrc_t srdA = __builtin_amdgcn_make_buffer_rsrc((void*)(P.ws + WS_A), 0, A_BYTES, RSRC3);
    {
        float pv[16];
#pragma unroll
        for (int e = 0; e < 16; ++e) pv[e] = *(const LAS float*)(lds + pa[e]);
        u32x4 o;
#pragma unroll
        for (int j = 0; j < 4; ++j) o[j] = pk_bf16(pv[4 * j] + pv[4 * j + 1], pv[4 * j + 2] + pv[4 * j + 3]);
        const int fr = w == 0 ? (cs < 22 ? 2 * cs : 22 + cs) : 2 * cs + 1;
        __builtin_amdgcn_raw_buffer_store_b128(o, srdA, lane < nact ? (unsigned)(fr * 832 + (26 * chh + p) * 16) : 0x7ffffff0u, 0, AUX_SC1);
    }
    STAMP(5);
    WAIT_VM0();
    STAMP(6);
    if (w == 1) { if (lane == 0) lds_st(lds, L_F + 24, 1); return; }
    const unsigned tagv = *(const LAS unsigned*)(lds + L_EP + (lane & 15) * 4);
    for (unsigned sp = 0; lds_ld(lds, L_F + 24) == 0 && sp < SPIN_CAP; ++sp) __builtin_amdgcn_s_sleep(0);
    if (lane < 16) __hip_atomic_store((gu32*)(P.ws + WS_FL) + lane * 32 + p, tagv + 1u, __ATOMIC_RELAXED, __HIP_MEMORY_SCOPE_AGENT);
}

constexpr unsigned A_PER = A_BYTES / 4;
DI void cin_role(const Params& P, lds8* lds, int cw, int lane, int g, int m, bool has_dnn, int pf_slot STAMP_ARG) {
    const int d = lane & 31, h = lane >> 5;
    const int b = 64 * g + 4 * m + cw;
    const unsigned ldsb = (unsigned)(size_t)lds;
    {
        const __amdgpu_buffer_rsrc_t srdT = __builtin_amdgcn_make_buffer_rsrc((void*)P.table, 0, NVOC * 64, RSRC3);
        const unsigned lb = ldsb + L_X0 + cw * 1664;
        WAIT_VM0();
        const int q = lane >> 2;
        const unsigned j0 = *(const LAS unsigned*)(lds + L_X0 + cw * 1664 + q * 4);
        const unsigned j1 = *(const LAS unsigned*)(lds + L_X0 + cw * 1664 + (q < 10 ? q + 16 : 25) * 4);
        unsigned o0 = j0 * 64 + (lane & 3) * 16, o1 = j1 * 64 + (lane & 3) * 16;
        asm volatile("" : "+v"(o0), "+v"(o1));
        STAMP(1);
        dma16(lb, o0, srdT, 0);
        if (lane < 40) dma16(lb + 1024, o1, srdT, 0);
    }
    const unsigned bo = P.bout[0];
    STAMP(2);
    __builtin_amdgcn_s_setprio(0);
    CBAR();
    __builtin_amdgcn_s_barrier();
    const unsigned epv = *(const LAS unsigned*)(lds + L_EP + g * 4);
    unsigned bov = bo;
    asm volatile("s_waitcnt vmcnt(0)" : "+v"(bov) :: "memory");
    STAMP(3);
    float x[26];
    {
        const lds8* xb = lds + L_X0 + cw * 1664 + h * 64 + d * 2;
        const lds8* xl = lds + L_X0 + cw * 1664 + (h ? 0 : 25 * 64) + d * 2;
#pragma unroll
        for (int a = 0; a < 13; ++a) {
            const unsigned lo = *(const LAS unsigned short*)(xb + 2 * a * 64);
            const unsigned hi = a < 12 ? *(const LAS unsigned short*)(xb + (2 * a + 1) * 64) : *(const LAS unsigned short*)xl;
            x[2 * a] = bf_lo(lo); x[2 * a + 1] = bf_lo(hi);
        }
    }
    u32x4 z[24];
#pragma unroll
    for (int s = 0; s < 22; ++s) {
        float fa[8], fb[8];
#pragma unroll
        for (int e = 0; e < 8; ++e) { const AB pe = slot_ab(8 * s + e); fa[e] = x[pe.a]; fb[e] = x[pe.b]; }
        float t0, t1, t2, t3, t4, t5;
        unsigned o0, o1, o2, o3;
        asm("v_mul_f32 %4, %10, %11\n\tv_mul_f32 %5, %12, %13\n\tv_mul_f32 %6, %14, %15\n\tv_mul_f32 %7, %16, %17\n\t"
            "v_cvt_pk_bf16_f32 %0, %4, %5\n\tv_mul_f32 %8, %18, %19\n\tv_mul_f32 %9, %20, %21\n\tv_cvt_pk_bf16_f32 %1, %6, %7\n\t"
            "v_mul_f32 %4, %22, %23\n\tv_mul_f32 %5, %24, %25\n\tv_cvt_pk_bf16_f32 %2, %8, %9\n\tv_cvt_pk_bf16_f32 %3, %4, %5"
            : "=&v"(o0), "=&v"(o1), "=&v"(o2), "=v"(o3), "=&v"(t0), "=&v"(t1), "=&v"(t2), "=&v"(t3), "=&v"(t4), "=&v"(t5)
            : "v"(fa[0]), "v"(fb[0]), "v"(fa[1]), "v"(fb[1]), "v"(fa[2]), "v"(fb[2]), "v"(fa[3]), "v"(fb[3]),
              "v"(fa[4]), "v"(fb[4]), "v"(fa[5]), "v"(fb[5]), "v"(fa[6]), "v"(fb[6]), "v"(fa[7]), "v"(fb[7]));
        z[s] = (u32x4){o0, o1, o2, o3};
    }
#pragma unroll
    for (int s = 22; s < 24; ++s) {
#pragma unroll
        for (int j = 0; j < 4; ++j) {
            const AB p0 = slot_ab(8 * s + 2 * j), p1 = slot_ab(8 * s + 2 * j + 1);
            const float a0 = p0.a < 26 ? x[p0.a] : (p0.a == 26 ? 1.f : 0.f);
            const float a1 = p1.a < 26 ? x[p1.a] : (p1.a == 26 ? 1.f : 0.f);
            z[s][j] = pk_bf16(a0, a1);
        }
    }
#pragma unroll
    for (int s = 0; s < 24; ++s) asm volatile("" : "+v"(z[s]));
    STAMP(4);
    const unsigned tag = epv + 1u;
    {
        const __amdgpu_buffer_rsrc_t srdA = __builtin_amdgcn_make_buffer_rsrc((void*)(P.ws + WS_A), 0, A_BYTES, RSRC3);
        if (cw == 0) {
            if (has_dnn) for (unsigned sp = 0; lds_ld(lds, L_F + 52) == 0 && sp < SPIN_CAP; ++sp) __builtin_amdgcn_s_sleep(1);
            const gu32* fl = (const gu32*)(P.ws + WS_FL) + g * 32 + (lane < 26 ? lane : 0);
            for (unsigned sp = 0; sp < SPIN_CAP; ++sp) { if (__all(ld_agent(fl) == tag)) break; __builtin_amdgcn_s_sleep(1); }
            if (lane == 0) lds_st(lds, L_F + 16, 1);
            if (!has_dnn) dma4_sc1(ldsb + L_DUMMY, lane < 50 ? (unsigned)((((g & 1) * 3 + pf_slot) * 50 + lane) * 128) : 0x7fffff00u, srdA, 0);
        } else {
            for (unsigned sp = 0; lds_ld(lds, L_F + 16) == 0 && sp < SPIN_CAP; ++sp) __builtin_amdgcn_s_sleep(1);
        }
        if (has_dnn) for (unsigned sp = 0; lds_ld(lds, L_F + 56) < 4 && sp < SPIN_CAP; ++sp) __builtin_amdgcn_s_sleep(0);
        CBAR();
        STAMP(5);
        {
            const unsigned l1 = ldsb + L_A + cw * 4096, l2 = ldsb + L_A + 16384 + cw * 6144, vo = lane * 16;
            asm volatile("s_mov_b32 m0, %0\n\ts_nop 0\n\t"
                         "buffer_load_dwordx4 %2, %3, %4 offen sc1 lds\n\tbuffer_load_dwordx4 %2, %3, %4 offen offset:1024 sc1 lds\n\t"
                         "buffer_load_dwordx4 %2, %3, %4 offen offset:2048 sc1 lds\n\tbuffer_load_dwordx4 %2, %3, %4 offen offset:3072 sc1 lds\n\t"
                         "s_mov_b32 m0, %1\n\ts_nop 0\n\t"
                         "buffer_load_dwordx4 %2, %3, %5 offen sc1 lds\n\tbuffer_load_dwordx4 %2, %3, %5 offen offset:1024 sc1 lds\n\t"
                         "buffer_load_dwordx4 %2, %3, %5 offen offset:2048 sc1 lds\n\tbuffer_load_dwordx4 %2, %3, %5 offen offset:3072 sc1 lds\n\t"
                         "s_add_u32 m0, %1, 4096\n\ts_nop 0\n\t"
                         "buffer_load_dwordx4 %2, %3, %6 offen sc1 lds\n\tbuffer_load_dwordx4 %2, %3, %6 offen offset:1024 sc1 lds"
                         :: "s"(l1), "s"(l2), "v"(vo), "s"(srdA), "s"(cw * 4096), "s"(16384 + cw * 6144), "s"(16384 + cw * 6144 + 4096) : "memory", "scc");
        }
        wait_vm<6>();
        if (lane == 0) lds_add(lds, L_F + 20, 1);
        for (unsigned sp = 0; lds_ld(lds, L_F + 20) < 4 && sp < SPIN_CAP; ++sp) __builtin_amdgcn_s_sleep(0);
        CBAR();
    }
    STAMP(6);
    const gu64* pp = (const gu64*)(P.ws + WS_PART) + ((g * 32 + (lane < 26 ? lane : 0)) * 64 + 4 * m + cw);
    unsigned long long pv = 0;
    f32x16 acc0, acc1;
#pragma unroll
    for (int i = 0; i < 16; ++i) { acc0[i] = 0.f; acc1[i] = 0.f; }
    {
        unsigned abo = L_A + (26 * h + d) * 16;
        asm volatile("" : "+v"(abo));
        const lds8* ab = lds + abo;
        constexpr int PF = 6;
        u32x4 af[46];
        unsigned cnt2 = 0;
        auto frag_of = [](int t) { return t; };
#pragma unroll
        for (int t = 0; t < PF; ++t) af[t] = *(const LAS u32x4*)(ab + frag_of(t) * 832);
        __builtin_amdgcn_sched_barrier(0);
#pragma unroll
        for (int t = 0; t < 46; ++t) {
            if (t == 4) { wait_vm<0>(); if (lane == 0) lds_add(lds, L_F + 44, 1); }
            if (t + PF == 19 - 3) cnt2 = lds_ld(lds, L_F + 44);
            if (t + PF == 19) {
                if (cnt2 < 4) for (unsigned sp = 0; lds_ld(lds, L_F + 44) < 4 && sp < SPIN_CAP; ++sp) __builtin_amdgcn_s_sleep(0);
                CBAR();
            }
            if (t + PF < 46) af[t + PF] = *(const LAS u32x4*)(ab + frag_of(t + PF) * 832);
            if (t < 44) {
                if (t & 1) acc0 = mfma32(as_frag(af[t]), as_frag(z[t >> 1]), acc0);
                else acc1 = mfma32(as_frag(af[t]), as_frag(z[t >> 1]), acc1);
            } else acc1 = mfma32(as_frag(af[t]), as_frag(z[t - 22]), acc1);
            if (t == 30) pv = __hip_atomic_load(pp, __ATOMIC_RELAXED, __HIP_MEMORY_SCOPE_AGENT);
            __builtin_amdgcn_sched_barrier(0);
        }
    }
    float sa = 0.f;
#pragma unroll
    for (int i = 0; i < 12; ++i) sa = __builtin_fmaf(acc0[i], acc1[i], sa);
    const float sb = __builtin_fmaf(acc0[12], acc1[12], acc0[13] * acc1[13]);
    float tot = h == 0 ? sa + sb : sa;
    asm volatile("" : "+v"(tot));
    STAMP(7);
    for (unsigned sp = 0; sp < SPIN_CAP; ++sp) {
        if (__all((unsigned)(pv >> 32) == tag)) break;
        pv = __hip_atomic_load(pp, __ATOMIC_RELAXED, __HIP_MEMORY_SCOPE_AGENT);
    }
    STAMP(8);
    if (lane < 26) tot += __uint_as_float((unsigned)pv);
    tot = wave_sum_to_lane63(tot);
    if (lane == 63) {
        const float o = tot + bf_lo(bov);
        P.out[b] = (unsigned short)(pk_bf16(o, o) & 0xffffu);
    }
    if (m == 0 && cw == 0) {
        const gu64* ap = (const gu64*)(P.ws + WS_PART) + ((g * 32 + 26 + (lane < 3 ? lane : 0)) * 64);
        for (unsigned sp = 0; sp < SPIN_CAP; ++sp) {
            const unsigned long long av = __hip_atomic_load(ap, __ATOMIC_RELAXED, __HIP_MEMORY_SCOPE_AGENT);
            if (__all((unsigned)(av >> 32) == tag)) break;
        }
        if (lane == 0) __hip_atomic_store((gu32*)(P.ws + WS_EP) + g, tag, __ATOMIC_RELAXED, __HIP_MEMORY_SCOPE_AGENT);
    }
}

DI void decode_block(int bid, int& g, int& m, int& ntile, int& k) {
    const int x = bid & 7, j = bid >> 3;
    g = 2 * x + (j >> 4); m = j & 15;
    ntile = m < 13 ? m : -1; k = m - 13;
}

__global__ void __launch_bounds__(512, 2) xdfm_kernel(Params P) {
    __shared__ __attribute__((aligned(16))) unsigned char smem[L_END];
    lds8* lds = (lds8*)smem;
    STAMP(0);
    const int w = __builtin_amdgcn_readfirstlane(threadIdx.x >> 6), lane = threadIdx.x & 63;
    {
        unsigned long long pc; unsigned len;
        asm volatile("s_getpc_b64 %0\n.Lxdfm_pc:\n\ts_mov_b32 %1, (.Lxdfm_end-.Lxdfm_pc)&4294967295" : "=s"(pc), "=s"(len));
        const __amdgpu_buffer_rsrc_t srdP = __builtin_amdgcn_make_buffer_rsrc((void*)pc, 0, len, RSRC3);
        dma4((unsigned)(size_t)lds + L_DUMMY, lane == 0 ? ((blockIdx.x >> 3) * 8 + w) * 128 : 0x7fffff00u, srdP, 0);
    }
    asm volatile("" :: "s"(P.idx), "s"(P.table), "s"(P.wcin), "s"(P.wout), "s"(P.w1), "s"(P.b1), "s"(P.w2), "s"(P.bout), "s"(P.out), "s"(P.ws));
    int g, m, ntile, k;
    decode_block(blockIdx.x, g, m, ntile, k);
    const bool is_cin = w >= 4;
    if (is_cin) {
        __builtin_amdgcn_s_setprio(3);
        dma16((unsigned)(size_t)lds + L_X0 + (w - 4) * 1664, lane < 7 ? (64 * g + 4 * m + w - 4) * 104 + lane * 16 : 0x7fffff00u,
              __builtin_amdgcn_make_buffer_rsrc((void*)P.idx, 0, NBATCH * 26 * 4, RSRC3), 0);
        if (w == 7) dma4((unsigned)(size_t)lds + L_DUMMY, lane < 26 ? ((blockIdx.x >> 3) * 26 + lane) * 128 : 0x7fffff00u,
                         __builtin_amdgcn_make_buffer_rsrc((void*)P.wcin, 0, 3 * 17576 * 2, RSRC3), 0);
    }
    if (w == 0) {
        if (lane < 16) lds_st(lds, L_F + 4 * lane, 0);
        dma4_sc1((unsigned)(size_t)lds + L_EP, (lane & 15) * 4, __builtin_amdgcn_make_buffer_rsrc((void*)(P.ws + WS_EP), 0, 64, RSRC3), 0);
    }
    if (__builtin_expect(!is_cin && ntile >= 0, 1)) {
        dnn_role(P, lds, w, lane, g, ntile STAMP_PASS);
    } else if (is_cin) {
        cin_role(P, lds, w - 4, lane, g, m, ntile >= 0, k STAMP_PASS);
    } else {
        const int p = g * 3 + k;
        if (p < 26) prep_role(P, lds, w, lane, p STAMP_PASS);
        else { asm volatile("s_waitcnt vmcnt(0) lgkmcnt(0)" ::: "memory"); __builtin_amdgcn_s_barrier(); }
        if (w == 0) {
            const unsigned ep = *(const LAS unsigned*)(lds + L_EP + g * 4);
            if (lane == 0) st_granule((gu64*)(P.ws + WS_PART) + ((g * 32 + 26 + k) * 64), ep + 1u, 0u);
        }
    }
    STAMP(11);
    STAMP_FLUSH(P.ws, w, lane);
    asm volatile(".Lxdfm_end:");
}

extern "C" void kernel_launch(void* const* d_in, const int* in_sizes, int n_in,
                              void* d_out, int out_size, void* d_ws, size_t ws_size,
                              hipStream_t stream) {
    Params P;
    P.idx = (const int*)d_in[0]; P.table = (const unsigned short*)d_in[1]; P.wcin = (const unsigned short*)d_in[2];
    P.wout = (const unsigned short*)d_in[3]; P.w1 = (const unsigned short*)d_in[4]; P.b1 = (const unsigned short*)d_in[5];
    P.w2 = (const unsigned short*)d_in[6]; P.bout = (const unsigned short*)d_in[7];
    P.out = (unsigned short*)d_out; P.ws = (unsigned char*)d_ws;
    xdfm_kernel<<<256, 512, 0, stream>>>(P);
}
```

```cpp
#include <hip/hip_runtime.h>
#include <stdint.h>

#define DI __device__ __forceinline__
#define GAS __attribute__((address_space(1)))
#define LAS __attribute__((address_space(3)))

typedef short    s16x4 __attribute__((ext_vector_type(4)));
typedef short    s16x8 __attribute__((ext_vector_type(8)));
typedef float    f32x2 __attribute__((ext_vector_type(2)));
typedef float    f32x4 __attribute__((ext_vector_type(4)));
typedef float    f32x16 __attribute__((ext_vector_type(16)));
typedef unsigned u32x2 __attribute__((ext_vector_type(2)));
typedef unsigned u32x4 __attribute__((ext_vector_type(4)));
typedef __bf16   bf16x2 __attribute__((ext_vector_type(2)));
typedef GAS unsigned gu32;
typedef GAS unsigned long long gu64;
typedef LAS unsigned char lds8;

#define MAPPING 1

constexpr int DIN = 832, NBATCH = 1024, NVOC = 100000;
#define A_AFTER_X 1
#define W_EARLY 1
#define A_PF 1
#define CODE_PF 1
#define WC_PF 1
#define POLL_STEP 10
#define CIN_PRIO 2
#define PKMUL 0
#define A_DMA 1
constexpr unsigned RSRC3 = 0x00020000u;
constexpr int AUX_SC1 = 16;
constexpr unsigned SPIN_CAP = 1u << 20;

constexpr unsigned WS_EP = 0;
constexpr unsigned WS_FL = 4096;
constexpr unsigned WS_PART = 8192;
constexpr unsigned WS_A = 8192 + 16 * 32 * 64 * 8;
constexpr unsigned A_BYTES = 46 * 832;

#define XD 6
constexpr unsigned L_W = 0;
constexpr unsigned L_A = 832 * 128;
constexpr unsigned L_X0 = L_A + 4 * XD * 2048;
constexpr unsigned L_F = L_X0 + 4 * 1664;
constexpr unsigned L_DUMMY = L_F + 256;
constexpr unsigned L_EP = L_DUMMY + 256;
constexpr unsigned L_BI = L_EP + 256;
constexpr unsigned L_W2 = L_BI + 256;
constexpr unsigned L_END = L_W2 + 256;
static_assert(4 * XD * 2048 >= A_BYTES && L_END <= 163840, "LDS map");
constexpr unsigned LP_VT = 0;
constexpr unsigned LP_T0 = 2048;
constexpr unsigned LP_T1 = 2048 + 2304;

struct AB { int a, b; };
constexpr AB slot_ab(int t) {
    if (t < 156) { int a = 2 * (t / 12); return {a, (a + t % 12) % 26}; }
    if (t < 170) { int u = t - 156; int a = 2 * (u / 2); return {a, (a + 12 + (u & 1)) % 26}; }
    if (t < 176) { int a = 2 * (7 + t - 170); return {a, (a + 12) % 26}; }
    if (t < 189) return {2 * (t - 176), 26};
    if (t == 189) return {26, 26};
    return {27, 27};
}
constexpr unsigned T_ZERO = 1024, T_CONST = 1025;
struct PrepTab { unsigned v[2][24 * 2 * 8]; };
constexpr PrepTab make_tab() {
    PrepTab tb{};
    for (int m = 0; m < 2; ++m) for (int s = 0; s < 24; ++s) for (int h = 0; h < 2; ++h) for (int e = 0; e < 8; ++e) {
        const int st = m ? 32 : 26;
        int t = 8 * s + e; AB ab = slot_ab(t);
        unsigned o1 = T_ZERO, o2 = T_ZERO;
        if (ab.a == 27) {}
        else if (ab.a == 26) { if (h == 0 && m) o1 = T_CONST; }
        else if (ab.b == 26) { if (m) o1 = ((ab.a + h) % 26) * st + 26; }
        else {
            int u = (ab.a + h) % 26, v = (ab.b + h) % 26;
            bool dup = (t == 169 && h == 1);
            if (dup) {}
            else if (u == v) o1 = u * st + u;
            else { o1 = u * st + v; o2 = v * st + u; }
        }
        tb.v[m][(s * 2 + h) * 8 + e] = o1 | (o2 << 16);
    }
    return tb;
}
static_assert(slot_ab(169).a == 12 && slot_ab(169).b == 25, "dup slot");
__device__ const PrepTab g_tab = make_tab();

struct Params {
    const int* idx; const unsigned short* table; const unsigned short* wcin; const unsigned short* wout;
    const unsigned short* w1; const unsigned short* b1; const unsigned short* w2; const unsigned short* bout;
    unsigned short* out; unsigned char* ws;
};

DI unsigned sel(bool c, unsigned a, unsigned b) { unsigned r = c ? a : b; return r; }
DI float bf_lo(unsigned v) { return __uint_as_float(v << 16); }
DI float bf_hi(unsigned v) { return __uint_as_float(v & 0xffff0000u); }
DI unsigned pk_bf16(float lo, float hi) { f32x2 v = {lo, hi}; return __builtin_bit_cast(unsigned, __builtin_convertvector(v, bf16x2)); }
DI f32x4 mfma16(s16x8 a, s16x8 b, f32x4 c) { return __builtin_amdgcn_mfma_f32_16x16x32_bf16(a, b, c, 0, 0, 0); }
DI f32x16 mfma32(s16x8 a, s16x8 b, f32x16 c) { return __builtin_amdgcn_mfma_f32_32x32x16_bf16(a, b, c, 0, 0, 0); }
DI s16x8 as_frag(u32x4 v) { return __builtin_bit_cast(s16x8, v); }
template <int CTRL, int ROWMASK> DI float dpp_add(float v) {
    return v + __int_as_float(__builtin_amdgcn_update_dpp(0, __float_as_int(v), CTRL, ROWMASK, 0xf, true));
}
DI float wave_sum_to_lane63(float v) {
    v = dpp_add<0x111, 0xf>(v); v = dpp_add<0x112, 0xf>(v); v = dpp_add<0x114, 0xf>(v); v = dpp_add<0x118, 0xf>(v);
    v = dpp_add<0x142, 0xa>(v); v = dpp_add<0x143, 0xc>(v);
    return v;
}
DI float sum_lane_groups(float v) {
    auto a = __builtin_amdgcn_permlane16_swap(__float_as_uint(v), __float_as_uint(v), false, false);
    v = __uint_as_float(a[0]) + __uint_as_float(a[1]);
    auto b = __builtin_amdgcn_permlane32_swap(__float_as_uint(v), __float_as_uint(v), false, false);
    return __uint_as_float(b[0]) + __uint_as_float(b[1]);
}
DI float gelu_tanh(float x) {
    const float k1 = -2.0f * 0.7978845608028654f * 1.4426950408889634f;
    const float k2 = k1 * 0.044715f;
    float arg = x * __builtin_fmaf(x * x, k2, k1);
    float e = __builtin_amdgcn_exp2f(arg);
    return x * __builtin_amdgcn_rcpf(1.0f + e);
}
DI void dma16(unsigned lds_addr, unsigned voff, __amdgpu_buffer_rsrc_t srd, unsigned soff) {
    asm volatile("s_mov_b32 m0, %0\n\ts_nop 0\n\tbuffer_load_dwordx4 %1, %2, %3 offen lds"
                 :: "s"(lds_addr), "v"(voff), "s"(srd), "s"(soff) : "memory");
}
DI void dma16_sc1(unsigned lds_addr, unsigned voff, __amdgpu_buffer_rsrc_t srd, unsigned soff) {
    asm volatile("s_mov_b32 m0, %0\n\ts_nop 0\n\tbuffer_load_dwordx4 %1, %2, %3 offen sc1 lds"
                 :: "s"(lds_addr), "v"(voff), "s"(srd), "s"(soff) : "memory");
}
DI void dma4_sc1(unsigned lds_addr, unsigned voff, __amdgpu_buffer_rsrc_t srd, unsigned soff) {
    asm volatile("s_mov_b32 m0, %0\n\ts_nop 0\n\tbuffer_load_dword %1, %2, %3 offen sc1 lds"
                 :: "s"(lds_addr), "v"(voff), "s"(srd), "s"(soff) : "memory");
}
DI void dma4(unsigned lds_addr, unsigned voff, __amdgpu_buffer_rsrc_t srd, unsigned soff) {
    asm volatile("s_mov_b32 m0, %0\n\ts_nop 0\n\tbuffer_load_dword %1, %2, %3 offen lds"
                 :: "s"(lds_addr), "v"(voff), "s"(srd), "s"(soff) : "memory");
}
DI void st_granule(gu64* g, unsigned tag, unsigned val) {
    __hip_atomic_store(g, ((unsigned long long)tag << 32) | val, __ATOMIC_RELAXED, __HIP_MEMORY_SCOPE_AGENT);
}
DI unsigned ld_agent(const gu32* p) { return __hip_atomic_load(p, __ATOMIC_RELAXED, __HIP_MEMORY_SCOPE_AGENT); }
DI unsigned lds_ld(lds8* l, unsigned off) { return __hip_atomic_load((LAS unsigned*)(l + off), __ATOMIC_RELAXED, __HIP_MEMORY_SCOPE_WORKGROUP); }
DI void lds_st(lds8* l, unsigned off, unsigned v) { __hip_atomic_store((LAS unsigned*)(l + off), v, __ATOMIC_RELAXED, __HIP_MEMORY_SCOPE_WORKGROUP); }
DI void lds_add(lds8* l, unsigned off, unsigned v) { __hip_atomic_fetch_add((LAS unsigned*)(l + off), v, __ATOMIC_RELAXED, __HIP_MEMORY_SCOPE_WORKGROUP); }
DI void sig_add(lds8* l, unsigned off) {
    asm volatile("s_mov_b64 exec, 1\n\tds_add_u32 %0, %1\n\ts_mov_b64 exec, -1" :: "v"((unsigned)(size_t)l + off), "v"(1u) : "memory");
}
DI void sig_set(lds8* l, unsigned off) {
    asm volatile("s_mov_b64 exec, 1\n\tds_write_b32 %0, %1\n\ts_mov_b64 exec, -1" :: "v"((unsigned)(size_t)l + off), "v"(1u) : "memory");
}
#define WAIT_LGKM() asm volatile("s_waitcnt lgkmcnt(0)" ::: "memory")
#define WAIT_VM0() asm volatile("s_waitcnt vmcnt(0)" ::: "memory")
#define CBAR() asm volatile("" ::: "memory")
#define STAMP(i)
#define STAMP_ARG
#define STAMP_PASS
#define STAMP_FLUSH(ws, w, lane)

template <int K> DI unsigned quad_bcast(unsigned v) { return __builtin_amdgcn_update_dpp(0u, v, K * 0x55, 0xf, 0xf, true); }
template <int N> DI void wait_vm() { asm volatile("s_waitcnt vmcnt(%0)" :: "i"(N) : "memory"); }
DI void dma_w4(unsigned lds_addr, unsigned voff, __amdgpu_buffer_rsrc_t srd, unsigned soff) {
    unsigned t;
    asm volatile("s_mov_b32 m0, %1\n\ts_add_u32 %0, %4, 12288\n\tbuffer_load_dwordx4 %2, %3, %4 offen lds\n\t"
                 "buffer_load_dwordx4 %2, %3, %0 offen offset:1024 lds\n\ts_add_u32 %0, %0, 12288\n\t"
                 "buffer_load_dwordx4 %2, %3, %0 offen offset:2048 lds\n\ts_add_u32 %0, %0, 12288\n\t"
                 "buffer_load_dwordx4 %2, %3, %0 offen offset:3072 lds"
                 : "=&s"(t) : "s"(lds_addr), "v"(voff), "s"(srd), "s"(soff) : "memory", "scc");
}
DI void dnn_start(const Params& P, lds8* lds, int w, int lane, int g, int ntile) {
    const int mh = w & 1, kh = w >> 1;
    const unsigned ldsb = (unsigned)(size_t)lds;
    const unsigned ring = ldsb + L_A + w * (XD * 2048);
    const __amdgpu_buffer_rsrc_t srdI = __builtin_amdgcn_make_buffer_rsrc((void*)P.idx, 0, NBATCH * 26 * 4, RSRC3);
    const unsigned vo = ((64 * g + 32 * mh + (lane >> 2)) * 26 + 13 * kh + 4 * (lane & 3)) * 4;
    dma16(ring, vo, srdI, 0);
    dma16(ring + 1024, vo, srdI, 1664);
    if (__builtin_expect(w == 1, 1)) dma4(ldsb + L_BI, ntile * 128 + lane * 4, __builtin_amdgcn_make_buffer_rsrc((void*)P.b1, 0, DIN * 2, RSRC3), 0);
    if (__builtin_expect(w == 2, 1)) dma4(ldsb + L_W2, ntile * 128 + lane * 4, __builtin_amdgcn_make_buffer_rsrc((void*)P.w2, 0, DIN * 2, RSRC3), 0);
    const __amdgpu_buffer_rsrc_t srdW = __builtin_amdgcn_make_buffer_rsrc((void*)P.w1, 0, DIN * DIN * 2, RSRC3);
    const unsigned vw = (lane >> 3) * 1664 + (((lane & 7) ^ (((lane >> 4) & 1) << 2)) << 4);
    const unsigned rowA = 416 * kh + 208 * mh + 104 * (g & 1);
    dma_w4(ldsb + L_W + rowA * 128, vw, srdW, rowA * 1664 + ntile * 128);
}
DI void dnn_role(const Params& P, lds8* lds, int w, int lane, int g, int ntile STAMP_ARG) {
    const int mh = w & 1, kh = w >> 1;
    const int h = lane >> 5;
    const unsigned ldsb = (unsigned)(size_t)lds;
    const unsigned ring = ldsb + L_A + w * (XD * 2048);
    const __amdgpu_buffer_rsrc_t srdW = __builtin_amdgcn_make_buffer_rsrc((void*)P.w1, 0, DIN * DIN * 2, RSRC3);
    const unsigned vw = (lane >> 3) * 1664 + (((lane & 7) ^ (((lane >> 4) & 1) << 2)) << 4);
    const unsigned rowA = 416 * kh + 208 * mh + 104 * (g & 1), rowB = 416 * kh + 208 * mh + 104 * ((g & 1) ^ 1);
    auto wdma = [&](unsigned row, int a) {
        const unsigned sb = row * 1664 + ntile * 128, lb = ldsb + L_W + row * 128;
        if (a < 3) dma_w4(lb + a * 4096, vw, srdW, sb + a * 4 * 13312);
        else dma16(lb + 12288, vw, srdW, sb + 12 * 13312);
    };
    STAMP(1);
    wait_vm<4>();
    WAIT_LGKM();
    __builtin_amdgcn_s_barrier();
    STAMP(2);
    const u32x4 i0 = *(const LAS u32x4*)(lds + L_A + w * (XD * 2048) + lane * 16);
    const u32x4 i1 = *(const LAS u32x4*)(lds + L_A + w * (XD * 2048) + 1024 + lane * 16);
    const unsigned ep = *(const LAS unsigned*)(lds + L_EP + g * 4);
#pragma unroll
    for (int a = 1; a < 4; ++a) wdma(rowA, a);
#pragma unroll
    for (int a = 0; a < 4; ++a) wdma(rowB, a);
    const __amdgpu_buffer_rsrc_t srdT = __builtin_amdgcn_make_buffer_rsrc((void*)P.table, 0, NVOC * 64, RSRC3);
    const unsigned c16 = (((lane & 3) ^ ((lane >> 4) & 3)) << 4);
    u32x4 t0 = i0 << 6, t1 = i1 << 6;
    asm volatile("" : "+v"(t0), "+v"(t1));
    auto xload = [&](int s) {
        const unsigned a0 = t0[s & 3], a1 = t1[s & 3];
        const unsigned o0 = (s < 4 ? quad_bcast<0>(a0) : s < 8 ? quad_bcast<1>(a0) : s < 12 ? quad_bcast<2>(a0) : quad_bcast<3>(a0)) + c16;
        const unsigned o1 = (s < 4 ? quad_bcast<0>(a1) : s < 8 ? quad_bcast<1>(a1) : s < 12 ? quad_bcast<2>(a1) : quad_bcast<3>(a1)) + c16;
        dma16(ring + (s % XD) * 2048, o0, srdT, 0);
        dma16(ring + (s % XD) * 2048 + 1024, o1, srdT, 0);
    };
#pragma unroll
    for (int s = 0; s < XD; ++s) xload(s);
    STAMP(3);
    wait_vm<2 * XD>();
    sig_add(lds, L_F + 32 + 4 * kh);
    _Pragma("unroll 1") for (unsigned sp = 0; lds_ld(lds, L_F + 32 + 4 * kh) < 2 && sp < SPIN_CAP; ++sp) __builtin_amdgcn_s_sleep(0);
    CBAR();
    STAMP(4);
    f32x16 acc[2];
    u32x2 bia[4], wv[4];
#pragma unroll
    for (int r4 = 0; r4 < 4; ++r4) {
        bia[r4] = *(const LAS u32x2*)(lds + L_BI + kh * 64 + h * 8 + r4 * 16);
        wv[r4] = *(const LAS u32x2*)(lds + L_W2 + kh * 64 + h * 8 + r4 * 16);
    }
#pragma unroll
    for (int r4 = 0; r4 < 4; ++r4) {
        acc[0][4 * r4] = bf_lo(bia[r4][0]); acc[0][4 * r4 + 1] = bf_hi(bia[r4][0]); acc[0][4 * r4 + 2] = bf_lo(bia[r4][1]); acc[0][4 * r4 + 3] = bf_hi(bia[r4][1]);
        acc[1][4 * r4] = 0.f; acc[1][4 * r4 + 1] = 0.f; acc[1][4 * r4 + 2] = 0.f; acc[1][4 * r4 + 3] = 0.f;
    }
    {
        const int blk = (lane >> 4) & 1, q = (lane & 15) >> 2, pp = lane & 3, n = lane & 31;
        unsigned ad[2];
#pragma unroll
        for (int j = 0; j < 2; ++j) {
            const unsigned c = 4 * (j ^ kh) + 2 * blk + (pp >> 1);
            ad[j] = L_W + (416 * kh + 8 * h + q) * 128 + ((c ^ (((q >> 1) & 1) << 2)) << 4) + (pp & 1) * 8;
        }
        const unsigned xb = L_A + w * (XD * 2048) + n * 64;
        const unsigned xa0 = xb + ((h ^ ((n >> 2) & 3)) << 4), xa1 = xb + (((2 + h) ^ ((n >> 2) & 3)) << 4);
        u32x4 xf[3][2];
        s16x8 wf[2][4];
        auto xread = [&](int s) {
            xf[s % 3][0] = *(const LAS u32x4*)(lds + xa0 + (s % XD) * 2048);
            xf[s % 3][1] = *(const LAS u32x4*)(lds + xa1 + (s % XD) * 2048);
        };
        auto wread = [&](int s) {
#pragma unroll
            for (int a = 0; a < 2; ++a)
#pragma unroll
                for (int j = 0; j < 2; ++j) {
                    const s16x4 lo = __builtin_amdgcn_ds_read_tr16_b64_v4i16((LAS s16x4*)(lds + ad[j] + s * 4096 + a * 2048));
                    const s16x4 hi = __builtin_amdgcn_ds_read_tr16_b64_v4i16((LAS s16x4*)(lds + ad[j] + s * 4096 + a * 2048 + 512));
                    wf[s & 1][2 * a + j] = __builtin_shufflevector(lo, hi, 0, 1, 2, 3, 4, 5, 6, 7);
                }
        };
        wread(0);
        wait_vm<2 * (XD - 1)>();
        xread(0);
        wait_vm<2 * (XD - 2)>();
        xread(1);
        __builtin_amdgcn_sched_barrier(0);
#pragma unroll
        for (int s = 0; s < 13; ++s) {
            if (s + 1 < 13) wread(s + 1);
#pragma unroll
            for (int a = 0; a < 2; ++a)
#pragma unroll
                for (int j = 0; j < 2; ++j) acc[j] = mfma32(wf[s & 1][2 * a + j], as_frag(xf[s % 3][a]), acc[j]);
            if (s + 1 < 13) {
#pragma unroll
                for (int j = 0; j < 4; ++j) { __builtin_amdgcn_sched_group_barrier(0x100, 2, 0); __builtin_amdgcn_sched_group_barrier(0x8, 1, 0); }
            }
            __builtin_amdgcn_sched_barrier(0);
            if (s + XD < 13) xload(s + XD);
            if (s + 2 < 13) {
                constexpr int LASTI = 12;
                const int last = s + XD < LASTI ? s + XD : LASTI;
                const int younger = last - (s + 2);
                if (younger >= 4) wait_vm<8>(); else if (younger == 3) wait_vm<6>(); else if (younger == 2) wait_vm<4>(); else if (younger == 1) wait_vm<2>(); else wait_vm<0>();
                if (s + 2 == 12) sig_add(lds, L_F + 52);
                xread(s + 2);
            }
            __builtin_amdgcn_sched_barrier(0);
        }
    }
    STAMP(6);
    {
        _Pragma("unroll 1") for (unsigned sp = 0; lds_ld(lds, L_F + 52) < 4 && sp < SPIN_CAP; ++sp) __builtin_amdgcn_s_sleep(0);
        CBAR();
        lds8* xw = lds + L_W + kh * 53248 + (5 + mh) * 4096 + lane * 16;
#pragma unroll
        for (int r4 = 0; r4 < 4; ++r4) *(LAS f32x4*)(xw + r4 * 1024) = (f32x4){acc[1][4 * r4], acc[1][4 * r4 + 1], acc[1][4 * r4 + 2], acc[1][4 * r4 + 3]};
        CBAR();
        sig_set(lds, L_F + 4 * w);
        _Pragma("unroll 1") for (unsigned sp = 0; lds_ld(lds, L_F + 4 * (w ^ 2)) == 0 && sp < SPIN_CAP; ++sp) __builtin_amdgcn_s_sleep(0);
        CBAR();
        const lds8* xr = lds + L_W + (kh ^ 1) * 53248 + (5 + mh) * 4096 + lane * 16;
#pragma unroll
        for (int r4 = 0; r4 < 4; ++r4) {
            const f32x4 o = *(const LAS f32x4*)(xr + r4 * 1024);
            acc[0][4 * r4] += o[0]; acc[0][4 * r4 + 1] += o[1]; acc[0][4 * r4 + 2] += o[2]; acc[0][4 * r4 + 3] += o[3];
        }
    }
    STAMP(7);
    float s0 = 0.f, s1 = 0.f;
#pragma unroll
    for (int r4 = 0; r4 < 4; ++r4) {
        s0 = __builtin_fmaf(gelu_tanh(acc[0][4 * r4]), bf_lo(wv[r4][0]), s0);
        s1 = __builtin_fmaf(gelu_tanh(acc[0][4 * r4 + 1]), bf_hi(wv[r4][0]), s1);
        s0 = __builtin_fmaf(gelu_tanh(acc[0][4 * r4 + 2]), bf_lo(wv[r4][1]), s0);
        s1 = __builtin_fmaf(gelu_tanh(acc[0][4 * r4 + 3]), bf_hi(wv[r4][1]), s1);
    }
    s0 += s1;
    const auto sw = __builtin_amdgcn_permlane32_swap(__float_as_uint(s0), __float_as_uint(s0), false, false);
    const float part = __uint_as_float(sw[0]) + __uint_as_float(sw[1]);
    STAMP(8);
    if (lane < 32) st_granule((gu64*)(P.ws + WS_PART) + ((g * 32 + 2 * ntile + kh) * 64 + 32 * mh + lane), ep + 1u, __float_as_uint(part));
}

constexpr unsigned LP_W2 = 12288;
constexpr unsigned LP_WO = 14336;
constexpr unsigned LP_DM = 15360;
constexpr unsigned LP_W3 = 16384;
static_assert(LP_T1 + 4 * 1026 <= LP_W2, "prep scratch");
constexpr unsigned w3_slot(int j) { return j * 1280 + (j & 1) * 64 + ((j >> 2) & 1) * 128; }
DI void dma16_add(unsigned lds_base, unsigned lds_off, unsigned voff, __amdgpu_buffer_rsrc_t srd, unsigned sbase, unsigned soff) {
    unsigned t;
    asm volatile("s_add_u32 m0, %1, %2\n\ts_add_u32 %0, %5, %6\n\tbuffer_load_dwordx4 %3, %4, %0 offen lds"
                 : "=&s"(t) : "s"(lds_base), "i"(lds_off), "v"(voff), "s"(srd), "s"(sbase), "i"(soff) : "memory", "scc");
}
DI void prep_start(const Params& P, lds8* lds, int w, int lane, int p) {
    const unsigned ldsb = (unsigned)(size_t)lds;
    const __amdgpu_buffer_rsrc_t srdC = __builtin_amdgcn_make_buffer_rsrc((void*)P.wcin, 0, 3 * 17576 * 2, RSRC3);
    const int i0 = w == 0 ? 0 : w == 1 ? 6 : w == 2 ? 14 : 20;
    dma4(ldsb + LP_WO + w * 256, lane * 4, __builtin_amdgcn_make_buffer_rsrc((void*)P.wout, 0, 78 * 2, RSRC3), 0);
    {
        const unsigned hp = lane >> 5;
        const unsigned vo = hp * 1352 + ((lane >> 2) & 7) * 52 + (((lane & 3) ^ (hp << 1)) << 4);
        const unsigned lb = ldsb + LP_W3 + w * 16640, sb = 52 * 1352 + 52 * i0;
        dma16_add(lb, w3_slot(0), vo, srdC, sb, 0 * 2704);  dma16_add(lb, w3_slot(1), vo, srdC, sb, 1 * 2704);
        dma16_add(lb, w3_slot(2), vo, srdC, sb, 2 * 2704);  dma16_add(lb, w3_slot(3), vo, srdC, sb, 3 * 2704);
        dma16_add(lb, w3_slot(4), vo, srdC, sb, 4 * 2704);  dma16_add(lb, w3_slot(5), vo, srdC, sb, 5 * 2704);
        dma16_add(lb, w3_slot(6), vo, srdC, sb, 6 * 2704);  dma16_add(lb, w3_slot(7), vo, srdC, sb, 7 * 2704);
        dma16_add(lb, w3_slot(8), vo, srdC, sb, 8 * 2704);  dma16_add(lb, w3_slot(9), vo, srdC, sb, 9 * 2704);
        dma16_add(lb, w3_slot(10), vo, srdC, sb, 10 * 2704); dma16_add(lb, w3_slot(11), vo, srdC, sb, 11 * 2704);
        dma16_add(lb, w3_slot(12), vo, srdC, sb, 12 * 2704);
    }
    if (__builtin_expect(w < 2, 1)) {
        const unsigned vo = w == 0 ? 35152 + (lane >> 2) * 1352 + p * 52 + (lane & 3) * 16 : p * 1352 + lane * 16;
        const unsigned lb = ldsb + (w == 0 ? LP_W2 : LP_T0);
        dma16(lb, vo, srdC, 0);
        dma16(lb + 1024, vo, srdC, w == 0 ? 16 * 1352 : 1024);
    }
}
DI void prep_role(const Params& P, lds8* lds, int w, int lane, int p STAMP_ARG) {
    const int n = lane & 15, gq = lane >> 4;
    const int i0 = w == 0 ? 0 : w == 1 ? 6 : w == 2 ? 14 : 20;
    const int nact = w == 0 ? 48 : 44;
    const int ch = lane < nact ? lane : 0;
    const int cs = ch >> 1, chh = ch & 1;
    const GAS u32x4* tp = (const GAS u32x4*)&g_tab.v[w == 0 ? 1 : 0][(cs * 2 + chh) * 8];
    const u32x4 ta = tp[0], tb = tp[1];
    STAMP(1);
    const int q = (lane & 15) >> 2, pp = lane & 3;
    int r0 = 8 * gq + q, r1 = r0 + 4;
    r0 = r0 < 26 ? r0 : 25; r1 = r1 < 26 ? r1 : 25;
    unsigned b0a, b0b, b1a, b1b;
    {
        const unsigned wb = LP_W3 + w * 16640 + pp * 8;
        const unsigned j0 = r0 >> 1, j1 = r1 >> 1;
        const unsigned s0 = wb + j0 * 1280 + (j0 & 1) * 64 + ((j0 >> 2) & 1) * 128 + (r0 & 1) * 512;
        const unsigned s1 = wb + j1 * 1280 + (j1 & 1) * 64 + ((j1 >> 2) & 1) * 128 + (r1 & 1) * 512;
        b0a = s0 + (r0 & 1) * 32; b0b = s0 + 32 - (r0 & 1) * 32; b1a = s1 + (r1 & 1) * 32; b1b = s1 + 32 - (r1 & 1) * 32;
    }
    unsigned va = LP_VT + n * 64 + 2 * i0, vb = n < 10 ? va + 1024 : LP_DM;
    unsigned vl = w == 3 ? LP_DM : 0;
    unsigned ac = LP_T1 + 4 * (4 * gq * 32 + n);
    unsigned a20 = LP_W2 + r0 * 64 + pp * 8, a21 = LP_W2 + r1 * 64 + pp * 8, abv = LP_VT + n * 64 + gq * 16;
    asm volatile("" : "+v"(b0a), "+v"(b0b), "+v"(b1a), "+v"(b1b), "+v"(va), "+v"(vb), "+v"(ac), "+v"(a20), "+v"(a21), "+v"(abv));
    if (w == 3) {
        LAS unsigned* z = (LAS unsigned*)(lds + LP_VT + (lane < 27 ? lane : 31) * 64 + 52);
        z[0] = 0; z[1] = 0; z[2] = 0;
        *(LAS unsigned*)(lds + LP_T1 + 4 * T_ZERO) = 0;
    }
    if (w == 1) *(LAS unsigned*)(lds + LP_T0 + 2 * T_ZERO) = 0;
    asm volatile("s_waitcnt vmcnt(0) lgkmcnt(0)" ::: "memory");
    __builtin_amdgcn_s_barrier();
    STAMP(2);
    {
        const u32x4 wo3 = *(const LAS u32x4*)(lds + LP_WO + w * 256 + 104 + 16 * gq);
        s16x8 bfr[16];
#pragma unroll
        for (int t = 0; t < 16; ++t) {
            const int ii = t >> 1, u = t & 1;
            const s16x4 lo = __builtin_amdgcn_ds_read_tr16_b64_v4i16((LAS s16x4*)(lds + (u ? b0b : b0a) + ii * 64));
            const s16x4 hi = __builtin_amdgcn_ds_read_tr16_b64_v4i16((LAS s16x4*)(lds + (u ? b1b : b1a) + ii * 64));
            bfr[t] = __builtin_shufflevector(lo, hi, 0, 1, 2, 3, 4, 5, 6, 7);
        }
        if (w == 3) {
            const unsigned o2 = *(const LAS unsigned*)(lds + LP_WO + 768 + 52 + (lane < 13 ? lane : 0) * 4);
            const unsigned o1 = *(const LAS unsigned short*)(lds + LP_WO + 768 + p * 2);
            *(LAS unsigned*)(lds + (lane < 13 ? LP_VT + 26 * 64 + lane * 4 : LP_DM)) = o2;
            *(LAS float*)(lds + LP_T1 + 4 * T_CONST) = bf_lo(o1);
        }
        float res[16];
#pragma unroll
        for (int t = 0; t < 16; ++t) res[t] = mfma16(as_frag(wo3), bfr[t], (f32x4){0.f, 0.f, 0.f, 0.f})[0];
#pragma unroll
        for (int k2 = 0; k2 < 4; ++k2) {
            const unsigned pa0 = pk_bf16(res[4 * k2], res[4 * k2 + 2]), pb0 = pk_bf16(res[4 * k2 + 1], res[4 * k2 + 3]);
            if (k2 < 3) { *(LAS unsigned*)(lds + va + 4 * k2) = pa0; *(LAS unsigned*)(lds + vb + 4 * k2) = pb0; }
            else { *(LAS unsigned*)(lds + (vl ? vl : va + 12)) = pa0; *(LAS unsigned*)(lds + (vl ? vl : vb + 12)) = pb0; }
        }
    }
    CBAR();
    sig_add(lds, L_F + 28);
    if (w > 1) return;
    STAMP(3);
    const __amdgpu_buffer_rsrc_t srdA = __builtin_amdgcn_make_buffer_rsrc((void*)(P.ws + WS_A), 0, A_BYTES, RSRC3);
    u32x4 o;
    if (w == 1) {
        float pv[16];
#pragma unroll
        for (int e = 0; e < 8; ++e) {
            const unsigned x = e < 4 ? ta[e] : tb[e - 4];
            pv[2 * e] = bf_lo(*(const LAS unsigned short*)(lds + LP_T0 + 2 * (x & 0xffff)));
            pv[2 * e + 1] = bf_lo(*(const LAS unsigned short*)(lds + LP_T0 + 2 * (x >> 16)));
        }
#pragma unroll
        for (int j = 0; j < 4; ++j) o[j] = pk_bf16(pv[4 * j] + pv[4 * j + 1], pv[4 * j + 2] + pv[4 * j + 3]);
    } else {
        s16x8 a2[2];
#pragma unroll
        for (int mt = 0; mt < 2; ++mt) {
            const s16x4 lo = __builtin_amdgcn_ds_read_tr16_b64_v4i16((LAS s16x4*)(lds + a20 + mt * 32));
            const s16x4 hi = __builtin_amdgcn_ds_read_tr16_b64_v4i16((LAS s16x4*)(lds + a21 + mt * 32));
            a2[mt] = __builtin_shufflevector(lo, hi, 0, 1, 2, 3, 4, 5, 6, 7);
        }
        unsigned pa[16];
#pragma unroll
        for (int e = 0; e < 8; ++e) {
            const unsigned x = e < 4 ? ta[e] : tb[e - 4];
            pa[2 * e] = LP_T1 + 4 * (x & 0xffff); pa[2 * e + 1] = LP_T1 + 4 * (x >> 16);
        }
#pragma unroll
        for (int e = 0; e < 16; ++e) asm volatile("" : "+v"(pa[e]));
        _Pragma("unroll 1") for (unsigned sp = 0; lds_ld(lds, L_F + 28) < 4 && sp < SPIN_CAP; ++sp) __builtin_amdgcn_s_sleep(0);
        CBAR();
        const u32x4 bf0 = *(const LAS u32x4*)(lds + abv), bf1 = *(const LAS u32x4*)(lds + abv + 1024);
        const f32x4 z4 = {0.f, 0.f, 0.f, 0.f};
        const f32x4 d00 = mfma16(a2[0], as_frag(bf0), z4), d01 = mfma16(a2[1], as_frag(bf0), z4);
        const f32x4 d10 = mfma16(a2[0], as_frag(bf1), z4), d11 = mfma16(a2[1], as_frag(bf1), z4);
#pragma unroll
        for (int e = 0; e < 4; ++e) {
            *(LAS float*)(lds + ac + e * 128) = d00[e]; *(LAS float*)(lds + ac + 2048 + e * 128) = d01[e];
            *(LAS float*)(lds + ac + 64 + e * 128) = d10[e]; *(LAS float*)(lds + ac + 2048 + 64 + e * 128) = d11[e];
        }
        STAMP(4);
        float pv[16];
#pragma unroll
        for (int e = 0; e < 16; ++e) pv[e] = *(const LAS float*)(lds + pa[e]);
#pragma unroll
        for (int j = 0; j < 4; ++j) o[j] = pk_bf16(pv[4 * j] + pv[4 * j + 1], pv[4 * j + 2] + pv[4 * j + 3]);
    }
    {
        const int fr = w == 0 ? (cs < 22 ? 2 * cs : 22 + cs) : 2 * cs + 1;
        __builtin_amdgcn_raw_buffer_store_b128(o, srdA, lane < nact ? (unsigned)(fr * 832 + (26 * chh + p) * 16) : 0x7ffffff0u, 0, AUX_SC1);
    }
    STAMP(5);
    WAIT_VM0();
    STAMP(6);
    if (w == 1) { sig_set(lds, L_F + 24); return; }
    const unsigned tagv = *(const LAS unsigned*)(lds + L_EP + (lane & 15) * 4);
    _Pragma("unroll 1") for (unsigned sp = 0; lds_ld(lds, L_F + 24) == 0 && sp < SPIN_CAP; ++sp) __builtin_amdgcn_s_sleep(0);
    if (lane < 16) __hip_atomic_store((gu32*)(P.ws + WS_FL) + lane * 32 + p, tagv + 1u, __ATOMIC_RELAXED, __HIP_MEMORY_SCOPE_AGENT);
}

constexpr unsigned A_PER = A_BYTES / 4;
DI void cin_role(const Params& P, lds8* lds, int cw, int lane, int g, int m, bool has_dnn, int pf_slot STAMP_ARG) {
    const int d = lane & 31, h = lane >> 5;
    const int b = 64 * g + 4 * m + cw;
    const unsigned ldsb = (unsigned)(size_t)lds;
    {
        const __amdgpu_buffer_rsrc_t srdT = __builtin_amdgcn_make_buffer_rsrc((void*)P.table, 0, NVOC * 64, RSRC3);
        const unsigned lb = ldsb + L_X0 + cw * 1664;
        WAIT_VM0();
        const int q = lane >> 2;
        const unsigned j0 = *(const LAS unsigned*)(lds + L_X0 + cw * 1664 + q * 4);
        const unsigned j1 = *(const LAS unsigned*)(lds + L_X0 + cw * 1664 + (q < 10 ? q + 16 : 25) * 4);
        unsigned o0 = j0 * 64 + (lane & 3) * 16, o1 = j1 * 64 + (lane & 3) * 16;
        asm volatile("" : "+v"(o0), "+v"(o1));
        STAMP(1);
        dma16(lb, o0, srdT, 0);
        if (lane < 40) dma16(lb + 1024, o1, srdT, 0);
    }
    const unsigned bo = P.bout[0];
    STAMP(2);
    __builtin_amdgcn_s_setprio(0);
    CBAR();
    __builtin_amdgcn_s_barrier();
    const unsigned epv = *(const LAS unsigned*)(lds + L_EP + g * 4);
    unsigned bov = bo;
    asm volatile("s_waitcnt vmcnt(0)" : "+v"(bov) :: "memory");
    STAMP(3);
    float x[26];
    {
        const lds8* xb = lds + L_X0 + cw * 1664 + h * 64 + d * 2;
        const lds8* xl = lds + L_X0 + cw * 1664 + (h ? 0 : 25 * 64) + d * 2;
#pragma unroll
        for (int a = 0; a < 13; ++a) {
            const unsigned lo = *(const LAS unsigned short*)(xb + 2 * a * 64);
            const unsigned hi = a < 12 ? *(const LAS unsigned short*)(xb + (2 * a + 1) * 64) : *(const LAS unsigned short*)xl;
            x[2 * a] = bf_lo(lo); x[2 * a + 1] = bf_lo(hi);
        }
    }
    u32x4 z[24];
#pragma unroll
    for (int s = 0; s < 22; ++s) {
        float fa[8], fb[8];
#pragma unroll
        for (int e = 0; e < 8; ++e) { const AB pe = slot_ab(8 * s + e); fa[e] = x[pe.a]; fb[e] = x[pe.b]; }
        float t0, t1, t2, t3, t4, t5;
        unsigned o0, o1, o2, o3;
        asm("v_mul_f32 %4, %10, %11\n\tv_mul_f32 %5, %12, %13\n\tv_mul_f32 %6, %14, %15\n\tv_mul_f32 %7, %16, %17\n\t"
            "v_cvt_pk_bf16_f32 %0, %4, %5\n\tv_mul_f32 %8, %18, %19\n\tv_mul_f32 %9, %20, %21\n\tv_cvt_pk_bf16_f32 %1, %6, %7\n\t"
            "v_mul_f32 %4, %22, %23\n\tv_mul_f32 %5, %24, %25\n\tv_cvt_pk_bf16_f32 %2, %8, %9\n\tv_cvt_pk_bf16_f32 %3, %4, %5"
            : "=&v"(o0), "=&v"(o1), "=&v"(o2), "=v"(o3), "=&v"(t0), "=&v"(t1), "=&v"(t2), "=&v"(t3), "=&v"(t4), "=&v"(t5)
            : "v"(fa[0]), "v"(fb[0]), "v"(fa[1]), "v"(fb[1]), "v"(fa[2]), "v"(fb[2]), "v"(fa[3]), "v"(fb[3]),
              "v"(fa[4]), "v"(fb[4]), "v"(fa[5]), "v"(fb[5]), "v"(fa[6]), "v"(fb[6]), "v"(fa[7]), "v"(fb[7]));
        z[s] = (u32x4){o0, o1, o2, o3};
    }
#pragma unroll
    for (int s = 22; s < 24; ++s) {
#pragma unroll
        for (int j = 0; j < 4; ++j) {
            const AB p0 = slot_ab(8 * s + 2 * j), p1 = slot_ab(8 * s + 2 * j + 1);
            const float a0 = p0.a < 26 ? x[p0.a] : (p0.a == 26 ? 1.f : 0.f);
            const float a1 = p1.a < 26 ? x[p1.a] : (p1.a == 26 ? 1.f : 0.f);
            z[s][j] = pk_bf16(a0, a1);
        }
    }
#pragma unroll
    for (int s = 0; s < 24; ++s) asm volatile("" : "+v"(z[s]));
    STAMP(4);
    const unsigned tag = epv + 1u;
    {
        const __amdgpu_buffer_rsrc_t srdA = __builtin_amdgcn_make_buffer_rsrc((void*)(P.ws + WS_A), 0, A_BYTES, RSRC3);
        if (cw == 0) {
            if (has_dnn) _Pragma("unroll 1") for (unsigned sp = 0; lds_ld(lds, L_F + 52) < 4 && sp < SPIN_CAP; ++sp) __builtin_amdgcn_s_sleep(1);
            const gu32* fl = (const gu32*)(P.ws + WS_FL) + g * 32 + (lane < 26 ? lane : 0);
            _Pragma("unroll 1") for (unsigned sp = 0; sp < SPIN_CAP; ++sp) { if (__all(ld_agent(fl) == tag)) break; __builtin_amdgcn_s_sleep(1); }
            sig_set(lds, L_F + 16);
            if (!has_dnn) dma4_sc1(ldsb + L_DUMMY, lane < 50 ? (unsigned)((((g & 1) * 3 + pf_slot) * 50 + lane) * 128) : 0x7fffff00u, srdA, 0);
        } else {
            _Pragma("unroll 1") for (unsigned sp = 0; lds_ld(lds, L_F + 16) == 0 && sp < SPIN_CAP; ++sp) __builtin_amdgcn_s_sleep(0);
        }
        CBAR();
        __builtin_amdgcn_s_setprio(CIN_PRIO);
        STAMP(5);
        {
            const unsigned l1 = ldsb + L_W + cw * 4096, vo = lane * 16;
            asm volatile("s_mov_b32 m0, %0\n\ts_nop 0\n\t"
                         "buffer_load_dwordx4 %1, %2, %3 offen sc1 lds\n\tbuffer_load_dwordx4 %1, %2, %3 offen offset:1024 sc1 lds\n\t"
                         "buffer_load_dwordx4 %1, %2, %3 offen offset:2048 sc1 lds\n\tbuffer_load_dwordx4 %1, %2, %3 offen offset:3072 sc1 lds"
                         :: "s"(l1), "v"(vo), "s"(srdA), "s"(cw * 4096) : "memory");
#pragma unroll
            for (int i = 0; i < 6; ++i) {
                const int q = 6 * cw + i;
                const unsigned go = q < 4 ? 16384 + 1024 * q : 19968 + 1024 * (q - 4), lo = q < 4 ? 16384 + 1024 * q : 53248 + 1024 * (q - 4);
                dma16_sc1(ldsb + L_W + lo, vo, srdA, go);
            }
        }
        wait_vm<6>();
        sig_add(lds, L_F + 20);
        _Pragma("unroll 1") for (unsigned sp = 0; lds_ld(lds, L_F + 20) < 4 && sp < SPIN_CAP; ++sp) __builtin_amdgcn_s_sleep(0);
        CBAR();
    }
    STAMP(6);
    const gu64* pp = (const gu64*)(P.ws + WS_PART) + ((g * 32 + (lane < 26 ? lane : 0)) * 64 + 4 * m + cw);
    unsigned long long pv = 0;
    f32x16 acc0, acc1;
#pragma unroll
    for (int i = 0; i < 16; ++i) { acc0[i] = 0.f; acc1[i] = 0.f; }
    {
        unsigned abo = L_W + (26 * h + d) * 16;
        asm volatile("" : "+v"(abo));
        const lds8* ab = lds + abo;
        constexpr int PF = 6;
        u32x4 af[46];
        unsigned cnt2 = 0;
        auto frag_of = [](int t) { return t < 24 ? t : t + 40; };
#pragma unroll
        for (int t = 0; t < PF; ++t) af[t] = *(const LAS u32x4*)(ab + frag_of(t) * 832);
        __builtin_amdgcn_sched_barrier(0);
#pragma unroll
        for (int t = 0; t < 46; ++t) {
            if (t == 9) { wait_vm<0>(); sig_add(lds, L_F + 44); }
            if (t + PF == 19 - 2) cnt2 = lds_ld(lds, L_F + 44);
            if (t + PF == 19) {
                if (cnt2 < 4) _Pragma("unroll 1") for (unsigned sp = 0; lds_ld(lds, L_F + 44) < 4 && sp < SPIN_CAP; ++sp) __builtin_amdgcn_s_sleep(0);
                CBAR();
            }
            if (t + PF < 46) af[t + PF] = *(const LAS u32x4*)(ab + frag_of(t + PF) * 832);
            if (t < 44) {
                if (t & 1) acc0 = mfma32(as_frag(af[t]), as_frag(z[t >> 1]), acc0);
                else acc1 = mfma32(as_frag(af[t]), as_frag(z[t >> 1]), acc1);
            } else acc1 = mfma32(as_frag(af[t]), as_frag(z[t - 22]), acc1);
            if (t == 30) pv = __hip_atomic_load(pp, __ATOMIC_RELAXED, __HIP_MEMORY_SCOPE_AGENT);
            __builtin_amdgcn_sched_barrier(0);
        }
    }
    float sa = 0.f;
#pragma unroll
    for (int i = 0; i < 12; ++i) sa = __builtin_fmaf(acc0[i], acc1[i], sa);
    const float sb = __builtin_fmaf(acc0[12], acc1[12], acc0[13] * acc1[13]);
    float tot = h == 0 ? sa + sb : sa;
    asm volatile("" : "+v"(tot));
    STAMP(7);
    _Pragma("unroll 1") for (unsigned sp = 0; sp < SPIN_CAP; ++sp) {
        if (__all((unsigned)(pv >> 32) == tag)) break;
        pv = __hip_atomic_load(pp, __ATOMIC_RELAXED, __HIP_MEMORY_SCOPE_AGENT);
    }
    STAMP(8);
    if (lane < 26) tot += __uint_as_float((unsigned)pv);
    tot = wave_sum_to_lane63(tot);
    if (lane == 63) {
        const float o = tot + bf_lo(bov);
        P.out[b] = (unsigned short)(pk_bf16(o, o) & 0xffffu);
    }
    if (m == 0 && cw == 0) {
        const gu64* ap = (const gu64*)(P.ws + WS_PART) + ((g * 32 + 26 + (lane < 3 ? lane : 0)) * 64);
        _Pragma("unroll 1") for (unsigned sp = 0; sp < SPIN_CAP; ++sp) {
            const unsigned long long av = __hip_atomic_load(ap, __ATOMIC_RELAXED, __HIP_MEMORY_SCOPE_AGENT);
            if (__all((unsigned)(av >> 32) == tag)) break;
        }
        if (lane == 0) __hip_atomic_store((gu32*)(P.ws + WS_EP) + g, tag, __ATOMIC_RELAXED, __HIP_MEMORY_SCOPE_AGENT);
    }
}

DI void decode_block(int bid, int& g, int& m, int& ntile, int& k) {
    const int x = bid & 7, j = bid >> 3;
    g = 2 * x + (j >> 4); m = j & 15;
    ntile = m < 13 ? m : -1; k = m - 13;
}

__global__ void __launch_bounds__(512, 2) xdfm_kernel(Params P) {
    __shared__ __attribute__((aligned(16))) unsigned char smem[L_END];
    lds8* lds = (lds8*)smem;
    STAMP(0);
    const int w = __builtin_amdgcn_readfirstlane(threadIdx.x >> 6), lane = threadIdx.x & 63;
    {
        unsigned long long pc; unsigned len;
        asm volatile("s_getpc_b64 %0\n.Lxdfm_pc:\n\ts_mov_b32 %1, (.Lxdfm_end-.Lxdfm_pc)&4294967295" : "=s"(pc), "=s"(len));
        const __amdgpu_buffer_rsrc_t srdP = __builtin_amdgcn_make_buffer_rsrc((void*)pc, 0, len, RSRC3);
        dma4((unsigned)(size_t)lds + L_DUMMY, lane == 0 ? ((blockIdx.x >> 3) * 8 + w) * 128 : 0x7fffff00u, srdP, 0);
    }
    asm volatile("" :: "s"(P.idx), "s"(P.table), "s"(P.wcin), "s"(P.wout), "s"(P.w1), "s"(P.b1), "s"(P.w2), "s"(P.bout), "s"(P.out), "s"(P.ws));
    int g, m, ntile, k;
    decode_block(blockIdx.x, g, m, ntile, k);
    const bool is_cin = w >= 4;
    if (__builtin_expect(is_cin, 1)) {
        __builtin_amdgcn_s_setprio(3);
        dma16((unsigned)(size_t)lds + L_X0 + (w - 4) * 1664, lane < 7 ? (64 * g + 4 * m + w - 4) * 104 + lane * 16 : 0x7fffff00u,
              __builtin_amdgcn_make_buffer_rsrc((void*)P.idx, 0, NBATCH * 26 * 4, RSRC3), 0);
        if (__builtin_expect(w == 7, 1)) dma4((unsigned)(size_t)lds + L_DUMMY, lane < 26 ? ((blockIdx.x >> 3) * 26 + lane) * 128 : 0x7fffff00u,
                         __builtin_amdgcn_make_buffer_rsrc((void*)P.wcin, 0, 3 * 17576 * 2, RSRC3), 0);
    }
    if (__builtin_expect(w == 0, 1)) {
        if (lane < 16) lds_st(lds, L_F + 4 * lane, 0);
        dma4_sc1((unsigned)(size_t)lds + L_EP, (lane & 15) * 4, __builtin_amdgcn_make_buffer_rsrc((void*)(P.ws + WS_EP), 0, 64, RSRC3), 0);
    }
    const int p = g * 3 + k;
    if (__builtin_expect(!is_cin && ntile >= 0, 1)) dnn_start(P, lds, w, lane, g, ntile);
    if (__builtin_expect(!is_cin && ntile < 0 && p < 26, 1)) prep_start(P, lds, w, lane, p);
    if (__builtin_expect(!is_cin && ntile >= 0, 1)) {
        dnn_role(P, lds, w, lane, g, ntile STAMP_PASS);
    } else if (is_cin) {
        cin_role(P, lds, w - 4, lane, g, m, ntile >= 0, k STAMP_PASS);
        __builtin_amdgcn_s_waitcnt(0x0F70);
    } else {
        if (p < 26) prep_role(P, lds, w, lane, p STAMP_PASS);
        else { asm volatile("s_waitcnt vmcnt(0) lgkmcnt(0)" ::: "memory"); __builtin_amdgcn_s_barrier(); }
        if (w == 0) {
            const unsigned ep = *(const LAS unsigned*)(lds + L_EP + g * 4);
            if (lane == 0) st_granule((gu64*)(P.ws + WS_PART) + ((g * 32 + 26 + k) * 64), ep + 1u, 0u);
        }
        __builtin_amdgcn_s_waitcnt(0x0F70);
    }
    STAMP(11);
    STAMP_FLUSH(P.ws, w, lane);
    asm volatile(".Lxdfm_end:");
}

extern "C" void kernel_launch(void* const* d_in, const int* in_sizes, int n_in,
                              void* d_out, int out_size, void* d_ws, size_t ws_size,
                              hipStream_t stream) {
    Params P;
    P.idx = (const int*)d_in[0]; P.table = (const unsigned short*)d_in[1]; P.wcin = (const unsigned short*)d_in[2];
    P.wout = (const unsigned short*)d_in[3]; P.w1 = (const unsigned short*)d_in[4]; P.b1 = (const unsigned short*)d_in[5];
    P.w2 = (const unsigned short*)d_in[6]; P.bout = (const unsigned short*)d_in[7];
    P.out = (unsigned short*)d_out; P.ws = (unsigned char*)d_ws;
    xdfm_kernel<<<256, 512, 0, stream>>>(P);
}
```

```cpp
#define AMP_BLK 40
#define AMP_W 0
#define AMP_J 2
#define AMP_K 4
#include <hip/hip_runtime.h>
#include <stdint.h>

#define DI __device__ __forceinline__
#define GAS __attribute__((address_space(1)))
#define LAS __attribute__((address_space(3)))

typedef short    s16x4 __attribute__((ext_vector_type(4)));
typedef short    s16x8 __attribute__((ext_vector_type(8)));
typedef float    f32x2 __attribute__((ext_vector_type(2)));
typedef float    f32x4 __attribute__((ext_vector_type(4)));
typedef float    f32x16 __attribute__((ext_vector_type(16)));
typedef unsigned u32x2 __attribute__((ext_vector_type(2)));
typedef unsigned u32x4 __attribute__((ext_vector_type(4)));
typedef __bf16   bf16x2 __attribute__((ext_vector_type(2)));
typedef GAS unsigned gu32;
typedef GAS unsigned long long gu64;
typedef LAS unsigned char lds8;

#define MAPPING 1

constexpr int DIN = 832, NBATCH = 1024, NVOC = 100000;
#define A_AFTER_X 1
#define W_EARLY 1
#define A_PF 1
#define CODE_PF 1
#define WC_PF 1
#define POLL_STEP 10
#define CIN_PRIO 2
#define PKMUL 0
#define A_DMA 1
constexpr unsigned RSRC3 = 0x00020000u;
constexpr int AUX_SC1 = 16;
constexpr unsigned SPIN_CAP = 1u << 20;

constexpr unsigned WS_EP = 0;
constexpr unsigned WS_FL = 4096;
constexpr unsigned WS_PART = 8192;
constexpr unsigned WS_A = 8192 + 16 * 32 * 64 * 8;
constexpr unsigned A_BYTES = 46 * 832;

#define XD 6
constexpr unsigned L_W = 0;
constexpr unsigned L_A = 832 * 128;
constexpr unsigned L_X0 = L_A + 4 * XD * 2048;
constexpr unsigned L_F = L_X0 + 4 * 1664;
constexpr unsigned L_DUMMY = L_F + 256;
constexpr unsigned L_EP = L_DUMMY + 256;
constexpr unsigned L_BI = L_EP + 256;
constexpr unsigned L_W2 = L_BI + 256;
constexpr unsigned L_END = L_W2 + 256;
static_assert(4 * XD * 2048 >= A_BYTES && L_END <= 163840, "LDS map");
constexpr unsigned LP_VT = 0;
constexpr unsigned LP_T0 = 2048;
constexpr unsigned LP_T1 = 2048 + 2304;

struct AB { int a, b; };
constexpr AB slot_ab(int t) {
    if (t < 156) { int a = 2 * (t / 12); return {a, (a + t % 12) % 26}; }
    if (t < 170) { int u = t - 156; int a = 2 * (u / 2); return {a, (a + 12 + (u & 1)) % 26}; }
    if (t < 176) { int a = 2 * (7 + t - 170); return {a, (a + 12) % 26}; }
    if (t < 189) return {2 * (t - 176), 26};
    if (t == 189) return {26, 26};
    return {27, 27};
}
constexpr unsigned T_ZERO = 1024, T_CONST = 1025;
struct PrepTab { unsigned v[2][24 * 2 * 8]; };
constexpr PrepTab make_tab() {
    PrepTab tb{};
    for (int m = 0; m < 2; ++m) for (int s = 0; s < 24; ++s) for (int h = 0; h < 2; ++h) for (int e = 0; e < 8; ++e) {
        const int st = m ? 32 : 26;
        int t = 8 * s + e; AB ab = slot_ab(t);
        unsigned o1 = T_ZERO, o2 = T_ZERO;
        if (ab.a == 27) {}
        else if (ab.a == 26) { if (h == 0 && m) o1 = T_CONST; }
        else if (ab.b == 26) { if (m) o1 = ((ab.a + h) % 26) * st + 26; }
        else {
            int u = (ab.a + h) % 26, v = (ab.b + h) % 26;
            bool dup = (t == 169 && h == 1);
            if (dup) {}
            else if (u == v) o1 = u * st + u;
            else { o1 = u * st + v; o2 = v * st + u; }
        }
        tb.v[m][(s * 2 + h) * 8 + e] = o1 | (o2 << 16);
    }
    return tb;
}
static_assert(slot_ab(169).a == 12 && slot_ab(169).b == 25, "dup slot");
__device__ const PrepTab g_tab = make_tab();

struct Params {
    const int* idx; const unsigned short* table; const unsigned short* wcin; const unsigned short* wout;
    const unsigned short* w1; const unsigned short* b1; const unsigned short* w2; const unsigned short* bout;
    unsigned short* out; unsigned char* ws;
};

DI unsigned sel(bool c, unsigned a, unsigned b) { unsigned r = c ? a : b; return r; }
DI float bf_lo(unsigned v) { return __uint_as_float(v << 16); }
DI float bf_hi(unsigned v) { return __uint_as_float(v & 0xffff0000u); }
DI unsigned pk_bf16(float lo, float hi) { f32x2 v = {lo, hi}; return __builtin_bit_cast(unsigned, __builtin_convertvector(v, bf16x2)); }
DI f32x4 mfma16(s16x8 a, s16x8 b, f32x4 c) { return __builtin_amdgcn_mfma_f32_16x16x32_bf16(a, b, c, 0, 0, 0); }
DI f32x16 mfma32(s16x8 a, s16x8 b, f32x16 c) { return __builtin_amdgcn_mfma_f32_32x32x16_bf16(a, b, c, 0, 0, 0); }
DI s16x8 as_frag(u32x4 v) { return __builtin_bit_cast(s16x8, v); }
template <int CTRL, int ROWMASK> DI float dpp_add(float v) {
    return v + __int_as_float(__builtin_amdgcn_update_dpp(0, __float_as_int(v), CTRL, ROWMASK, 0xf, true));
}
DI float wave_sum_to_lane63(float v) {
    v = dpp_add<0x111, 0xf>(v); v = dpp_add<0x112, 0xf>(v); v = dpp_add<0x114, 0xf>(v); v = dpp_add<0x118, 0xf>(v);
    v = dpp_add<0x142, 0xa>(v); v = dpp_add<0x143, 0xc>(v);
    return v;
}
DI float sum_lane_groups(float v) {
    auto a = __builtin_amdgcn_permlane16_swap(__float_as_uint(v), __float_as_uint(v), false, false);
    v = __uint_as_float(a[0]) + __uint_as_float(a[1]);
    auto b = __builtin_amdgcn_permlane32_swap(__float_as_uint(v), __float_as_uint(v), false, false);
    return __uint_as_float(b[0]) + __uint_as_float(b[1]);
}
DI float gelu_tanh(float x) {
    const float k1 = -2.0f * 0.7978845608028654f * 1.4426950408889634f;
    const float k2 = k1 * 0.044715f;
    float arg = x * __builtin_fmaf(x * x, k2, k1);
    float e = __builtin_amdgcn_exp2f(arg);
    return x * __builtin_amdgcn_rcpf(1.0f + e);
}
DI void dma16(unsigned lds_addr, unsigned voff, __amdgpu_buffer_rsrc_t srd, unsigned soff) {
    asm volatile("s_mov_b32 m0, %0\n\ts_nop 0\n\tbuffer_load_dwordx4 %1, %2, %3 offen lds"
                 :: "s"(lds_addr), "v"(voff), "s"(srd), "s"(soff) : "memory");
}
DI void dma16_sc1(unsigned lds_addr, unsigned voff, __amdgpu_buffer_rsrc_t srd, unsigned soff) {
    asm volatile("s_mov_b32 m0, %0\n\ts_nop 0\n\tbuffer_load_dwordx4 %1, %2, %3 offen sc1 lds"
                 :: "s"(lds_addr), "v"(voff), "s"(srd), "s"(soff) : "memory");
}
DI void dma4_sc1(unsigned lds_addr, unsigned voff, __amdgpu_buffer_rsrc_t srd, unsigned soff) {
    asm volatile("s_mov_b32 m0, %0\n\ts_nop 0\n\tbuffer_load_dword %1, %2, %3 offen sc1 lds"
                 :: "s"(lds_addr), "v"(voff), "s"(srd), "s"(soff) : "memory");
}
DI void dma4(unsigned lds_addr, unsigned voff, __amdgpu_buffer_rsrc_t srd, unsigned soff) {
    asm volatile("s_mov_b32 m0, %0\n\ts_nop 0\n\tbuffer_load_dword %1, %2, %3 offen lds"
                 :: "s"(lds_addr), "v"(voff), "s"(srd), "s"(soff) : "memory");
}
DI void st_granule(gu64* g, unsigned tag, unsigned val) {
    __hip_atomic_store(g, ((unsigned long long)tag << 32) | val, __ATOMIC_RELAXED, __HIP_MEMORY_SCOPE_AGENT);
}
DI unsigned ld_agent(const gu32* p) { return __hip_atomic_load(p, __ATOMIC_RELAXED, __HIP_MEMORY_SCOPE_AGENT); }
DI unsigned lds_ld(lds8* l, unsigned off) { return __hip_atomic_load((LAS unsigned*)(l + off), __ATOMIC_RELAXED, __HIP_MEMORY_SCOPE_WORKGROUP); }
DI void lds_st(lds8* l, unsigned off, unsigned v) { __hip_atomic_store((LAS unsigned*)(l + off), v, __ATOMIC_RELAXED, __HIP_MEMORY_SCOPE_WORKGROUP); }
DI void lds_add(lds8* l, unsigned off, unsigned v) { __hip_atomic_fetch_add((LAS unsigned*)(l + off), v, __ATOMIC_RELAXED, __HIP_MEMORY_SCOPE_WORKGROUP); }
DI void sig_add(lds8* l, unsigned off) {
    asm volatile("s_mov_b64 exec, 1\n\tds_add_u32 %0, %1\n\ts_mov_b64 exec, -1" :: "v"((unsigned)(size_t)l + off), "v"(1u) : "memory");
}
DI void sig_set(lds8* l, unsigned off) {
    asm volatile("s_mov_b64 exec, 1\n\tds_write_b32 %0, %1\n\ts_mov_b64 exec, -1" :: "v"((unsigned)(size_t)l + off), "v"(1u) : "memory");
}
#define WAIT_LGKM() asm volatile("s_waitcnt lgkmcnt(0)" ::: "memory")
#define WAIT_VM0() asm volatile("s_waitcnt vmcnt(0)" ::: "memory")
#define CBAR() asm volatile("" ::: "memory")
struct Stamps { unsigned long long t0, t1; };
#define STAMP(i) do { if ((i) == 0) { asm volatile("s_memrealtime %0\n\ts_waitcnt lgkmcnt(0)" : "=s"(st.t0) :: "memory"); } else if ((i) == AMP_J) { asm volatile("s_memrealtime %0\n\ts_waitcnt lgkmcnt(0)" : "=s"(st.t1) :: "memory"); } } while (0)
#define STAMP_ARG , Stamps& st
#define STAMP_PASS , st
#define STAMP_FLUSH(ws, w, lane) do { if (blockIdx.x == AMP_BLK && (w) == AMP_W) { const unsigned long long d_ = (st.t1 - st.t0) * AMP_K; const unsigned long long e_ = __builtin_amdgcn_s_memrealtime(); while (__builtin_amdgcn_s_memrealtime() - e_ < d_) __builtin_amdgcn_s_sleep(4); } } while (0)

template <int K> DI unsigned quad_bcast(unsigned v) { return __builtin_amdgcn_update_dpp(0u, v, K * 0x55, 0xf, 0xf, true); }
template <int N> DI void wait_vm() { asm volatile("s_waitcnt vmcnt(%0)" :: "i"(N) : "memory"); }
DI void dma_w4(unsigned lds_addr, unsigned voff, __amdgpu_buffer_rsrc_t srd, unsigned soff) {
    unsigned t;
    asm volatile("s_mov_b32 m0, %1\n\ts_add_u32 %0, %4, 12288\n\tbuffer_load_dwordx4 %2, %3, %4 offen lds\n\t"
                 "buffer_load_dwordx4 %2, %3, %0 offen offset:1024 lds\n\ts_add_u32 %0, %0, 12288\n\t"
                 "buffer_load_dwordx4 %2, %3, %0 offen offset:2048 lds\n\ts_add_u32 %0, %0, 12288\n\t"
                 "buffer_load_dwordx4 %2, %3, %0 offen offset:3072 lds"
                 : "=&s"(t) : "s"(lds_addr), "v"(voff), "s"(srd), "s"(soff) : "memory", "scc");
}
DI void dnn_start(const Params& P, lds8* lds, int w, int lane, int g, int ntile) {
    const int mh = w & 1, kh = w >> 1;
    const unsigned ldsb = (unsigned)(size_t)lds;
    const unsigned ring = ldsb + L_A + w * (XD * 2048);
    const __amdgpu_buffer_rsrc_t srdI = __builtin_amdgcn_make_buffer_rsrc((void*)P.idx, 0, NBATCH * 26 * 4, RSRC3);
    const unsigned vo = ((64 * g + 32 * mh + (lane >> 2)) * 26 + 13 * kh + 4 * (lane & 3)) * 4;
    dma16(ring, vo, srdI, 0);
    dma16(ring + 1024, vo, srdI, 1664);
    if (__builtin_expect(w == 1, 1)) dma4(ldsb + L_BI, ntile * 128 + lane * 4, __builtin_amdgcn_make_buffer_rsrc((void*)P.b1, 0, DIN * 2, RSRC3), 0);
    if (__builtin_expect(w == 2, 1)) dma4(ldsb + L_W2, ntile * 128 + lane * 4, __builtin_amdgcn_make_buffer_rsrc((void*)P.w2, 0, DIN * 2, RSRC3), 0);
    const __amdgpu_buffer_rsrc_t srdW = __builtin_amdgcn_make_buffer_rsrc((void*)P.w1, 0, DIN * DIN * 2, RSRC3);
    const unsigned vw = (lane >> 3) * 1664 + (((lane & 7) ^ (((lane >> 4) & 1) << 2)) << 4);
    const unsigned rowA = 416 * kh + 208 * mh + 104 * (g & 1);
    dma_w4(ldsb + L_W + rowA * 128, vw, srdW, rowA * 1664 + ntile * 128);
}
DI void dnn_role(const Params& P, lds8* lds, int w, int lane, int g, int ntile STAMP_ARG) {
    const int mh = w & 1, kh = w >> 1;
    const int h = lane >> 5;
    const unsigned ldsb = (unsigned)(size_t)lds;
    const unsigned ring = ldsb + L_A + w * (XD * 2048);
    const __amdgpu_buffer_rsrc_t srdW = __builtin_amdgcn_make_buffer_rsrc((void*)P.w1, 0, DIN * DIN * 2, RSRC3);
    const unsigned vw = (lane >> 3) * 1664 + (((lane & 7) ^ (((lane >> 4) & 1) << 2)) << 4);
    const unsigned rowA = 416 * kh + 208 * mh + 104 * (g & 1), rowB = 416 * kh + 208 * mh + 104 * ((g & 1) ^ 1);
    auto wdma = [&](unsigned row, int a) {
        const unsigned sb = row * 1664 + ntile * 128, lb = ldsb + L_W + row * 128;
        if (a < 3) dma_w4(lb + a * 4096, vw, srdW, sb + a * 4 * 13312);
        else dma16(lb + 12288, vw, srdW, sb + 12 * 13312);
    };
    STAMP(1);
    wait_vm<4>();
    WAIT_LGKM();
    __builtin_amdgcn_s_barrier();
    STAMP(2);
    const u32x4 i0 = *(const LAS u32x4*)(lds + L_A + w * (XD * 2048) + lane * 16);
    const u32x4 i1 = *(const LAS u32x4*)(lds + L_A + w * (XD * 2048) + 1024 + lane * 16);
    const unsigned ep = *(const LAS unsigned*)(lds + L_EP + g * 4);
#pragma unroll
    for (int a = 1; a < 4; ++a) wdma(rowA, a);
#pragma unroll
    for (int a = 0; a < 4; ++a) wdma(rowB, a);
    const __amdgpu_buffer_rsrc_t srdT = __builtin_amdgcn_make_buffer_rsrc((void*)P.table, 0, NVOC * 64, RSRC3);
    const unsigned c16 = (((lane & 3) ^ ((lane >> 4) & 3)) << 4);
    u32x4 t0 = i0 << 6, t1 = i1 << 6;
    asm volatile("" : "+v"(t0), "+v"(t1));
    auto xload = [&](int s) {
        const unsigned a0 = t0[s & 3], a1 = t1[s & 3];
        const unsigned o0 = (s < 4 ? quad_bcast<0>(a0) : s < 8 ? quad_bcast<1>(a0) : s < 12 ? quad_bcast<2>(a0) : quad_bcast<3>(a0)) + c16;
        const unsigned o1 = (s < 4 ? quad_bcast<0>(a1) : s < 8 ? quad_bcast<1>(a1) : s < 12 ? quad_bcast<2>(a1) : quad_bcast<3>(a1)) + c16;
        dma16(ring + (s % XD) * 2048, o0, srdT, 0);
        dma16(ring + (s % XD) * 2048 + 1024, o1, srdT, 0);
    };
#pragma unroll
    for (int s = 0; s < XD; ++s) xload(s);
    STAMP(3);
    wait_vm<2 * XD>();
    sig_add(lds, L_F + 32 + 4 * kh);
    _Pragma("unroll 1") for (unsigned sp = 0; lds_ld(lds, L_F + 32 + 4 * kh) < 2 && sp < SPIN_CAP; ++sp) __builtin_amdgcn_s_sleep(0);
    CBAR();
    STAMP(4);
    f32x16 acc[2];
    u32x2 bia[4], wv[4];
#pragma unroll
    for (int r4 = 0; r4 < 4; ++r4) {
        bia[r4] = *(const LAS u32x2*)(lds + L_BI + kh * 64 + h * 8 + r4 * 16);
        wv[r4] = *(const LAS u32x2*)(lds + L_W2 + kh * 64 + h * 8 + r4 * 16);
    }
#pragma unroll
    for (int r4 = 0; r4 < 4; ++r4) {
        acc[0][4 * r4] = bf_lo(bia[r4][0]); acc[0][4 * r4 + 1] = bf_hi(bia[r4][0]); acc[0][4 * r4 + 2] = bf_lo(bia[r4][1]); acc[0][4 * r4 + 3] = bf_hi(bia[r4][1]);
        acc[1][4 * r4] = 0.f; acc[1][4 * r4 + 1] = 0.f; acc[1][4 * r4 + 2] = 0.f; acc[1][4 * r4 + 3] = 0.f;
    }
    {
        const int blk = (lane >> 4) & 1, q = (lane & 15) >> 2, pp = lane & 3, n = lane & 31;
        unsigned ad[2];
#pragma unroll
        for (int j = 0; j < 2; ++j) {
            const unsigned c = 4 * (j ^ kh) + 2 * blk + (pp >> 1);
            ad[j] = L_W + (416 * kh + 8 * h + q) * 128 + ((c ^ (((q >> 1) & 1) << 2)) << 4) + (pp & 1) * 8;
        }
        const unsigned xb = L_A + w * (XD * 2048) + n * 64;
        const unsigned xa0 = xb + ((h ^ ((n >> 2) & 3)) << 4), xa1 = xb + (((2 + h) ^ ((n >> 2) & 3)) << 4);
        u32x4 xf[3][2];
        s16x8 wf[2][4];
        auto xread = [&](int s) {
            xf[s % 3][0] = *(const LAS u32x4*)(lds + xa0 + (s % XD) * 2048);
            xf[s % 3][1] = *(const LAS u32x4*)(lds + xa1 + (s % XD) * 2048);
        };
        auto wread = [&](int s) {
#pragma unroll
            for (int a = 0; a < 2; ++a)
#pragma unroll
                for (int j = 0; j < 2; ++j) {
                    const s16x4 lo = __builtin_amdgcn_ds_read_tr16_b64_v4i16((LAS s16x4*)(lds + ad[j] + s * 4096 + a * 2048));
                    const s16x4 hi = __builtin_amdgcn_ds_read_tr16_b64_v4i16((LAS s16x4*)(lds + ad[j] + s * 4096 + a * 2048 + 512));
                    wf[s & 1][2 * a + j] = __builtin_shufflevector(lo, hi, 0, 1, 2, 3, 4, 5, 6, 7);
                }
        };
        wread(0);
        wait_vm<2 * (XD - 1)>();
        xread(0);
        wait_vm<2 * (XD - 2)>();
        xread(1);
        __builtin_amdgcn_sched_barrier(0);
#pragma unroll
        for (int s = 0; s < 13; ++s) {
            if (s + 1 < 13) wread(s + 1);
#pragma unroll
            for (int a = 0; a < 2; ++a)
#pragma unroll
                for (int j = 0; j < 2; ++j) acc[j] = mfma32(wf[s & 1][2 * a + j], as_frag(xf[s % 3][a]), acc[j]);
            if (s + 1 < 13) {
#pragma unroll
                for (int j = 0; j < 4; ++j) { __builtin_amdgcn_sched_group_barrier(0x100, 2, 0); __builtin_amdgcn_sched_group_barrier(0x8, 1, 0); }
            }
            __builtin_amdgcn_sched_barrier(0);
            if (s + XD < 13) xload(s + XD);
            if (s + 2 < 13) {
                constexpr int LASTI = 12;
                const int last = s + XD < LASTI ? s + XD : LASTI;
                const int younger = last - (s + 2);
                if (younger >= 4) wait_vm<8>(); else if (younger == 3) wait_vm<6>(); else if (younger == 2) wait_vm<4>(); else if (younger == 1) wait_vm<2>(); else wait_vm<0>();
                if (s + 2 == 12) sig_add(lds, L_F + 52);
                xread(s + 2);
            }
            __builtin_amdgcn_sched_barrier(0);
        }
    }
    STAMP(6);
    {
        _Pragma("unroll 1") for (unsigned sp = 0; lds_ld(lds, L_F + 52) < 4 && sp < SPIN_CAP; ++sp) __builtin_amdgcn_s_sleep(0);
        CBAR();
        lds8* xw = lds + L_W + kh * 53248 + (5 + mh) * 4096 + lane * 16;
#pragma unroll
        for (int r4 = 0; r4 < 4; ++r4) *(LAS f32x4*)(xw + r4 * 1024) = (f32x4){acc[1][4 * r4], acc[1][4 * r4 + 1], acc[1][4 * r4 + 2], acc[1][4 * r4 + 3]};
        CBAR();
        sig_set(lds, L_F + 4 * w);
        _Pragma("unroll 1") for (unsigned sp = 0; lds_ld(lds, L_F + 4 * (w ^ 2)) == 0 && sp < SPIN_CAP; ++sp) __builtin_amdgcn_s_sleep(0);
        CBAR();
        const lds8* xr = lds + L_W + (kh ^ 1) * 53248 + (5 + mh) * 4096 + lane * 16;
#pragma unroll
        for (int r4 = 0; r4 < 4; ++r4) {
            const f32x4 o = *(const LAS f32x4*)(xr + r4 * 1024);
            acc[0][4 * r4] += o[0]; acc[0][4 * r4 + 1] += o[1]; acc[0][4 * r4 + 2] += o[2]; acc[0][4 * r4 + 3] += o[3];
        }
    }
    STAMP(7);
    float s0 = 0.f, s1 = 0.f;
#pragma unroll
    for (int r4 = 0; r4 < 4; ++r4) {
        s0 = __builtin_fmaf(gelu_tanh(acc[0][4 * r4]), bf_lo(wv[r4][0]), s0);
        s1 = __builtin_fmaf(gelu_tanh(acc[0][4 * r4 + 1]), bf_hi(wv[r4][0]), s1);
        s0 = __builtin_fmaf(gelu_tanh(acc[0][4 * r4 + 2]), bf_lo(wv[r4][1]), s0);
        s1 = __builtin_fmaf(gelu_tanh(acc[0][4 * r4 + 3]), bf_hi(wv[r4][1]), s1);
    }
    s0 += s1;
    const auto sw = __builtin_amdgcn_permlane32_swap(__float_as_uint(s0), __float_as_uint(s0), false, false);
    const float part = __uint_as_float(sw[0]) + __uint_as_float(sw[1]);
    STAMP(8);
    if (lane < 32) st_granule((gu64*)(P.ws + WS_PART) + ((g * 32 + 2 * ntile + kh) * 64 + 32 * mh + lane), ep + 1u, __float_as_uint(part));
}

constexpr unsigned LP_W2 = 12288;
constexpr unsigned LP_WO = 14336;
constexpr unsigned LP_DM = 15360;
constexpr unsigned LP_W3 = 16384;
static_assert(LP_T1 + 4 * 1026 <= LP_W2, "prep scratch");
constexpr unsigned w3_slot(int j) { return j * 1280 + (j & 1) * 64 + ((j >> 2) & 1) * 128; }
DI void dma16_add(unsigned lds_base, unsigned lds_off, unsigned voff, __amdgpu_buffer_rsrc_t srd, unsigned sbase, unsigned soff) {
    unsigned t;
    asm volatile("s_add_u32 m0, %1, %2\n\ts_add_u32 %0, %5, %6\n\tbuffer_load_dwordx4 %3, %4, %0 offen lds"
                 : "=&s"(t) : "s"(lds_base), "i"(lds_off), "v"(voff), "s"(srd), "s"(sbase), "i"(soff) : "memory", "scc");
}
DI void prep_start(const Params& P, lds8* lds, int w, int lane, int p) {
    const unsigned ldsb = (unsigned)(size_t)lds;
    const __amdgpu_buffer_rsrc_t srdC = __builtin_amdgcn_make_buffer_rsrc((void*)P.wcin, 0, 3 * 17576 * 2, RSRC3);
    const int i0 = w == 0 ? 0 : w == 1 ? 6 : w == 2 ? 14 : 20;
    dma4(ldsb + LP_WO + w * 256, lane * 4, __builtin_amdgcn_make_buffer_rsrc((void*)P.wout, 0, 78 * 2, RSRC3), 0);
    {
        const unsigned hp = lane >> 5;
        const unsigned vo = hp * 1352 + ((lane >> 2) & 7) * 52 + (((lane & 3) ^ (hp << 1)) << 4);
        const unsigned lb = ldsb + LP_W3 + w * 16640, sb = 52 * 1352 + 52 * i0;
        dma16_add(lb, w3_slot(0), vo, srdC, sb, 0 * 2704);  dma16_add(lb, w3_slot(1), vo, srdC, sb, 1 * 2704);
        dma16_add(lb, w3_slot(2), vo, srdC, sb, 2 * 2704);  dma16_add(lb, w3_slot(3), vo, srdC, sb, 3 * 2704);
        dma16_add(lb, w3_slot(4), vo, srdC, sb, 4 * 2704);  dma16_add(lb, w3_slot(5), vo, srdC, sb, 5 * 2704);
        dma16_add(lb, w3_slot(6), vo, srdC, sb, 6 * 2704);  dma16_add(lb, w3_slot(7), vo, srdC, sb, 7 * 2704);
        dma16_add(lb, w3_slot(8), vo, srdC, sb, 8 * 2704);  dma16_add(lb, w3_slot(9), vo, srdC, sb, 9 * 2704);
        dma16_add(lb, w3_slot(10), vo, srdC, sb, 10 * 2704); dma16_add(lb, w3_slot(11), vo, srdC, sb, 11 * 2704);
        dma16_add(lb, w3_slot(12), vo, srdC, sb, 12 * 2704);
    }
    if (__builtin_expect(w < 2, 1)) {
        const unsigned vo = w == 0 ? 35152 + (lane >> 2) * 1352 + p * 52 + (lane & 3) * 16 : p * 1352 + lane * 16;
        const unsigned lb = ldsb + (w == 0 ? LP_W2 : LP_T0);
        dma16(lb, vo, srdC, 0);
        dma16(lb + 1024, vo, srdC, w == 0 ? 16 * 1352 : 1024);
    }
}
DI void prep_role(const Params& P, lds8* lds, int w, int lane, int p STAMP_ARG) {
    const int n = lane & 15, gq = lane >> 4;
    const int i0 = w == 0 ? 0 : w == 1 ? 6 : w == 2 ? 14 : 20;
    const int nact = w == 0 ? 48 : 44;
    const int ch = lane < nact ? lane : 0;
    const int cs = ch >> 1, chh = ch & 1;
    const GAS u32x4* tp = (const GAS u32x4*)&g_tab.v[w == 0 ? 1 : 0][(cs * 2 + chh) * 8];
    const u32x4 ta = tp[0], tb = tp[1];
    STAMP(1);
    const int q = (lane & 15) >> 2, pp = lane & 3;
    int r0 = 8 * gq + q, r1 = r0 + 4;
    r0 = r0 < 26 ? r0 : 25; r1 = r1 < 26 ? r1 : 25;
    unsigned b0a, b0b, b1a, b1b;
    {
        const unsigned wb = LP_W3 + w * 16640 + pp * 8;
        const unsigned j0 = r0 >> 1, j1 = r1 >> 1;
        const unsigned s0 = wb + j0 * 1280 + (j0 & 1) * 64 + ((j0 >> 2) & 1) * 128 + (r0 & 1) * 512;
        const unsigned s1 = wb + j1 * 1280 + (j1 & 1) * 64 + ((j1 >> 2) & 1) * 128 + (r1 & 1) * 512;
        b0a = s0 + (r0 & 1) * 32; b0b = s0 + 32 - (r0 & 1) * 32; b1a = s1 + (r1 & 1) * 32; b1b = s1 + 32 - (r1 & 1) * 32;
    }
    unsigned va = LP_VT + n * 64 + 2 * i0, vb = n < 10 ? va + 1024 : LP_DM;
    unsigned vl = w == 3 ? LP_DM : 0;
    unsigned ac = LP_T1 + 4 * (4 * gq * 32 + n);
    unsigned a20 = LP_W2 + r0 * 64 + pp * 8, a21 = LP_W2 + r1 * 64 + pp * 8, abv = LP_VT + n * 64 + gq * 16;
    asm volatile("" : "+v"(b0a), "+v"(b0b), "+v"(b1a), "+v"(b1b), "+v"(va), "+v"(vb), "+v"(ac), "+v"(a20), "+v"(a21), "+v"(abv));
    if (w == 3) {
        LAS unsigned* z = (LAS unsigned*)(lds + LP_VT + (lane < 27 ? lane : 31) * 64 + 52);
        z[0] = 0; z[1] = 0; z[2] = 0;
        *(LAS unsigned*)(lds + LP_T1 + 4 * T_ZERO) = 0;
    }
    if (w == 1) *(LAS unsigned*)(lds + LP_T0 + 2 * T_ZERO) = 0;
    asm volatile("s_waitcnt vmcnt(0) lgkmcnt(0)" ::: "memory");
    __builtin_amdgcn_s_barrier();
    STAMP(2);
    {
        const u32x4 wo3 = *(const LAS u32x4*)(lds + LP_WO + w * 256 + 104 + 16 * gq);
        s16x8 bfr[16];
#pragma unroll
        for (int t = 0; t < 16; ++t) {
            const int ii = t >> 1, u = t & 1;
            const s16x4 lo = __builtin_amdgcn_ds_read_tr16_b64_v4i16((LAS s16x4*)(lds + (u ? b0b : b0a) + ii * 64));
            const s16x4 hi = __builtin_amdgcn_ds_read_tr16_b64_v4i16((LAS s16x4*)(lds + (u ? b1b : b1a) + ii * 64));
            bfr[t] = __builtin_shufflevector(lo, hi, 0, 1, 2, 3, 4, 5, 6, 7);
        }
        if (w == 3) {
            const unsigned o2 = *(const LAS unsigned*)(lds + LP_WO + 768 + 52 + (lane < 13 ? lane : 0) * 4);
            const unsigned o1 = *(const LAS unsigned short*)(lds + LP_WO + 768 + p * 2);
            *(LAS unsigned*)(lds + (lane < 13 ? LP_VT + 26 * 64 + lane * 4 : LP_DM)) = o2;
            *(LAS float*)(lds + LP_T1 + 4 * T_CONST) = bf_lo(o1);
        }
        float res[16];
#pragma unroll
        for (int t = 0; t < 16; ++t) res[t] = mfma16(as_frag(wo3), bfr[t], (f32x4){0.f, 0.f, 0.f, 0.f})[0];
#pragma unroll
        for (int k2 = 0; k2 < 4; ++k2) {
            const unsigned pa0 = pk_bf16(res[4 * k2], res[4 * k2 + 2]), pb0 = pk_bf16(res[4 * k2 + 1], res[4 * k2 + 3]);
            if (k2 < 3) { *(LAS unsigned*)(lds + va + 4 * k2) = pa0; *(LAS unsigned*)(lds + vb + 4 * k2) = pb0; }
            else { *(LAS unsigned*)(lds + (vl ? vl : va + 12)) = pa0; *(LAS unsigned*)(lds + (vl ? vl : vb + 12)) = pb0; }
        }
    }
    CBAR();
    sig_add(lds, L_F + 28);
    if (w > 1) return;
    STAMP(3);
    const __amdgpu_buffer_rsrc_t srdA = __builtin_amdgcn_make_buffer_rsrc((void*)(P.ws + WS_A), 0, A_BYTES, RSRC3);
    u32x4 o;
    if (w == 1) {
        float pv[16];
#pragma unroll
        for (int e = 0; e < 8; ++e) {
            const unsigned x = e < 4 ? ta[e] : tb[e - 4];
            pv[2 * e] = bf_lo(*(const LAS unsigned short*)(lds + LP_T0 + 2 * (x & 0xffff)));
            pv[2 * e + 1] = bf_lo(*(const LAS unsigned short*)(lds + LP_T0 + 2 * (x >> 16)));
        }
#pragma unroll
        for (int j = 0; j < 4; ++j) o[j] = pk_bf16(pv[4 * j] + pv[4 * j + 1], pv[4 * j + 2] + pv[4 * j + 3]);
    } else {
        s16x8 a2[2];
#pragma unroll
        for (int mt = 0; mt < 2; ++mt) {
            const s16x4 lo = __builtin_amdgcn_ds_read_tr16_b64_v4i16((LAS s16x4*)(lds + a20 + mt * 32));
            const s16x4 hi = __builtin_amdgcn_ds_read_tr16_b64_v4i16((LAS s16x4*)(lds + a21 + mt * 32));
            a2[mt] = __builtin_shufflevector(lo, hi, 0, 1, 2, 3, 4, 5, 6, 7);
        }
        unsigned pa[16];
#pragma unroll
        for (int e = 0; e < 8; ++e) {
            const unsigned x = e < 4 ? ta[e] : tb[e - 4];
            pa[2 * e] = LP_T1 + 4 * (x & 0xffff); pa[2 * e + 1] = LP_T1 + 4 * (x >> 16);
        }
#pragma unroll
        for (int e = 0; e < 16; ++e) asm volatile("" : "+v"(pa[e]));
        _Pragma("unroll 1") for (unsigned sp = 0; lds_ld(lds, L_F + 28) < 4 && sp < SPIN_CAP; ++sp) __builtin_amdgcn_s_sleep(0);
        CBAR();
        const u32x4 bf0 = *(const LAS u32x4*)(lds + abv), bf1 = *(const LAS u32x4*)(lds + abv + 1024);
        const f32x4 z4 = {0.f, 0.f, 0.f, 0.f};
        const f32x4 d00 = mfma16(a2[0], as_frag(bf0), z4), d01 = mfma16(a2[1], as_frag(bf0), z4);
        const f32x4 d10 = mfma16(a2[0], as_frag(bf1), z4), d11 = mfma16(a2[1], as_frag(bf1), z4);
#pragma unroll
        for (int e = 0; e < 4; ++e) {
            *(LAS float*)(lds + ac + e * 128) = d00[e]; *(LAS float*)(lds + ac + 2048 + e * 128) = d01[e];
            *(LAS float*)(lds + ac + 64 + e * 128) = d10[e]; *(LAS float*)(lds + ac + 2048 + 64 + e * 128) = d11[e];
        }
        STAMP(4);
        float pv[16];
#pragma unroll
        for (int e = 0; e < 16; ++e) pv[e] = *(const LAS float*)(lds + pa[e]);
#pragma unroll
        for (int j = 0; j < 4; ++j) o[j] = pk_bf16(pv[4 * j] + pv[4 * j + 1], pv[4 * j + 2] + pv[4 * j + 3]);
    }
    {
        const int fr = w == 0 ? (cs < 22 ? 2 * cs : 22 + cs) : 2 * cs + 1;
        __builtin_amdgcn_raw_buffer_store_b128(o, srdA, lane < nact ? (unsigned)(fr * 832 + (26 * chh + p) * 16) : 0x7ffffff0u, 0, AUX_SC1);
    }
    STAMP(5);
    WAIT_VM0();
    STAMP(6);
    if (w == 1) { sig_set(lds, L_F + 24); return; }
    const unsigned tagv = *(const LAS unsigned*)(lds + L_EP + (lane & 15) * 4);
    _Pragma("unroll 1") for (unsigned sp = 0; lds_ld(lds, L_F + 24) == 0 && sp < SPIN_CAP; ++sp) __builtin_amdgcn_s_sleep(0);
    if (lane < 16) __hip_atomic_store((gu32*)(P.ws + WS_FL) + lane * 32 + p, tagv + 1u, __ATOMIC_RELAXED, __HIP_MEMORY_SCOPE_AGENT);
}

constexpr unsigned A_PER = A_BYTES / 4;
DI void cin_role(const Params& P, lds8* lds, int cw, int lane, int g, int m, bool has_dnn, int pf_slot STAMP_ARG) {
    const int d = lane & 31, h = lane >> 5;
    const int b = 64 * g + 4 * m + cw;
    const unsigned ldsb = (unsigned)(size_t)lds;
    {
        const __amdgpu_buffer_rsrc_t srdT = __builtin_amdgcn_make_buffer_rsrc((void*)P.table, 0, NVOC * 64, RSRC3);
        const unsigned lb = ldsb + L_X0 + cw * 1664;
        WAIT_VM0();
        const int q = lane >> 2;
        const unsigned j0 = *(const LAS unsigned*)(lds + L_X0 + cw * 1664 + q * 4);
        const unsigned j1 = *(const LAS unsigned*)(lds + L_X0 + cw * 1664 + (q < 10 ? q + 16 : 25) * 4);
        unsigned o0 = j0 * 64 + (lane & 3) * 16, o1 = j1 * 64 + (lane & 3) * 16;
        asm volatile("" : "+v"(o0), "+v"(o1));
        STAMP(1);
        dma16(lb, o0, srdT, 0);
        if (lane < 40) dma16(lb + 1024, o1, srdT, 0);
    }
    const unsigned bo = P.bout[0];
    STAMP(2);
    __builtin_amdgcn_s_setprio(0);
    CBAR();
    __builtin_amdgcn_s_barrier();
    const unsigned epv = *(const LAS unsigned*)(lds + L_EP + g * 4);
    unsigned bov = bo;
    asm volatile("s_waitcnt vmcnt(0)" : "+v"(bov) :: "memory");
    STAMP(3);
    float x[26];
    {
        const lds8* xb = lds + L_X0 + cw * 1664 + h * 64 + d * 2;
        const lds8* xl = lds + L_X0 + cw * 1664 + (h ? 0 : 25 * 64) + d * 2;
#pragma unroll
        for (int a = 0; a < 13; ++a) {
            const unsigned lo = *(const LAS unsigned short*)(xb + 2 * a * 64);
            const unsigned hi = a < 12 ? *(const LAS unsigned short*)(xb + (2 * a + 1) * 64) : *(const LAS unsigned short*)xl;
            x[2 * a] = bf_lo(lo); x[2 * a + 1] = bf_lo(hi);
        }
    }
    u32x4 z[24];
#pragma unroll
    for (int s = 0; s < 22; ++s) {
        float fa[8], fb[8];
#pragma unroll
        for (int e = 0; e < 8; ++e) { const AB pe = slot_ab(8 * s + e); fa[e] = x[pe.a]; fb[e] = x[pe.b]; }
        float t0, t1, t2, t3, t4, t5;
        unsigned o0, o1, o2, o3;
        asm("v_mul_f32 %4, %10, %11\n\tv_mul_f32 %5, %12, %13\n\tv_mul_f32 %6, %14, %15\n\tv_mul_f32 %7, %16, %17\n\t"
            "v_cvt_pk_bf16_f32 %0, %4, %5\n\tv_mul_f32 %8, %18, %19\n\tv_mul_f32 %9, %20, %21\n\tv_cvt_pk_bf16_f32 %1, %6, %7\n\t"
            "v_mul_f32 %4, %22, %23\n\tv_mul_f32 %5, %24, %25\n\tv_cvt_pk_bf16_f32 %2, %8, %9\n\tv_cvt_pk_bf16_f32 %3, %4, %5"
            : "=&v"(o0), "=&v"(o1), "=&v"(o2), "=v"(o3), "=&v"(t0), "=&v"(t1), "=&v"(t2), "=&v"(t3), "=&v"(t4), "=&v"(t5)
            : "v"(fa[0]), "v"(fb[0]), "v"(fa[1]), "v"(fb[1]), "v"(fa[2]), "v"(fb[2]), "v"(fa[3]), "v"(fb[3]),
              "v"(fa[4]), "v"(fb[4]), "v"(fa[5]), "v"(fb[5]), "v"(fa[6]), "v"(fb[6]), "v"(fa[7]), "v"(fb[7]));
        z[s] = (u32x4){o0, o1, o2, o3};
    }
#pragma unroll
    for (int s = 22; s < 24; ++s) {
#pragma unroll
        for (int j = 0; j < 4; ++j) {
            const AB p0 = slot_ab(8 * s + 2 * j), p1 = slot_ab(8 * s + 2 * j + 1);
            const float a0 = p0.a < 26 ? x[p0.a] : (p0.a == 26 ? 1.f : 0.f);
            const float a1 = p1.a < 26 ? x[p1.a] : (p1.a == 26 ? 1.f : 0.f);
            z[s][j] = pk_bf16(a0, a1);
        }
    }
#pragma unroll
    for (int s = 0; s < 24; ++s) asm volatile("" : "+v"(z[s]));
    STAMP(4);
    const unsigned tag = epv + 1u;
    {
        const __amdgpu_buffer_rsrc_t srdA = __builtin_amdgcn_make_buffer_rsrc((void*)(P.ws + WS_A), 0, A_BYTES, RSRC3);
        if (cw == 0) {
            if (has_dnn) _Pragma("unroll 1") for (unsigned sp = 0; lds_ld(lds, L_F + 52) < 4 && sp < SPIN_CAP; ++sp) __builtin_amdgcn_s_sleep(1);
            const gu32* fl = (const gu32*)(P.ws + WS_FL) + g * 32 + (lane < 26 ? lane : 0);
            _Pragma("unroll 1") for (unsigned sp = 0; sp < SPIN_CAP; ++sp) { if (__all(ld_agent(fl) == tag)) break; __builtin_amdgcn_s_sleep(1); }
            sig_set(lds, L_F + 16);
            if (!has_dnn) dma4_sc1(ldsb + L_DUMMY, lane < 50 ? (unsigned)((((g & 1) * 3 + pf_slot) * 50 + lane) * 128) : 0x7fffff00u, srdA, 0);
        } else {
            _Pragma("unroll 1") for (unsigned sp = 0; lds_ld(lds, L_F + 16) == 0 && sp < SPIN_CAP; ++sp) __builtin_amdgcn_s_sleep(0);
        }
        CBAR();
        __builtin_amdgcn_s_setprio(CIN_PRIO);
        STAMP(5);
        {
            const unsigned l1 = ldsb + L_W + cw * 4096, vo = lane * 16;
            asm volatile("s_mov_b32 m0, %0\n\ts_nop 0\n\t"
                         "buffer_load_dwordx4 %1, %2, %3 offen sc1 lds\n\tbuffer_load_dwordx4 %1, %2, %3 offen offset:1024 sc1 lds\n\t"
                         "buffer_load_dwordx4 %1, %2, %3 offen offset:2048 sc1 lds\n\tbuffer_load_dwordx4 %1, %2, %3 offen offset:3072 sc1 lds"
                         :: "s"(l1), "v"(vo), "s"(srdA), "s"(cw * 4096) : "memory");
#pragma unroll
            for (int i = 0; i < 6; ++i) {
                const int q = 6 * cw + i;
                const unsigned go = q < 4 ? 16384 + 1024 * q : 19968 + 1024 * (q - 4), lo = q < 4 ? 16384 + 1024 * q : 53248 + 1024 * (q - 4);
                dma16_sc1(ldsb + L_W + lo, vo, srdA, go);
            }
        }
        wait_vm<6>();
        sig_add(lds, L_F + 20);
        _Pragma("unroll 1") for (unsigned sp = 0; lds_ld(lds, L_F + 20) < 4 && sp < SPIN_CAP; ++sp) __builtin_amdgcn_s_sleep(0);
        CBAR();
    }
    STAMP(6);
    const gu64* pp = (const gu64*)(P.ws + WS_PART) + ((g * 32 + (lane < 26 ? lane : 0)) * 64 + 4 * m + cw);
    unsigned long long pv = 0;
    f32x16 acc0, acc1;
#pragma unroll
    for (int i = 0; i < 16; ++i) { acc0[i] = 0.f; acc1[i] = 0.f; }
    {
        unsigned abo = L_W + (26 * h + d) * 16;
        asm volatile("" : "+v"(abo));
        const lds8* ab = lds + abo;
        constexpr int PF = 6;
        u32x4 af[46];
        unsigned cnt2 = 0;
        auto frag_of = [](int t) { return t < 24 ? t : t + 40; };
#pragma unroll
        for (int t = 0; t < PF; ++t) af[t] = *(const LAS u32x4*)(ab + frag_of(t) * 832);
        __builtin_amdgcn_sched_barrier(0);
#pragma unroll
        for (int t = 0; t < 46; ++t) {
            if (t == 9) { wait_vm<0>(); sig_add(lds, L_F + 44); }
            if (t + PF == 19 - 2) cnt2 = lds_ld(lds, L_F + 44);
            if (t + PF == 19) {
                if (cnt2 < 4) _Pragma("unroll 1") for (unsigned sp = 0; lds_ld(lds, L_F + 44) < 4 && sp < SPIN_CAP; ++sp) __builtin_amdgcn_s_sleep(0);
                CBAR();
            }
            if (t + PF < 46) af[t + PF] = *(const LAS u32x4*)(ab + frag_of(t + PF) * 832);
            if (t < 44) {
                if (t & 1) acc0 = mfma32(as_frag(af[t]), as_frag(z[t >> 1]), acc0);
                else acc1 = mfma32(as_frag(af[t]), as_frag(z[t >> 1]), acc1);
            } else acc1 = mfma32(as_frag(af[t]), as_frag(z[t - 22]), acc1);
            if (t == 30) pv = __hip_atomic_load(pp, __ATOMIC_RELAXED, __HIP_MEMORY_SCOPE_AGENT);
            __builtin_amdgcn_sched_barrier(0);
        }
    }
    float sa = 0.f;
#pragma unroll
    for (int i = 0; i < 12; ++i) sa = __builtin_fmaf(acc0[i], acc1[i], sa);
    const float sb = __builtin_fmaf(acc0[12], acc1[12], acc0[13] * acc1[13]);
    float tot = h == 0 ? sa + sb : sa;
    asm volatile("" : "+v"(tot));
    STAMP(7);
    _Pragma("unroll 1") for (unsigned sp = 0; sp < SPIN_CAP; ++sp) {
        if (__all((unsigned)(pv >> 32) == tag)) break;
        pv = __hip_atomic_load(pp, __ATOMIC_RELAXED, __HIP_MEMORY_SCOPE_AGENT);
    }
    STAMP(8);
    if (lane < 26) tot += __uint_as_float((unsigned)pv);
    tot = wave_sum_to_lane63(tot);
    if (lane == 63) {
        const float o = tot + bf_lo(bov);
        P.out[b] = (unsigned short)(pk_bf16(o, o) & 0xffffu);
    }
    if (m == 0 && cw == 0) {
        const gu64* ap = (const gu64*)(P.ws + WS_PART) + ((g * 32 + 26 + (lane < 3 ? lane : 0)) * 64);
        _Pragma("unroll 1") for (unsigned sp = 0; sp < SPIN_CAP; ++sp) {
            const unsigned long long av = __hip_atomic_load(ap, __ATOMIC_RELAXED, __HIP_MEMORY_SCOPE_AGENT);
            if (__all((unsigned)(av >> 32) == tag)) break;
        }
        if (lane == 0) __hip_atomic_store((gu32*)(P.ws + WS_EP) + g, tag, __ATOMIC_RELAXED, __HIP_MEMORY_SCOPE_AGENT);
    }
}

DI void decode_block(int bid, int& g, int& m, int& ntile, int& k) {
    const int x = bid & 7, j = bid >> 3;
    g = 2 * x + (j >> 4); m = j & 15;
    ntile = m < 13 ? m : -1; k = m - 13;
}

__global__ void __launch_bounds__(512, 2) xdfm_kernel(Params P) {
    __shared__ __attribute__((aligned(16))) unsigned char smem[L_END];
    lds8* lds = (lds8*)smem;
    Stamps st; st.t0 = 0; st.t1 = 0;
    STAMP(0);
    const int w = __builtin_amdgcn_readfirstlane(threadIdx.x >> 6), lane = threadIdx.x & 63;
    {
        unsigned long long pc; unsigned len;
        asm volatile("s_getpc_b64 %0\n.Lxdfm_pc:\n\ts_mov_b32 %1, (.Lxdfm_end-.Lxdfm_pc)&4294967295" : "=s"(pc), "=s"(len));
        const __amdgpu_buffer_rsrc_t srdP = __builtin_amdgcn_make_buffer_rsrc((void*)pc, 0, len, RSRC3);
        dma4((unsigned)(size_t)lds + L_DUMMY, lane == 0 ? ((blockIdx.x >> 3) * 8 + w) * 128 : 0x7fffff00u, srdP, 0);
    }
    asm volatile("" :: "s"(P.idx), "s"(P.table), "s"(P.wcin), "s"(P.wout), "s"(P.w1), "s"(P.b1), "s"(P.w2), "s"(P.bout), "s"(P.out), "s"(P.ws));
    int g, m, ntile, k;
    decode_block(blockIdx.x, g, m, ntile, k);
    const bool is_cin = w >= 4;
    if (__builtin_expect(is_cin, 1)) {
        __builtin_amdgcn_s_setprio(3);
        dma16((unsigned)(size_t)lds + L_X0 + (w - 4) * 1664, lane < 7 ? (64 * g + 4 * m + w - 4) * 104 + lane * 16 : 0x7fffff00u,
              __builtin_amdgcn_make_buffer_rsrc((void*)P.idx, 0, NBATCH * 26 * 4, RSRC3), 0);
        if (__builtin_expect(w == 7, 1)) dma4((unsigned)(size_t)lds + L_DUMMY, lane < 26 ? ((blockIdx.x >> 3) * 26 + lane) * 128 : 0x7fffff00u,
                         __builtin_amdgcn_make_buffer_rsrc((void*)P.wcin, 0, 3 * 17576 * 2, RSRC3), 0);
    }
    if (__builtin_expect(w == 0, 1)) {
        if (lane < 16) lds_st(lds, L_F + 4 * lane, 0);
        dma4_sc1((unsigned)(size_t)lds + L_EP, (lane & 15) * 4, __builtin_amdgcn_make_buffer_rsrc((void*)(P.ws + WS_EP), 0, 64, RSRC3), 0);
    }
    const int p = g * 3 + k;
    if (__builtin_expect(!is_cin && ntile >= 0, 1)) dnn_start(P, lds, w, lane, g, ntile);
    if (__builtin_expect(!is_cin && ntile < 0 && p < 26, 1)) prep_start(P, lds, w, lane, p);
    if (__builtin_expect(!is_cin && ntile >= 0, 1)) {
        dnn_role(P, lds, w, lane, g, ntile STAMP_PASS);
    } else if (is_cin) {
        cin_role(P, lds, w - 4, lane, g, m, ntile >= 0, k STAMP_PASS);
        __builtin_amdgcn_s_waitcnt(0x0F70);
    } else {
        if (p < 26) prep_role(P, lds, w, lane, p STAMP_PASS);
        else { asm volatile("s_waitcnt vmcnt(0) lgkmcnt(0)" ::: "memory"); __builtin_amdgcn_s_barrier(); }
        if (w == 0) {
            const unsigned ep = *(const LAS unsigned*)(lds + L_EP + g * 4);
            if (lane == 0) st_granule((gu64*)(P.ws + WS_PART) + ((g * 32 + 26 + k) * 64), ep + 1u, 0u);
        }
        __builtin_amdgcn_s_waitcnt(0x0F70);
    }
    STAMP(11);
    STAMP_FLUSH(P.ws, w, lane);
    asm volatile(".Lxdfm_end:");
}

extern "C" void kernel_launch(void* const* d_in, const int* in_sizes, int n_in,
                              void* d_out, int out_size, void* d_ws, size_t ws_size,
                              hipStream_t stream) {
    Params P;
    P.idx = (const int*)d_in[0]; P.table = (const unsigned short*)d_in[1]; P.wcin = (const unsigned short*)d_in[2];
    P.wout = (const unsigned short*)d_in[3]; P.w1 = (const unsigned short*)d_in[4]; P.b1 = (const unsigned short*)d_in[5];
    P.w2 = (const unsigned short*)d_in[6]; P.bout = (const unsigned short*)d_in[7];
    P.out = (unsigned short*)d_out; P.ws = (unsigned char*)d_ws;
    xdfm_kernel<<<256, 512, 0, stream>>>(P);
}
```

```cpp
#include <hip/hip_runtime.h>
#include <stdint.h>

#define DI __device__ __forceinline__
#define GAS __attribute__((address_space(1)))
#define LAS __attribute__((address_space(3)))

typedef short    s16x4 __attribute__((ext_vector_type(4)));
typedef short    s16x8 __attribute__((ext_vector_type(8)));
typedef float    f32x2 __attribute__((ext_vector_type(2)));
typedef float    f32x4 __attribute__((ext_vector_type(4)));
typedef float    f32x16 __attribute__((ext_vector_type(16)));
typedef unsigned u32x2 __attribute__((ext_vector_type(2)));
typedef unsigned u32x4 __attribute__((ext_vector_type(4)));
typedef __bf16   bf16x2 __attribute__((ext_vector_type(2)));
typedef GAS unsigned gu32;
typedef GAS unsigned long long gu64;
typedef LAS unsigned char lds8;

#define MAPPING 1

constexpr int DIN = 832, NBATCH = 1024, NVOC = 100000;
#define A_AFTER_X 1
#define W_EARLY 1
#define A_PF 1
#define CODE_PF 1
#define WC_PF 1
#define POLL_STEP 10
#define CIN_PRIO 2
#define WO_LAST 0
#define PKMUL 0
#define A_DMA 1
constexpr unsigned RSRC3 = 0x00020000u;
constexpr int AUX_SC1 = 16;
constexpr unsigned SPIN_CAP = 1u << 20;

constexpr unsigned WS_EP = 0;
constexpr unsigned WS_FL = 4096;
constexpr unsigned WS_PART = 8192;
constexpr unsigned WS_A = 8192 + 16 * 32 * 64 * 8;
constexpr unsigned A_BYTES = 46 * 832;

#define XD 6
constexpr unsigned L_W = 0;
constexpr unsigned L_A = 832 * 128;
constexpr unsigned L_X0 = L_A + 4 * XD * 2048;
constexpr unsigned L_F = L_X0 + 4 * 1664;
constexpr unsigned L_DUMMY = L_F + 256;
constexpr unsigned L_EP = L_DUMMY + 256;
constexpr unsigned L_BI = L_EP + 256;
constexpr unsigned L_W2 = L_BI + 256;
constexpr unsigned L_END = L_W2 + 256;
static_assert(4 * XD * 2048 >= A_BYTES && L_END <= 163840, "LDS map");
constexpr unsigned LP_VT = 0;
constexpr unsigned LP_T0 = 2048;
constexpr unsigned LP_T1 = 2048 + 2304;

struct AB { int a, b; };
constexpr AB slot_ab(int t) {
    if (t < 156) { int a = 2 * (t / 12); return {a, (a + t % 12) % 26}; }
    if (t < 170) { int u = t - 156; int a = 2 * (u / 2); return {a, (a + 12 + (u & 1)) % 26}; }
    if (t < 176) { int a = 2 * (7 + t - 170); return {a, (a + 12) % 26}; }
    if (t < 189) return {2 * (t - 176), 26};
    if (t == 189) return {26, 26};
    return {27, 27};
}
constexpr unsigned T_ZERO = 1024, T_CONST = 1025;
struct PrepTab { unsigned v[2][24 * 2 * 8]; };
constexpr PrepTab make_tab() {
    PrepTab tb{};
    for (int m = 0; m < 2; ++m) for (int s = 0; s < 24; ++s) for (int h = 0; h < 2; ++h) for (int e = 0; e < 8; ++e) {
        const int st = m ? 32 : 26;
        int t = 8 * s + e; AB ab = slot_ab(t);
        unsigned o1 = T_ZERO, o2 = T_ZERO;
        if (ab.a == 27) {}
        else if (ab.a == 26) { if (h == 0 && m) o1 = T_CONST; }
        else if (ab.b == 26) { if (m) o1 = ((ab.a + h) % 26) * st + 26; }
        else {
            int u = (ab.a + h) % 26, v = (ab.b + h) % 26;
            bool dup = (t == 169 && h == 1);
            if (dup) {}
            else if (u == v) o1 = u * st + u;
            else { o1 = u * st + v; o2 = v * st + u; }
        }
        tb.v[m][(s * 2 + h) * 8 + e] = o1 | (o2 << 16);
    }
    return tb;
}
static_assert(slot_ab(169).a == 12 && slot_ab(169).b == 25, "dup slot");

struct Params {
    const int* idx; const unsigned short* table; const unsigned short* wcin; const unsigned short* wout;
    const unsigned short* w1; const unsigned short* b1; const unsigned short* w2; const unsigned short* bout;
    unsigned short* out; unsigned char* ws;
};
struct KArgs { Params P; PrepTab tab; };

DI unsigned sel(bool c, unsigned a, unsigned b) { unsigned r = c ? a : b; return r; }
DI float bf_lo(unsigned v) { return __uint_as_float(v << 16); }
DI float bf_hi(unsigned v) { return __uint_as_float(v & 0xffff0000u); }
DI unsigned pk_bf16(float lo, float hi) { f32x2 v = {lo, hi}; return __builtin_bit_cast(unsigned, __builtin_convertvector(v, bf16x2)); }
DI f32x4 mfma16(s16x8 a, s16x8 b, f32x4 c) { return __builtin_amdgcn_mfma_f32_16x16x32_bf16(a, b, c, 0, 0, 0); }
DI f32x16 mfma32(s16x8 a, s16x8 b, f32x16 c) { return __builtin_amdgcn_mfma_f32_32x32x16_bf16(a, b, c, 0, 0, 0); }
DI s16x8 as_frag(u32x4 v) { return __builtin_bit_cast(s16x8, v); }
template <int CTRL, int ROWMASK> DI float dpp_add(float v) {
    return v + __int_as_float(__builtin_amdgcn_update_dpp(0, __float_as_int(v), CTRL, ROWMASK, 0xf, true));
}
DI float wave_sum_to_lane63(float v) {
    v = dpp_add<0x111, 0xf>(v); v = dpp_add<0x112, 0xf>(v); v = dpp_add<0x114, 0xf>(v); v = dpp_add<0x118, 0xf>(v);
    v = dpp_add<0x142, 0xa>(v); v = dpp_add<0x143, 0xc>(v);
    return v;
}
DI float sum_lane_groups(float v) {
    auto a = __builtin_amdgcn_permlane16_swap(__float_as_uint(v), __float_as_uint(v), false, false);
    v = __uint_as_float(a[0]) + __uint_as_float(a[1]);
    auto b = __builtin_amdgcn_permlane32_swap(__float_as_uint(v), __float_as_uint(v), false, false);
    return __uint_as_float(b[0]) + __uint_as_float(b[1]);
}
DI float gelu_tanh(float x) {
    const float k1 = -2.0f * 0.7978845608028654f * 1.4426950408889634f;
    const float k2 = k1 * 0.044715f;
    float arg = x * __builtin_fmaf(x * x, k2, k1);
    float e = __builtin_amdgcn_exp2f(arg);
    return x * __builtin_amdgcn_rcpf(1.0f + e);
}
DI void dma16(unsigned lds_addr, unsigned voff, __amdgpu_buffer_rsrc_t srd, unsigned soff) {
    asm volatile("s_mov_b32 m0, %0\n\ts_nop 0\n\tbuffer_load_dwordx4 %1, %2, %3 offen lds"
                 :: "s"(lds_addr), "v"(voff), "s"(srd), "s"(soff) : "memory");
}
DI void dma16_sc1(unsigned lds_addr, unsigned voff, __amdgpu_buffer_rsrc_t srd, unsigned soff) {
    asm volatile("s_mov_b32 m0, %0\n\ts_nop 0\n\tbuffer_load_dwordx4 %1, %2, %3 offen sc1 lds"
                 :: "s"(lds_addr), "v"(voff), "s"(srd), "s"(soff) : "memory");
}
DI void dma4_sc1(unsigned lds_addr, unsigned voff, __amdgpu_buffer_rsrc_t srd, unsigned soff) {
    asm volatile("s_mov_b32 m0, %0\n\ts_nop 0\n\tbuffer_load_dword %1, %2, %3 offen sc1 lds"
                 :: "s"(lds_addr), "v"(voff), "s"(srd), "s"(soff) : "memory");
}
DI void dma4(unsigned lds_addr, unsigned voff, __amdgpu_buffer_rsrc_t srd, unsigned soff) {
    asm volatile("s_mov_b32 m0, %0\n\ts_nop 0\n\tbuffer_load_dword %1, %2, %3 offen lds"
                 :: "s"(lds_addr), "v"(voff), "s"(srd), "s"(soff) : "memory");
}
DI void st_granule(gu64* g, unsigned tag, unsigned val) {
    __hip_atomic_store(g, ((unsigned long long)tag << 32) | val, __ATOMIC_RELAXED, __HIP_MEMORY_SCOPE_AGENT);
}
DI unsigned ld_agent(const gu32* p) { return __hip_atomic_load(p, __ATOMIC_RELAXED, __HIP_MEMORY_SCOPE_AGENT); }
DI unsigned lds_ld(lds8* l, unsigned off) { return __hip_atomic_load((LAS unsigned*)(l + off), __ATOMIC_RELAXED, __HIP_MEMORY_SCOPE_WORKGROUP); }
DI void lds_st(lds8* l, unsigned off, unsigned v) { __hip_atomic_store((LAS unsigned*)(l + off), v, __ATOMIC_RELAXED, __HIP_MEMORY_SCOPE_WORKGROUP); }
DI void lds_add(lds8* l, unsigned off, unsigned v) { __hip_atomic_fetch_add((LAS unsigned*)(l + off), v, __ATOMIC_RELAXED, __HIP_MEMORY_SCOPE_WORKGROUP); }
DI void sig_add(lds8* l, unsigned off) {
    asm volatile("s_mov_b64 exec, 1\n\tds_add_u32 %0, %1\n\ts_mov_b64 exec, -1" :: "v"((unsigned)(size_t)l + off), "v"(1u) : "memory");
}
DI void sig_set(lds8* l, unsigned off) {
    asm volatile("s_mov_b64 exec, 1\n\tds_write_b32 %0, %1\n\ts_mov_b64 exec, -1" :: "v"((unsigned)(size_t)l + off), "v"(1u) : "memory");
}
#define WAIT_LGKM() asm volatile("s_waitcnt lgkmcnt(0)" ::: "memory")
#define WAIT_VM0() asm volatile("s_waitcnt vmcnt(0)" ::: "memory")
#define CBAR() asm volatile("" ::: "memory")
#define STAMP(i)
#define STAMP_ARG
#define STAMP_PASS
#define STAMP_FLUSH(ws, w, lane)

template <int K> DI unsigned quad_bcast(unsigned v) { return __builtin_amdgcn_update_dpp(0u, v, K * 0x55, 0xf, 0xf, true); }
template <int N> DI void wait_vm() { asm volatile("s_waitcnt vmcnt(%0)" :: "i"(N) : "memory"); }
DI void dma_w4(unsigned lds_addr, unsigned voff, __amdgpu_buffer_rsrc_t srd, unsigned soff) {
    unsigned t;
    asm volatile("s_mov_b32 m0, %1\n\ts_add_u32 %0, %4, 12288\n\tbuffer_load_dwordx4 %2, %3, %4 offen lds\n\t"
                 "buffer_load_dwordx4 %2, %3, %0 offen offset:1024 lds\n\ts_add_u32 %0, %0, 12288\n\t"
                 "buffer_load_dwordx4 %2, %3, %0 offen offset:2048 lds\n\ts_add_u32 %0, %0, 12288\n\t"
                 "buffer_load_dwordx4 %2, %3, %0 offen offset:3072 lds"
                 : "=&s"(t) : "s"(lds_addr), "v"(voff), "s"(srd), "s"(soff) : "memory", "scc");
}
DI void dnn_start(const Params& P, lds8* lds, int w, int lane, int g, int ntile) {
    const int mh = w & 1, kh = w >> 1;
    const unsigned ldsb = (unsigned)(size_t)lds;
    const unsigned ring = ldsb + L_A + w * (XD * 2048);
    const __amdgpu_buffer_rsrc_t srdI = __builtin_amdgcn_make_buffer_rsrc((void*)P.idx, 0, NBATCH * 26 * 4, RSRC3);
    const unsigned vo = ((64 * g + 32 * mh + (lane >> 2)) * 26 + 13 * kh + 4 * (lane & 3)) * 4;
    dma16(ring, vo, srdI, 0);
    dma16(ring + 1024, vo, srdI, 1664);
    if (__builtin_expect(w == 1, 1)) dma4(ldsb + L_BI, ntile * 128 + lane * 4, __builtin_amdgcn_make_buffer_rsrc((void*)P.b1, 0, DIN * 2, RSRC3), 0);
    if (__builtin_expect(w == 2, 1)) dma4(ldsb + L_W2, ntile * 128 + lane * 4, __builtin_amdgcn_make_buffer_rsrc((void*)P.w2, 0, DIN * 2, RSRC3), 0);
    const __amdgpu_buffer_rsrc_t srdW = __builtin_amdgcn_make_buffer_rsrc((void*)P.w1, 0, DIN * DIN * 2, RSRC3);
    const unsigned vw = (lane >> 3) * 1664 + (((lane & 7) ^ (((lane >> 4) & 1) << 2)) << 4);
    const unsigned rowA = 416 * kh + 208 * mh + 104 * (g & 1);
    dma_w4(ldsb + L_W + rowA * 128, vw, srdW, rowA * 1664 + ntile * 128);
}
DI void dnn_role(const Params& P, lds8* lds, int w, int lane, int g, int ntile STAMP_ARG) {
    const int mh = w & 1, kh = w >> 1;
    const int h = lane >> 5;
    const unsigned ldsb = (unsigned)(size_t)lds;
    const unsigned ring = ldsb + L_A + w * (XD * 2048);
    const __amdgpu_buffer_rsrc_t srdW = __builtin_amdgcn_make_buffer_rsrc((void*)P.w1, 0, DIN * DIN * 2, RSRC3);
    const unsigned vw = (lane >> 3) * 1664 + (((lane & 7) ^ (((lane >> 4) & 1) << 2)) << 4);
    const unsigned rowA = 416 * kh + 208 * mh + 104 * (g & 1), rowB = 416 * kh + 208 * mh + 104 * ((g & 1) ^ 1);
    auto wdma = [&](unsigned row, int a) {
        const unsigned sb = row * 1664 + ntile * 128, lb = ldsb + L_W + row * 128;
        if (a < 3) dma_w4(lb + a * 4096, vw, srdW, sb + a * 4 * 13312);
        else dma16(lb + 12288, vw, srdW, sb + 12 * 13312);
    };
    STAMP(1);
    wait_vm<4>();
    WAIT_LGKM();
    __builtin_amdgcn_s_barrier();
    STAMP(2);
    const u32x4 i0 = *(const LAS u32x4*)(lds + L_A + w * (XD * 2048) + lane * 16);
    const u32x4 i1 = *(const LAS u32x4*)(lds + L_A + w * (XD * 2048) + 1024 + lane * 16);
    const unsigned ep = *(const LAS unsigned*)(lds + L_EP + g * 4);
#pragma unroll
    for (int a = 1; a < 4; ++a) wdma(rowA, a);
#pragma unroll
    for (int a = 0; a < 4; ++a) wdma(rowB, a);
    const __amdgpu_buffer_rsrc_t srdT = __builtin_amdgcn_make_buffer_rsrc((void*)P.table, 0, NVOC * 64, RSRC3);
    const unsigned c16 = (((lane & 3) ^ ((lane >> 4) & 3)) << 4);
    u32x4 t0 = i0 << 6, t1 = i1 << 6;
    asm volatile("" : "+v"(t0), "+v"(t1));
    auto xload = [&](int s) {
        const unsigned a0 = t0[s & 3], a1 = t1[s & 3];
        const unsigned o0 = (s < 4 ? quad_bcast<0>(a0) : s < 8 ? quad_bcast<1>(a0) : s < 12 ? quad_bcast<2>(a0) : quad_bcast<3>(a0)) + c16;
        const unsigned o1 = (s < 4 ? quad_bcast<0>(a1) : s < 8 ? quad_bcast<1>(a1) : s < 12 ? quad_bcast<2>(a1) : quad_bcast<3>(a1)) + c16;
        dma16(ring + (s % XD) * 2048, o0, srdT, 0);
        dma16(ring + (s % XD) * 2048 + 1024, o1, srdT, 0);
    };
#pragma unroll
    for (int s = 0; s < XD; ++s) xload(s);
    STAMP(3);
    wait_vm<2 * XD>();
    sig_add(lds, L_F + 32 + 4 * kh);
    _Pragma("unroll 1") for (unsigned sp = 0; lds_ld(lds, L_F + 32 + 4 * kh) < 2 && sp < SPIN_CAP; ++sp) __builtin_amdgcn_s_sleep(0);
    CBAR();
    STAMP(4);
    f32x16 acc[2];
    u32x2 bia[4], wv[4];
#pragma unroll
    for (int r4 = 0; r4 < 4; ++r4) {
        bia[r4] = *(const LAS u32x2*)(lds + L_BI + kh * 64 + h * 8 + r4 * 16);
        wv[r4] = *(const LAS u32x2*)(lds + L_W2 + kh * 64 + h * 8 + r4 * 16);
    }
#pragma unroll
    for (int r4 = 0; r4 < 4; ++r4) {
        acc[0][4 * r4] = bf_lo(bia[r4][0]); acc[0][4 * r4 + 1] = bf_hi(bia[r4][0]); acc[0][4 * r4 + 2] = bf_lo(bia[r4][1]); acc[0][4 * r4 + 3] = bf_hi(bia[r4][1]);
        acc[1][4 * r4] = 0.f; acc[1][4 * r4 + 1] = 0.f; acc[1][4 * r4 + 2] = 0.f; acc[1][4 * r4 + 3] = 0.f;
    }
    {
        const int blk = (lane >> 4) & 1, q = (lane & 15) >> 2, pp = lane & 3, n = lane & 31;
        unsigned ad[2];
#pragma unroll
        for (int j = 0; j < 2; ++j) {
            const unsigned c = 4 * (j ^ kh) + 2 * blk + (pp >> 1);
            ad[j] = L_W + (416 * kh + 8 * h + q) * 128 + ((c ^ (((q >> 1) & 1) << 2)) << 4) + (pp & 1) * 8;
        }
        const unsigned xb = L_A + w * (XD * 2048) + n * 64;
        const unsigned xa0 = xb + ((h ^ ((n >> 2) & 3)) << 4), xa1 = xb + (((2 + h) ^ ((n >> 2) & 3)) << 4);
        u32x4 xf[3][2];
        s16x8 wf[2][4];
        auto xread = [&](int s) {
            xf[s % 3][0] = *(const LAS u32x4*)(lds + xa0 + (s % XD) * 2048);
            xf[s % 3][1] = *(const LAS u32x4*)(lds + xa1 + (s % XD) * 2048);
        };
        auto wread = [&](int s) {
#pragma unroll
            for (int a = 0; a < 2; ++a)
#pragma unroll
                for (int j = 0; j < 2; ++j) {
                    const s16x4 lo = __builtin_amdgcn_ds_read_tr16_b64_v4i16((LAS s16x4*)(lds + ad[j] + s * 4096 + a * 2048));
                    const s16x4 hi = __builtin_amdgcn_ds_read_tr16_b64_v4i16((LAS s16x4*)(lds + ad[j] + s * 4096 + a * 2048 + 512));
                    wf[s & 1][2 * a + j] = __builtin_shufflevector(lo, hi, 0, 1, 2, 3, 4, 5, 6, 7);
                }
        };
        wread(0);
        wait_vm<2 * (XD - 1)>();
        xread(0);
        wait_vm<2 * (XD - 2)>();
        xread(1);
        __builtin_amdgcn_sched_barrier(0);
#pragma unroll
        for (int s = 0; s < 13; ++s) {
            if (s + 1 < 13) wread(s + 1);
#pragma unroll
            for (int a = 0; a < 2; ++a)
#pragma unroll
                for (int j = 0; j < 2; ++j) acc[j] = mfma32(wf[s & 1][2 * a + j], as_frag(xf[s % 3][a]), acc[j]);
            if (s + 1 < 13) {
#pragma unroll
                for (int j = 0; j < 4; ++j) { __builtin_amdgcn_sched_group_barrier(0x100, 2, 0); __builtin_amdgcn_sched_group_barrier(0x8, 1, 0); }
            }
            __builtin_amdgcn_sched_barrier(0);
            if (s + XD < 13) xload(s + XD);
            if (s + 2 < 13) {
                constexpr int LASTI = 12;
                const int last = s + XD < LASTI ? s + XD : LASTI;
                const int younger = last - (s + 2);
                if (younger >= 4) wait_vm<8>(); else if (younger == 3) wait_vm<6>(); else if (younger == 2) wait_vm<4>(); else if (younger == 1) wait_vm<2>(); else wait_vm<0>();
                if (s + 2 == 12) sig_add(lds, L_F + 52);
                xread(s + 2);
            }
            __builtin_amdgcn_sched_barrier(0);
        }
    }
    STAMP(6);
    {
        _Pragma("unroll 1") for (unsigned sp = 0; lds_ld(lds, L_F + 52) < 4 && sp < SPIN_CAP; ++sp) __builtin_amdgcn_s_sleep(0);
        CBAR();
        lds8* xw = lds + L_W + kh * 53248 + (5 + mh) * 4096 + lane * 16;
#pragma unroll
        for (int r4 = 0; r4 < 4; ++r4) *(LAS f32x4*)(xw + r4 * 1024) = (f32x4){acc[1][4 * r4], acc[1][4 * r4 + 1], acc[1][4 * r4 + 2], acc[1][4 * r4 + 3]};
        CBAR();
        sig_set(lds, L_F + 4 * w);
        _Pragma("unroll 1") for (unsigned sp = 0; lds_ld(lds, L_F + 4 * (w ^ 2)) == 0 && sp < SPIN_CAP; ++sp) __builtin_amdgcn_s_sleep(0);
        CBAR();
        const lds8* xr = lds + L_W + (kh ^ 1) * 53248 + (5 + mh) * 4096 + lane * 16;
#pragma unroll
        for (int r4 = 0; r4 < 4; ++r4) {
            const f32x4 o = *(const LAS f32x4*)(xr + r4 * 1024);
            acc[0][4 * r4] += o[0]; acc[0][4 * r4 + 1] += o[1]; acc[0][4 * r4 + 2] += o[2]; acc[0][4 * r4 + 3] += o[3];
        }
    }
    STAMP(7);
    float s0 = 0.f, s1 = 0.f;
#pragma unroll
    for (int r4 = 0; r4 < 4; ++r4) {
        s0 = __builtin_fmaf(gelu_tanh(acc[0][4 * r4]), bf_lo(wv[r4][0]), s0);
        s1 = __builtin_fmaf(gelu_tanh(acc[0][4 * r4 + 1]), bf_hi(wv[r4][0]), s1);
        s0 = __builtin_fmaf(gelu_tanh(acc[0][4 * r4 + 2]), bf_lo(wv[r4][1]), s0);
        s1 = __builtin_fmaf(gelu_tanh(acc[0][4 * r4 + 3]), bf_hi(wv[r4][1]), s1);
    }
    s0 += s1;
    const auto sw = __builtin_amdgcn_permlane32_swap(__float_as_uint(s0), __float_as_uint(s0), false, false);
    const float part = __uint_as_float(sw[0]) + __uint_as_float(sw[1]);
    STAMP(8);
    if (lane < 32) st_granule((gu64*)(P.ws + WS_PART) + ((g * 32 + 2 * ntile + kh) * 64 + 32 * mh + lane), ep + 1u, __float_as_uint(part));
}

constexpr unsigned LP_W2 = 12288;
constexpr unsigned LP_WO = 14336;
constexpr unsigned LP_DM = 15360;
constexpr unsigned LP_W3 = 16384;
static_assert(LP_T1 + 4 * 1026 <= LP_W2, "prep scratch");
constexpr unsigned w3_slot(int j) { return j * 1280 + (j & 1) * 64 + ((j >> 2) & 1) * 128; }
DI void dma16_add(unsigned lds_base, unsigned lds_off, unsigned voff, __amdgpu_buffer_rsrc_t srd, unsigned sbase, unsigned soff) {
    unsigned t;
    asm volatile("s_add_u32 m0, %1, %2\n\ts_add_u32 %0, %5, %6\n\tbuffer_load_dwordx4 %3, %4, %0 offen lds"
                 : "=&s"(t) : "s"(lds_base), "i"(lds_off), "v"(voff), "s"(srd), "s"(sbase), "i"(soff) : "memory", "scc");
}
DI void prep_start(const Params& P, lds8* lds, int w, int lane, int p) {
    const unsigned ldsb = (unsigned)(size_t)lds;
    const __amdgpu_buffer_rsrc_t srdC = __builtin_amdgcn_make_buffer_rsrc((void*)P.wcin, 0, 3 * 17576 * 2, RSRC3);
    const int i0 = w == 0 ? 0 : w == 1 ? 6 : w == 2 ? 14 : 20;
    dma4(ldsb + LP_WO + w * 256, lane * 4, __builtin_amdgcn_make_buffer_rsrc((void*)P.wout, 0, 78 * 2, RSRC3), 0);
    {
        const unsigned hp = lane >> 5;
        const unsigned vo = hp * 1352 + ((lane >> 2) & 7) * 52 + (((lane & 3) ^ (hp << 1)) << 4);
        const unsigned lb = ldsb + LP_W3 + w * 16640, sb = 52 * 1352 + 52 * i0;
        dma16_add(lb, w3_slot(0), vo, srdC, sb, 0 * 2704);  dma16_add(lb, w3_slot(1), vo, srdC, sb, 1 * 2704);
        dma16_add(lb, w3_slot(2), vo, srdC, sb, 2 * 2704);  dma16_add(lb, w3_slot(3), vo, srdC, sb, 3 * 2704);
        dma16_add(lb, w3_slot(4), vo, srdC, sb, 4 * 2704);  dma16_add(lb, w3_slot(5), vo, srdC, sb, 5 * 2704);
        dma16_add(lb, w3_slot(6), vo, srdC, sb, 6 * 2704);  dma16_add(lb, w3_slot(7), vo, srdC, sb, 7 * 2704);
        dma16_add(lb, w3_slot(8), vo, srdC, sb, 8 * 2704);  dma16_add(lb, w3_slot(9), vo, srdC, sb, 9 * 2704);
        dma16_add(lb, w3_slot(10), vo, srdC, sb, 10 * 2704); dma16_add(lb, w3_slot(11), vo, srdC, sb, 11 * 2704);
        dma16_add(lb, w3_slot(12), vo, srdC, sb, 12 * 2704);
    }
    if (__builtin_expect(w < 2, 1)) {
        const unsigned vo = w == 0 ? 35152 + (lane >> 2) * 1352 + p * 52 + (lane & 3) * 16 : p * 1352 + lane * 16;
        const unsigned lb = ldsb + (w == 0 ? LP_W2 : LP_T0);
        dma16(lb, vo, srdC, 0);
        dma16(lb + 1024, vo, srdC, w == 0 ? 16 * 1352 : 1024);
    }
}
DI void prep_role(const Params& P, lds8* lds, int w, int lane, int p STAMP_ARG) {
    const int n = lane & 15, gq = lane >> 4;
    const int i0 = w == 0 ? 0 : w == 1 ? 6 : w == 2 ? 14 : 20;
    const int nact = w == 0 ? 48 : 44;
    const int ch = lane < nact ? lane : 0;
    const int cs = ch >> 1, chh = ch & 1;
    const GAS u32x4* tp = (const GAS u32x4*)((const GAS unsigned char*)(size_t)__builtin_amdgcn_kernarg_segment_ptr() + sizeof(Params) + ((w == 0 ? 384 : 0) + (cs * 2 + chh) * 8) * 4);
    const u32x4 ta = tp[0], tb = tp[1];
    STAMP(1);
    const int q = (lane & 15) >> 2, pp = lane & 3;
    int r0 = 8 * gq + q, r1 = r0 + 4;
    r0 = r0 < 26 ? r0 : 25; r1 = r1 < 26 ? r1 : 25;
    unsigned b0a, b0b, b1a, b1b;
    {
        const unsigned wb = LP_W3 + w * 16640 + pp * 8;
        const unsigned j0 = r0 >> 1, j1 = r1 >> 1;
        const unsigned s0 = wb + j0 * 1280 + (j0 & 1) * 64 + ((j0 >> 2) & 1) * 128 + (r0 & 1) * 512;
        const unsigned s1 = wb + j1 * 1280 + (j1 & 1) * 64 + ((j1 >> 2) & 1) * 128 + (r1 & 1) * 512;
        b0a = s0 + (r0 & 1) * 32; b0b = s0 + 32 - (r0 & 1) * 32; b1a = s1 + (r1 & 1) * 32; b1b = s1 + 32 - (r1 & 1) * 32;
    }
    unsigned va = LP_VT + n * 64 + 2 * i0, vb = n < 10 ? va + 1024 : LP_DM;
    unsigned vl = w == 3 ? LP_DM : 0;
    unsigned ac = LP_T1 + 4 * (4 * gq * 32 + n);
    unsigned a20 = LP_W2 + r0 * 64 + pp * 8, a21 = LP_W2 + r1 * 64 + pp * 8, abv = LP_VT + n * 64 + gq * 16;
    asm volatile("" : "+v"(b0a), "+v"(b0b), "+v"(b1a), "+v"(b1b), "+v"(va), "+v"(vb), "+v"(ac), "+v"(a20), "+v"(a21), "+v"(abv));
    if (w == 3) {
        LAS unsigned* z = (LAS unsigned*)(lds + LP_VT + (lane < 27 ? lane : 31) * 64 + 52);
        z[0] = 0; z[1] = 0; z[2] = 0;
        *(LAS unsigned*)(lds + LP_T1 + 4 * T_ZERO) = 0;
    }
    if (w == 1) *(LAS unsigned*)(lds + LP_T0 + 2 * T_ZERO) = 0;
    asm volatile("s_waitcnt vmcnt(0) lgkmcnt(0)" ::: "memory");
    __builtin_amdgcn_s_barrier();
    STAMP(2);
    {
        const u32x4 wo3 = *(const LAS u32x4*)(lds + LP_WO + w * 256 + 104 + 16 * gq);
        s16x8 bfr[16];
#pragma unroll
        for (int t = 0; t < 16; ++t) {
            const int ii = t >> 1, u = t & 1;
            const s16x4 lo = __builtin_amdgcn_ds_read_tr16_b64_v4i16((LAS s16x4*)(lds + (u ? b0b : b0a) + ii * 64));
            const s16x4 hi = __builtin_amdgcn_ds_read_tr16_b64_v4i16((LAS s16x4*)(lds + (u ? b1b : b1a) + ii * 64));
            bfr[t] = __builtin_shufflevector(lo, hi, 0, 1, 2, 3, 4, 5, 6, 7);
        }
        if (w == 3) {
            const unsigned o2 = *(const LAS unsigned*)(lds + LP_WO + 768 + 52 + (lane < 13 ? lane : 0) * 4);
            const unsigned o1 = *(const LAS unsigned short*)(lds + LP_WO + 768 + p * 2);
            *(LAS unsigned*)(lds + (lane < 13 ? LP_VT + 26 * 64 + lane * 4 : LP_DM)) = o2;
            *(LAS float*)(lds + LP_T1 + 4 * T_CONST) = bf_lo(o1);
        }
        float res[16];
#pragma unroll
        for (int t = 0; t < 16; ++t) res[t] = mfma16(as_frag(wo3), bfr[t], (f32x4){0.f, 0.f, 0.f, 0.f})[0];
#pragma unroll
        for (int k2 = 0; k2 < 4; ++k2) {
            const unsigned pa0 = pk_bf16(res[4 * k2], res[4 * k2 + 2]), pb0 = pk_bf16(res[4 * k2 + 1], res[4 * k2 + 3]);
            if (k2 < 3) { *(LAS unsigned*)(lds + va + 4 * k2) = pa0; *(LAS unsigned*)(lds + vb + 4 * k2) = pb0; }
            else { *(LAS unsigned*)(lds + (vl ? vl : va + 12)) = pa0; *(LAS unsigned*)(lds + (vl ? vl : vb + 12)) = pb0; }
        }
    }
    CBAR();
    sig_add(lds, L_F + 28);
    if (w > 1) return;
    STAMP(3);
    const __amdgpu_buffer_rsrc_t srdA = __builtin_amdgcn_make_buffer_rsrc((void*)(P.ws + WS_A), 0, A_BYTES, RSRC3);
    u32x4 o;
    if (w == 1) {
        float pv[16];
#pragma unroll
        for (int e = 0; e < 8; ++e) {
            const unsigned x = e < 4 ? ta[e] : tb[e - 4];
            pv[2 * e] = bf_lo(*(const LAS unsigned short*)(lds + LP_T0 + 2 * (x & 0xffff)));
            pv[2 * e + 1] = bf_lo(*(const LAS unsigned short*)(lds + LP_T0 + 2 * (x >> 16)));
        }
#pragma unroll
        for (int j = 0; j < 4; ++j) o[j] = pk_bf16(pv[4 * j] + pv[4 * j + 1], pv[4 * j + 2] + pv[4 * j + 3]);
    } else {
        s16x8 a2[2];
#pragma unroll
        for (int mt = 0; mt < 2; ++mt) {
            const s16x4 lo = __builtin_amdgcn_ds_read_tr16_b64_v4i16((LAS s16x4*)(lds + a20 + mt * 32));
            const s16x4 hi = __builtin_amdgcn_ds_read_tr16_b64_v4i16((LAS s16x4*)(lds + a21 + mt * 32));
            a2[mt] = __builtin_shufflevector(lo, hi, 0, 1, 2, 3, 4, 5, 6, 7);
        }
        unsigned pa[16];
#pragma unroll
        for (int e = 0; e < 8; ++e) {
            const unsigned x = e < 4 ? ta[e] : tb[e - 4];
            pa[2 * e] = LP_T1 + 4 * (x & 0xffff); pa[2 * e + 1] = LP_T1 + 4 * (x >> 16);
        }
#pragma unroll
        for (int e = 0; e < 16; ++e) asm volatile("" : "+v"(pa[e]));
        _Pragma("unroll 1") for (unsigned sp = 0; lds_ld(lds, L_F + 28) < 4 && sp < SPIN_CAP; ++sp) __builtin_amdgcn_s_sleep(0);
        CBAR();
        const u32x4 bf0 = *(const LAS u32x4*)(lds + abv), bf1 = *(const LAS u32x4*)(lds + abv + 1024);
        const f32x4 z4 = {0.f, 0.f, 0.f, 0.f};
        const f32x4 d00 = mfma16(a2[0], as_frag(bf0), z4), d01 = mfma16(a2[1], as_frag(bf0), z4);
        const f32x4 d10 = mfma16(a2[0], as_frag(bf1), z4), d11 = mfma16(a2[1], as_frag(bf1), z4);
#pragma unroll
        for (int e = 0; e < 4; ++e) {
            *(LAS float*)(lds + ac + e * 128) = d00[e]; *(LAS float*)(lds + ac + 2048 + e * 128) = d01[e];
            *(LAS float*)(lds + ac + 64 + e * 128) = d10[e]; *(LAS float*)(lds + ac + 2048 + 64 + e * 128) = d11[e];
        }
        STAMP(4);
        float pv[16];
#pragma unroll
        for (int e = 0; e < 16; ++e) pv[e] = *(const LAS float*)(lds + pa[e]);
#pragma unroll
        for (int j = 0; j < 4; ++j) o[j] = pk_bf16(pv[4 * j] + pv[4 * j + 1], pv[4 * j + 2] + pv[4 * j + 3]);
    }
    {
        const int fr = w == 0 ? (cs < 22 ? 2 * cs : 22 + cs) : 2 * cs + 1;
        __builtin_amdgcn_raw_buffer_store_b128(o, srdA, lane < nact ? (unsigned)(fr * 832 + (26 * chh + p) * 16) : 0x7ffffff0u, 0, AUX_SC1);
    }
    STAMP(5);
    WAIT_VM0();
    STAMP(6);
    if (w == 1) { sig_set(lds, L_F + 24); return; }
    const unsigned tagv = *(const LAS unsigned*)(lds + L_EP + (lane & 15) * 4);
    _Pragma("unroll 1") for (unsigned sp = 0; lds_ld(lds, L_F + 24) == 0 && sp < SPIN_CAP; ++sp) __builtin_amdgcn_s_sleep(0);
    if (lane < 16) __hip_atomic_store((gu32*)(P.ws + WS_FL) + lane * 32 + p, tagv + 1u, __ATOMIC_RELAXED, __HIP_MEMORY_SCOPE_AGENT);
}

constexpr unsigned A_PER = A_BYTES / 4;
DI void cin_role(const Params& P, lds8* lds, int cw, int lane, int g, int m, bool has_dnn, int pf_slot STAMP_ARG) {
    const int d = lane & 31, h = lane >> 5;
    const int b = 64 * g + 4 * m + cw;
    const unsigned ldsb = (unsigned)(size_t)lds;
    {
        const __amdgpu_buffer_rsrc_t srdT = __builtin_amdgcn_make_buffer_rsrc((void*)P.table, 0, NVOC * 64, RSRC3);
        const unsigned lb = ldsb + L_X0 + cw * 1664;
        WAIT_VM0();
        const int q = lane >> 2;
        const unsigned j0 = *(const LAS unsigned*)(lds + L_X0 + cw * 1664 + q * 4);
        const unsigned j1 = *(const LAS unsigned*)(lds + L_X0 + cw * 1664 + (q < 10 ? q + 16 : 25) * 4);
        unsigned o0 = j0 * 64 + (lane & 3) * 16, o1 = j1 * 64 + (lane & 3) * 16;
        asm volatile("" : "+v"(o0), "+v"(o1));
        STAMP(1);
        dma16(lb, o0, srdT, 0);
        if (lane < 40) dma16(lb + 1024, o1, srdT, 0);
    }
    const unsigned bo = P.bout[0];
    STAMP(2);
    __builtin_amdgcn_s_setprio(0);
    CBAR();
    __builtin_amdgcn_s_barrier();
    const unsigned epv = *(const LAS unsigned*)(lds + L_EP + g * 4);
    unsigned bov = bo;
    asm volatile("s_waitcnt vmcnt(0)" : "+v"(bov) :: "memory");
    STAMP(3);
    float x[26];
    {
        const lds8* xb = lds + L_X0 + cw * 1664 + h * 64 + d * 2;
        const lds8* xl = lds + L_X0 + cw * 1664 + (h ? 0 : 25 * 64) + d * 2;
#pragma unroll
        for (int a = 0; a < 13; ++a) {
            const unsigned lo = *(const LAS unsigned short*)(xb + 2 * a * 64);
            const unsigned hi = a < 12 ? *(const LAS unsigned short*)(xb + (2 * a + 1) * 64) : *(const LAS unsigned short*)xl;
            x[2 * a] = bf_lo(lo); x[2 * a + 1] = bf_lo(hi);
        }
    }
    u32x4 z[24];
#pragma unroll
    for (int s = 0; s < 22; ++s) {
        float fa[8], fb[8];
#pragma unroll
        for (int e = 0; e < 8; ++e) { const AB pe = slot_ab(8 * s + e); fa[e] = x[pe.a]; fb[e] = x[pe.b]; }
        float t0, t1, t2, t3, t4, t5;
        unsigned o0, o1, o2, o3;
        asm("v_mul_f32 %4, %10, %11\n\tv_mul_f32 %5, %12, %13\n\tv_mul_f32 %6, %14, %15\n\tv_mul_f32 %7, %16, %17\n\t"
            "v_cvt_pk_bf16_f32 %0, %4, %5\n\tv_mul_f32 %8, %18, %19\n\tv_mul_f32 %9, %20, %21\n\tv_cvt_pk_bf16_f32 %1, %6, %7\n\t"
            "v_mul_f32 %4, %22, %23\n\tv_mul_f32 %5, %24, %25\n\tv_cvt_pk_bf16_f32 %2, %8, %9\n\tv_cvt_pk_bf16_f32 %3, %4, %5"
            : "=&v"(o0), "=&v"(o1), "=&v"(o2), "=v"(o3), "=&v"(t0), "=&v"(t1), "=&v"(t2), "=&v"(t3), "=&v"(t4), "=&v"(t5)
            : "v"(fa[0]), "v"(fb[0]), "v"(fa[1]), "v"(fb[1]), "v"(fa[2]), "v"(fb[2]), "v"(fa[3]), "v"(fb[3]),
              "v"(fa[4]), "v"(fb[4]), "v"(fa[5]), "v"(fb[5]), "v"(fa[6]), "v"(fb[6]), "v"(fa[7]), "v"(fb[7]));
        z[s] = (u32x4){o0, o1, o2, o3};
    }
#pragma unroll
    for (int s = 22; s < 24; ++s) {
#pragma unroll
        for (int j = 0; j < 4; ++j) {
            const AB p0 = slot_ab(8 * s + 2 * j), p1 = slot_ab(8 * s + 2 * j + 1);
            const float a0 = p0.a < 26 ? x[p0.a] : (p0.a == 26 ? 1.f : 0.f);
            const float a1 = p1.a < 26 ? x[p1.a] : (p1.a == 26 ? 1.f : 0.f);
            z[s][j] = pk_bf16(a0, a1);
        }
    }
#pragma unroll
    for (int s = 0; s < 24; ++s) asm volatile("" : "+v"(z[s]));
    STAMP(4);
    const unsigned tag = epv + 1u;
    {
        const __amdgpu_buffer_rsrc_t srdA = __builtin_amdgcn_make_buffer_rsrc((void*)(P.ws + WS_A), 0, A_BYTES, RSRC3);
        if (cw == 0) {
            if (has_dnn) _Pragma("unroll 1") for (unsigned sp = 0; lds_ld(lds, L_F + 52) < 4 && sp < SPIN_CAP; ++sp) __builtin_amdgcn_s_sleep(1);
            const gu32* fl = (const gu32*)(P.ws + WS_FL) + g * 32 + (lane < 26 ? lane : 0);
            _Pragma("unroll 1") for (unsigned sp = 0; sp < SPIN_CAP; ++sp) { if (__all(ld_agent(fl) == tag)) break; __builtin_amdgcn_s_sleep(1); }
            sig_set(lds, L_F + 16);
            if (!has_dnn) dma4_sc1(ldsb + L_DUMMY, lane < 50 ? (unsigned)((((g & 1) * 3 + pf_slot) * 50 + lane) * 128) : 0x7fffff00u, srdA, 0);
        } else {
            _Pragma("unroll 1") for (unsigned sp = 0; lds_ld(lds, L_F + 16) == 0 && sp < SPIN_CAP; ++sp) __builtin_amdgcn_s_sleep(0);
        }
        CBAR();
        __builtin_amdgcn_s_setprio(CIN_PRIO);
        STAMP(5);
        {
            const unsigned l1 = ldsb + L_W + cw * 4096, vo = lane * 16;
            asm volatile("s_mov_b32 m0, %0\n\ts_nop 0\n\t"
                         "buffer_load_dwordx4 %1, %2, %3 offen sc1 lds\n\tbuffer_load_dwordx4 %1, %2, %3 offen offset:1024 sc1 lds\n\t"
                         "buffer_load_dwordx4 %1, %2, %3 offen offset:2048 sc1 lds\n\tbuffer_load_dwordx4 %1, %2, %3 offen offset:3072 sc1 lds"
                         :: "s"(l1), "v"(vo), "s"(srdA), "s"(cw * 4096) : "memory");
#pragma unroll
            for (int i = 0; i < 6; ++i) {
                const int q = 6 * cw + i;
                const unsigned go = q < 4 ? 16384 + 1024 * q : 19968 + 1024 * (q - 4), lo = q < 4 ? 16384 + 1024 * q : 53248 + 1024 * (q - 4);
                dma16_sc1(ldsb + L_W + lo, vo, srdA, go);
            }
        }
        wait_vm<6>();
        sig_add(lds, L_F + 20);
        _Pragma("unroll 1") for (unsigned sp = 0; lds_ld(lds, L_F + 20) < 4 && sp < SPIN_CAP; ++sp) __builtin_amdgcn_s_sleep(0);
        CBAR();
    }
    STAMP(6);
    const gu64* pp = (const gu64*)(P.ws + WS_PART) + ((g * 32 + (lane < 26 ? lane : 0)) * 64 + 4 * m + cw);
    unsigned long long pv = 0;
    f32x16 acc0, acc1;
#pragma unroll
    for (int i = 0; i < 16; ++i) { acc0[i] = 0.f; acc1[i] = 0.f; }
    {
        unsigned abo = L_W + (26 * h + d) * 16;
        asm volatile("" : "+v"(abo));
        const lds8* ab = lds + abo;
        constexpr int PF = 6;
        u32x4 af[46];
        unsigned cnt2 = 0;
        auto frag_of = [](int t) { return t < 24 ? t : t + 40; };
#pragma unroll
        for (int t = 0; t < PF; ++t) af[t] = *(const LAS u32x4*)(ab + frag_of(t) * 832);
        __builtin_amdgcn_sched_barrier(0);
#pragma unroll
        for (int t = 0; t < 46; ++t) {
            if (t == 9) { wait_vm<0>(); sig_add(lds, L_F + 44); }
            if (t + PF == 19 - 2) cnt2 = lds_ld(lds, L_F + 44);
            if (t + PF == 19) {
                if (cnt2 < 4) _Pragma("unroll 1") for (unsigned sp = 0; lds_ld(lds, L_F + 44) < 4 && sp < SPIN_CAP; ++sp) __builtin_amdgcn_s_sleep(0);
                CBAR();
            }
            if (t + PF < 46) af[t + PF] = *(const LAS u32x4*)(ab + frag_of(t + PF) * 832);
            if (t < 44) {
                if (t & 1) acc0 = mfma32(as_frag(af[t]), as_frag(z[t >> 1]), acc0);
                else acc1 = mfma32(as_frag(af[t]), as_frag(z[t >> 1]), acc1);
            } else acc1 = mfma32(as_frag(af[t]), as_frag(z[t - 22]), acc1);
            if (t == 30) pv = __hip_atomic_load(pp, __ATOMIC_RELAXED, __HIP_MEMORY_SCOPE_AGENT);
            __builtin_amdgcn_sched_barrier(0);
        }
    }
    float sa = 0.f;
#pragma unroll
    for (int i = 0; i < 12; ++i) sa = __builtin_fmaf(acc0[i], acc1[i], sa);
    const float sb = __builtin_fmaf(acc0[12], acc1[12], acc0[13] * acc1[13]);
    float tot = h == 0 ? sa + sb : sa;
    asm volatile("" : "+v"(tot));
    STAMP(7);
    _Pragma("unroll 1") for (unsigned sp = 0; sp < SPIN_CAP; ++sp) {
        if (__all((unsigned)(pv >> 32) == tag)) break;
        pv = __hip_atomic_load(pp, __ATOMIC_RELAXED, __HIP_MEMORY_SCOPE_AGENT);
    }
    STAMP(8);
    if (lane < 26) tot += __uint_as_float((unsigned)pv);
    tot = wave_sum_to_lane63(tot);
    if (lane == 63) {
        const float o = tot + bf_lo(bov);
        P.out[b] = (unsigned short)(pk_bf16(o, o) & 0xffffu);
    }
    if (m == 0 && cw == 0) {
        const gu64* ap = (const gu64*)(P.ws + WS_PART) + ((g * 32 + 26 + (lane < 3 ? lane : 0)) * 64);
        _Pragma("unroll 1") for (unsigned sp = 0; sp < SPIN_CAP; ++sp) {
            const unsigned long long av = __hip_atomic_load(ap, __ATOMIC_RELAXED, __HIP_MEMORY_SCOPE_AGENT);
            if (__all((unsigned)(av >> 32) == tag)) break;
        }
        if (lane == 0) __hip_atomic_store((gu32*)(P.ws + WS_EP) + g, tag, __ATOMIC_RELAXED, __HIP_MEMORY_SCOPE_AGENT);
    }
}

DI void decode_block(int bid, int& g, int& m, int& ntile, int& k) {
    const int x = bid & 7, j = bid >> 3;
    g = 2 * x + (j >> 4); m = j & 15;
    ntile = m < 13 ? m : -1; k = m - 13;
}

__global__ void __launch_bounds__(512, 2) xdfm_kernel(KArgs KA) {
    const Params& P = KA.P;
    __shared__ __attribute__((aligned(16))) unsigned char smem[L_END];
    lds8* lds = (lds8*)smem;
    STAMP(0);
    const int w = __builtin_amdgcn_readfirstlane(threadIdx.x >> 6), lane = threadIdx.x & 63;
    {
        unsigned long long pc; unsigned len;
        asm volatile("s_getpc_b64 %0\n.Lxdfm_pc:\n\ts_mov_b32 %1, (.Lxdfm_end-.Lxdfm_pc)&4294967295" : "=s"(pc), "=s"(len));
        const __amdgpu_buffer_rsrc_t srdP = __builtin_amdgcn_make_buffer_rsrc((void*)pc, 0, len, RSRC3);
        dma4((unsigned)(size_t)lds + L_DUMMY, lane == 0 ? ((blockIdx.x >> 3) * 8 + w) * 128 : 0x7fffff00u, srdP, 0);
    }
    asm volatile("" :: "s"(P.idx), "s"(P.table), "s"(P.wcin), "s"(P.wout), "s"(P.w1), "s"(P.b1), "s"(P.w2), "s"(P.bout), "s"(P.out), "s"(P.ws));
    int g, m, ntile, k;
    decode_block(blockIdx.x, g, m, ntile, k);
    const bool is_cin = w >= 4;
    if (__builtin_expect(is_cin, 1)) {
        __builtin_amdgcn_s_setprio(3);
        dma16((unsigned)(size_t)lds + L_X0 + (w - 4) * 1664, lane < 7 ? (64 * g + 4 * m + w - 4) * 104 + lane * 16 : 0x7fffff00u,
              __builtin_amdgcn_make_buffer_rsrc((void*)P.idx, 0, NBATCH * 26 * 4, RSRC3), 0);
        if (__builtin_expect(w == 7, 1)) dma4((unsigned)(size_t)lds + L_DUMMY, lane < 26 ? ((blockIdx.x >> 3) * 26 + lane) * 128 : 0x7fffff00u,
                         __builtin_amdgcn_make_buffer_rsrc((void*)P.wcin, 0, 3 * 17576 * 2, RSRC3), 0);
    }
    if (__builtin_expect(w == 0, 1)) {
        if (lane < 16) lds_st(lds, L_F + 4 * lane, 0);
        dma4_sc1((unsigned)(size_t)lds + L_EP, (lane & 15) * 4, __builtin_amdgcn_make_buffer_rsrc((void*)(P.ws + WS_EP), 0, 64, RSRC3), 0);
    }
    const int p = g * 3 + k;
    if (__builtin_expect(!is_cin && ntile >= 0, 1)) dnn_start(P, lds, w, lane, g, ntile);
    if (__builtin_expect(!is_cin && ntile < 0 && p < 26, 1)) prep_start(P, lds, w, lane, p);
    if (__builtin_expect(!is_cin && ntile >= 0, 1)) {
        dnn_role(P, lds, w, lane, g, ntile STAMP_PASS);
    } else if (is_cin) {
        cin_role(P, lds, w - 4, lane, g, m, ntile >= 0, k STAMP_PASS);
        __builtin_amdgcn_s_waitcnt(0x0F70);
    } else {
        if (p < 26) prep_role(P, lds, w, lane, p STAMP_PASS);
        else { asm volatile("s_waitcnt vmcnt(0) lgkmcnt(0)" ::: "memory"); __builtin_amdgcn_s_barrier(); }
        if (w == 0) {
            const unsigned ep = *(const LAS unsigned*)(lds + L_EP + g * 4);
            if (lane == 0) st_granule((gu64*)(P.ws + WS_PART) + ((g * 32 + 26 + k) * 64), ep + 1u, 0u);
        }
        __builtin_amdgcn_s_waitcnt(0x0F70);
    }
    STAMP(11);
    STAMP_FLUSH(P.ws, w, lane);
    asm volatile(".Lxdfm_end:");
}

extern "C" void kernel_launch(void* const* d_in, const int* in_sizes, int n_in,
                              void* d_out, int out_size, void* d_ws, size_t ws_size,
                              hipStream_t stream) {
    KArgs KA;
    KA.tab = make_tab();
    Params& P = KA.P;
    P.idx = (const int*)d_in[0]; P.table = (const unsigned short*)d_in[1]; P.wcin = (const unsigned short*)d_in[2];
    P.wout = (const unsigned short*)d_in[3]; P.w1 = (const unsigned short*)d_in[4]; P.b1 = (const unsigned short*)d_in[5];
    P.w2 = (const unsigned short*)d_in[6]; P.bout = (const unsigned short*)d_in[7];
    P.out = (unsigned short*)d_out; P.ws = (unsigned char*)d_ws;
    xdfm_kernel<<<256, 512, 0, stream>>>(KA);
}
```

```cpp
#include <hip/hip_runtime.h>
#include <stdint.h>

#define DI __device__ __forceinline__
#define GAS __attribute__((address_space(1)))
#define LAS __attribute__((address_space(3)))

typedef short    s16x4 __attribute__((ext_vector_type(4)));
typedef short    s16x8 __attribute__((ext_vector_type(8)));
typedef float    f32x2 __attribute__((ext_vector_type(2)));
typedef float    f32x4 __attribute__((ext_vector_type(4)));
typedef float    f32x16 __attribute__((ext_vector_type(16)));
typedef unsigned u32x2 __attribute__((ext_vector_type(2)));
typedef unsigned u32x4 __attribute__((ext_vector_type(4)));
typedef __bf16   bf16x2 __attribute__((ext_vector_type(2)));
typedef GAS unsigned gu32;
typedef GAS unsigned long long gu64;
typedef LAS unsigned char lds8;

#define MAPPING 1

constexpr int DIN = 832, NBATCH = 1024, NVOC = 100000;
#define A_AFTER_X 1
#define W_EARLY 1
#define A_PF 1
#define CODE_PF 0
#define WC_PF 1
#define POLL_STEP 10
#define CIN_PRIO 2
#define WO_LAST 0
#define PKMUL 0
#define A_DMA 1
constexpr unsigned RSRC3 = 0x00020000u;
constexpr int AUX_SC1 = 16;
constexpr unsigned SPIN_CAP = 1u << 20;

constexpr unsigned WS_EP = 0;
constexpr unsigned WS_FL = 4096;
constexpr unsigned WS_PART = 8192;
constexpr unsigned WS_A = 8192 + 16 * 32 * 64 * 8;
constexpr unsigned A_BYTES = 46 * 832;

#define XD 6
constexpr unsigned L_W = 0;
constexpr unsigned L_A = 832 * 128;
constexpr unsigned L_X0 = L_A + 4 * XD * 2048;
constexpr unsigned L_F = L_X0 + 4 * 1664;
constexpr unsigned L_DUMMY = L_F + 256;
constexpr unsigned L_EP = L_DUMMY + 256;
constexpr unsigned L_BI = L_EP + 256;
constexpr unsigned L_W2 = L_BI + 256;
constexpr unsigned L_END = L_W2 + 256;
static_assert(4 * XD * 2048 >= A_BYTES && L_END <= 163840, "LDS map");
constexpr unsigned LP_VT = 0;
constexpr unsigned LP_T0 = 2048;
constexpr unsigned LP_T1 = 2048 + 2304;

struct AB { int a, b; };
constexpr AB slot_ab(int t) {
    if (t < 156) { int a = 2 * (t / 12); return {a, (a + t % 12) % 26}; }
    if (t < 170) { int u = t - 156; int a = 2 * (u / 2); return {a, (a + 12 + (u & 1)) % 26}; }
    if (t < 176) { int a = 2 * (7 + t - 170); return {a, (a + 12) % 26}; }
    if (t < 189) return {2 * (t - 176), 26};
    if (t == 189) return {26, 26};
    return {27, 27};
}
constexpr unsigned T_ZERO = 1024, T_CONST = 1025;
struct PrepTab { unsigned v[2][24 * 2 * 8]; };
constexpr PrepTab make_tab() {
    PrepTab tb{};
    for (int m = 0; m < 2; ++m) for (int s = 0; s < 24; ++s) for (int h = 0; h < 2; ++h) for (int e = 0; e < 8; ++e) {
        const int st = m ? 32 : 26;
        int t = 8 * s + e; AB ab = slot_ab(t);
        unsigned o1 = T_ZERO, o2 = T_ZERO;
        if (ab.a == 27) {}
        else if (ab.a == 26) { if (h == 0 && m) o1 = T_CONST; }
        else if (ab.b == 26) { if (m) o1 = ((ab.a + h) % 26) * st + 26; }
        else {
            int u = (ab.a + h) % 26, v = (ab.b + h) % 26;
            bool dup = (t == 169 && h == 1);
            if (dup) {}
            else if (u == v) o1 = u * st + u;
            else { o1 = u * st + v; o2 = v * st + u; }
        }
        tb.v[m][(s * 2 + h) * 8 + e] = o1 | (o2 << 16);
    }
    return tb;
}
static_assert(slot_ab(169).a == 12 && slot_ab(169).b == 25, "dup slot");

struct Params {
    const int* idx; const unsigned short* table; const unsigned short* wcin; const unsigned short* wout;
    const unsigned short* w1; const unsigned short* b1; const unsigned short* w2; const unsigned short* bout;
    unsigned short* out; unsigned char* ws;
};
struct KArgs { Params P; PrepTab tab; };

DI unsigned sel(bool c, unsigned a, unsigned b) { unsigned r = c ? a : b; return r; }
DI float bf_lo(unsigned v) { return __uint_as_float(v << 16); }
DI float bf_hi(unsigned v) { return __uint_as_float(v & 0xffff0000u); }
DI unsigned pk_bf16(float lo, float hi) { f32x2 v = {lo, hi}; return __builtin_bit_cast(unsigned, __builtin_convertvector(v, bf16x2)); }
DI f32x4 mfma16(s16x8 a, s16x8 b, f32x4 c) { return __builtin_amdgcn_mfma_f32_16x16x32_bf16(a, b, c, 0, 0, 0); }
DI f32x16 mfma32(s16x8 a, s16x8 b, f32x16 c) { return __builtin_amdgcn_mfma_f32_32x32x16_bf16(a, b, c, 0, 0, 0); }
DI s16x8 as_frag(u32x4 v) { return __builtin_bit_cast(s16x8, v); }
template <int CTRL, int ROWMASK> DI float dpp_add(float v) {
    return v + __int_as_float(__builtin_amdgcn_update_dpp(0, __float_as_int(v), CTRL, ROWMASK, 0xf, true));
}
DI float wave_sum_to_lane63(float v) {
    v = dpp_add<0x111, 0xf>(v); v = dpp_add<0x112, 0xf>(v); v = dpp_add<0x114, 0xf>(v); v = dpp_add<0x118, 0xf>(v);
    v = dpp_add<0x142, 0xa>(v); v = dpp_add<0x143, 0xc>(v);
    return v;
}
DI float sum_lane_groups(float v) {
    auto a = __builtin_amdgcn_permlane16_swap(__float_as_uint(v), __float_as_uint(v), false, false);
    v = __uint_as_float(a[0]) + __uint_as_float(a[1]);
    auto b = __builtin_amdgcn_permlane32_swap(__float_as_uint(v), __float_as_uint(v), false, false);
    return __uint_as_float(b[0]) + __uint_as_float(b[1]);
}
DI float gelu_tanh(float x) {
    const float k1 = -2.0f * 0.7978845608028654f * 1.4426950408889634f;
    const float k2 = k1 * 0.044715f;
    float arg = x * __builtin_fmaf(x * x, k2, k1);
    float e = __builtin_amdgcn_exp2f(arg);
    return x * __builtin_amdgcn_rcpf(1.0f + e);
}
DI void dma16(unsigned lds_addr, unsigned voff, __amdgpu_buffer_rsrc_t srd, unsigned soff) {
    asm volatile("s_mov_b32 m0, %0\n\ts_nop 0\n\tbuffer_load_dwordx4 %1, %2, %3 offen lds"
                 :: "s"(lds_addr), "v"(voff), "s"(srd), "s"(soff) : "memory");
}
DI void dma16_sc1(unsigned lds_addr, unsigned voff, __amdgpu_buffer_rsrc_t srd, unsigned soff) {
    asm volatile("s_mov_b32 m0, %0\n\ts_nop 0\n\tbuffer_load_dwordx4 %1, %2, %3 offen sc1 lds"
                 :: "s"(lds_addr), "v"(voff), "s"(srd), "s"(soff) : "memory");
}
DI void dma4_sc1(unsigned lds_addr, unsigned voff, __amdgpu_buffer_rsrc_t srd, unsigned soff) {
    asm volatile("s_mov_b32 m0, %0\n\ts_nop 0\n\tbuffer_load_dword %1, %2, %3 offen sc1 lds"
                 :: "s"(lds_addr), "v"(voff), "s"(srd), "s"(soff) : "memory");
}
DI void dma4(unsigned lds_addr, unsigned voff, __amdgpu_buffer_rsrc_t srd, unsigned soff) {
    asm volatile("s_mov_b32 m0, %0\n\ts_nop 0\n\tbuffer_load_dword %1, %2, %3 offen lds"
                 :: "s"(lds_addr), "v"(voff), "s"(srd), "s"(soff) : "memory");
}
DI void st_granule(gu64* g, unsigned tag, unsigned val) {
    __hip_atomic_store(g, ((unsigned long long)tag << 32) | val, __ATOMIC_RELAXED, __HIP_MEMORY_SCOPE_AGENT);
}
DI unsigned ld_agent(const gu32* p) { return __hip_atomic_load(p, __ATOMIC_RELAXED, __HIP_MEMORY_SCOPE_AGENT); }
DI unsigned lds_ld(lds8* l, unsigned off) { return __hip_atomic_load((LAS unsigned*)(l + off), __ATOMIC_RELAXED, __HIP_MEMORY_SCOPE_WORKGROUP); }
DI void lds_st(lds8* l, unsigned off, unsigned v) { __hip_atomic_store((LAS unsigned*)(l + off), v, __ATOMIC_RELAXED, __HIP_MEMORY_SCOPE_WORKGROUP); }
DI void lds_add(lds8* l, unsigned off, unsigned v) { __hip_atomic_fetch_add((LAS unsigned*)(l + off), v, __ATOMIC_RELAXED, __HIP_MEMORY_SCOPE_WORKGROUP); }
DI void sig_add(lds8* l, unsigned off) {
    asm volatile("s_mov_b64 exec, 1\n\tds_add_u32 %0, %1\n\ts_mov_b64 exec, -1" :: "v"((unsigned)(size_t)l + off), "v"(1u) : "memory");
}
DI void sig_set(lds8* l, unsigned off) {
    asm volatile("s_mov_b64 exec, 1\n\tds_write_b32 %0, %1\n\ts_mov_b64 exec, -1" :: "v"((unsigned)(size_t)l + off), "v"(1u) : "memory");
}
#define WAIT_LGKM() asm volatile("s_waitcnt lgkmcnt(0)" ::: "memory")
#define WAIT_VM0() asm volatile("s_waitcnt vmcnt(0)" ::: "memory")
#define CBAR() asm volatile("" ::: "memory")
#define STAMP(i)
#define STAMP_ARG
#define STAMP_PASS
#define STAMP_FLUSH(ws, w, lane)

template <int K> DI unsigned quad_bcast(unsigned v) { return __builtin_amdgcn_update_dpp(0u, v, K * 0x55, 0xf, 0xf, true); }
template <int N> DI void wait_vm() { asm volatile("s_waitcnt vmcnt(%0)" :: "i"(N) : "memory"); }
DI void dma_w4(unsigned lds_addr, unsigned voff, __amdgpu_buffer_rsrc_t srd, unsigned soff) {
    unsigned t;
    asm volatile("s_mov_b32 m0, %1\n\ts_add_u32 %0, %4, 12288\n\tbuffer_load_dwordx4 %2, %3, %4 offen lds\n\t"
                 "buffer_load_dwordx4 %2, %3, %0 offen offset:1024 lds\n\ts_add_u32 %0, %0, 12288\n\t"
                 "buffer_load_dwordx4 %2, %3, %0 offen offset:2048 lds\n\ts_add_u32 %0, %0, 12288\n\t"
                 "buffer_load_dwordx4 %2, %3, %0 offen offset:3072 lds"
                 : "=&s"(t) : "s"(lds_addr), "v"(voff), "s"(srd), "s"(soff) : "memory", "scc");
}
DI void dnn_start(const Params& P, lds8* lds, int w, int lane, int g, int ntile) {
    const int mh = w & 1, kh = w >> 1;
    const unsigned ldsb = (unsigned)(size_t)lds;
    const unsigned ring = ldsb + L_A + w * (XD * 2048);
    const __amdgpu_buffer_rsrc_t srdI = __builtin_amdgcn_make_buffer_rsrc((void*)P.idx, 0, NBATCH * 26 * 4, RSRC3);
    const unsigned vo = ((64 * g + 32 * mh + (lane >> 2)) * 26 + 13 * kh + 4 * (lane & 3)) * 4;
    dma16(ring, vo, srdI, 0);
    dma16(ring + 1024, vo, srdI, 1664);
    if (__builtin_expect(w == 1, 1)) dma4(ldsb + L_BI, ntile * 128 + lane * 4, __builtin_amdgcn_make_buffer_rsrc((void*)P.b1, 0, DIN * 2, RSRC3), 0);
    if (__builtin_expect(w == 2, 1)) dma4(ldsb + L_W2, ntile * 128 + lane * 4, __builtin_amdgcn_make_buffer_rsrc((void*)P.w2, 0, DIN * 2, RSRC3), 0);
    const __amdgpu_buffer_rsrc_t srdW = __builtin_amdgcn_make_buffer_rsrc((void*)P.w1, 0, DIN * DIN * 2, RSRC3);
    const unsigned vw = (lane >> 3) * 1664 + (((lane & 7) ^ (((lane >> 4) & 1) << 2)) << 4);
    const unsigned rowA = 416 * kh + 208 * mh + 104 * (g & 1);
    dma_w4(ldsb + L_W + rowA * 128, vw, srdW, rowA * 1664 + ntile * 128);
}
DI void dnn_role(const Params& P, lds8* lds, int w, int lane, int g, int ntile STAMP_ARG) {
    const int mh = w & 1, kh = w >> 1;
    const int h = lane >> 5;
    const unsigned ldsb = (unsigned)(size_t)lds;
    const unsigned ring = ldsb + L_A + w * (XD * 2048);
    const __amdgpu_buffer_rsrc_t srdW = __builtin_amdgcn_make_buffer_rsrc((void*)P.w1, 0, DIN * DIN * 2, RSRC3);
    const unsigned vw = (lane >> 3) * 1664 + (((lane & 7) ^ (((lane >> 4) & 1) << 2)) << 4);
    const unsigned rowA = 416 * kh + 208 * mh + 104 * (g & 1), rowB = 416 * kh + 208 * mh + 104 * ((g & 1) ^ 1);
    auto wdma = [&](unsigned row, int a) {
        const unsigned sb = row * 1664 + ntile * 128, lb = ldsb + L_W + row * 128;
        if (a < 3) dma_w4(lb + a * 4096, vw, srdW, sb + a * 4 * 13312);
        else dma16(lb + 12288, vw, srdW, sb + 12 * 13312);
    };
    STAMP(1);
    wait_vm<4>();
    WAIT_LGKM();
    __builtin_amdgcn_s_barrier();
    STAMP(2);
    const u32x4 i0 = *(const LAS u32x4*)(lds + L_A + w * (XD * 2048) + lane * 16);
    const u32x4 i1 = *(const LAS u32x4*)(lds + L_A + w * (XD * 2048) + 1024 + lane * 16);
    const unsigned ep = *(const LAS unsigned*)(lds + L_EP + g * 4);
#pragma unroll
    for (int a = 1; a < 4; ++a) wdma(rowA, a);
#pragma unroll
    for (int a = 0; a < 4; ++a) wdma(rowB, a);
    const __amdgpu_buffer_rsrc_t srdT = __builtin_amdgcn_make_buffer_rsrc((void*)P.table, 0, NVOC * 64, RSRC3);
    const unsigned c16 = (((lane & 3) ^ ((lane >> 4) & 3)) << 4);
    u32x4 t0 = i0 << 6, t1 = i1 << 6;
    asm volatile("" : "+v"(t0), "+v"(t1));
    auto xload = [&](int s) {
        const unsigned a0 = t0[s & 3], a1 = t1[s & 3];
        const unsigned o0 = (s < 4 ? quad_bcast<0>(a0) : s < 8 ? quad_bcast<1>(a0) : s < 12 ? quad_bcast<2>(a0) : quad_bcast<3>(a0)) + c16;
        const unsigned o1 = (s < 4 ? quad_bcast<0>(a1) : s < 8 ? quad_bcast<1>(a1) : s < 12 ? quad_bcast<2>(a1) : quad_bcast<3>(a1)) + c16;
        dma16(ring + (s % XD) * 2048, o0, srdT, 0);
        dma16(ring + (s % XD) * 2048 + 1024, o1, srdT, 0);
    };
#pragma unroll
    for (int s = 0; s < XD; ++s) xload(s);
    STAMP(3);
    wait_vm<2 * XD>();
    sig_add(lds, L_F + 32 + 4 * kh);
    _Pragma("unroll 1") for (unsigned sp = 0; lds_ld(lds, L_F + 32 + 4 * kh) < 2 && sp < SPIN_CAP; ++sp) __builtin_amdgcn_s_sleep(0);
    CBAR();
    STAMP(4);
    f32x16 acc[2];
    u32x2 bia[4], wv[4];
#pragma unroll
    for (int r4 = 0; r4 < 4; ++r4) {
        bia[r4] = *(const LAS u32x2*)(lds + L_BI + kh * 64 + h * 8 + r4 * 16);
        wv[r4] = *(const LAS u32x2*)(lds + L_W2 + kh * 64 + h * 8 + r4 * 16);
    }
#pragma unroll
    for (int r4 = 0; r4 < 4; ++r4) {
        acc[0][4 * r4] = bf_lo(bia[r4][0]); acc[0][4 * r4 + 1] = bf_hi(bia[r4][0]); acc[0][4 * r4 + 2] = bf_lo(bia[r4][1]); acc[0][4 * r4 + 3] = bf_hi(bia[r4][1]);
        acc[1][4 * r4] = 0.f; acc[1][4 * r4 + 1] = 0.f; acc[1][4 * r4 + 2] = 0.f; acc[1][4 * r4 + 3] = 0.f;
    }
    {
        const int blk = (lane >> 4) & 1, q = (lane & 15) >> 2, pp = lane & 3, n = lane & 31;
        unsigned ad[2];
#pragma unroll
        for (int j = 0; j < 2; ++j) {
            const unsigned c = 4 * (j ^ kh) + 2 * blk + (pp >> 1);
            ad[j] = L_W + (416 * kh + 8 * h + q) * 128 + ((c ^ (((q >> 1) & 1) << 2)) << 4) + (pp & 1) * 8;
        }
        const unsigned xb = L_A + w * (XD * 2048) + n * 64;
        const unsigned xa0 = xb + ((h ^ ((n >> 2) & 3)) << 4), xa1 = xb + (((2 + h) ^ ((n >> 2) & 3)) << 4);
        u32x4 xf[3][2];
        s16x8 wf[2][4];
        auto xread = [&](int s) {
            xf[s % 3][0] = *(const LAS u32x4*)(lds + xa0 + (s % XD) * 2048);
            xf[s % 3][1] = *(const LAS u32x4*)(lds + xa1 + (s % XD) * 2048);
        };
        auto wread = [&](int s) {
#pragma unroll
            for (int a = 0; a < 2; ++a)
#pragma unroll
                for (int j = 0; j < 2; ++j) {
                    const s16x4 lo = __builtin_amdgcn_ds_read_tr16_b64_v4i16((LAS s16x4*)(lds + ad[j] + s * 4096 + a * 2048));
                    const s16x4 hi = __builtin_amdgcn_ds_read_tr16_b64_v4i16((LAS s16x4*)(lds + ad[j] + s * 4096 + a * 2048 + 512));
                    wf[s & 1][2 * a + j] = __builtin_shufflevector(lo, hi, 0, 1, 2, 3, 4, 5, 6, 7);
                }
        };
        wread(0);
        wait_vm<2 * (XD - 1)>();
        xread(0);
        wait_vm<2 * (XD - 2)>();
        xread(1);
        __builtin_amdgcn_sched_barrier(0);
#pragma unroll
        for (int s = 0; s < 13; ++s) {
            if (s + 1 < 13) wread(s + 1);
#pragma unroll
            for (int a = 0; a < 2; ++a)
#pragma unroll
                for (int j = 0; j < 2; ++j) acc[j] = mfma32(wf[s & 1][2 * a + j], as_frag(xf[s % 3][a]), acc[j]);
            if (s + 1 < 13) {
#pragma unroll
                for (int j = 0; j < 4; ++j) { __builtin_amdgcn_sched_group_barrier(0x100, 2, 0); __builtin_amdgcn_sched_group_barrier(0x8, 1, 0); }
            }
            __builtin_amdgcn_sched_barrier(0);
            if (s + XD < 13) xload(s + XD);
            if (s + 2 < 13) {
                constexpr int LASTI = 12;
                const int last = s + XD < LASTI ? s + XD : LASTI;
                const int younger = last - (s + 2);
                if (younger >= 4) wait_vm<8>(); else if (younger == 3) wait_vm<6>(); else if (younger == 2) wait_vm<4>(); else if (younger == 1) wait_vm<2>(); else wait_vm<0>();
                if (s + 2 == 12) sig_add(lds, L_F + 52);
                xread(s + 2);
            }
            __builtin_amdgcn_sched_barrier(0);
        }
    }
    STAMP(6);
    {
        _Pragma("unroll 1") for (unsigned sp = 0; lds_ld(lds, L_F + 52) < 4 && sp < SPIN_CAP; ++sp) __builtin_amdgcn_s_sleep(0);
        CBAR();
        lds8* xw = lds + L_W + kh * 53248 + (5 + mh) * 4096 + lane * 16;
#pragma unroll
        for (int r4 = 0; r4 < 4; ++r4) *(LAS f32x4*)(xw + r4 * 1024) = (f32x4){acc[1][4 * r4], acc[1][4 * r4 + 1], acc[1][4 * r4 + 2], acc[1][4 * r4 + 3]};
        CBAR();
        sig_set(lds, L_F + 4 * w);
        _Pragma("unroll 1") for (unsigned sp = 0; lds_ld(lds, L_F + 4 * (w ^ 2)) == 0 && sp < SPIN_CAP; ++sp) __builtin_amdgcn_s_sleep(0);
        CBAR();
        const lds8* xr = lds + L_W + (kh ^ 1) * 53248 + (5 + mh) * 4096 + lane * 16;
#pragma unroll
        for (int r4 = 0; r4 < 4; ++r4) {
            const f32x4 o = *(const LAS f32x4*)(xr + r4 * 1024);
            acc[0][4 * r4] += o[0]; acc[0][4 * r4 + 1] += o[1]; acc[0][4 * r4 + 2] += o[2]; acc[0][4 * r4 + 3] += o[3];
        }
    }
    STAMP(7);
    float s0 = 0.f, s1 = 0.f;
#pragma unroll
    for (int r4 = 0; r4 < 4; ++r4) {
        s0 = __builtin_fmaf(gelu_tanh(acc[0][4 * r4]), bf_lo(wv[r4][0]), s0);
        s1 = __builtin_fmaf(gelu_tanh(acc[0][4 * r4 + 1]), bf_hi(wv[r4][0]), s1);
        s0 = __builtin_fmaf(gelu_tanh(acc[0][4 * r4 + 2]), bf_lo(wv[r4][1]), s0);
        s1 = __builtin_fmaf(gelu_tanh(acc[0][4 * r4 + 3]), bf_hi(wv[r4][1]), s1);
    }
    s0 += s1;
    const auto sw = __builtin_amdgcn_permlane32_swap(__float_as_uint(s0), __float_as_uint(s0), false, false);
    const float part = __uint_as_float(sw[0]) + __uint_as_float(sw[1]);
    STAMP(8);
    if (lane < 32) st_granule((gu64*)(P.ws + WS_PART) + ((g * 32 + 2 * ntile + kh) * 64 + 32 * mh + lane), ep + 1u, __float_as_uint(part));
}

constexpr unsigned LP_W2 = 12288;
constexpr unsigned LP_WO = 14336;
constexpr unsigned LP_DM = 15360;
constexpr unsigned LP_W3 = 16384;
static_assert(LP_T1 + 4 * 1026 <= LP_W2, "prep scratch");
constexpr unsigned w3_slot(int j) { return j * 1280 + (j & 1) * 64 + ((j >> 2) & 1) * 128; }
DI void dma16_add(unsigned lds_base, unsigned lds_off, unsigned voff, __amdgpu_buffer_rsrc_t srd, unsigned sbase, unsigned soff) {
    unsigned t;
    asm volatile("s_add_u32 m0, %1, %2\n\ts_add_u32 %0, %5, %6\n\tbuffer_load_dwordx4 %3, %4, %0 offen lds"
                 : "=&s"(t) : "s"(lds_base), "i"(lds_off), "v"(voff), "s"(srd), "s"(sbase), "i"(soff) : "memory", "scc");
}
DI void prep_start(const Params& P, lds8* lds, int w, int lane, int p) {
    const unsigned ldsb = (unsigned)(size_t)lds;
    const __amdgpu_buffer_rsrc_t srdC = __builtin_amdgcn_make_buffer_rsrc((void*)P.wcin, 0, 3 * 17576 * 2, RSRC3);
    const int i0 = w == 0 ? 0 : w == 1 ? 6 : w == 2 ? 14 : 20;
    dma4(ldsb + LP_WO + w * 256, lane * 4, __builtin_amdgcn_make_buffer_rsrc((void*)P.wout, 0, 78 * 2, RSRC3), 0);
    {
        const unsigned hp = lane >> 5;
        const unsigned vo = hp * 1352 + ((lane >> 2) & 7) * 52 + (((lane & 3) ^ (hp << 1)) << 4);
        const unsigned lb = ldsb + LP_W3 + w * 16640, sb = 52 * 1352 + 52 * i0;
        dma16_add(lb, w3_slot(0), vo, srdC, sb, 0 * 2704);  dma16_add(lb, w3_slot(1), vo, srdC, sb, 1 * 2704);
        dma16_add(lb, w3_slot(2), vo, srdC, sb, 2 * 2704);  dma16_add(lb, w3_slot(3), vo, srdC, sb, 3 * 2704);
        dma16_add(lb, w3_slot(4), vo, srdC, sb, 4 * 2704);  dma16_add(lb, w3_slot(5), vo, srdC, sb, 5 * 2704);
        dma16_add(lb, w3_slot(6), vo, srdC, sb, 6 * 2704);  dma16_add(lb, w3_slot(7), vo, srdC, sb, 7 * 2704);
        dma16_add(lb, w3_slot(8), vo, srdC, sb, 8 * 2704);  dma16_add(lb, w3_slot(9), vo, srdC, sb, 9 * 2704);
        dma16_add(lb, w3_slot(10), vo, srdC, sb, 10 * 2704); dma16_add(lb, w3_slot(11), vo, srdC, sb, 11 * 2704);
        dma16_add(lb, w3_slot(12), vo, srdC, sb, 12 * 2704);
    }
    if (__builtin_expect(w < 2, 1)) {
        const unsigned vo = w == 0 ? 35152 + (lane >> 2) * 1352 + p * 52 + (lane & 3) * 16 : p * 1352 + lane * 16;
        const unsigned lb = ldsb + (w == 0 ? LP_W2 : LP_T0);
        dma16(lb, vo, srdC, 0);
        dma16(lb + 1024, vo, srdC, w == 0 ? 16 * 1352 : 1024);
    }
}
DI void prep_role(const Params& P, lds8* lds, int w, int lane, int p STAMP_ARG) {
    const int n = lane & 15, gq = lane >> 4;
    const int i0 = w == 0 ? 0 : w == 1 ? 6 : w == 2 ? 14 : 20;
    const int nact = w == 0 ? 48 : 44;
    const int ch = lane < nact ? lane : 0;
    const int cs = ch >> 1, chh = ch & 1;
    const GAS u32x4* tp = (const GAS u32x4*)((const GAS unsigned char*)(size_t)__builtin_amdgcn_kernarg_segment_ptr() + sizeof(Params) + ((w == 0 ? 384 : 0) + (cs * 2 + chh) * 8) * 4);
    const u32x4 ta = tp[0], tb = tp[1];
    STAMP(1);
    const int q = (lane & 15) >> 2, pp = lane & 3;
    int r0 = 8 * gq + q, r1 = r0 + 4;
    r0 = r0 < 26 ? r0 : 25; r1 = r1 < 26 ? r1 : 25;
    unsigned b0a, b0b, b1a, b1b;
    {
        const unsigned wb = LP_W3 + w * 16640 + pp * 8;
        const unsigned j0 = r0 >> 1, j1 = r1 >> 1;
        const unsigned s0 = wb + j0 * 1280 + (j0 & 1) * 64 + ((j0 >> 2) & 1) * 128 + (r0 & 1) * 512;
        const unsigned s1 = wb + j1 * 1280 + (j1 & 1) * 64 + ((j1 >> 2) & 1) * 128 + (r1 & 1) * 512;
        b0a = s0 + (r0 & 1) * 32; b0b = s0 + 32 - (r0 & 1) * 32; b1a = s1 + (r1 & 1) * 32; b1b = s1 + 32 - (r1 & 1) * 32;
    }
    unsigned va = LP_VT + n * 64 + 2 * i0, vb = n < 10 ? va + 1024 : LP_DM;
    unsigned vl = w == 3 ? LP_DM : 0;
    unsigned ac = LP_T1 + 4 * (4 * gq * 32 + n);
    unsigned a20 = LP_W2 + r0 * 64 + pp * 8, a21 = LP_W2 + r1 * 64 + pp * 8, abv = LP_VT + n * 64 + gq * 16;
    asm volatile("" : "+v"(b0a), "+v"(b0b), "+v"(b1a), "+v"(b1b), "+v"(va), "+v"(vb), "+v"(ac), "+v"(a20), "+v"(a21), "+v"(abv));
    if (w == 3) {
        LAS unsigned* z = (LAS unsigned*)(lds + LP_VT + (lane < 27 ? lane : 31) * 64 + 52);
        z[0] = 0; z[1] = 0; z[2] = 0;
        *(LAS unsigned*)(lds + LP_T1 + 4 * T_ZERO) = 0;
    }
    if (w == 1) *(LAS unsigned*)(lds + LP_T0 + 2 * T_ZERO) = 0;
    asm volatile("s_waitcnt vmcnt(0) lgkmcnt(0)" ::: "memory");
    __builtin_amdgcn_s_barrier();
    STAMP(2);
    {
        const u32x4 wo3 = *(const LAS u32x4*)(lds + LP_WO + w * 256 + 104 + 16 * gq);
        s16x8 bfr[16];
#pragma unroll
        for (int t = 0; t < 16; ++t) {
            const int ii = t >> 1, u = t & 1;
            const s16x4 lo = __builtin_amdgcn_ds_read_tr16_b64_v4i16((LAS s16x4*)(lds + (u ? b0b : b0a) + ii * 64));
            const s16x4 hi = __builtin_amdgcn_ds_read_tr16_b64_v4i16((LAS s16x4*)(lds + (u ? b1b : b1a) + ii * 64));
            bfr[t] = __builtin_shufflevector(lo, hi, 0, 1, 2, 3, 4, 5, 6, 7);
        }
        if (w == 3) {
            const unsigned o2 = *(const LAS unsigned*)(lds + LP_WO + 768 + 52 + (lane < 13 ? lane : 0) * 4);
            const unsigned o1 = *(const LAS unsigned short*)(lds + LP_WO + 768 + p * 2);
            *(LAS unsigned*)(lds + (lane < 13 ? LP_VT + 26 * 64 + lane * 4 : LP_DM)) = o2;
            *(LAS float*)(lds + LP_T1 + 4 * T_CONST) = bf_lo(o1);
        }
        float res[16];
#pragma unroll
        for (int t = 0; t < 16; ++t) res[t] = mfma16(as_frag(wo3), bfr[t], (f32x4){0.f, 0.f, 0.f, 0.f})[0];
#pragma unroll
        for (int k2 = 0; k2 < 4; ++k2) {
            const unsigned pa0 = pk_bf16(res[4 * k2], res[4 * k2 + 2]), pb0 = pk_bf16(res[4 * k2 + 1], res[4 * k2 + 3]);
            if (k2 < 3) { *(LAS unsigned*)(lds + va + 4 * k2) = pa0; *(LAS unsigned*)(lds + vb + 4 * k2) = pb0; }
            else { *(LAS unsigned*)(lds + (vl ? vl : va + 12)) = pa0; *(LAS unsigned*)(lds + (vl ? vl : vb + 12)) = pb0; }
        }
    }
    CBAR();
    sig_add(lds, L_F + 28);
    if (w > 1) return;
    STAMP(3);
    const __amdgpu_buffer_rsrc_t srdA = __builtin_amdgcn_make_buffer_rsrc((void*)(P.ws + WS_A), 0, A_BYTES, RSRC3);
    u32x4 o;
    if (w == 1) {
        float pv[16];
#pragma unroll
        for (int e = 0; e < 8; ++e) {
            const unsigned x = e < 4 ? ta[e] : tb[e - 4];
            pv[2 * e] = bf_lo(*(const LAS unsigned short*)(lds + LP_T0 + 2 * (x & 0xffff)));
            pv[2 * e + 1] = bf_lo(*(const LAS unsigned short*)(lds + LP_T0 + 2 * (x >> 16)));
        }
#pragma unroll
        for (int j = 0; j < 4; ++j) o[j] = pk_bf16(pv[4 * j] + pv[4 * j + 1], pv[4 * j + 2] + pv[4 * j + 3]);
    } else {
        s16x8 a2[2];
#pragma unroll
        for (int mt = 0; mt < 2; ++mt) {
            const s16x4 lo = __builtin_amdgcn_ds_read_tr16_b64_v4i16((LAS s16x4*)(lds + a20 + mt * 32));
            const s16x4 hi = __builtin_amdgcn_ds_read_tr16_b64_v4i16((LAS s16x4*)(lds + a21 + mt * 32));
            a2[mt] = __builtin_shufflevector(lo, hi, 0, 1, 2, 3, 4, 5, 6, 7);
        }
        unsigned pa[16];
#pragma unroll
        for (int e = 0; e < 8; ++e) {
            const unsigned x = e < 4 ? ta[e] : tb[e - 4];
            pa[2 * e] = LP_T1 + 4 * (x & 0xffff); pa[2 * e + 1] = LP_T1 + 4 * (x >> 16);
        }
#pragma unroll
        for (int e = 0; e < 16; ++e) asm volatile("" : "+v"(pa[e]));
        _Pragma("unroll 1") for (unsigned sp = 0; lds_ld(lds, L_F + 28) < 4 && sp < SPIN_CAP; ++sp) __builtin_amdgcn_s_sleep(0);
        CBAR();
        const u32x4 bf0 = *(const LAS u32x4*)(lds + abv), bf1 = *(const LAS u32x4*)(lds + abv + 1024);
        const f32x4 z4 = {0.f, 0.f, 0.f, 0.f};
        const f32x4 d00 = mfma16(a2[0], as_frag(bf0), z4), d01 = mfma16(a2[1], as_frag(bf0), z4);
        const f32x4 d10 = mfma16(a2[0], as_frag(bf1), z4), d11 = mfma16(a2[1], as_frag(bf1), z4);
#pragma unroll
        for (int e = 0; e < 4; ++e) {
            *(LAS float*)(lds + ac + e * 128) = d00[e]; *(LAS float*)(lds + ac + 2048 + e * 128) = d01[e];
            *(LAS float*)(lds + ac + 64 + e * 128) = d10[e]; *(LAS float*)(lds + ac + 2048 + 64 + e * 128) = d11[e];
        }
        STAMP(4);
        float pv[16];
#pragma unroll
        for (int e = 0; e < 16; ++e) pv[e] = *(const LAS float*)(lds + pa[e]);
#pragma unroll
        for (int j = 0; j < 4; ++j) o[j] = pk_bf16(pv[4 * j] + pv[4 * j + 1], pv[4 * j + 2] + pv[4 * j + 3]);
    }
    {
        const int fr = w == 0 ? (cs < 22 ? 2 * cs : 22 + cs) : 2 * cs + 1;
        __builtin_amdgcn_raw_buffer_store_b128(o, srdA, lane < nact ? (unsigned)(fr * 832 + (26 * chh + p) * 16) : 0x7ffffff0u, 0, AUX_SC1);
    }
    STAMP(5);
    WAIT_VM0();
    STAMP(6);
    if (w == 1) { sig_set(lds, L_F + 24); return; }
    const unsigned tagv = *(const LAS unsigned*)(lds + L_EP + (lane & 15) * 4);
    _Pragma("unroll 1") for (unsigned sp = 0; lds_ld(lds, L_F + 24) == 0 && sp < SPIN_CAP; ++sp) __builtin_amdgcn_s_sleep(0);
    if (lane < 16) __hip_atomic_store((gu32*)(P.ws + WS_FL) + lane * 32 + p, tagv + 1u, __ATOMIC_RELAXED, __HIP_MEMORY_SCOPE_AGENT);
}

constexpr unsigned A_PER = A_BYTES / 4;
DI void cin_role(const Params& P, lds8* lds, int cw, int lane, int g, int m, bool has_dnn, int pf_slot STAMP_ARG) {
    const int d = lane & 31, h = lane >> 5;
    const int b = 64 * g + 4 * m + cw;
    const unsigned ldsb = (unsigned)(size_t)lds;
    {
        const __amdgpu_buffer_rsrc_t srdT = __builtin_amdgcn_make_buffer_rsrc((void*)P.table, 0, NVOC * 64, RSRC3);
        const unsigned lb = ldsb + L_X0 + cw * 1664;
        WAIT_VM0();
        const int q = lane >> 2;
        const unsigned j0 = *(const LAS unsigned*)(lds + L_X0 + cw * 1664 + q * 4);
        const unsigned j1 = *(const LAS unsigned*)(lds + L_X0 + cw * 1664 + (q < 10 ? q + 16 : 25) * 4);
        unsigned o0 = j0 * 64 + (lane & 3) * 16, o1 = j1 * 64 + (lane & 3) * 16;
        asm volatile("" : "+v"(o0), "+v"(o1));
        STAMP(1);
        dma16(lb, o0, srdT, 0);
        if (lane < 40) dma16(lb + 1024, o1, srdT, 0);
    }
    const unsigned bo = P.bout[0];
    STAMP(2);
    __builtin_amdgcn_s_setprio(0);
    CBAR();
    __builtin_amdgcn_s_barrier();
    const unsigned epv = *(const LAS unsigned*)(lds + L_EP + g * 4);
    unsigned bov = bo;
    asm volatile("s_waitcnt vmcnt(0)" : "+v"(bov) :: "memory");
    STAMP(3);
    float x[26];
    {
        const lds8* xb = lds + L_X0 + cw * 1664 + h * 64 + d * 2;
        const lds8* xl = lds + L_X0 + cw * 1664 + (h ? 0 : 25 * 64) + d * 2;
#pragma unroll
        for (int a = 0; a < 13; ++a) {
            const unsigned lo = *(const LAS unsigned short*)(xb + 2 * a * 64);
            const unsigned hi = a < 12 ? *(const LAS unsigned short*)(xb + (2 * a + 1) * 64) : *(const LAS unsigned short*)xl;
            x[2 * a] = bf_lo(lo); x[2 * a + 1] = bf_lo(hi);
        }
    }
    u32x4 z[24];
#pragma unroll
    for (int s = 0; s < 22; ++s) {
        float fa[8], fb[8];
#pragma unroll
        for (int e = 0; e < 8; ++e) { const AB pe = slot_ab(8 * s + e); fa[e] = x[pe.a]; fb[e] = x[pe.b]; }
        float t0, t1, t2, t3, t4, t5;
        unsigned o0, o1, o2, o3;
        asm("v_mul_f32 %4, %10, %11\n\tv_mul_f32 %5, %12, %13\n\tv_mul_f32 %6, %14, %15\n\tv_mul_f32 %7, %16, %17\n\t"
            "v_cvt_pk_bf16_f32 %0, %4, %5\n\tv_mul_f32 %8, %18, %19\n\tv_mul_f32 %9, %20, %21\n\tv_cvt_pk_bf16_f32 %1, %6, %7\n\t"
            "v_mul_f32 %4, %22, %23\n\tv_mul_f32 %5, %24, %25\n\tv_cvt_pk_bf16_f32 %2, %8, %9\n\tv_cvt_pk_bf16_f32 %3, %4, %5"
            : "=&v"(o0), "=&v"(o1), "=&v"(o2), "=v"(o3), "=&v"(t0), "=&v"(t1), "=&v"(t2), "=&v"(t3), "=&v"(t4), "=&v"(t5)
            : "v"(fa[0]), "v"(fb[0]), "v"(fa[1]), "v"(fb[1]), "v"(fa[2]), "v"(fb[2]), "v"(fa[3]), "v"(fb[3]),
              "v"(fa[4]), "v"(fb[4]), "v"(fa[5]), "v"(fb[5]), "v"(fa[6]), "v"(fb[6]), "v"(fa[7]), "v"(fb[7]));
        z[s] = (u32x4){o0, o1, o2, o3};
    }
#pragma unroll
    for (int s = 22; s < 24; ++s) {
#pragma unroll
        for (int j = 0; j < 4; ++j) {
            const AB p0 = slot_ab(8 * s + 2 * j), p1 = slot_ab(8 * s + 2 * j + 1);
            const float a0 = p0.a < 26 ? x[p0.a] : (p0.a == 26 ? 1.f : 0.f);
            const float a1 = p1.a < 26 ? x[p1.a] : (p1.a == 26 ? 1.f : 0.f);
            z[s][j] = pk_bf16(a0, a1);
        }
    }
#pragma unroll
    for (int s = 0; s < 24; ++s) asm volatile("" : "+v"(z[s]));
    STAMP(4);
    const unsigned tag = epv + 1u;
    {
        const __amdgpu_buffer_rsrc_t srdA = __builtin_amdgcn_make_buffer_rsrc((void*)(P.ws + WS_A), 0, A_BYTES, RSRC3);
        if (cw == 0) {
            if (has_dnn) _Pragma("unroll 1") for (unsigned sp = 0; lds_ld(lds, L_F + 52) < 4 && sp < SPIN_CAP; ++sp) __builtin_amdgcn_s_sleep(1);
            const gu32* fl = (const gu32*)(P.ws + WS_FL) + g * 32 + (lane < 26 ? lane : 0);
            _Pragma("unroll 1") for (unsigned sp = 0; sp < SPIN_CAP; ++sp) { if (__all(ld_agent(fl) == tag)) break; __builtin_amdgcn_s_sleep(1); }
            sig_set(lds, L_F + 16);
            if (!has_dnn) dma4_sc1(ldsb + L_DUMMY, lane < 50 ? (unsigned)((((g & 1) * 3 + pf_slot) * 50 + lane) * 128) : 0x7fffff00u, srdA, 0);
        } else {
            _Pragma("unroll 1") for (unsigned sp = 0; lds_ld(lds, L_F + 16) == 0 && sp < SPIN_CAP; ++sp) __builtin_amdgcn_s_sleep(0);
        }
        CBAR();
        __builtin_amdgcn_s_setprio(CIN_PRIO);
        STAMP(5);
        {
            const unsigned l1 = ldsb + L_W + cw * 4096, vo = lane * 16;
            asm volatile("s_mov_b32 m0, %0\n\ts_nop 0\n\t"
                         "buffer_load_dwordx4 %1, %2, %3 offen sc1 lds\n\tbuffer_load_dwordx4 %1, %2, %3 offen offset:1024 sc1 lds\n\t"
                         "buffer_load_dwordx4 %1, %2, %3 offen offset:2048 sc1 lds\n\tbuffer_load_dwordx4 %1, %2, %3 offen offset:3072 sc1 lds"
                         :: "s"(l1), "v"(vo), "s"(srdA), "s"(cw * 4096) : "memory");
#pragma unroll
            for (int i = 0; i < 6; ++i) {
                const int q = 6 * cw + i;
                const unsigned go = q < 4 ? 16384 + 1024 * q : 19968 + 1024 * (q - 4), lo = q < 4 ? 16384 + 1024 * q : 53248 + 1024 * (q - 4);
                dma16_sc1(ldsb + L_W + lo, vo, srdA, go);
            }
        }
        wait_vm<6>();
        sig_add(lds, L_F + 20);
        _Pragma("unroll 1") for (unsigned sp = 0; lds_ld(lds, L_F + 20) < 4 && sp < SPIN_CAP; ++sp) __builtin_amdgcn_s_sleep(0);
        CBAR();
    }
    STAMP(6);
    const gu64* pp = (const gu64*)(P.ws + WS_PART) + ((g * 32 + (lane < 26 ? lane : 0)) * 64 + 4 * m + cw);
    unsigned long long pv = 0;
    f32x16 acc0, acc1;
#pragma unroll
    for (int i = 0; i < 16; ++i) { acc0[i] = 0.f; acc1[i] = 0.f; }
    {
        unsigned abo = L_W + (26 * h + d) * 16;
        asm volatile("" : "+v"(abo));
        const lds8* ab = lds + abo;
        constexpr int PF = 6;
        u32x4 af[46];
        unsigned cnt2 = 0;
        auto frag_of = [](int t) { return t < 24 ? t : t + 40; };
#pragma unroll
        for (int t = 0; t < PF; ++t) af[t] = *(const LAS u32x4*)(ab + frag_of(t) * 832);
        __builtin_amdgcn_sched_barrier(0);
#pragma unroll
        for (int t = 0; t < 46; ++t) {
            if (t == 9) { wait_vm<0>(); sig_add(lds, L_F + 44); }
            if (t + PF == 19 - 2) cnt2 = lds_ld(lds, L_F + 44);
            if (t + PF == 19) {
                if (cnt2 < 4) _Pragma("unroll 1") for (unsigned sp = 0; lds_ld(lds, L_F + 44) < 4 && sp < SPIN_CAP; ++sp) __builtin_amdgcn_s_sleep(0);
                CBAR();
            }
            if (t + PF < 46) af[t + PF] = *(const LAS u32x4*)(ab + frag_of(t + PF) * 832);
            if (t < 44) {
                if (t & 1) acc0 = mfma32(as_frag(af[t]), as_frag(z[t >> 1]), acc0);
                else acc1 = mfma32(as_frag(af[t]), as_frag(z[t >> 1]), acc1);
            } else acc1 = mfma32(as_frag(af[t]), as_frag(z[t - 22]), acc1);
            if (t == 30) pv = __hip_atomic_load(pp, __ATOMIC_RELAXED, __HIP_MEMORY_SCOPE_AGENT);
            __builtin_amdgcn_sched_barrier(0);
        }
    }
    float sa = 0.f;
#pragma unroll
    for (int i = 0; i < 12; ++i) sa = __builtin_fmaf(acc0[i], acc1[i], sa);
    const float sb = __builtin_fmaf(acc0[12], acc1[12], acc0[13] * acc1[13]);
    float tot = h == 0 ? sa + sb : sa;
    asm volatile("" : "+v"(tot));
    STAMP(7);
    _Pragma("unroll 1") for (unsigned sp = 0; sp < SPIN_CAP; ++sp) {
        if (__all((unsigned)(pv >> 32) == tag)) break;
        pv = __hip_atomic_load(pp, __ATOMIC_RELAXED, __HIP_MEMORY_SCOPE_AGENT);
    }
    STAMP(8);
    if (lane < 26) tot += __uint_as_float((unsigned)pv);
    tot = wave_sum_to_lane63(tot);
    if (lane == 63) {
        const float o = tot + bf_lo(bov);
        P.out[b] = (unsigned short)(pk_bf16(o, o) & 0xffffu);
    }
    if (m == 0 && cw == 0) {
        const gu64* ap = (const gu64*)(P.ws + WS_PART) + ((g * 32 + 26 + (lane < 3 ? lane : 0)) * 64);
        _Pragma("unroll 1") for (unsigned sp = 0; sp < SPIN_CAP; ++sp) {
            const unsigned long long av = __hip_atomic_load(ap, __ATOMIC_RELAXED, __HIP_MEMORY_SCOPE_AGENT);
            if (__all((unsigned)(av >> 32) == tag)) break;
        }
        if (lane == 0) __hip_atomic_store((gu32*)(P.ws + WS_EP) + g, tag, __ATOMIC_RELAXED, __HIP_MEMORY_SCOPE_AGENT);
    }
}

DI void decode_block(int bid, int& g, int& m, int& ntile, int& k) {
    const int x = bid & 7, j = bid >> 3;
    g = 2 * x + (j >> 4); m = j & 15;
    ntile = m < 13 ? m : -1; k = m - 13;
}

__global__ void __launch_bounds__(512, 2) xdfm_kernel(KArgs KA) {
    const Params& P = KA.P;
    __shared__ __attribute__((aligned(16))) unsigned char smem[L_END];
    lds8* lds = (lds8*)smem;
    STAMP(0);
    const int w = __builtin_amdgcn_readfirstlane(threadIdx.x >> 6), lane = threadIdx.x & 63;
    asm volatile("" :: "s"(P.idx), "s"(P.table), "s"(P.wcin), "s"(P.wout), "s"(P.w1), "s"(P.b1), "s"(P.w2), "s"(P.bout), "s"(P.out), "s"(P.ws));
    int g, m, ntile, k;
    decode_block(blockIdx.x, g, m, ntile, k);
    const bool is_cin = w >= 4;
    if (__builtin_expect(is_cin, 1)) {
        __builtin_amdgcn_s_setprio(3);
        dma16((unsigned)(size_t)lds + L_X0 + (w - 4) * 1664, lane < 7 ? (64 * g + 4 * m + w - 4) * 104 + lane * 16 : 0x7fffff00u,
              __builtin_amdgcn_make_buffer_rsrc((void*)P.idx, 0, NBATCH * 26 * 4, RSRC3), 0);
        if (__builtin_expect(w == 7, 1)) dma4((unsigned)(size_t)lds + L_DUMMY, lane < 26 ? ((blockIdx.x >> 3) * 26 + lane) * 128 : 0x7fffff00u,
                         __builtin_amdgcn_make_buffer_rsrc((void*)P.wcin, 0, 3 * 17576 * 2, RSRC3), 0);
    }
    if (__builtin_expect(w == 0, 1)) {
        if (lane < 16) lds_st(lds, L_F + 4 * lane, 0);
        dma4_sc1((unsigned)(size_t)lds + L_EP, (lane & 15) * 4, __builtin_amdgcn_make_buffer_rsrc((void*)(P.ws + WS_EP), 0, 64, RSRC3), 0);
    }
    const int p = g * 3 + k;
    if (__builtin_expect(!is_cin && ntile >= 0, 1)) dnn_start(P, lds, w, lane, g, ntile);
    if (__builtin_expect(!is_cin && ntile < 0 && p < 26, 1)) prep_start(P, lds, w, lane, p);
    if (__builtin_expect(!is_cin && ntile >= 0, 1)) {
        dnn_role(P, lds, w, lane, g, ntile STAMP_PASS);
    } else if (is_cin) {
        cin_role(P, lds, w - 4, lane, g, m, ntile >= 0, k STAMP_PASS);
        __builtin_amdgcn_s_waitcnt(0x0F70);
    } else {
        if (p < 26) prep_role(P, lds, w, lane, p STAMP_PASS);
        else { asm volatile("s_waitcnt vmcnt(0) lgkmcnt(0)" ::: "memory"); __builtin_amdgcn_s_barrier(); }
        if (w == 0) {
            const unsigned ep = *(const LAS unsigned*)(lds + L_EP + g * 4);
            if (lane == 0) st_granule((gu64*)(P.ws + WS_PART) + ((g * 32 + 26 + k) * 64), ep + 1u, 0u);
        }
        __builtin_amdgcn_s_waitcnt(0x0F70);
    }
    STAMP(11);
    STAMP_FLUSH(P.ws, w, lane);
}

extern "C" void kernel_launch(void* const* d_in, const int* in_sizes, int n_in,
                              void* d_out, int out_size, void* d_ws, size_t ws_size,
                              hipStream_t stream) {
    KArgs KA;
    KA.tab = make_tab();
    Params& P = KA.P;
    P.idx = (const int*)d_in[0]; P.table = (const unsigned short*)d_in[1]; P.wcin = (const unsigned short*)d_in[2];
    P.wout = (const unsigned short*)d_in[3]; P.w1 = (const unsigned short*)d_in[4]; P.b1 = (const unsigned short*)d_in[5];
    P.w2 = (const unsigned short*)d_in[6]; P.bout = (const unsigned short*)d_in[7];
    P.out = (unsigned short*)d_out; P.ws = (unsigned char*)d_ws;
    xdfm_kernel<<<256, 512, 0, stream>>>(KA);
}
```

```cpp
#define AMP_BLK 40
#define AMP_W 4
#define AMP_J 23
#define AMP_K 10
#include <hip/hip_runtime.h>
#include <stdint.h>

#define DI __device__ __forceinline__
#define GAS __attribute__((address_space(1)))
#define LAS __attribute__((address_space(3)))

typedef short    s16x4 __attribute__((ext_vector_type(4)));
typedef short    s16x8 __attribute__((ext_vector_type(8)));
typedef float    f32x2 __attribute__((ext_vector_type(2)));
typedef float    f32x4 __attribute__((ext_vector_type(4)));
typedef float    f32x16 __attribute__((ext_vector_type(16)));
typedef unsigned u32x2 __attribute__((ext_vector_type(2)));
typedef unsigned u32x4 __attribute__((ext_vector_type(4)));
typedef __bf16   bf16x2 __attribute__((ext_vector_type(2)));
typedef GAS unsigned gu32;
typedef GAS unsigned long long gu64;
typedef LAS unsigned char lds8;

#define MAPPING 1

constexpr int DIN = 832, NBATCH = 1024, NVOC = 100000;
#define A_AFTER_X 1
#define W_EARLY 1
#define A_PF 1
#define CODE_PF 1
#define WC_PF 1
#define POLL_STEP 10
#define CIN_PRIO 2
#define WO_LAST 0
#define PKMUL 0
#define A_DMA 1
constexpr unsigned RSRC3 = 0x00020000u;
constexpr int AUX_SC1 = 16;
constexpr unsigned SPIN_CAP = 1u << 20;

constexpr unsigned WS_EP = 0;
constexpr unsigned WS_FL = 4096;
constexpr unsigned WS_PART = 8192;
constexpr unsigned WS_A = 8192 + 16 * 32 * 64 * 8;
constexpr unsigned A_BYTES = 46 * 832;

#define XD 6
constexpr unsigned L_W = 0;
constexpr unsigned L_A = 832 * 128;
constexpr unsigned L_X0 = L_A + 4 * XD * 2048;
constexpr unsigned L_F = L_X0 + 4 * 1664;
constexpr unsigned L_DUMMY = L_F + 256;
constexpr unsigned L_EP = L_DUMMY + 256;
constexpr unsigned L_BI = L_EP + 256;
constexpr unsigned L_W2 = L_BI + 256;
constexpr unsigned L_END = L_W2 + 256;
static_assert(4 * XD * 2048 >= A_BYTES && L_END <= 163840, "LDS map");
constexpr unsigned LP_VT = 0;
constexpr unsigned LP_T0 = 2048;
constexpr unsigned LP_T1 = 2048 + 2304;

struct AB { int a, b; };
constexpr AB slot_ab(int t) {
    if (t < 156) { int a = 2 * (t / 12); return {a, (a + t % 12) % 26}; }
    if (t < 170) { int u = t - 156; int a = 2 * (u / 2); return {a, (a + 12 + (u & 1)) % 26}; }
    if (t < 176) { int a = 2 * (7 + t - 170); return {a, (a + 12) % 26}; }
    if (t < 189) return {2 * (t - 176), 26};
    if (t == 189) return {26, 26};
    return {27, 27};
}
constexpr unsigned T_ZERO = 1024, T_CONST = 1025;
struct PrepTab { unsigned v[2][24 * 2 * 8]; };
constexpr PrepTab make_tab() {
    PrepTab tb{};
    for (int m = 0; m < 2; ++m) for (int s = 0; s < 24; ++s) for (int h = 0; h < 2; ++h) for (int e = 0; e < 8; ++e) {
        const int st = m ? 32 : 26;
        int t = 8 * s + e; AB ab = slot_ab(t);
        unsigned o1 = T_ZERO, o2 = T_ZERO;
        if (ab.a == 27) {}
        else if (ab.a == 26) { if (h == 0 && m) o1 = T_CONST; }
        else if (ab.b == 26) { if (m) o1 = ((ab.a + h) % 26) * st + 26; }
        else {
            int u = (ab.a + h) % 26, v = (ab.b + h) % 26;
            bool dup = (t == 169 && h == 1);
            if (dup) {}
            else if (u == v) o1 = u * st + u;
            else { o1 = u * st + v; o2 = v * st + u; }
        }
        tb.v[m][(s * 2 + h) * 8 + e] = o1 | (o2 << 16);
    }
    return tb;
}
static_assert(slot_ab(169).a == 12 && slot_ab(169).b == 25, "dup slot");

struct Params {
    const int* idx; const unsigned short* table; const unsigned short* wcin; const unsigned short* wout;
    const unsigned short* w1; const unsigned short* b1; const unsigned short* w2; const unsigned short* bout;
    unsigned short* out; unsigned char* ws;
};
struct KArgs { Params P; PrepTab tab; };

DI unsigned sel(bool c, unsigned a, unsigned b) { unsigned r = c ? a : b; return r; }
DI float bf_lo(unsigned v) { return __uint_as_float(v << 16); }
DI float bf_hi(unsigned v) { return __uint_as_float(v & 0xffff0000u); }
DI unsigned pk_bf16(float lo, float hi) { f32x2 v = {lo, hi}; return __builtin_bit_cast(unsigned, __builtin_convertvector(v, bf16x2)); }
DI f32x4 mfma16(s16x8 a, s16x8 b, f32x4 c) { return __builtin_amdgcn_mfma_f32_16x16x32_bf16(a, b, c, 0, 0, 0); }
DI f32x16 mfma32(s16x8 a, s16x8 b, f32x16 c) { return __builtin_amdgcn_mfma_f32_32x32x16_bf16(a, b, c, 0, 0, 0); }
DI s16x8 as_frag(u32x4 v) { return __builtin_bit_cast(s16x8, v); }
template <int CTRL, int ROWMASK> DI float dpp_add(float v) {
    return v + __int_as_float(__builtin_amdgcn_update_dpp(0, __float_as_int(v), CTRL, ROWMASK, 0xf, true));
}
DI float wave_sum_to_lane63(float v) {
    v = dpp_add<0x111, 0xf>(v); v = dpp_add<0x112, 0xf>(v); v = dpp_add<0x114, 0xf>(v); v = dpp_add<0x118, 0xf>(v);
    v = dpp_add<0x142, 0xa>(v); v = dpp_add<0x143, 0xc>(v);
    return v;
}
DI float sum_lane_groups(float v) {
    auto a = __builtin_amdgcn_permlane16_swap(__float_as_uint(v), __float_as_uint(v), false, false);
    v = __uint_as_float(a[0]) + __uint_as_float(a[1]);
    auto b = __builtin_amdgcn_permlane32_swap(__float_as_uint(v), __float_as_uint(v), false, false);
    return __uint_as_float(b[0]) + __uint_as_float(b[1]);
}
DI float gelu_tanh(float x) {
    const float k1 = -2.0f * 0.7978845608028654f * 1.4426950408889634f;
    const float k2 = k1 * 0.044715f;
    float arg = x * __builtin_fmaf(x * x, k2, k1);
    float e = __builtin_amdgcn_exp2f(arg);
    return x * __builtin_amdgcn_rcpf(1.0f + e);
}
DI void dma16(unsigned lds_addr, unsigned voff, __amdgpu_buffer_rsrc_t srd, unsigned soff) {
    asm volatile("s_mov_b32 m0, %0\n\ts_nop 0\n\tbuffer_load_dwordx4 %1, %2, %3 offen lds"
                 :: "s"(lds_addr), "v"(voff), "s"(srd), "s"(soff) : "memory");
}
DI void dma16_sc1(unsigned lds_addr, unsigned voff, __amdgpu_buffer_rsrc_t srd, unsigned soff) {
    asm volatile("s_mov_b32 m0, %0\n\ts_nop 0\n\tbuffer_load_dwordx4 %1, %2, %3 offen sc1 lds"
                 :: "s"(lds_addr), "v"(voff), "s"(srd), "s"(soff) : "memory");
}
DI void dma4_sc1(unsigned lds_addr, unsigned voff, __amdgpu_buffer_rsrc_t srd, unsigned soff) {
    asm volatile("s_mov_b32 m0, %0\n\ts_nop 0\n\tbuffer_load_dword %1, %2, %3 offen sc1 lds"
                 :: "s"(lds_addr), "v"(voff), "s"(srd), "s"(soff) : "memory");
}
DI void dma4(unsigned lds_addr, unsigned voff, __amdgpu_buffer_rsrc_t srd, unsigned soff) {
    asm volatile("s_mov_b32 m0, %0\n\ts_nop 0\n\tbuffer_load_dword %1, %2, %3 offen lds"
                 :: "s"(lds_addr), "v"(voff), "s"(srd), "s"(soff) : "memory");
}
DI void st_granule(gu64* g, unsigned tag, unsigned val) {
    __hip_atomic_store(g, ((unsigned long long)tag << 32) | val, __ATOMIC_RELAXED, __HIP_MEMORY_SCOPE_AGENT);
}
DI unsigned ld_agent(const gu32* p) { return __hip_atomic_load(p, __ATOMIC_RELAXED, __HIP_MEMORY_SCOPE_AGENT); }
DI unsigned lds_ld(lds8* l, unsigned off) { return __hip_atomic_load((LAS unsigned*)(l + off), __ATOMIC_RELAXED, __HIP_MEMORY_SCOPE_WORKGROUP); }
DI void lds_st(lds8* l, unsigned off, unsigned v) { __hip_atomic_store((LAS unsigned*)(l + off), v, __ATOMIC_RELAXED, __HIP_MEMORY_SCOPE_WORKGROUP); }
DI void lds_add(lds8* l, unsigned off, unsigned v) { __hip_atomic_fetch_add((LAS unsigned*)(l + off), v, __ATOMIC_RELAXED, __HIP_MEMORY_SCOPE_WORKGROUP); }
DI void sig_add(lds8* l, unsigned off) {
    asm volatile("s_mov_b64 exec, 1\n\tds_add_u32 %0, %1\n\ts_mov_b64 exec, -1" :: "v"((unsigned)(size_t)l + off), "v"(1u) : "memory");
}
DI void sig_set(lds8* l, unsigned off) {
    asm volatile("s_mov_b64 exec, 1\n\tds_write_b32 %0, %1\n\ts_mov_b64 exec, -1" :: "v"((unsigned)(size_t)l + off), "v"(1u) : "memory");
}
#define WAIT_LGKM() asm volatile("s_waitcnt lgkmcnt(0)" ::: "memory")
#define WAIT_VM0() asm volatile("s_waitcnt vmcnt(0)" ::: "memory")
#define CBAR() asm volatile("" ::: "memory")
struct Stamps { unsigned long long t0, t1; };
#define STAMP(i) do { if ((i) == 0) { asm volatile("s_memrealtime %0\n\ts_waitcnt lgkmcnt(0)" : "=s"(st.t0) :: "memory"); } else if ((i) == AMP_J) { asm volatile("s_memrealtime %0\n\ts_waitcnt lgkmcnt(0)" : "=s"(st.t1) :: "memory"); } } while (0)
#define STAMP_ARG , Stamps& st
#define STAMP_PASS , st
#define STAMP_FLUSH(ws, w, lane) do { if (blockIdx.x == AMP_BLK && (w) == AMP_W) { const unsigned long long d_ = (st.t1 - st.t0) * AMP_K; const unsigned long long e_ = __builtin_amdgcn_s_memrealtime(); while (__builtin_amdgcn_s_memrealtime() - e_ < d_) __builtin_amdgcn_s_sleep(4); } } while (0)

template <int K> DI unsigned quad_bcast(unsigned v) { return __builtin_amdgcn_update_dpp(0u, v, K * 0x55, 0xf, 0xf, true); }
template <int N> DI void wait_vm() { asm volatile("s_waitcnt vmcnt(%0)" :: "i"(N) : "memory"); }
DI void dma_w4(unsigned lds_addr, unsigned voff, __amdgpu_buffer_rsrc_t srd, unsigned soff) {
    unsigned t;
    asm volatile("s_mov_b32 m0, %1\n\ts_add_u32 %0, %4, 12288\n\tbuffer_load_dwordx4 %2, %3, %4 offen lds\n\t"
                 "buffer_load_dwordx4 %2, %3, %0 offen offset:1024 lds\n\ts_add_u32 %0, %0, 12288\n\t"
                 "buffer_load_dwordx4 %2, %3, %0 offen offset:2048 lds\n\ts_add_u32 %0, %0, 12288\n\t"
                 "buffer_load_dwordx4 %2, %3, %0 offen offset:3072 lds"
                 : "=&s"(t) : "s"(lds_addr), "v"(voff), "s"(srd), "s"(soff) : "memory", "scc");
}
DI void dnn_start(const Params& P, lds8* lds, int w, int lane, int g, int ntile) {
    const int mh = w & 1, kh = w >> 1;
    const unsigned ldsb = (unsigned)(size_t)lds;
    const unsigned ring = ldsb + L_A + w * (XD * 2048);
    const __amdgpu_buffer_rsrc_t srdI = __builtin_amdgcn_make_buffer_rsrc((void*)P.idx, 0, NBATCH * 26 * 4, RSRC3);
    const unsigned vo = ((64 * g + 32 * mh + (lane >> 2)) * 26 + 13 * kh + 4 * (lane & 3)) * 4;
    dma16(ring, vo, srdI, 0);
    dma16(ring + 1024, vo, srdI, 1664);
    if (__builtin_expect(w == 1, 1)) dma4(ldsb + L_BI, ntile * 128 + lane * 4, __builtin_amdgcn_make_buffer_rsrc((void*)P.b1, 0, DIN * 2, RSRC3), 0);
    if (__builtin_expect(w == 2, 1)) dma4(ldsb + L_W2, ntile * 128 + lane * 4, __builtin_amdgcn_make_buffer_rsrc((void*)P.w2, 0, DIN * 2, RSRC3), 0);
    const __amdgpu_buffer_rsrc_t srdW = __builtin_amdgcn_make_buffer_rsrc((void*)P.w1, 0, DIN * DIN * 2, RSRC3);
    const unsigned vw = (lane >> 3) * 1664 + (((lane & 7) ^ (((lane >> 4) & 1) << 2)) << 4);
    const unsigned rowA = 416 * kh + 208 * mh + 104 * (g & 1);
    dma_w4(ldsb + L_W + rowA * 128, vw, srdW, rowA * 1664 + ntile * 128);
}
DI void dnn_role(const Params& P, lds8* lds, int w, int lane, int g, int ntile STAMP_ARG) {
    const int mh = w & 1, kh = w >> 1;
    const int h = lane >> 5;
    const unsigned ldsb = (unsigned)(size_t)lds;
    const unsigned ring = ldsb + L_A + w * (XD * 2048);
    const __amdgpu_buffer_rsrc_t srdW = __builtin_amdgcn_make_buffer_rsrc((void*)P.w1, 0, DIN * DIN * 2, RSRC3);
    const unsigned vw = (lane >> 3) * 1664 + (((lane & 7) ^ (((lane >> 4) & 1) << 2)) << 4);
    const unsigned rowA = 416 * kh + 208 * mh + 104 * (g & 1), rowB = 416 * kh + 208 * mh + 104 * ((g & 1) ^ 1);
    auto wdma = [&](unsigned row, int a) {
        const unsigned sb = row * 1664 + ntile * 128, lb = ldsb + L_W + row * 128;
        if (a < 3) dma_w4(lb + a * 4096, vw, srdW, sb + a * 4 * 13312);
        else dma16(lb + 12288, vw, srdW, sb + 12 * 13312);
    };
    STAMP(1);
    wait_vm<4>();
    WAIT_LGKM();
    __builtin_amdgcn_s_barrier();
    STAMP(2);
    const u32x4 i0 = *(const LAS u32x4*)(lds + L_A + w * (XD * 2048) + lane * 16);
    const u32x4 i1 = *(const LAS u32x4*)(lds + L_A + w * (XD * 2048) + 1024 + lane * 16);
    const unsigned ep = *(const LAS unsigned*)(lds + L_EP + g * 4);
#pragma unroll
    for (int a = 1; a < 4; ++a) wdma(rowA, a);
#pragma unroll
    for (int a = 0; a < 4; ++a) wdma(rowB, a);
    const __amdgpu_buffer_rsrc_t srdT = __builtin_amdgcn_make_buffer_rsrc((void*)P.table, 0, NVOC * 64, RSRC3);
    const unsigned c16 = (((lane & 3) ^ ((lane >> 4) & 3)) << 4);
    u32x4 t0 = i0 << 6, t1 = i1 << 6;
    asm volatile("" : "+v"(t0), "+v"(t1));
    auto xload = [&](int s) {
        const unsigned a0 = t0[s & 3], a1 = t1[s & 3];
        const unsigned o0 = (s < 4 ? quad_bcast<0>(a0) : s < 8 ? quad_bcast<1>(a0) : s < 12 ? quad_bcast<2>(a0) : quad_bcast<3>(a0)) + c16;
        const unsigned o1 = (s < 4 ? quad_bcast<0>(a1) : s < 8 ? quad_bcast<1>(a1) : s < 12 ? quad_bcast<2>(a1) : quad_bcast<3>(a1)) + c16;
        dma16(ring + (s % XD) * 2048, o0, srdT, 0);
        dma16(ring + (s % XD) * 2048 + 1024, o1, srdT, 0);
    };
#pragma unroll
    for (int s = 0; s < XD; ++s) xload(s);
    STAMP(3);
    wait_vm<2 * XD>();
    sig_add(lds, L_F + 32 + 4 * kh);
    _Pragma("unroll 1") for (unsigned sp = 0; lds_ld(lds, L_F + 32 + 4 * kh) < 2 && sp < SPIN_CAP; ++sp) __builtin_amdgcn_s_sleep(0);
    CBAR();
    STAMP(4);
    f32x16 acc[2];
    u32x2 bia[4], wv[4];
#pragma unroll
    for (int r4 = 0; r4 < 4; ++r4) {
        bia[r4] = *(const LAS u32x2*)(lds + L_BI + kh * 64 + h * 8 + r4 * 16);
        wv[r4] = *(const LAS u32x2*)(lds + L_W2 + kh * 64 + h * 8 + r4 * 16);
    }
#pragma unroll
    for (int r4 = 0; r4 < 4; ++r4) {
        acc[0][4 * r4] = bf_lo(bia[r4][0]); acc[0][4 * r4 + 1] = bf_hi(bia[r4][0]); acc[0][4 * r4 + 2] = bf_lo(bia[r4][1]); acc[0][4 * r4 + 3] = bf_hi(bia[r4][1]);
        acc[1][4 * r4] = 0.f; acc[1][4 * r4 + 1] = 0.f; acc[1][4 * r4 + 2] = 0.f; acc[1][4 * r4 + 3] = 0.f;
    }
    {
        const int blk = (lane >> 4) & 1, q = (lane & 15) >> 2, pp = lane & 3, n = lane & 31;
        unsigned ad[2];
#pragma unroll
        for (int j = 0; j < 2; ++j) {
            const unsigned c = 4 * (j ^ kh) + 2 * blk + (pp >> 1);
            ad[j] = L_W + (416 * kh + 8 * h + q) * 128 + ((c ^ (((q >> 1) & 1) << 2)) << 4) + (pp & 1) * 8;
        }
        const unsigned xb = L_A + w * (XD * 2048) + n * 64;
        const unsigned xa0 = xb + ((h ^ ((n >> 2) & 3)) << 4), xa1 = xb + (((2 + h) ^ ((n >> 2) & 3)) << 4);
        u32x4 xf[3][2];
        s16x8 wf[2][4];
        auto xread = [&](int s) {
            xf[s % 3][0] = *(const LAS u32x4*)(lds + xa0 + (s % XD) * 2048);
            xf[s % 3][1] = *(const LAS u32x4*)(lds + xa1 + (s % XD) * 2048);
        };
        auto wread = [&](int s) {
#pragma unroll
            for (int a = 0; a < 2; ++a)
#pragma unroll
                for (int j = 0; j < 2; ++j) {
                    const s16x4 lo = __builtin_amdgcn_ds_read_tr16_b64_v4i16((LAS s16x4*)(lds + ad[j] + s * 4096 + a * 2048));
                    const s16x4 hi = __builtin_amdgcn_ds_read_tr16_b64_v4i16((LAS s16x4*)(lds + ad[j] + s * 4096 + a * 2048 + 512));
                    wf[s & 1][2 * a + j] = __builtin_shufflevector(lo, hi, 0, 1, 2, 3, 4, 5, 6, 7);
                }
        };
        wread(0);
        wait_vm<2 * (XD - 1)>();
        xread(0);
        wait_vm<2 * (XD - 2)>();
        xread(1);
        __builtin_amdgcn_sched_barrier(0);
#pragma unroll
        for (int s = 0; s < 13; ++s) {
            if (s + 1 < 13) wread(s + 1);
#pragma unroll
            for (int a = 0; a < 2; ++a)
#pragma unroll
                for (int j = 0; j < 2; ++j) acc[j] = mfma32(wf[s & 1][2 * a + j], as_frag(xf[s % 3][a]), acc[j]);
            if (s + 1 < 13) {
#pragma unroll
                for (int j = 0; j < 4; ++j) { __builtin_amdgcn_sched_group_barrier(0x100, 2, 0); __builtin_amdgcn_sched_group_barrier(0x8, 1, 0); }
            }
            __builtin_amdgcn_sched_barrier(0);
            if (s + XD < 13) xload(s + XD);
            if (s + 2 < 13) {
                constexpr int LASTI = 12;
                const int last = s + XD < LASTI ? s + XD : LASTI;
                const int younger = last - (s + 2);
                if (younger >= 4) wait_vm<8>(); else if (younger == 3) wait_vm<6>(); else if (younger == 2) wait_vm<4>(); else if (younger == 1) wait_vm<2>(); else wait_vm<0>();
                if (s + 2 == 12) sig_add(lds, L_F + 52);
                xread(s + 2);
            }
            __builtin_amdgcn_sched_barrier(0);
        }
    }
    STAMP(6);
    {
        _Pragma("unroll 1") for (unsigned sp = 0; lds_ld(lds, L_F + 52) < 4 && sp < SPIN_CAP; ++sp) __builtin_amdgcn_s_sleep(0);
        CBAR();
        lds8* xw = lds + L_W + kh * 53248 + (5 + mh) * 4096 + lane * 16;
#pragma unroll
        for (int r4 = 0; r4 < 4; ++r4) *(LAS f32x4*)(xw + r4 * 1024) = (f32x4){acc[1][4 * r4], acc[1][4 * r4 + 1], acc[1][4 * r4 + 2], acc[1][4 * r4 + 3]};
        CBAR();
        sig_set(lds, L_F + 4 * w);
        _Pragma("unroll 1") for (unsigned sp = 0; lds_ld(lds, L_F + 4 * (w ^ 2)) == 0 && sp < SPIN_CAP; ++sp) __builtin_amdgcn_s_sleep(0);
        CBAR();
        const lds8* xr = lds + L_W + (kh ^ 1) * 53248 + (5 + mh) * 4096 + lane * 16;
#pragma unroll
        for (int r4 = 0; r4 < 4; ++r4) {
            const f32x4 o = *(const LAS f32x4*)(xr + r4 * 1024);
            acc[0][4 * r4] += o[0]; acc[0][4 * r4 + 1] += o[1]; acc[0][4 * r4 + 2] += o[2]; acc[0][4 * r4 + 3] += o[3];
        }
    }
    STAMP(7);
    float s0 = 0.f, s1 = 0.f;
#pragma unroll
    for (int r4 = 0; r4 < 4; ++r4) {
        s0 = __builtin_fmaf(gelu_tanh(acc[0][4 * r4]), bf_lo(wv[r4][0]), s0);
        s1 = __builtin_fmaf(gelu_tanh(acc[0][4 * r4 + 1]), bf_hi(wv[r4][0]), s1);
        s0 = __builtin_fmaf(gelu_tanh(acc[0][4 * r4 + 2]), bf_lo(wv[r4][1]), s0);
        s1 = __builtin_fmaf(gelu_tanh(acc[0][4 * r4 + 3]), bf_hi(wv[r4][1]), s1);
    }
    s0 += s1;
    const auto sw = __builtin_amdgcn_permlane32_swap(__float_as_uint(s0), __float_as_uint(s0), false, false);
    const float part = __uint_as_float(sw[0]) + __uint_as_float(sw[1]);
    STAMP(8);
    if (lane < 32) st_granule((gu64*)(P.ws + WS_PART) + ((g * 32 + 2 * ntile + kh) * 64 + 32 * mh + lane), ep + 1u, __float_as_uint(part));
}

constexpr unsigned LP_W2 = 12288;
constexpr unsigned LP_WO = 14336;
constexpr unsigned LP_DM = 15360;
constexpr unsigned LP_W3 = 16384;
static_assert(LP_T1 + 4 * 1026 <= LP_W2, "prep scratch");
constexpr unsigned w3_slot(int j) { return j * 1280 + (j & 1) * 64 + ((j >> 2) & 1) * 128; }
DI void dma16_add(unsigned lds_base, unsigned lds_off, unsigned voff, __amdgpu_buffer_rsrc_t srd, unsigned sbase, unsigned soff) {
    unsigned t;
    asm volatile("s_add_u32 m0, %1, %2\n\ts_add_u32 %0, %5, %6\n\tbuffer_load_dwordx4 %3, %4, %0 offen lds"
                 : "=&s"(t) : "s"(lds_base), "i"(lds_off), "v"(voff), "s"(srd), "s"(sbase), "i"(soff) : "memory", "scc");
}
DI void prep_start(const Params& P, lds8* lds, int w, int lane, int p) {
    const unsigned ldsb = (unsigned)(size_t)lds;
    const __amdgpu_buffer_rsrc_t srdC = __builtin_amdgcn_make_buffer_rsrc((void*)P.wcin, 0, 3 * 17576 * 2, RSRC3);
    const int i0 = w == 0 ? 0 : w == 1 ? 6 : w == 2 ? 14 : 20;
    dma4(ldsb + LP_WO + w * 256, lane * 4, __builtin_amdgcn_make_buffer_rsrc((void*)P.wout, 0, 78 * 2, RSRC3), 0);
    {
        const unsigned hp = lane >> 5;
        const unsigned vo = hp * 1352 + ((lane >> 2) & 7) * 52 + (((lane & 3) ^ (hp << 1)) << 4);
        const unsigned lb = ldsb + LP_W3 + w * 16640, sb = 52 * 1352 + 52 * i0;
        dma16_add(lb, w3_slot(0), vo, srdC, sb, 0 * 2704);  dma16_add(lb, w3_slot(1), vo, srdC, sb, 1 * 2704);
        dma16_add(lb, w3_slot(2), vo, srdC, sb, 2 * 2704);  dma16_add(lb, w3_slot(3), vo, srdC, sb, 3 * 2704);
        dma16_add(lb, w3_slot(4), vo, srdC, sb, 4 * 2704);  dma16_add(lb, w3_slot(5), vo, srdC, sb, 5 * 2704);
        dma16_add(lb, w3_slot(6), vo, srdC, sb, 6 * 2704);  dma16_add(lb, w3_slot(7), vo, srdC, sb, 7 * 2704);
        dma16_add(lb, w3_slot(8), vo, srdC, sb, 8 * 2704);  dma16_add(lb, w3_slot(9), vo, srdC, sb, 9 * 2704);
        dma16_add(lb, w3_slot(10), vo, srdC, sb, 10 * 2704); dma16_add(lb, w3_slot(11), vo, srdC, sb, 11 * 2704);
        dma16_add(lb, w3_slot(12), vo, srdC, sb, 12 * 2704);
    }
    if (__builtin_expect(w < 2, 1)) {
        const unsigned vo = w == 0 ? 35152 + (lane >> 2) * 1352 + p * 52 + (lane & 3) * 16 : p * 1352 + lane * 16;
        const unsigned lb = ldsb + (w == 0 ? LP_W2 : LP_T0);
        dma16(lb, vo, srdC, 0);
        dma16(lb + 1024, vo, srdC, w == 0 ? 16 * 1352 : 1024);
    }
}
DI void prep_role(const Params& P, lds8* lds, int w, int lane, int p STAMP_ARG) {
    STAMP(21);
    const int n = lane & 15, gq = lane >> 4;
    const int i0 = w == 0 ? 0 : w == 1 ? 6 : w == 2 ? 14 : 20;
    const int nact = w == 0 ? 48 : 44;
    const int ch = lane < nact ? lane : 0;
    const int cs = ch >> 1, chh = ch & 1;
    const GAS u32x4* tp = (const GAS u32x4*)((const GAS unsigned char*)(size_t)__builtin_amdgcn_kernarg_segment_ptr() + sizeof(Params) + ((w == 0 ? 384 : 0) + (cs * 2 + chh) * 8) * 4);
    const u32x4 ta = tp[0], tb = tp[1];
    STAMP(1);
    const int q = (lane & 15) >> 2, pp = lane & 3;
    int r0 = 8 * gq + q, r1 = r0 + 4;
    r0 = r0 < 26 ? r0 : 25; r1 = r1 < 26 ? r1 : 25;
    unsigned b0a, b0b, b1a, b1b;
    {
        const unsigned wb = LP_W3 + w * 16640 + pp * 8;
        const unsigned j0 = r0 >> 1, j1 = r1 >> 1;
        const unsigned s0 = wb + j0 * 1280 + (j0 & 1) * 64 + ((j0 >> 2) & 1) * 128 + (r0 & 1) * 512;
        const unsigned s1 = wb + j1 * 1280 + (j1 & 1) * 64 + ((j1 >> 2) & 1) * 128 + (r1 & 1) * 512;
        b0a = s0 + (r0 & 1) * 32; b0b = s0 + 32 - (r0 & 1) * 32; b1a = s1 + (r1 & 1) * 32; b1b = s1 + 32 - (r1 & 1) * 32;
    }
    unsigned va = LP_VT + n * 64 + 2 * i0, vb = n < 10 ? va + 1024 : LP_DM;
    unsigned vl = w == 3 ? LP_DM : 0;
    unsigned ac = LP_T1 + 4 * (4 * gq * 32 + n);
    unsigned a20 = LP_W2 + r0 * 64 + pp * 8, a21 = LP_W2 + r1 * 64 + pp * 8, abv = LP_VT + n * 64 + gq * 16;
    asm volatile("" : "+v"(b0a), "+v"(b0b), "+v"(b1a), "+v"(b1b), "+v"(va), "+v"(vb), "+v"(ac), "+v"(a20), "+v"(a21), "+v"(abv));
    if (w == 3) {
        LAS unsigned* z = (LAS unsigned*)(lds + LP_VT + (lane < 27 ? lane : 31) * 64 + 52);
        z[0] = 0; z[1] = 0; z[2] = 0;
        *(LAS unsigned*)(lds + LP_T1 + 4 * T_ZERO) = 0;
    }
    if (w == 1) *(LAS unsigned*)(lds + LP_T0 + 2 * T_ZERO) = 0;
    asm volatile("s_waitcnt vmcnt(0) lgkmcnt(0)" ::: "memory");
    __builtin_amdgcn_s_barrier();
    STAMP(2);
    {
        const u32x4 wo3 = *(const LAS u32x4*)(lds + LP_WO + w * 256 + 104 + 16 * gq);
        s16x8 bfr[16];
#pragma unroll
        for (int t = 0; t < 16; ++t) {
            const int ii = t >> 1, u = t & 1;
            const s16x4 lo = __builtin_amdgcn_ds_read_tr16_b64_v4i16((LAS s16x4*)(lds + (u ? b0b : b0a) + ii * 64));
            const s16x4 hi = __builtin_amdgcn_ds_read_tr16_b64_v4i16((LAS s16x4*)(lds + (u ? b1b : b1a) + ii * 64));
            bfr[t] = __builtin_shufflevector(lo, hi, 0, 1, 2, 3, 4, 5, 6, 7);
        }
        if (w == 3) {
            const unsigned o2 = *(const LAS unsigned*)(lds + LP_WO + 768 + 52 + (lane < 13 ? lane : 0) * 4);
            const unsigned o1 = *(const LAS unsigned short*)(lds + LP_WO + 768 + p * 2);
            *(LAS unsigned*)(lds + (lane < 13 ? LP_VT + 26 * 64 + lane * 4 : LP_DM)) = o2;
            *(LAS float*)(lds + LP_T1 + 4 * T_CONST) = bf_lo(o1);
        }
        float res[16];
#pragma unroll
        for (int t = 0; t < 16; ++t) res[t] = mfma16(as_frag(wo3), bfr[t], (f32x4){0.f, 0.f, 0.f, 0.f})[0];
#pragma unroll
        for (int k2 = 0; k2 < 4; ++k2) {
            const unsigned pa0 = pk_bf16(res[4 * k2], res[4 * k2 + 2]), pb0 = pk_bf16(res[4 * k2 + 1], res[4 * k2 + 3]);
            if (k2 < 3) { *(LAS unsigned*)(lds + va + 4 * k2) = pa0; *(LAS unsigned*)(lds + vb + 4 * k2) = pb0; }
            else { *(LAS unsigned*)(lds + (vl ? vl : va + 12)) = pa0; *(LAS unsigned*)(lds + (vl ? vl : vb + 12)) = pb0; }
        }
    }
    CBAR();
    sig_add(lds, L_F + 28);
    if (w > 1) return;
    STAMP(3);
    const __amdgpu_buffer_rsrc_t srdA = __builtin_amdgcn_make_buffer_rsrc((void*)(P.ws + WS_A), 0, A_BYTES, RSRC3);
    u32x4 o;
    if (w == 1) {
        float pv[16];
#pragma unroll
        for (int e = 0; e < 8; ++e) {
            const unsigned x = e < 4 ? ta[e] : tb[e - 4];
            pv[2 * e] = bf_lo(*(const LAS unsigned short*)(lds + LP_T0 + 2 * (x & 0xffff)));
            pv[2 * e + 1] = bf_lo(*(const LAS unsigned short*)(lds + LP_T0 + 2 * (x >> 16)));
        }
#pragma unroll
        for (int j = 0; j < 4; ++j) o[j] = pk_bf16(pv[4 * j] + pv[4 * j + 1], pv[4 * j + 2] + pv[4 * j + 3]);
    } else {
        s16x8 a2[2];
#pragma unroll
        for (int mt = 0; mt < 2; ++mt) {
            const s16x4 lo = __builtin_amdgcn_ds_read_tr16_b64_v4i16((LAS s16x4*)(lds + a20 + mt * 32));
            const s16x4 hi = __builtin_amdgcn_ds_read_tr16_b64_v4i16((LAS s16x4*)(lds + a21 + mt * 32));
            a2[mt] = __builtin_shufflevector(lo, hi, 0, 1, 2, 3, 4, 5, 6, 7);
        }
        unsigned pa[16];
#pragma unroll
        for (int e = 0; e < 8; ++e) {
            const unsigned x = e < 4 ? ta[e] : tb[e - 4];
            pa[2 * e] = LP_T1 + 4 * (x & 0xffff); pa[2 * e + 1] = LP_T1 + 4 * (x >> 16);
        }
#pragma unroll
        for (int e = 0; e < 16; ++e) asm volatile("" : "+v"(pa[e]));
        _Pragma("unroll 1") for (unsigned sp = 0; lds_ld(lds, L_F + 28) < 4 && sp < SPIN_CAP; ++sp) __builtin_amdgcn_s_sleep(0);
        CBAR();
        const u32x4 bf0 = *(const LAS u32x4*)(lds + abv), bf1 = *(const LAS u32x4*)(lds + abv + 1024);
        const f32x4 z4 = {0.f, 0.f, 0.f, 0.f};
        const f32x4 d00 = mfma16(a2[0], as_frag(bf0), z4), d01 = mfma16(a2[1], as_frag(bf0), z4);
        const f32x4 d10 = mfma16(a2[0], as_frag(bf1), z4), d11 = mfma16(a2[1], as_frag(bf1), z4);
#pragma unroll
        for (int e = 0; e < 4; ++e) {
            *(LAS float*)(lds + ac + e * 128) = d00[e]; *(LAS float*)(lds + ac + 2048 + e * 128) = d01[e];
            *(LAS float*)(lds + ac + 64 + e * 128) = d10[e]; *(LAS float*)(lds + ac + 2048 + 64 + e * 128) = d11[e];
        }
        STAMP(4);
        float pv[16];
#pragma unroll
        for (int e = 0; e < 16; ++e) pv[e] = *(const LAS float*)(lds + pa[e]);
#pragma unroll
        for (int j = 0; j < 4; ++j) o[j] = pk_bf16(pv[4 * j] + pv[4 * j + 1], pv[4 * j + 2] + pv[4 * j + 3]);
    }
    {
        const int fr = w == 0 ? (cs < 22 ? 2 * cs : 22 + cs) : 2 * cs + 1;
        __builtin_amdgcn_raw_buffer_store_b128(o, srdA, lane < nact ? (unsigned)(fr * 832 + (26 * chh + p) * 16) : 0x7ffffff0u, 0, AUX_SC1);
    }
    STAMP(5);
    WAIT_VM0();
    STAMP(6);
    if (w == 1) { sig_set(lds, L_F + 24); return; }
    const unsigned tagv = *(const LAS unsigned*)(lds + L_EP + (lane & 15) * 4);
    _Pragma("unroll 1") for (unsigned sp = 0; lds_ld(lds, L_F + 24) == 0 && sp < SPIN_CAP; ++sp) __builtin_amdgcn_s_sleep(0);
    if (lane < 16) __hip_atomic_store((gu32*)(P.ws + WS_FL) + lane * 32 + p, tagv + 1u, __ATOMIC_RELAXED, __HIP_MEMORY_SCOPE_AGENT);
}

constexpr unsigned A_PER = A_BYTES / 4;
DI void cin_role(const Params& P, lds8* lds, int cw, int lane, int g, int m, bool has_dnn, int pf_slot STAMP_ARG) {
    STAMP(22);
    const int d = lane & 31, h = lane >> 5;
    const int b = 64 * g + 4 * m + cw;
    const unsigned ldsb = (unsigned)(size_t)lds;
    {
        const __amdgpu_buffer_rsrc_t srdT = __builtin_amdgcn_make_buffer_rsrc((void*)P.table, 0, NVOC * 64, RSRC3);
        const unsigned lb = ldsb + L_X0 + cw * 1664;
        WAIT_VM0();
        const int q = lane >> 2;
        const unsigned j0 = *(const LAS unsigned*)(lds + L_X0 + cw * 1664 + q * 4);
        const unsigned j1 = *(const LAS unsigned*)(lds + L_X0 + cw * 1664 + (q < 10 ? q + 16 : 25) * 4);
        unsigned o0 = j0 * 64 + (lane & 3) * 16, o1 = j1 * 64 + (lane & 3) * 16;
        asm volatile("" : "+v"(o0), "+v"(o1));
        STAMP(1);
        dma16(lb, o0, srdT, 0);
        if (lane < 40) dma16(lb + 1024, o1, srdT, 0);
    }
    const unsigned bo = P.bout[0];
    STAMP(2);
    __builtin_amdgcn_s_setprio(0);
    CBAR();
    __builtin_amdgcn_s_barrier();
    const unsigned epv = *(const LAS unsigned*)(lds + L_EP + g * 4);
    unsigned bov = bo;
    asm volatile("s_waitcnt vmcnt(0)" : "+v"(bov) :: "memory");
    STAMP(3);
    float x[26];
    {
        const lds8* xb = lds + L_X0 + cw * 1664 + h * 64 + d * 2;
        const lds8* xl = lds + L_X0 + cw * 1664 + (h ? 0 : 25 * 64) + d * 2;
#pragma unroll
        for (int a = 0; a < 13; ++a) {
            const unsigned lo = *(const LAS unsigned short*)(xb + 2 * a * 64);
            const unsigned hi = a < 12 ? *(const LAS unsigned short*)(xb + (2 * a + 1) * 64) : *(const LAS unsigned short*)xl;
            x[2 * a] = bf_lo(lo); x[2 * a + 1] = bf_lo(hi);
        }
    }
    u32x4 z[24];
#pragma unroll
    for (int s = 0; s < 22; ++s) {
        float fa[8], fb[8];
#pragma unroll
        for (int e = 0; e < 8; ++e) { const AB pe = slot_ab(8 * s + e); fa[e] = x[pe.a]; fb[e] = x[pe.b]; }
        float t0, t1, t2, t3, t4, t5;
        unsigned o0, o1, o2, o3;
        asm("v_mul_f32 %4, %10, %11\n\tv_mul_f32 %5, %12, %13\n\tv_mul_f32 %6, %14, %15\n\tv_mul_f32 %7, %16, %17\n\t"
            "v_cvt_pk_bf16_f32 %0, %4, %5\n\tv_mul_f32 %8, %18, %19\n\tv_mul_f32 %9, %20, %21\n\tv_cvt_pk_bf16_f32 %1, %6, %7\n\t"
            "v_mul_f32 %4, %22, %23\n\tv_mul_f32 %5, %24, %25\n\tv_cvt_pk_bf16_f32 %2, %8, %9\n\tv_cvt_pk_bf16_f32 %3, %4, %5"
            : "=&v"(o0), "=&v"(o1), "=&v"(o2), "=v"(o3), "=&v"(t0), "=&v"(t1), "=&v"(t2), "=&v"(t3), "=&v"(t4), "=&v"(t5)
            : "v"(fa[0]), "v"(fb[0]), "v"(fa[1]), "v"(fb[1]), "v"(fa[2]), "v"(fb[2]), "v"(fa[3]), "v"(fb[3]),
              "v"(fa[4]), "v"(fb[4]), "v"(fa[5]), "v"(fb[5]), "v"(fa[6]), "v"(fb[6]), "v"(fa[7]), "v"(fb[7]));
        z[s] = (u32x4){o0, o1, o2, o3};
    }
#pragma unroll
    for (int s = 22; s < 24; ++s) {
#pragma unroll
        for (int j = 0; j < 4; ++j) {
            const AB p0 = slot_ab(8 * s + 2 * j), p1 = slot_ab(8 * s + 2 * j + 1);
            const float a0 = p0.a < 26 ? x[p0.a] : (p0.a == 26 ? 1.f : 0.f);
            const float a1 = p1.a < 26 ? x[p1.a] : (p1.a == 26 ? 1.f : 0.f);
            z[s][j] = pk_bf16(a0, a1);
        }
    }
#pragma unroll
    for (int s = 0; s < 24; ++s) asm volatile("" : "+v"(z[s]));
    STAMP(4);
    const unsigned tag = epv + 1u;
    {
        const __amdgpu_buffer_rsrc_t srdA = __builtin_amdgcn_make_buffer_rsrc((void*)(P.ws + WS_A), 0, A_BYTES, RSRC3);
        if (cw == 0) {
            if (has_dnn) _Pragma("unroll 1") for (unsigned sp = 0; lds_ld(lds, L_F + 52) < 4 && sp < SPIN_CAP; ++sp) __builtin_amdgcn_s_sleep(1);
            const gu32* fl = (const gu32*)(P.ws + WS_FL) + g * 32 + (lane < 26 ? lane : 0);
            _Pragma("unroll 1") for (unsigned sp = 0; sp < SPIN_CAP; ++sp) { if (__all(ld_agent(fl) == tag)) break; __builtin_amdgcn_s_sleep(1); }
            sig_set(lds, L_F + 16);
            if (!has_dnn) dma4_sc1(ldsb + L_DUMMY, lane < 50 ? (unsigned)((((g & 1) * 3 + pf_slot) * 50 + lane) * 128) : 0x7fffff00u, srdA, 0);
        } else {
            _Pragma("unroll 1") for (unsigned sp = 0; lds_ld(lds, L_F + 16) == 0 && sp < SPIN_CAP; ++sp) __builtin_amdgcn_s_sleep(0);
        }
        CBAR();
        __builtin_amdgcn_s_setprio(CIN_PRIO);
        STAMP(5);
        {
            const unsigned l1 = ldsb + L_W + cw * 4096, vo = lane * 16;
            asm volatile("s_mov_b32 m0, %0\n\ts_nop 0\n\t"
                         "buffer_load_dwordx4 %1, %2, %3 offen sc1 lds\n\tbuffer_load_dwordx4 %1, %2, %3 offen offset:1024 sc1 lds\n\t"
                         "buffer_load_dwordx4 %1, %2, %3 offen offset:2048 sc1 lds\n\tbuffer_load_dwordx4 %1, %2, %3 offen offset:3072 sc1 lds"
                         :: "s"(l1), "v"(vo), "s"(srdA), "s"(cw * 4096) : "memory");
#pragma unroll
            for (int i = 0; i < 6; ++i) {
                const int q = 6 * cw + i;
                const unsigned go = q < 4 ? 16384 + 1024 * q : 19968 + 1024 * (q - 4), lo = q < 4 ? 16384 + 1024 * q : 53248 + 1024 * (q - 4);
                dma16_sc1(ldsb + L_W + lo, vo, srdA, go);
            }
        }
        wait_vm<6>();
        sig_add(lds, L_F + 20);
        _Pragma("unroll 1") for (unsigned sp = 0; lds_ld(lds, L_F + 20) < 4 && sp < SPIN_CAP; ++sp) __builtin_amdgcn_s_sleep(0);
        CBAR();
    }
    STAMP(6);
    const gu64* pp = (const gu64*)(P.ws + WS_PART) + ((g * 32 + (lane < 26 ? lane : 0)) * 64 + 4 * m + cw);
    unsigned long long pv = 0;
    f32x16 acc0, acc1;
#pragma unroll
    for (int i = 0; i < 16; ++i) { acc0[i] = 0.f; acc1[i] = 0.f; }
    {
        unsigned abo = L_W + (26 * h + d) * 16;
        asm volatile("" : "+v"(abo));
        const lds8* ab = lds + abo;
        constexpr int PF = 6;
        u32x4 af[46];
        unsigned cnt2 = 0;
        auto frag_of = [](int t) { return t < 24 ? t : t + 40; };
#pragma unroll
        for (int t = 0; t < PF; ++t) af[t] = *(const LAS u32x4*)(ab + frag_of(t) * 832);
        __builtin_amdgcn_sched_barrier(0);
#pragma unroll
        for (int t = 0; t < 46; ++t) {
            if (t == 9) { wait_vm<0>(); sig_add(lds, L_F + 44); }
            if (t + PF == 19 - 2) cnt2 = lds_ld(lds, L_F + 44);
            if (t + PF == 19) {
                if (cnt2 < 4) _Pragma("unroll 1") for (unsigned sp = 0; lds_ld(lds, L_F + 44) < 4 && sp < SPIN_CAP; ++sp) __builtin_amdgcn_s_sleep(0);
                CBAR();
            }
            if (t + PF < 46) af[t + PF] = *(const LAS u32x4*)(ab + frag_of(t + PF) * 832);
            if (t < 44) {
                if (t & 1) acc0 = mfma32(as_frag(af[t]), as_frag(z[t >> 1]), acc0);
                else acc1 = mfma32(as_frag(af[t]), as_frag(z[t >> 1]), acc1);
            } else acc1 = mfma32(as_frag(af[t]), as_frag(z[t - 22]), acc1);
            if (t == 30) pv = __hip_atomic_load(pp, __ATOMIC_RELAXED, __HIP_MEMORY_SCOPE_AGENT);
            __builtin_amdgcn_sched_barrier(0);
        }
    }
    float sa = 0.f;
#pragma unroll
    for (int i = 0; i < 12; ++i) sa = __builtin_fmaf(acc0[i], acc1[i], sa);
    const float sb = __builtin_fmaf(acc0[12], acc1[12], acc0[13] * acc1[13]);
    float tot = h == 0 ? sa + sb : sa;
    asm volatile("" : "+v"(tot));
    STAMP(7);
    _Pragma("unroll 1") for (unsigned sp = 0; sp < SPIN_CAP; ++sp) {
        if (__all((unsigned)(pv >> 32) == tag)) break;
        pv = __hip_atomic_load(pp, __ATOMIC_RELAXED, __HIP_MEMORY_SCOPE_AGENT);
    }
    STAMP(8);
    if (lane < 26) tot += __uint_as_float((unsigned)pv);
    tot = wave_sum_to_lane63(tot);
    if (lane == 63) {
        const float o = tot + bf_lo(bov);
        P.out[b] = (unsigned short)(pk_bf16(o, o) & 0xffffu);
    }
    if (m == 0 && cw == 0) {
        const gu64* ap = (const gu64*)(P.ws + WS_PART) + ((g * 32 + 26 + (lane < 3 ? lane : 0)) * 64);
        _Pragma("unroll 1") for (unsigned sp = 0; sp < SPIN_CAP; ++sp) {
            const unsigned long long av = __hip_atomic_load(ap, __ATOMIC_RELAXED, __HIP_MEMORY_SCOPE_AGENT);
            if (__all((unsigned)(av >> 32) == tag)) break;
        }
        if (lane == 0) __hip_atomic_store((gu32*)(P.ws + WS_EP) + g, tag, __ATOMIC_RELAXED, __HIP_MEMORY_SCOPE_AGENT);
    }
}

DI void decode_block(int bid, int& g, int& m, int& ntile, int& k) {
    const int x = bid & 7, j = bid >> 3;
    g = 2 * x + (j >> 4); m = j & 15;
    ntile = m < 13 ? m : -1; k = m - 13;
}

__global__ void __launch_bounds__(512, 2) xdfm_kernel(KArgs KA) {
    const Params& P = KA.P;
    __shared__ __attribute__((aligned(16))) unsigned char smem[L_END];
    lds8* lds = (lds8*)smem;
    Stamps st; st.t0 = 0; st.t1 = 0;
    STAMP(0);
    const int w = __builtin_amdgcn_readfirstlane(threadIdx.x >> 6), lane = threadIdx.x & 63;
    {
        unsigned long long pc; unsigned len;
        asm volatile("s_getpc_b64 %0\n.Lxdfm_pc:\n\ts_mov_b32 %1, (.Lxdfm_end-.Lxdfm_pc)&4294967295" : "=s"(pc), "=s"(len));
        const __amdgpu_buffer_rsrc_t srdP = __builtin_amdgcn_make_buffer_rsrc((void*)pc, 0, len, RSRC3);
        dma4((unsigned)(size_t)lds + L_DUMMY, lane == 0 ? ((blockIdx.x >> 3) * 8 + w) * 128 : 0x7fffff00u, srdP, 0);
    }
    asm volatile("" :: "s"(P.idx), "s"(P.table), "s"(P.wcin), "s"(P.wout), "s"(P.w1), "s"(P.b1), "s"(P.w2), "s"(P.bout), "s"(P.out), "s"(P.ws));
    STAMP(23);
    int g, m, ntile, k;
    decode_block(blockIdx.x, g, m, ntile, k);
    const bool is_cin = w >= 4;
    if (__builtin_expect(is_cin, 1)) {
        __builtin_amdgcn_s_setprio(3);
        dma16((unsigned)(size_t)lds + L_X0 + (w - 4) * 1664, lane < 7 ? (64 * g + 4 * m + w - 4) * 104 + lane * 16 : 0x7fffff00u,
              __builtin_amdgcn_make_buffer_rsrc((void*)P.idx, 0, NBATCH * 26 * 4, RSRC3), 0);
        if (__builtin_expect(w == 7, 1)) dma4((unsigned)(size_t)lds + L_DUMMY, lane < 26 ? ((blockIdx.x >> 3) * 26 + lane) * 128 : 0x7fffff00u,
                         __builtin_amdgcn_make_buffer_rsrc((void*)P.wcin, 0, 3 * 17576 * 2, RSRC3), 0);
    }
    if (__builtin_expect(w == 0, 1)) {
        if (lane < 16) lds_st(lds, L_F + 4 * lane, 0);
        dma4_sc1((unsigned)(size_t)lds + L_EP, (lane & 15) * 4, __builtin_amdgcn_make_buffer_rsrc((void*)(P.ws + WS_EP), 0, 64, RSRC3), 0);
    }
    const int p = g * 3 + k;
    if (__builtin_expect(!is_cin && ntile >= 0, 1)) dnn_start(P, lds, w, lane, g, ntile);
    if (__builtin_expect(!is_cin && ntile < 0 && p < 26, 1)) prep_start(P, lds, w, lane, p);
    STAMP(20);
    if (__builtin_expect(!is_cin && ntile >= 0, 1)) {
        dnn_role(P, lds, w, lane, g, ntile STAMP_PASS);
    } else if (is_cin) {
        cin_role(P, lds, w - 4, lane, g, m, ntile >= 0, k STAMP_PASS);
        __builtin_amdgcn_s_waitcnt(0x0F70);
    } else {
        if (p < 26) prep_role(P, lds, w, lane, p STAMP_PASS);
        else { asm volatile("s_waitcnt vmcnt(0) lgkmcnt(0)" ::: "memory"); __builtin_amdgcn_s_barrier(); }
        if (w == 0) {
            const unsigned ep = *(const LAS unsigned*)(lds + L_EP + g * 4);
            if (lane == 0) st_granule((gu64*)(P.ws + WS_PART) + ((g * 32 + 26 + k) * 64), ep + 1u, 0u);
        }
        __builtin_amdgcn_s_waitcnt(0x0F70);
    }
    STAMP(11);
    STAMP_FLUSH(P.ws, w, lane);
    asm volatile(".Lxdfm_end:");
}

extern "C" void kernel_launch(void* const* d_in, const int* in_sizes, int n_in,
                              void* d_out, int out_size, void* d_ws, size_t ws_size,
                              hipStream_t stream) {
    KArgs KA;
    KA.tab = make_tab();
    Params& P = KA.P;
    P.idx = (const int*)d_in[0]; P.table = (const unsigned short*)d_in[1]; P.wcin = (const unsigned short*)d_in[2];
    P.wout = (const unsigned short*)d_in[3]; P.w1 = (const unsigned short*)d_in[4]; P.b1 = (const unsigned short*)d_in[5];
    P.w2 = (const unsigned short*)d_in[6]; P.bout = (const unsigned short*)d_in[7];
    P.out = (unsigned short*)d_out; P.ws = (unsigned char*)d_ws;
    xdfm_kernel<<<256, 512, 0, stream>>>(KA);
}
```

```cpp
#include <hip/hip_runtime.h>
#include <stdint.h>

#define DI __device__ __forceinline__
#define GAS __attribute__((address_space(1)))
#define LAS __attribute__((address_space(3)))

typedef short    s16x4 __attribute__((ext_vector_type(4)));
typedef short    s16x8 __attribute__((ext_vector_type(8)));
typedef float    f32x2 __attribute__((ext_vector_type(2)));
typedef float    f32x4 __attribute__((ext_vector_type(4)));
typedef float    f32x16 __attribute__((ext_vector_type(16)));
typedef unsigned u32x2 __attribute__((ext_vector_type(2)));
typedef unsigned u32x4 __attribute__((ext_vector_type(4)));
typedef unsigned u32x16 __attribute__((ext_vector_type(16)));
typedef __bf16   bf16x2 __attribute__((ext_vector_type(2)));
typedef GAS unsigned gu32;
typedef GAS unsigned long long gu64;
typedef LAS unsigned char lds8;

#define MAPPING 1

constexpr int DIN = 832, NBATCH = 1024, NVOC = 100000;
#define A_AFTER_X 1
#define W_EARLY 1
#define A_PF 1
#define CODE_PF 1
#define WC_PF 1
#define POLL_STEP 10
#define CIN_PRIO 2
#define WO_LAST 0
#define S_HINT 0
#define PKMUL 0
#define A_DMA 1
constexpr unsigned RSRC3 = 0x00020000u;
constexpr int AUX_SC1 = 16;
constexpr unsigned SPIN_CAP = 1u << 20;

constexpr unsigned WS_EP = 0;
constexpr unsigned WS_FL = 4096;
constexpr unsigned WS_PART = 8192;
constexpr unsigned WS_A = 8192 + 16 * 32 * 64 * 8;
constexpr unsigned A_BYTES = 46 * 832;

#define XD 6
constexpr unsigned L_W = 0;
constexpr unsigned L_A = 832 * 128;
constexpr unsigned L_X0 = L_A + 4 * XD * 2048;
constexpr unsigned L_F = L_X0 + 4 * 1664;
constexpr unsigned L_DUMMY = L_F + 256;
constexpr unsigned L_EP = L_DUMMY + 256;
constexpr unsigned L_BI = L_EP + 256;
constexpr unsigned L_W2 = L_BI + 256;
constexpr unsigned L_END = L_W2 + 256;
static_assert(4 * XD * 2048 >= A_BYTES && L_END <= 163840, "LDS map");
constexpr unsigned LP_VT = 0;
constexpr unsigned LP_T0 = 2048;
constexpr unsigned LP_T1 = 2048 + 2304;

struct AB { int a, b; };
constexpr AB slot_ab(int t) {
    if (t < 156) { int a = 2 * (t / 12); return {a, (a + t % 12) % 26}; }
    if (t < 170) { int u = t - 156; int a = 2 * (u / 2); return {a, (a + 12 + (u & 1)) % 26}; }
    if (t < 176) { int a = 2 * (7 + t - 170); return {a, (a + 12) % 26}; }
    if (t < 189) return {2 * (t - 176), 26};
    if (t == 189) return {26, 26};
    return {27, 27};
}
constexpr unsigned T_ZERO = 1024, T_CONST = 1025;
struct PrepTab { unsigned v[2][24 * 2 * 8]; };
constexpr PrepTab make_tab() {
    PrepTab tb{};
    for (int m = 0; m < 2; ++m) for (int s = 0; s < 24; ++s) for (int h = 0; h < 2; ++h) for (int e = 0; e < 8; ++e) {
        const int st = m ? 32 : 26;
        int t = 8 * s + e; AB ab = slot_ab(t);
        unsigned o1 = T_ZERO, o2 = T_ZERO;
        if (ab.a == 27) {}
        else if (ab.a == 26) { if (h == 0 && m) o1 = T_CONST; }
        else if (ab.b == 26) { if (m) o1 = ((ab.a + h) % 26) * st + 26; }
        else {
            int u = (ab.a + h) % 26, v = (ab.b + h) % 26;
            bool dup = (t == 169 && h == 1);
            if (dup) {}
            else if (u == v) o1 = u * st + u;
            else { o1 = u * st + v; o2 = v * st + u; }
        }
        tb.v[m][(s * 2 + h) * 8 + e] = o1 | (o2 << 16);
    }
    return tb;
}
static_assert(slot_ab(169).a == 12 && slot_ab(169).b == 25, "dup slot");

struct Params {
    const int* idx; const unsigned short* table; const unsigned short* wcin; const unsigned short* wout;
    const unsigned short* w1; const unsigned short* b1; const unsigned short* w2; const unsigned short* bout;
    unsigned short* out; unsigned char* ws;
};
struct KArgs { Params P; PrepTab tab; };

DI unsigned sel(bool c, unsigned a, unsigned b) { unsigned r = c ? a : b; return r; }
DI float bf_lo(unsigned v) { return __uint_as_float(v << 16); }
DI float bf_hi(unsigned v) { return __uint_as_float(v & 0xffff0000u); }
DI unsigned pk_bf16(float lo, float hi) { f32x2 v = {lo, hi}; return __builtin_bit_cast(unsigned, __builtin_convertvector(v, bf16x2)); }
DI f32x4 mfma16(s16x8 a, s16x8 b, f32x4 c) { return __builtin_amdgcn_mfma_f32_16x16x32_bf16(a, b, c, 0, 0, 0); }
DI f32x16 mfma32(s16x8 a, s16x8 b, f32x16 c) { return __builtin_amdgcn_mfma_f32_32x32x16_bf16(a, b, c, 0, 0, 0); }
DI s16x8 as_frag(u32x4 v) { return __builtin_bit_cast(s16x8, v); }
template <int CTRL, int ROWMASK> DI float dpp_add(float v) {
    return v + __int_as_float(__builtin_amdgcn_update_dpp(0, __float_as_int(v), CTRL, ROWMASK, 0xf, true));
}
DI float wave_sum_to_lane63(float v) {
    v = dpp_add<0x111, 0xf>(v); v = dpp_add<0x112, 0xf>(v); v = dpp_add<0x114, 0xf>(v); v = dpp_add<0x118, 0xf>(v);
    v = dpp_add<0x142, 0xa>(v); v = dpp_add<0x143, 0xc>(v);
    return v;
}
DI float sum_lane_groups(float v) {
    auto a = __builtin_amdgcn_permlane16_swap(__float_as_uint(v), __float_as_uint(v), false, false);
    v = __uint_as_float(a[0]) + __uint_as_float(a[1]);
    auto b = __builtin_amdgcn_permlane32_swap(__float_as_uint(v), __float_as_uint(v), false, false);
    return __uint_as_float(b[0]) + __uint_as_float(b[1]);
}
DI float gelu_tanh(float x) {
    const float k1 = -2.0f * 0.7978845608028654f * 1.4426950408889634f;
    const float k2 = k1 * 0.044715f;
    float arg = x * __builtin_fmaf(x * x, k2, k1);
    float e = __builtin_amdgcn_exp2f(arg);
    return x * __builtin_amdgcn_rcpf(1.0f + e);
}
DI void dma16(unsigned lds_addr, unsigned voff, __amdgpu_buffer_rsrc_t srd, unsigned soff) {
    asm volatile("s_mov_b32 m0, %0\n\ts_nop 0\n\tbuffer_load_dwordx4 %1, %2, %3 offen lds"
                 :: "s"(lds_addr), "v"(voff), "s"(srd), "s"(soff) : "memory");
}
DI void dma16_sc1(unsigned lds_addr, unsigned voff, __amdgpu_buffer_rsrc_t srd, unsigned soff) {
    asm volatile("s_mov_b32 m0, %0\n\ts_nop 0\n\tbuffer_load_dwordx4 %1, %2, %3 offen sc1 lds"
                 :: "s"(lds_addr), "v"(voff), "s"(srd), "s"(soff) : "memory");
}
DI void dma4_sc1(unsigned lds_addr, unsigned voff, __amdgpu_buffer_rsrc_t srd, unsigned soff) {
    asm volatile("s_mov_b32 m0, %0\n\ts_nop 0\n\tbuffer_load_dword %1, %2, %3 offen sc1 lds"
                 :: "s"(lds_addr), "v"(voff), "s"(srd), "s"(soff) : "memory");
}
DI void dma4(unsigned lds_addr, unsigned voff, __amdgpu_buffer_rsrc_t srd, unsigned soff) {
    asm volatile("s_mov_b32 m0, %0\n\ts_nop 0\n\tbuffer_load_dword %1, %2, %3 offen lds"
                 :: "s"(lds_addr), "v"(voff), "s"(srd), "s"(soff) : "memory");
}
DI void st_granule(gu64* g, unsigned tag, unsigned val) {
    __hip_atomic_store(g, ((unsigned long long)tag << 32) | val, __ATOMIC_RELAXED, __HIP_MEMORY_SCOPE_AGENT);
}
DI unsigned ld_agent(const gu32* p) { return __hip_atomic_load(p, __ATOMIC_RELAXED, __HIP_MEMORY_SCOPE_AGENT); }
DI unsigned lds_ld(lds8* l, unsigned off) { return __hip_atomic_load((LAS unsigned*)(l + off), __ATOMIC_RELAXED, __HIP_MEMORY_SCOPE_WORKGROUP); }
DI void lds_st(lds8* l, unsigned off, unsigned v) { __hip_atomic_store((LAS unsigned*)(l + off), v, __ATOMIC_RELAXED, __HIP_MEMORY_SCOPE_WORKGROUP); }
DI void lds_add(lds8* l, unsigned off, unsigned v) { __hip_atomic_fetch_add((LAS unsigned*)(l + off), v, __ATOMIC_RELAXED, __HIP_MEMORY_SCOPE_WORKGROUP); }
DI void sig_add(lds8* l, unsigned off) {
    asm volatile("s_mov_b64 exec, 1\n\tds_add_u32 %0, %1\n\ts_mov_b64 exec, -1" :: "v"((unsigned)(size_t)l + off), "v"(1u) : "memory");
}
DI void sig_set(lds8* l, unsigned off) {
    asm volatile("s_mov_b64 exec, 1\n\tds_write_b32 %0, %1\n\ts_mov_b64 exec, -1" :: "v"((unsigned)(size_t)l + off), "v"(1u) : "memory");
}
#define WAIT_LGKM() asm volatile("s_waitcnt lgkmcnt(0)" ::: "memory")
#define WAIT_VM0() asm volatile("s_waitcnt vmcnt(0)" ::: "memory")
#define CBAR() asm volatile("" ::: "memory")
#define STAMP(i)
#define STAMP_ARG
#define STAMP_PASS
#define STAMP_FLUSH(ws, w, lane)

template <int K> DI unsigned quad_bcast(unsigned v) { return __builtin_amdgcn_update_dpp(0u, v, K * 0x55, 0xf, 0xf, true); }
template <int N> DI void wait_vm() { asm volatile("s_waitcnt vmcnt(%0)" :: "i"(N) : "memory"); }
DI void dma_w4(unsigned lds_addr, unsigned voff, __amdgpu_buffer_rsrc_t srd, unsigned soff) {
    unsigned t;
    asm volatile("s_mov_b32 m0, %1\n\ts_add_u32 %0, %4, 12288\n\tbuffer_load_dwordx4 %2, %3, %4 offen lds\n\t"
                 "buffer_load_dwordx4 %2, %3, %0 offen offset:1024 lds\n\ts_add_u32 %0, %0, 12288\n\t"
                 "buffer_load_dwordx4 %2, %3, %0 offen offset:2048 lds\n\ts_add_u32 %0, %0, 12288\n\t"
                 "buffer_load_dwordx4 %2, %3, %0 offen offset:3072 lds"
                 : "=&s"(t) : "s"(lds_addr), "v"(voff), "s"(srd), "s"(soff) : "memory", "scc");
}
DI void dnn_start(const Params& P, lds8* lds, int w, int lane, int g, int ntile) {
    const int mh = w & 1, kh = w >> 1;
    const unsigned ldsb = (unsigned)(size_t)lds;
    const unsigned ring = ldsb + L_A + w * (XD * 2048);
    const __amdgpu_buffer_rsrc_t srdI = __builtin_amdgcn_make_buffer_rsrc((void*)P.idx, 0, NBATCH * 26 * 4, RSRC3);
    const unsigned vo = ((64 * g + 32 * mh + (lane >> 2)) * 26 + 13 * kh + 4 * (lane & 3)) * 4;
    dma16(ring, vo, srdI, 0);
    dma16(ring + 1024, vo, srdI, 1664);
    if (__builtin_expect(w == 1, 1)) dma4(ldsb + L_BI, ntile * 128 + lane * 4, __builtin_amdgcn_make_buffer_rsrc((void*)P.b1, 0, DIN * 2, RSRC3), 0);
    if (__builtin_expect(w == 2, 1)) dma4(ldsb + L_W2, ntile * 128 + lane * 4, __builtin_amdgcn_make_buffer_rsrc((void*)P.w2, 0, DIN * 2, RSRC3), 0);
    const __amdgpu_buffer_rsrc_t srdW = __builtin_amdgcn_make_buffer_rsrc((void*)P.w1, 0, DIN * DIN * 2, RSRC3);
    const unsigned vw = (lane >> 3) * 1664 + (((lane & 7) ^ (((lane >> 4) & 1) << 2)) << 4);
    const unsigned rowA = 416 * kh + 208 * mh + 104 * (g & 1);
    dma_w4(ldsb + L_W + rowA * 128, vw, srdW, rowA * 1664 + ntile * 128);
    if (W_EARLY > 1) dma_w4(ldsb + L_W + rowA * 128 + 4096, vw, srdW, rowA * 1664 + ntile * 128 + 4 * 13312);
    if (W_EARLY > 2) dma_w4(ldsb + L_W + rowA * 128 + 8192, vw, srdW, rowA * 1664 + ntile * 128 + 8 * 13312);
}
DI void dnn_role(const Params& P, lds8* lds, int w, int lane, int g, int ntile STAMP_ARG) {
    const int mh = w & 1, kh = w >> 1;
    const int h = lane >> 5;
    const unsigned ldsb = (unsigned)(size_t)lds;
    const unsigned ring = ldsb + L_A + w * (XD * 2048);
    const __amdgpu_buffer_rsrc_t srdW = __builtin_amdgcn_make_buffer_rsrc((void*)P.w1, 0, DIN * DIN * 2, RSRC3);
    const unsigned vw = (lane >> 3) * 1664 + (((lane & 7) ^ (((lane >> 4) & 1) << 2)) << 4);
    const unsigned rowA = 416 * kh + 208 * mh + 104 * (g & 1), rowB = 416 * kh + 208 * mh + 104 * ((g & 1) ^ 1);
    auto wdma = [&](unsigned row, int a) {
        const unsigned sb = row * 1664 + ntile * 128, lb = ldsb + L_W + row * 128;
        if (a < 3) dma_w4(lb + a * 4096, vw, srdW, sb + a * 4 * 13312);
        else dma16(lb + 12288, vw, srdW, sb + 12 * 13312);
    };
    STAMP(1);
    wait_vm<4 * W_EARLY>();
    WAIT_LGKM();
    __builtin_amdgcn_s_barrier();
    STAMP(2);
    const u32x4 i0 = *(const LAS u32x4*)(lds + L_A + w * (XD * 2048) + lane * 16);
    const u32x4 i1 = *(const LAS u32x4*)(lds + L_A + w * (XD * 2048) + 1024 + lane * 16);
    const unsigned ep = *(const LAS unsigned*)(lds + L_EP + g * 4);
#pragma unroll
    for (int a = W_EARLY; a < 4; ++a) wdma(rowA, a);
#pragma unroll
    for (int a = 0; a < 4; ++a) wdma(rowB, a);
    const __amdgpu_buffer_rsrc_t srdT = __builtin_amdgcn_make_buffer_rsrc((void*)P.table, 0, NVOC * 64, RSRC3);
    const unsigned c16 = (((lane & 3) ^ ((lane >> 4) & 3)) << 4);
    u32x4 t0 = i0 << 6, t1 = i1 << 6;
    asm volatile("" : "+v"(t0), "+v"(t1));
    auto xload = [&](int s) {
        const unsigned a0 = t0[s & 3], a1 = t1[s & 3];
        const unsigned o0 = (s < 4 ? quad_bcast<0>(a0) : s < 8 ? quad_bcast<1>(a0) : s < 12 ? quad_bcast<2>(a0) : quad_bcast<3>(a0)) + c16;
        const unsigned o1 = (s < 4 ? quad_bcast<0>(a1) : s < 8 ? quad_bcast<1>(a1) : s < 12 ? quad_bcast<2>(a1) : quad_bcast<3>(a1)) + c16;
        dma16(ring + (s % XD) * 2048, o0, srdT, 0);
        dma16(ring + (s % XD) * 2048 + 1024, o1, srdT, 0);
    };
#pragma unroll
    for (int s = 0; s < XD; ++s) xload(s);
    STAMP(3);
    wait_vm<2 * XD>();
    sig_add(lds, L_F + 32 + 4 * kh);
    _Pragma("unroll 1") for (unsigned sp = 0; lds_ld(lds, L_F + 32 + 4 * kh) < 2 && sp < SPIN_CAP; ++sp) __builtin_amdgcn_s_sleep(0);
    CBAR();
    STAMP(4);
    f32x16 acc[2];
    u32x2 bia[4], wv[4];
    float w2f[16];
    unsigned xdone = 0;
#pragma unroll
    for (int r4 = 0; r4 < 4; ++r4) {
        bia[r4] = *(const LAS u32x2*)(lds + L_BI + kh * 64 + h * 8 + r4 * 16);
        wv[r4] = *(const LAS u32x2*)(lds + L_W2 + kh * 64 + h * 8 + r4 * 16);
    }
#pragma unroll
    for (int r4 = 0; r4 < 4; ++r4) {
        acc[0][4 * r4] = bf_lo(bia[r4][0]); acc[0][4 * r4 + 1] = bf_hi(bia[r4][0]); acc[0][4 * r4 + 2] = bf_lo(bia[r4][1]); acc[0][4 * r4 + 3] = bf_hi(bia[r4][1]);
        acc[1][4 * r4] = 0.f; acc[1][4 * r4 + 1] = 0.f; acc[1][4 * r4 + 2] = 0.f; acc[1][4 * r4 + 3] = 0.f;
    }
    {
        const int blk = (lane >> 4) & 1, q = (lane & 15) >> 2, pp = lane & 3, n = lane & 31;
        unsigned ad[2];
#pragma unroll
        for (int j = 0; j < 2; ++j) {
            const unsigned c = 4 * (j ^ kh) + 2 * blk + (pp >> 1);
            ad[j] = L_W + (416 * kh + 8 * h + q) * 128 + ((c ^ (((q >> 1) & 1) << 2)) << 4) + (pp & 1) * 8;
        }
        const unsigned xb = L_A + w * (XD * 2048) + n * 64;
        const unsigned xa0 = xb + ((h ^ ((n >> 2) & 3)) << 4), xa1 = xb + (((2 + h) ^ ((n >> 2) & 3)) << 4);
        u32x4 xf[3][2];
        s16x8 wf[2][4];
        auto xread = [&](int s) {
            xf[s % 3][0] = *(const LAS u32x4*)(lds + xa0 + (s % XD) * 2048);
            xf[s % 3][1] = *(const LAS u32x4*)(lds + xa1 + (s % XD) * 2048);
        };
        auto wread = [&](int s) {
#pragma unroll
            for (int a = 0; a < 2; ++a)
#pragma unroll
                for (int j = 0; j < 2; ++j) {
                    const s16x4 lo = __builtin_amdgcn_ds_read_tr16_b64_v4i16((LAS s16x4*)(lds + ad[j] + s * 4096 + a * 2048));
                    const s16x4 hi = __builtin_amdgcn_ds_read_tr16_b64_v4i16((LAS s16x4*)(lds + ad[j] + s * 4096 + a * 2048 + 512));
                    wf[s & 1][2 * a + j] = __builtin_shufflevector(lo, hi, 0, 1, 2, 3, 4, 5, 6, 7);
                }
        };
        wread(0);
        wait_vm<2 * (XD - 1)>();
        xread(0);
        wait_vm<2 * (XD - 2)>();
        xread(1);
        __builtin_amdgcn_sched_barrier(0);
#pragma unroll
        for (int s = 0; s < 13; ++s) {
            if (s + 1 < 13) wread(s + 1);
#pragma unroll
            for (int a = 0; a < 2; ++a)
#pragma unroll
                for (int j = 0; j < 2; ++j) acc[j] = mfma32(wf[s & 1][2 * a + j], as_frag(xf[s % 3][a]), acc[j]);
            if (s + 1 < 13) {
#pragma unroll
                for (int j = 0; j < 4; ++j) { __builtin_amdgcn_sched_group_barrier(0x100, 2, 0); __builtin_amdgcn_sched_group_barrier(0x8, 1, 0); }
            }
            __builtin_amdgcn_sched_barrier(0);
            if (s + XD < 13) xload(s + XD);
            if (s == 11) {
                xdone = lds_ld(lds, L_F + 52);
#pragma unroll
                for (int r4 = 0; r4 < 4; ++r4) { w2f[4 * r4] = bf_lo(wv[r4][0]); w2f[4 * r4 + 1] = bf_hi(wv[r4][0]); w2f[4 * r4 + 2] = bf_lo(wv[r4][1]); w2f[4 * r4 + 3] = bf_hi(wv[r4][1]); }
#pragma unroll
                for (int e = 0; e < 16; ++e) asm volatile("" : "+v"(w2f[e]));
            }
            if (s + 2 < 13) {
                constexpr int LASTI = 12;
                const int last = s + XD < LASTI ? s + XD : LASTI;
                const int younger = last - (s + 2);
                if (younger >= 4) wait_vm<8>(); else if (younger == 3) wait_vm<6>(); else if (younger == 2) wait_vm<4>(); else if (younger == 1) wait_vm<2>(); else wait_vm<0>();
                if (s + 2 == 12) sig_add(lds, L_F + 52);
                xread(s + 2);
            }
            __builtin_amdgcn_sched_barrier(0);
        }
    }
    STAMP(6);
    {
        if (xdone < 4) _Pragma("unroll 1") for (unsigned sp = 0; lds_ld(lds, L_F + 52) < 4 && sp < SPIN_CAP; ++sp) __builtin_amdgcn_s_sleep(0);
        CBAR();
        lds8* xw = lds + L_W + kh * 53248 + (5 + mh) * 4096 + lane * 16;
#pragma unroll
        for (int r4 = 0; r4 < 4; ++r4) *(LAS f32x4*)(xw + r4 * 1024) = (f32x4){acc[1][4 * r4], acc[1][4 * r4 + 1], acc[1][4 * r4 + 2], acc[1][4 * r4 + 3]};
        CBAR();
        sig_set(lds, L_F + 4 * w);
        _Pragma("unroll 1") for (unsigned sp = 0; lds_ld(lds, L_F + 4 * (w ^ 2)) == 0 && sp < SPIN_CAP; ++sp) __builtin_amdgcn_s_sleep(0);
        CBAR();
        const lds8* xr = lds + L_W + (kh ^ 1) * 53248 + (5 + mh) * 4096 + lane * 16;
        f32x4 o[4];
#pragma unroll
        for (int r4 = 0; r4 < 4; ++r4) o[r4] = *(const LAS f32x4*)(xr + r4 * 1024);
        asm volatile("" : "+v"(o[0]), "+v"(o[1]), "+v"(o[2]), "+v"(o[3]));
#pragma unroll
        for (int r4 = 0; r4 < 4; ++r4) {
            acc[0][4 * r4] += o[r4][0]; acc[0][4 * r4 + 1] += o[r4][1]; acc[0][4 * r4 + 2] += o[r4][2]; acc[0][4 * r4 + 3] += o[r4][3];
        }
    }
    STAMP(7);
    float s0 = 0.f, s1 = 0.f;
#pragma unroll
    for (int r4 = 0; r4 < 4; ++r4) {
        s0 = __builtin_fmaf(gelu_tanh(acc[0][4 * r4]), w2f[4 * r4], s0);
        s1 = __builtin_fmaf(gelu_tanh(acc[0][4 * r4 + 1]), w2f[4 * r4 + 1], s1);
        s0 = __builtin_fmaf(gelu_tanh(acc[0][4 * r4 + 2]), w2f[4 * r4 + 2], s0);
        s1 = __builtin_fmaf(gelu_tanh(acc[0][4 * r4 + 3]), w2f[4 * r4 + 3], s1);
    }
    s0 += s1;
    const auto sw = __builtin_amdgcn_permlane32_swap(__float_as_uint(s0), __float_as_uint(s0), false, false);
    const float part = __uint_as_float(sw[0]) + __uint_as_float(sw[1]);
    STAMP(8);
    if (lane < 32) st_granule((gu64*)(P.ws + WS_PART) + ((g * 32 + 2 * ntile + kh) * 64 + 32 * mh + lane), ep + 1u, __float_as_uint(part));
}

constexpr unsigned LP_W2 = 12288;
constexpr unsigned LP_WO = 14336;
constexpr unsigned LP_DM = 15360;
constexpr unsigned LP_W3 = 16384;
static_assert(LP_T1 + 4 * 1026 <= LP_W2, "prep scratch");
constexpr unsigned w3_slot(int j) { return j * 1280 + (j & 1) * 64 + ((j >> 2) & 1) * 128; }
DI void dma16_add(unsigned lds_base, unsigned lds_off, unsigned voff, __amdgpu_buffer_rsrc_t srd, unsigned sbase, unsigned soff) {
    unsigned t;
    asm volatile("s_add_u32 m0, %1, %2\n\ts_add_u32 %0, %5, %6\n\tbuffer_load_dwordx4 %3, %4, %0 offen lds"
                 : "=&s"(t) : "s"(lds_base), "i"(lds_off), "v"(voff), "s"(srd), "s"(sbase), "i"(soff) : "memory", "scc");
}
DI void prep_start(const Params& P, lds8* lds, int w, int lane, int p) {
    const unsigned ldsb = (unsigned)(size_t)lds;
    const __amdgpu_buffer_rsrc_t srdC = __builtin_amdgcn_make_buffer_rsrc((void*)P.wcin, 0, 3 * 17576 * 2, RSRC3);
    const int i0 = w == 0 ? 0 : w == 1 ? 6 : w == 2 ? 14 : 20;
    dma4(ldsb + LP_WO + w * 256, lane * 4, __builtin_amdgcn_make_buffer_rsrc((void*)P.wout, 0, 78 * 2, RSRC3), 0);
    {
        const unsigned hp = lane >> 5;
        const unsigned vo = hp * 1352 + ((lane >> 2) & 7) * 52 + (((lane & 3) ^ (hp << 1)) << 4);
        const unsigned lb = ldsb + LP_W3 + w * 16640, sb = 52 * 1352 + 52 * i0;
        dma16_add(lb, w3_slot(0), vo, srdC, sb, 0 * 2704);  dma16_add(lb, w3_slot(1), vo, srdC, sb, 1 * 2704);
        dma16_add(lb, w3_slot(2), vo, srdC, sb, 2 * 2704);  dma16_add(lb, w3_slot(3), vo, srdC, sb, 3 * 2704);
        dma16_add(lb, w3_slot(4), vo, srdC, sb, 4 * 2704);  dma16_add(lb, w3_slot(5), vo, srdC, sb, 5 * 2704);
        dma16_add(lb, w3_slot(6), vo, srdC, sb, 6 * 2704);  dma16_add(lb, w3_slot(7), vo, srdC, sb, 7 * 2704);
        dma16_add(lb, w3_slot(8), vo, srdC, sb, 8 * 2704);  dma16_add(lb, w3_slot(9), vo, srdC, sb, 9 * 2704);
        dma16_add(lb, w3_slot(10), vo, srdC, sb, 10 * 2704); dma16_add(lb, w3_slot(11), vo, srdC, sb, 11 * 2704);
        dma16_add(lb, w3_slot(12), vo, srdC, sb, 12 * 2704);
    }
    if (__builtin_expect(w < 2, 1)) {
        const unsigned vo = w == 0 ? 35152 + (lane >> 2) * 1352 + p * 52 + (lane & 3) * 16 : p * 1352 + lane * 16;
        const unsigned lb = ldsb + (w == 0 ? LP_W2 : LP_T0);
        dma16(lb, vo, srdC, 0);
        dma16(lb + 1024, vo, srdC, w == 0 ? 16 * 1352 : 1024);
    }
}
DI void prep_role(const Params& P, lds8* lds, int w, int lane, int p STAMP_ARG) {
    const int n = lane & 15, gq = lane >> 4;
    const int i0 = w == 0 ? 0 : w == 1 ? 6 : w == 2 ? 14 : 20;
    const int nact = w == 0 ? 48 : 44;
    const int ch = lane < nact ? lane : 0;
    const int cs = ch >> 1, chh = ch & 1;
    const GAS u32x4* tp = (const GAS u32x4*)((const GAS unsigned char*)(size_t)__builtin_amdgcn_kernarg_segment_ptr() + sizeof(Params) + ((w == 0 ? 384 : 0) + (cs * 2 + chh) * 8) * 4);
    const u32x4 ta = tp[0], tb = tp[1];
    STAMP(1);
    const int q = (lane & 15) >> 2, pp = lane & 3;
    int r0 = 8 * gq + q, r1 = r0 + 4;
    r0 = r0 < 26 ? r0 : 25; r1 = r1 < 26 ? r1 : 25;
    unsigned b0a, b0b, b1a, b1b;
    {
        const unsigned wb = LP_W3 + w * 16640 + pp * 8;
        const unsigned j0 = r0 >> 1, j1 = r1 >> 1;
        const unsigned s0 = wb + j0 * 1280 + (j0 & 1) * 64 + ((j0 >> 2) & 1) * 128 + (r0 & 1) * 512;
        const unsigned s1 = wb + j1 * 1280 + (j1 & 1) * 64 + ((j1 >> 2) & 1) * 128 + (r1 & 1) * 512;
        b0a = s0 + (r0 & 1) * 32; b0b = s0 + 32 - (r0 & 1) * 32; b1a = s1 + (r1 & 1) * 32; b1b = s1 + 32 - (r1 & 1) * 32;
    }
    unsigned va = LP_VT + n * 64 + 2 * i0, vb = n < 10 ? va + 1024 : LP_DM;
    unsigned vl = w == 3 ? LP_DM : 0;
    unsigned ac = LP_T1 + 4 * (4 * gq * 32 + n);
    unsigned a20 = LP_W2 + r0 * 64 + pp * 8, a21 = LP_W2 + r1 * 64 + pp * 8, abv = LP_VT + n * 64 + gq * 16;
    asm volatile("" : "+v"(b0a), "+v"(b0b), "+v"(b1a), "+v"(b1b), "+v"(va), "+v"(vb), "+v"(ac), "+v"(a20), "+v"(a21), "+v"(abv));
    if (w == 3) {
        LAS unsigned* z = (LAS unsigned*)(lds + LP_VT + (lane < 27 ? lane : 31) * 64 + 52);
        z[0] = 0; z[1] = 0; z[2] = 0;
        *(LAS unsigned*)(lds + LP_T1 + 4 * T_ZERO) = 0;
    }
    if (w == 1) *(LAS unsigned*)(lds + LP_T0 + 2 * T_ZERO) = 0;
    asm volatile("s_waitcnt vmcnt(0) lgkmcnt(0)" ::: "memory");
    __builtin_amdgcn_s_barrier();
    STAMP(2);
    {
        const u32x4 wo3 = *(const LAS u32x4*)(lds + LP_WO + w * 256 + 104 + 16 * gq);
        s16x8 bfr[16];
#pragma unroll
        for (int t = 0; t < 16; ++t) {
            const int ii = t >> 1, u = t & 1;
            const s16x4 lo = __builtin_amdgcn_ds_read_tr16_b64_v4i16((LAS s16x4*)(lds + (u ? b0b : b0a) + ii * 64));
            const s16x4 hi = __builtin_amdgcn_ds_read_tr16_b64_v4i16((LAS s16x4*)(lds + (u ? b1b : b1a) + ii * 64));
            bfr[t] = __builtin_shufflevector(lo, hi, 0, 1, 2, 3, 4, 5, 6, 7);
        }
        if (w == 3) {
            const unsigned o2 = *(const LAS unsigned*)(lds + LP_WO + 768 + 52 + (lane < 13 ? lane : 0) * 4);
            const unsigned o1 = *(const LAS unsigned short*)(lds + LP_WO + 768 + p * 2);
            *(LAS unsigned*)(lds + (lane < 13 ? LP_VT + 26 * 64 + lane * 4 : LP_DM)) = o2;
            *(LAS float*)(lds + LP_T1 + 4 * T_CONST) = bf_lo(o1);
        }
        float res[16];
#pragma unroll
        for (int t = 0; t < 16; ++t) res[t] = mfma16(as_frag(wo3), bfr[t], (f32x4){0.f, 0.f, 0.f, 0.f})[0];
#pragma unroll
        for (int k2 = 0; k2 < 4; ++k2) {
            const unsigned pa0 = pk_bf16(res[4 * k2], res[4 * k2 + 2]), pb0 = pk_bf16(res[4 * k2 + 1], res[4 * k2 + 3]);
            if (k2 < 3) { *(LAS unsigned*)(lds + va + 4 * k2) = pa0; *(LAS unsigned*)(lds + vb + 4 * k2) = pb0; }
            else { *(LAS unsigned*)(lds + (vl ? vl : va + 12)) = pa0; *(LAS unsigned*)(lds + (vl ? vl : vb + 12)) = pb0; }
        }
    }
    CBAR();
    sig_add(lds, L_F + 28);
    if (w > 1) return;
    STAMP(3);
    const __amdgpu_buffer_rsrc_t srdA = __builtin_amdgcn_make_buffer_rsrc((void*)(P.ws + WS_A), 0, A_BYTES, RSRC3);
    u32x4 o;
    if (w == 1) {
        float pv[16];
#pragma unroll
        for (int e = 0; e < 8; ++e) {
            const unsigned x = e < 4 ? ta[e] : tb[e - 4];
            pv[2 * e] = bf_lo(*(const LAS unsigned short*)(lds + LP_T0 + 2 * (x & 0xffff)));
            pv[2 * e + 1] = bf_lo(*(const LAS unsigned short*)(lds + LP_T0 + 2 * (x >> 16)));
        }
#pragma unroll
        for (int j = 0; j < 4; ++j) o[j] = pk_bf16(pv[4 * j] + pv[4 * j + 1], pv[4 * j + 2] + pv[4 * j + 3]);
    } else {
        s16x8 a2[2];
#pragma unroll
        for (int mt = 0; mt < 2; ++mt) {
            const s16x4 lo = __builtin_amdgcn_ds_read_tr16_b64_v4i16((LAS s16x4*)(lds + a20 + mt * 32));
            const s16x4 hi = __builtin_amdgcn_ds_read_tr16_b64_v4i16((LAS s16x4*)(lds + a21 + mt * 32));
            a2[mt] = __builtin_shufflevector(lo, hi, 0, 1, 2, 3, 4, 5, 6, 7);
        }
        unsigned pa[16];
#pragma unroll
        for (int e = 0; e < 8; ++e) {
            const unsigned x = e < 4 ? ta[e] : tb[e - 4];
            pa[2 * e] = LP_T1 + 4 * (x & 0xffff); pa[2 * e + 1] = LP_T1 + 4 * (x >> 16);
        }
#pragma unroll
        for (int e = 0; e < 16; ++e) asm volatile("" : "+v"(pa[e]));
        _Pragma("unroll 1") for (unsigned sp = 0; lds_ld(lds, L_F + 28) < 4 && sp < SPIN_CAP; ++sp) __builtin_amdgcn_s_sleep(0);
        CBAR();
        const u32x4 bf0 = *(const LAS u32x4*)(lds + abv), bf1 = *(const LAS u32x4*)(lds + abv + 1024);
        const f32x4 z4 = {0.f, 0.f, 0.f, 0.f};
        const f32x4 d00 = mfma16(a2[0], as_frag(bf0), z4), d01 = mfma16(a2[1], as_frag(bf0), z4);
        const f32x4 d10 = mfma16(a2[0], as_frag(bf1), z4), d11 = mfma16(a2[1], as_frag(bf1), z4);
#pragma unroll
        for (int e = 0; e < 4; ++e) {
            *(LAS float*)(lds + ac + e * 128) = d00[e]; *(LAS float*)(lds + ac + 2048 + e * 128) = d01[e];
            *(LAS float*)(lds + ac + 64 + e * 128) = d10[e]; *(LAS float*)(lds + ac + 2048 + 64 + e * 128) = d11[e];
        }
        STAMP(4);
        float pv[16];
#pragma unroll
        for (int e = 0; e < 16; ++e) pv[e] = *(const LAS float*)(lds + pa[e]);
#pragma unroll
        for (int j = 0; j < 4; ++j) o[j] = pk_bf16(pv[4 * j] + pv[4 * j + 1], pv[4 * j + 2] + pv[4 * j + 3]);
    }
    {
        const int fr = w == 0 ? (cs < 22 ? 2 * cs : 22 + cs) : 2 * cs + 1;
        __builtin_amdgcn_raw_buffer_store_b128(o, srdA, lane < nact ? (unsigned)(fr * 832 + (26 * chh + p) * 16) : 0x7ffffff0u, 0, AUX_SC1);
    }
    STAMP(5);
    WAIT_VM0();
    STAMP(6);
    if (w == 1) { sig_set(lds, L_F + 24); return; }
    const unsigned tagv = *(const LAS unsigned*)(lds + L_EP + (lane & 15) * 4);
    _Pragma("unroll 1") for (unsigned sp = 0; lds_ld(lds, L_F + 24) == 0 && sp < SPIN_CAP; ++sp) __builtin_amdgcn_s_sleep(0);
    if (lane < 16) __hip_atomic_store((gu32*)(P.ws + WS_FL) + lane * 32 + p, tagv + 1u, __ATOMIC_RELAXED, __HIP_MEMORY_SCOPE_AGENT);
}

constexpr unsigned A_PER = A_BYTES / 4;
DI void cin_role(const Params& P, lds8* lds, int cw, int lane, int g, int m, bool has_dnn, int pf_slot STAMP_ARG) {
    const int d = lane & 31, h = lane >> 5;
    const int b = 64 * g + 4 * m + cw;
    const unsigned ldsb = (unsigned)(size_t)lds;
    {
        const __amdgpu_buffer_rsrc_t srdT = __builtin_amdgcn_make_buffer_rsrc((void*)P.table, 0, NVOC * 64, RSRC3);
        const unsigned lb = ldsb + L_X0 + cw * 1664;
        WAIT_VM0();
        const int q = lane >> 2;
        const unsigned j0 = *(const LAS unsigned*)(lds + L_X0 + cw * 1664 + q * 4);
        const unsigned j1 = *(const LAS unsigned*)(lds + L_X0 + cw * 1664 + (q < 10 ? q + 16 : 25) * 4);
        unsigned o0 = j0 * 64 + (lane & 3) * 16, o1 = j1 * 64 + (lane & 3) * 16;
        asm volatile("" : "+v"(o0), "+v"(o1));
        STAMP(1);
        dma16(lb, o0, srdT, 0);
        if (lane < 40) dma16(lb + 1024, o1, srdT, 0);
    }
    const unsigned bo = P.bout[0];
    STAMP(2);
    __builtin_amdgcn_s_setprio(0);
    CBAR();
    __builtin_amdgcn_s_barrier();
    const unsigned epv = *(const LAS unsigned*)(lds + L_EP + g * 4);
    unsigned bov = bo;
    asm volatile("s_waitcnt vmcnt(0)" : "+v"(bov) :: "memory");
    STAMP(3);
    float x[26];
    {
        const lds8* xb = lds + L_X0 + cw * 1664 + h * 64 + d * 2;
        const lds8* xl = lds + L_X0 + cw * 1664 + (h ? 0 : 25 * 64) + d * 2;
#pragma unroll
        for (int a = 0; a < 13; ++a) {
            const unsigned lo = *(const LAS unsigned short*)(xb + 2 * a * 64);
            const unsigned hi = a < 12 ? *(const LAS unsigned short*)(xb + (2 * a + 1) * 64) : *(const LAS unsigned short*)xl;
            x[2 * a] = bf_lo(lo); x[2 * a + 1] = bf_lo(hi);
        }
    }
    u32x4 z[24];
#pragma unroll
    for (int s = 0; s < 22; ++s) {
        float fa[8], fb[8];
#pragma unroll
        for (int e = 0; e < 8; ++e) { const AB pe = slot_ab(8 * s + e); fa[e] = x[pe.a]; fb[e] = x[pe.b]; }
        float t0, t1, t2, t3, t4, t5;
        unsigned o0, o1, o2, o3;
        asm("v_mul_f32 %4, %10, %11\n\tv_mul_f32 %5, %12, %13\n\tv_mul_f32 %6, %14, %15\n\tv_mul_f32 %7, %16, %17\n\t"
            "v_cvt_pk_bf16_f32 %0, %4, %5\n\tv_mul_f32 %8, %18, %19\n\tv_mul_f32 %9, %20, %21\n\tv_cvt_pk_bf16_f32 %1, %6, %7\n\t"
            "v_mul_f32 %4, %22, %23\n\tv_mul_f32 %5, %24, %25\n\tv_cvt_pk_bf16_f32 %2, %8, %9\n\tv_cvt_pk_bf16_f32 %3, %4, %5"
            : "=&v"(o0), "=&v"(o1), "=&v"(o2), "=v"(o3), "=&v"(t0), "=&v"(t1), "=&v"(t2), "=&v"(t3), "=&v"(t4), "=&v"(t5)
            : "v"(fa[0]), "v"(fb[0]), "v"(fa[1]), "v"(fb[1]), "v"(fa[2]), "v"(fb[2]), "v"(fa[3]), "v"(fb[3]),
              "v"(fa[4]), "v"(fb[4]), "v"(fa[5]), "v"(fb[5]), "v"(fa[6]), "v"(fb[6]), "v"(fa[7]), "v"(fb[7]));
        z[s] = (u32x4){o0, o1, o2, o3};
    }
#pragma unroll
    for (int s = 22; s < 24; ++s) {
#pragma unroll
        for (int j = 0; j < 4; ++j) {
            const AB p0 = slot_ab(8 * s + 2 * j), p1 = slot_ab(8 * s + 2 * j + 1);
            const float a0 = p0.a < 26 ? x[p0.a] : (p0.a == 26 ? 1.f : 0.f);
            const float a1 = p1.a < 26 ? x[p1.a] : (p1.a == 26 ? 1.f : 0.f);
            z[s][j] = pk_bf16(a0, a1);
        }
    }
#pragma unroll
    for (int s = 0; s < 24; ++s) asm volatile("" : "+v"(z[s]));
    STAMP(4);
    const unsigned tag = epv + 1u;
    {
        const __amdgpu_buffer_rsrc_t srdA = __builtin_amdgcn_make_buffer_rsrc((void*)(P.ws + WS_A), 0, A_BYTES, RSRC3);
        if (cw == 0) {
            if (has_dnn) _Pragma("unroll 1") for (unsigned sp = 0; lds_ld(lds, L_F + 52) < 4 && sp < SPIN_CAP; ++sp) __builtin_amdgcn_s_sleep(1);
            const gu32* fl = (const gu32*)(P.ws + WS_FL) + g * 32 + (lane < 26 ? lane : 0);
            _Pragma("unroll 1") for (unsigned sp = 0; sp < SPIN_CAP; ++sp) { if (__all(ld_agent(fl) == tag)) break; __builtin_amdgcn_s_sleep(1); }
            sig_set(lds, L_F + 16);
            if (!has_dnn) dma4_sc1(ldsb + L_DUMMY, lane < 50 ? (unsigned)((((g & 1) * 3 + pf_slot) * 50 + lane) * 128) : 0x7fffff00u, srdA, 0);
        } else {
            _Pragma("unroll 1") for (unsigned sp = 0; lds_ld(lds, L_F + 16) == 0 && sp < SPIN_CAP; ++sp) __builtin_amdgcn_s_sleep(0);
        }
        CBAR();
        __builtin_amdgcn_s_setprio(CIN_PRIO);
        STAMP(5);
        {
            const unsigned l1 = ldsb + L_W + cw * 4096, vo = lane * 16;
            asm volatile("s_mov_b32 m0, %0\n\ts_nop 0\n\t"
                         "buffer_load_dwordx4 %1, %2, %3 offen sc1 lds\n\tbuffer_load_dwordx4 %1, %2, %3 offen offset:1024 sc1 lds\n\t"
                         "buffer_load_dwordx4 %1, %2, %3 offen offset:2048 sc1 lds\n\tbuffer_load_dwordx4 %1, %2, %3 offen offset:3072 sc1 lds"
                         :: "s"(l1), "v"(vo), "s"(srdA), "s"(cw * 4096) : "memory");
#pragma unroll
            for (int i = 0; i < 6; ++i) {
                const int q = 6 * cw + i;
                const unsigned go = q < 4 ? 16384 + 1024 * q : 19968 + 1024 * (q - 4), lo = q < 4 ? 16384 + 1024 * q : 53248 + 1024 * (q - 4);
                dma16_sc1(ldsb + L_W + lo, vo, srdA, go);
            }
        }
        wait_vm<6>();
        sig_add(lds, L_F + 20);
        _Pragma("unroll 1") for (unsigned sp = 0; lds_ld(lds, L_F + 20) < 4 && sp < SPIN_CAP; ++sp) __builtin_amdgcn_s_sleep(0);
        CBAR();
    }
    STAMP(6);
    const gu64* pp = (const gu64*)(P.ws + WS_PART) + ((g * 32 + (lane < 26 ? lane : 0)) * 64 + 4 * m + cw);
    unsigned long long pv = 0;
    f32x16 acc0, acc1;
#pragma unroll
    for (int i = 0; i < 16; ++i) { acc0[i] = 0.f; acc1[i] = 0.f; }
    {
        unsigned abo = L_W + (26 * h + d) * 16;
        asm volatile("" : "+v"(abo));
        const lds8* ab = lds + abo;
        constexpr int PF = 6;
        u32x4 af[46];
        unsigned cnt2 = 0;
        auto frag_of = [](int t) { return t < 24 ? t : t + 40; };
#pragma unroll
        for (int t = 0; t < PF; ++t) af[t] = *(const LAS u32x4*)(ab + frag_of(t) * 832);
        __builtin_amdgcn_sched_barrier(0);
#pragma unroll
        for (int t = 0; t < 46; ++t) {
            if (t == 9) { wait_vm<0>(); sig_add(lds, L_F + 44); }
            if (t + PF == 19 - 2) cnt2 = lds_ld(lds, L_F + 44);
            if (t + PF == 19) {
                if (cnt2 < 4) _Pragma("unroll 1") for (unsigned sp = 0; lds_ld(lds, L_F + 44) < 4 && sp < SPIN_CAP; ++sp) __builtin_amdgcn_s_sleep(0);
                CBAR();
            }
            if (t + PF < 46) af[t + PF] = *(const LAS u32x4*)(ab + frag_of(t + PF) * 832);
            if (t < 44) {
                if (t & 1) acc0 = mfma32(as_frag(af[t]), as_frag(z[t >> 1]), acc0);
                else acc1 = mfma32(as_frag(af[t]), as_frag(z[t >> 1]), acc1);
            } else acc1 = mfma32(as_frag(af[t]), as_frag(z[t - 22]), acc1);
            if (t == 30) pv = __hip_atomic_load(pp, __ATOMIC_RELAXED, __HIP_MEMORY_SCOPE_AGENT);
            __builtin_amdgcn_sched_barrier(0);
        }
    }
    float sa = 0.f;
#pragma unroll
    for (int i = 0; i < 12; ++i) sa = __builtin_fmaf(acc0[i], acc1[i], sa);
    const float sb = __builtin_fmaf(acc0[12], acc1[12], acc0[13] * acc1[13]);
    float tot = h == 0 ? sa + sb : sa;
    asm volatile("" : "+v"(tot));
    STAMP(7);
    _Pragma("unroll 1") for (unsigned sp = 0; sp < SPIN_CAP; ++sp) {
        if (__all((unsigned)(pv >> 32) == tag)) break;
        pv = __hip_atomic_load(pp, __ATOMIC_RELAXED, __HIP_MEMORY_SCOPE_AGENT);
    }
    STAMP(8);
    if (lane < 26) tot += __uint_as_float((unsigned)pv);
    tot = wave_sum_to_lane63(tot);
    if (lane == 63) {
        const float o = tot + bf_lo(bov);
        P.out[b] = (unsigned short)(pk_bf16(o, o) & 0xffffu);
    }
    if (m == 0 && cw == 0) {
        const gu64* ap = (const gu64*)(P.ws + WS_PART) + ((g * 32 + 26 + (lane < 3 ? lane : 0)) * 64);
        _Pragma("unroll 1") for (unsigned sp = 0; sp < SPIN_CAP; ++sp) {
            const unsigned long long av = __hip_atomic_load(ap, __ATOMIC_RELAXED, __HIP_MEMORY_SCOPE_AGENT);
            if (__all((unsigned)(av >> 32) == tag)) break;
        }
        if (lane == 0) __hip_atomic_store((gu32*)(P.ws + WS_EP) + g, tag, __ATOMIC_RELAXED, __HIP_MEMORY_SCOPE_AGENT);
    }
}

DI void decode_block(int bid, int& g, int& m, int& ntile, int& k) {
    const int x = bid & 7, j = bid >> 3;
    g = 2 * x + (j >> 4); m = j & 15;
    ntile = m < 13 ? m : -1; k = m - 13;
}

__global__ void __launch_bounds__(512, 2) xdfm_kernel(KArgs KA) {
    const Params& P = KA.P;
    __shared__ __attribute__((aligned(16))) unsigned char smem[L_END];
    lds8* lds = (lds8*)smem;
    STAMP(0);
    const int w = __builtin_amdgcn_readfirstlane(threadIdx.x >> 6), lane = threadIdx.x & 63;
    {
        unsigned long long pc; unsigned len;
        asm volatile("s_getpc_b64 %0\n.Lxdfm_pc:\n\ts_mov_b32 %1, (.Lxdfm_end-.Lxdfm_pc)&4294967295" : "=s"(pc), "=s"(len));
        const __amdgpu_buffer_rsrc_t srdP = __builtin_amdgcn_make_buffer_rsrc((void*)pc, 0, len, RSRC3);
        dma4((unsigned)(size_t)lds + L_DUMMY, lane == 0 ? ((blockIdx.x >> 3) * 8 + w) * 128 : 0x7fffff00u, srdP, 0);
    }
    asm volatile("" :: "s"(P.idx), "s"(P.table), "s"(P.wcin), "s"(P.wout), "s"(P.w1), "s"(P.b1), "s"(P.w2), "s"(P.bout), "s"(P.out), "s"(P.ws));
    STAMP(9);
    int g, m, ntile, k;
    decode_block(blockIdx.x, g, m, ntile, k);
    const bool is_cin = w >= 4;
    if (__builtin_expect(is_cin, 1)) {
        __builtin_amdgcn_s_setprio(3);
        dma16((unsigned)(size_t)lds + L_X0 + (w - 4) * 1664, lane < 7 ? (64 * g + 4 * m + w - 4) * 104 + lane * 16 : 0x7fffff00u,
              __builtin_amdgcn_make_buffer_rsrc((void*)P.idx, 0, NBATCH * 26 * 4, RSRC3), 0);
        if (__builtin_expect(w == 7, 1)) dma4((unsigned)(size_t)lds + L_DUMMY, lane < 26 ? ((blockIdx.x >> 3) * 26 + lane) * 128 : 0x7fffff00u,
                         __builtin_amdgcn_make_buffer_rsrc((void*)P.wcin, 0, 3 * 17576 * 2, RSRC3), 0);
    }
    if (__builtin_expect(w == 0, 1)) {
        if (lane < 16) lds_st(lds, L_F + 4 * lane, 0);
        dma4_sc1((unsigned)(size_t)lds + L_EP, (lane & 15) * 4, __builtin_amdgcn_make_buffer_rsrc((void*)(P.ws + WS_EP), 0, 64, RSRC3), 0);
    }
    const int p = g * 3 + k;
    if (__builtin_expect(!is_cin && ntile >= 0, 1)) dnn_start(P, lds, w, lane, g, ntile);
    if (__builtin_expect(!is_cin && ntile < 0 && p < 26, 1)) prep_start(P, lds, w, lane, p);
    STAMP(10);
    if (__builtin_expect(!is_cin && ntile >= 0, 1)) {
        dnn_role(P, lds, w, lane, g, ntile STAMP_PASS);
    } else if (is_cin) {
        cin_role(P, lds, w - 4, lane, g, m, ntile >= 0, k STAMP_PASS);
        __builtin_amdgcn_s_waitcnt(0x0F70);
    } else {
        if (p < 26) prep_role(P, lds, w, lane, p STAMP_PASS);
        else { asm volatile("s_waitcnt vmcnt(0) lgkmcnt(0)" ::: "memory"); __builtin_amdgcn_s_barrier(); }
        if (w == 0) {
            const unsigned ep = *(const LAS unsigned*)(lds + L_EP + g * 4);
            if (lane == 0) st_granule((gu64*)(P.ws + WS_PART) + ((g * 32 + 26 + k) * 64), ep + 1u, 0u);
        }
        __builtin_amdgcn_s_waitcnt(0x0F70);
    }
    STAMP(11);
    STAMP_FLUSH(P.ws, w, lane);
    asm volatile(".Lxdfm_end:");
}

extern "C" void kernel_launch(void* const* d_in, const int* in_sizes, int n_in,
                              void* d_out, int out_size, void* d_ws, size_t ws_size,
                              hipStream_t stream) {
    KArgs KA;
    KA.tab = make_tab();
    Params& P = KA.P;
    P.idx = (const int*)d_in[0]; P.table = (const unsigned short*)d_in[1]; P.wcin = (const unsigned short*)d_in[2];
    P.wout = (const unsigned short*)d_in[3]; P.w1 = (const unsigned short*)d_in[4]; P.b1 = (const unsigned short*)d_in[5];
    P.w2 = (const unsigned short*)d_in[6]; P.bout = (const unsigned short*)d_in[7];
    P.out = (unsigned short*)d_out; P.ws = (unsigned char*)d_ws;
    xdfm_kernel<<<256, 512, 0, stream>>>(KA);
}
```

```cpp
#define SKEW_SEL 8
#define SKEW_K 4
#include <hip/hip_runtime.h>
#include <stdint.h>

#define DI __device__ __forceinline__
#define GAS __attribute__((address_space(1)))
#define LAS __attribute__((address_space(3)))

typedef short    s16x4 __attribute__((ext_vector_type(4)));
typedef short    s16x8 __attribute__((ext_vector_type(8)));
typedef float    f32x2 __attribute__((ext_vector_type(2)));
typedef float    f32x4 __attribute__((ext_vector_type(4)));
typedef float    f32x16 __attribute__((ext_vector_type(16)));
typedef unsigned u32x2 __attribute__((ext_vector_type(2)));
typedef unsigned u32x4 __attribute__((ext_vector_type(4)));
typedef __bf16   bf16x2 __attribute__((ext_vector_type(2)));
typedef GAS unsigned gu32;
typedef GAS unsigned long long gu64;
typedef LAS unsigned char lds8;

#define MAPPING 1

constexpr int DIN = 832, NBATCH = 1024, NVOC = 100000;
#define A_AFTER_X 1
#define W_EARLY 1
#define A_PF 1
#define CODE_PF 1
#define WC_PF 1
#define POLL_STEP 10
#define CIN_PRIO 2
#define WO_LAST 0
#define EARLY_FL 1
#define GATE_STEP 10
#define PKMUL 0
#define A_DMA 1
constexpr unsigned RSRC3 = 0x00020000u;
constexpr int AUX_SC1 = 16;
constexpr unsigned SPIN_CAP = 1u << 20;

constexpr unsigned WS_EP = 0;
constexpr unsigned WS_FL = 4096;
constexpr unsigned WS_PART = 8192;
constexpr unsigned WS_A = 8192 + 16 * 32 * 64 * 8;
constexpr unsigned A_BYTES = 46 * 832;

#define XD 6
constexpr unsigned L_W = 0;
constexpr unsigned L_A = 832 * 128;
constexpr unsigned L_X0 = L_A + 4 * XD * 2048;
constexpr unsigned L_F = L_X0 + 4 * 1664;
constexpr unsigned L_DUMMY = L_F + 256;
constexpr unsigned L_EP = L_DUMMY + 256;
constexpr unsigned L_BI = L_EP + 256;
constexpr unsigned L_W2 = L_BI + 256;
constexpr unsigned L_FL = L_W2 + 256;
constexpr unsigned L_END = L_FL + 256;
static_assert(4 * XD * 2048 >= A_BYTES && L_END <= 163840, "LDS map");
constexpr unsigned LP_VT = 0;
constexpr unsigned LP_T0 = 2048;
constexpr unsigned LP_T1 = 2048 + 2304;

struct AB { int a, b; };
constexpr AB slot_ab(int t) {
    if (t < 156) { int a = 2 * (t / 12); return {a, (a + t % 12) % 26}; }
    if (t < 170) { int u = t - 156; int a = 2 * (u / 2); return {a, (a + 12 + (u & 1)) % 26}; }
    if (t < 176) { int a = 2 * (7 + t - 170); return {a, (a + 12) % 26}; }
    if (t < 189) return {2 * (t - 176), 26};
    if (t == 189) return {26, 26};
    return {27, 27};
}
constexpr unsigned T_ZERO = 1024, T_CONST = 1025;
struct PrepTab { unsigned v[2][24 * 2 * 8]; };
constexpr PrepTab make_tab() {
    PrepTab tb{};
    for (int m = 0; m < 2; ++m) for (int s = 0; s < 24; ++s) for (int h = 0; h < 2; ++h) for (int e = 0; e < 8; ++e) {
        const int st = m ? 32 : 26;
        int t = 8 * s + e; AB ab = slot_ab(t);
        unsigned o1 = T_ZERO, o2 = T_ZERO;
        if (ab.a == 27) {}
        else if (ab.a == 26) { if (h == 0 && m) o1 = T_CONST; }
        else if (ab.b == 26) { if (m) o1 = ((ab.a + h) % 26) * st + 26; }
        else {
            int u = (ab.a + h) % 26, v = (ab.b + h) % 26;
            bool dup = (t == 169 && h == 1);
            if (dup) {}
            else if (u == v) o1 = u * st + u;
            else { o1 = u * st + v; o2 = v * st + u; }
        }
        tb.v[m][(s * 2 + h) * 8 + e] = o1 | (o2 << 16);
    }
    return tb;
}
static_assert(slot_ab(169).a == 12 && slot_ab(169).b == 25, "dup slot");

struct Params {
    const int* idx; const unsigned short* table; const unsigned short* wcin; const unsigned short* wout;
    const unsigned short* w1; const unsigned short* b1; const unsigned short* w2; const unsigned short* bout;
    unsigned short* out; unsigned char* ws;
};
struct KArgs { Params P; PrepTab tab; };

DI unsigned sel(bool c, unsigned a, unsigned b) { unsigned r = c ? a : b; return r; }
DI float bf_lo(unsigned v) { return __uint_as_float(v << 16); }
DI float bf_hi(unsigned v) { return __uint_as_float(v & 0xffff0000u); }
DI unsigned pk_bf16(float lo, float hi) { f32x2 v = {lo, hi}; return __builtin_bit_cast(unsigned, __builtin_convertvector(v, bf16x2)); }
DI f32x4 mfma16(s16x8 a, s16x8 b, f32x4 c) { return __builtin_amdgcn_mfma_f32_16x16x32_bf16(a, b, c, 0, 0, 0); }
DI f32x16 mfma32(s16x8 a, s16x8 b, f32x16 c) { return __builtin_amdgcn_mfma_f32_32x32x16_bf16(a, b, c, 0, 0, 0); }
DI s16x8 as_frag(u32x4 v) { return __builtin_bit_cast(s16x8, v); }
template <int CTRL, int ROWMASK> DI float dpp_add(float v) {
    return v + __int_as_float(__builtin_amdgcn_update_dpp(0, __float_as_int(v), CTRL, ROWMASK, 0xf, true));
}
DI float wave_sum_to_lane63(float v) {
    v = dpp_add<0x111, 0xf>(v); v = dpp_add<0x112, 0xf>(v); v = dpp_add<0x114, 0xf>(v); v = dpp_add<0x118, 0xf>(v);
    v = dpp_add<0x142, 0xa>(v); v = dpp_add<0x143, 0xc>(v);
    return v;
}
DI float sum_lane_groups(float v) {
    auto a = __builtin_amdgcn_permlane16_swap(__float_as_uint(v), __float_as_uint(v), false, false);
    v = __uint_as_float(a[0]) + __uint_as_float(a[1]);
    auto b = __builtin_amdgcn_permlane32_swap(__float_as_uint(v), __float_as_uint(v), false, false);
    return __uint_as_float(b[0]) + __uint_as_float(b[1]);
}
DI float gelu_tanh(float x) {
    const float k1 = -2.0f * 0.7978845608028654f * 1.4426950408889634f;
    const float k2 = k1 * 0.044715f;
    float arg = x * __builtin_fmaf(x * x, k2, k1);
    float e = __builtin_amdgcn_exp2f(arg);
    return x * __builtin_amdgcn_rcpf(1.0f + e);
}
DI void dma16(unsigned lds_addr, unsigned voff, __amdgpu_buffer_rsrc_t srd, unsigned soff) {
    asm volatile("s_mov_b32 m0, %0\n\ts_nop 0\n\tbuffer_load_dwordx4 %1, %2, %3 offen lds"
                 :: "s"(lds_addr), "v"(voff), "s"(srd), "s"(soff) : "memory");
}
DI void dma16_sc1(unsigned lds_addr, unsigned voff, __amdgpu_buffer_rsrc_t srd, unsigned soff) {
    asm volatile("s_mov_b32 m0, %0\n\ts_nop 0\n\tbuffer_load_dwordx4 %1, %2, %3 offen sc1 lds"
                 :: "s"(lds_addr), "v"(voff), "s"(srd), "s"(soff) : "memory");
}
DI void dma4_sc1(unsigned lds_addr, unsigned voff, __amdgpu_buffer_rsrc_t srd, unsigned soff) {
    asm volatile("s_mov_b32 m0, %0\n\ts_nop 0\n\tbuffer_load_dword %1, %2, %3 offen sc1 lds"
                 :: "s"(lds_addr), "v"(voff), "s"(srd), "s"(soff) : "memory");
}
DI void dma4(unsigned lds_addr, unsigned voff, __amdgpu_buffer_rsrc_t srd, unsigned soff) {
    asm volatile("s_mov_b32 m0, %0\n\ts_nop 0\n\tbuffer_load_dword %1, %2, %3 offen lds"
                 :: "s"(lds_addr), "v"(voff), "s"(srd), "s"(soff) : "memory");
}
DI void st_granule(gu64* g, unsigned tag, unsigned val) {
    __hip_atomic_store(g, ((unsigned long long)tag << 32) | val, __ATOMIC_RELAXED, __HIP_MEMORY_SCOPE_AGENT);
}
DI unsigned ld_agent(const gu32* p) { return __hip_atomic_load(p, __ATOMIC_RELAXED, __HIP_MEMORY_SCOPE_AGENT); }
DI unsigned lds_ld(lds8* l, unsigned off) { return __hip_atomic_load((LAS unsigned*)(l + off), __ATOMIC_RELAXED, __HIP_MEMORY_SCOPE_WORKGROUP); }
DI void lds_st(lds8* l, unsigned off, unsigned v) { __hip_atomic_store((LAS unsigned*)(l + off), v, __ATOMIC_RELAXED, __HIP_MEMORY_SCOPE_WORKGROUP); }
DI void lds_add(lds8* l, unsigned off, unsigned v) { __hip_atomic_fetch_add((LAS unsigned*)(l + off), v, __ATOMIC_RELAXED, __HIP_MEMORY_SCOPE_WORKGROUP); }
DI void sig_add(lds8* l, unsigned off) {
    asm volatile("s_mov_b64 exec, 1\n\tds_add_u32 %0, %1\n\ts_mov_b64 exec, -1" :: "v"((unsigned)(size_t)l + off), "v"(1u) : "memory");
}
DI void sig_set(lds8* l, unsigned off) {
    asm volatile("s_mov_b64 exec, 1\n\tds_write_b32 %0, %1\n\ts_mov_b64 exec, -1" :: "v"((unsigned)(size_t)l + off), "v"(1u) : "memory");
}
#define WAIT_LGKM() asm volatile("s_waitcnt lgkmcnt(0)" ::: "memory")
#define WAIT_VM0() asm volatile("s_waitcnt vmcnt(0)" ::: "memory")
#define CBAR() asm volatile("" ::: "memory")
#define STAMP(i)
#define STAMP_ARG
#define STAMP_PASS
#define STAMP_FLUSH(ws, w, lane)

template <int K> DI unsigned quad_bcast(unsigned v) { return __builtin_amdgcn_update_dpp(0u, v, K * 0x55, 0xf, 0xf, true); }
template <int N> DI void wait_vm() { asm volatile("s_waitcnt vmcnt(%0)" :: "i"(N) : "memory"); }
DI void dma_w4(unsigned lds_addr, unsigned voff, __amdgpu_buffer_rsrc_t srd, unsigned soff) {
    unsigned t;
    asm volatile("s_mov_b32 m0, %1\n\ts_add_u32 %0, %4, 12288\n\tbuffer_load_dwordx4 %2, %3, %4 offen lds\n\t"
                 "buffer_load_dwordx4 %2, %3, %0 offen offset:1024 lds\n\ts_add_u32 %0, %0, 12288\n\t"
                 "buffer_load_dwordx4 %2, %3, %0 offen offset:2048 lds\n\ts_add_u32 %0, %0, 12288\n\t"
                 "buffer_load_dwordx4 %2, %3, %0 offen offset:3072 lds"
                 : "=&s"(t) : "s"(lds_addr), "v"(voff), "s"(srd), "s"(soff) : "memory", "scc");
}
DI void dnn_start(const Params& P, lds8* lds, int w, int lane, int g, int ntile) {
    const int mh = w & 1, kh = w >> 1;
    const unsigned ldsb = (unsigned)(size_t)lds;
    const unsigned ring = ldsb + L_A + w * (XD * 2048);
    const __amdgpu_buffer_rsrc_t srdI = __builtin_amdgcn_make_buffer_rsrc((void*)P.idx, 0, NBATCH * 26 * 4, RSRC3);
    const unsigned vo = ((64 * g + 32 * mh + (lane >> 2)) * 26 + 13 * kh + 4 * (lane & 3)) * 4;
    dma16(ring, vo, srdI, 0);
    dma16(ring + 1024, vo, srdI, 1664);
    if (__builtin_expect(w == 1, 1)) dma4(ldsb + L_BI, ntile * 128 + lane * 4, __builtin_amdgcn_make_buffer_rsrc((void*)P.b1, 0, DIN * 2, RSRC3), 0);
    if (__builtin_expect(w == 2, 1)) dma4(ldsb + L_W2, ntile * 128 + lane * 4, __builtin_amdgcn_make_buffer_rsrc((void*)P.w2, 0, DIN * 2, RSRC3), 0);
    const __amdgpu_buffer_rsrc_t srdW = __builtin_amdgcn_make_buffer_rsrc((void*)P.w1, 0, DIN * DIN * 2, RSRC3);
    const unsigned vw = (lane >> 3) * 1664 + (((lane & 7) ^ (((lane >> 4) & 1) << 2)) << 4);
    const unsigned rowA = 416 * kh + 208 * mh + 104 * (g & 1);
    dma_w4(ldsb + L_W + rowA * 128, vw, srdW, rowA * 1664 + ntile * 128);
    if (W_EARLY > 1) dma_w4(ldsb + L_W + rowA * 128 + 4096, vw, srdW, rowA * 1664 + ntile * 128 + 4 * 13312);
    if (W_EARLY > 2) dma_w4(ldsb + L_W + rowA * 128 + 8192, vw, srdW, rowA * 1664 + ntile * 128 + 8 * 13312);
}
DI void dnn_role(const Params& P, lds8* lds, int w, int lane, int g, int ntile STAMP_ARG) {
    const int mh = w & 1, kh = w >> 1;
    const int h = lane >> 5;
    const unsigned ldsb = (unsigned)(size_t)lds;
    const unsigned ring = ldsb + L_A + w * (XD * 2048);
    const __amdgpu_buffer_rsrc_t srdW = __builtin_amdgcn_make_buffer_rsrc((void*)P.w1, 0, DIN * DIN * 2, RSRC3);
    const unsigned vw = (lane >> 3) * 1664 + (((lane & 7) ^ (((lane >> 4) & 1) << 2)) << 4);
    const unsigned rowA = 416 * kh + 208 * mh + 104 * (g & 1), rowB = 416 * kh + 208 * mh + 104 * ((g & 1) ^ 1);
    auto wdma = [&](unsigned row, int a) {
        const unsigned sb = row * 1664 + ntile * 128, lb = ldsb + L_W + row * 128;
        if (a < 3) dma_w4(lb + a * 4096, vw, srdW, sb + a * 4 * 13312);
        else dma16(lb + 12288, vw, srdW, sb + 12 * 13312);
    };
    STAMP(1);
    wait_vm<4 * W_EARLY>();
    WAIT_LGKM();
    __builtin_amdgcn_s_barrier();
    STAMP(2);
    const u32x4 i0 = *(const LAS u32x4*)(lds + L_A + w * (XD * 2048) + lane * 16);
    const u32x4 i1 = *(const LAS u32x4*)(lds + L_A + w * (XD * 2048) + 1024 + lane * 16);
    const unsigned ep = *(const LAS unsigned*)(lds + L_EP + g * 4);
#pragma unroll
    for (int a = W_EARLY; a < 4; ++a) wdma(rowA, a);
#pragma unroll
    for (int a = 0; a < 4; ++a) wdma(rowB, a);
    const __amdgpu_buffer_rsrc_t srdT = __builtin_amdgcn_make_buffer_rsrc((void*)P.table, 0, NVOC * 64, RSRC3);
    const unsigned c16 = (((lane & 3) ^ ((lane >> 4) & 3)) << 4);
    u32x4 t0 = i0 << 6, t1 = i1 << 6;
    asm volatile("" : "+v"(t0), "+v"(t1));
    auto xload = [&](int s) {
        const unsigned a0 = t0[s & 3], a1 = t1[s & 3];
        const unsigned o0 = (s < 4 ? quad_bcast<0>(a0) : s < 8 ? quad_bcast<1>(a0) : s < 12 ? quad_bcast<2>(a0) : quad_bcast<3>(a0)) + c16;
        const unsigned o1 = (s < 4 ? quad_bcast<0>(a1) : s < 8 ? quad_bcast<1>(a1) : s < 12 ? quad_bcast<2>(a1) : quad_bcast<3>(a1)) + c16;
        dma16(ring + (s % XD) * 2048, o0, srdT, 0);
        dma16(ring + (s % XD) * 2048 + 1024, o1, srdT, 0);
    };
#pragma unroll
    for (int s = 0; s < XD; ++s) xload(s);
    STAMP(3);
    wait_vm<2 * XD>();
    sig_add(lds, L_F + 32 + 4 * kh);
    _Pragma("unroll 1") for (unsigned sp = 0; lds_ld(lds, L_F + 32 + 4 * kh) < 2 && sp < SPIN_CAP; ++sp) __builtin_amdgcn_s_sleep(0);
    CBAR();
    STAMP(4);
    f32x16 acc[2];
    u32x2 bia[4], wv[4];
    float w2f[16];
    unsigned xdone = 0;
#pragma unroll
    for (int r4 = 0; r4 < 4; ++r4) {
        bia[r4] = *(const LAS u32x2*)(lds + L_BI + kh * 64 + h * 8 + r4 * 16);
        wv[r4] = *(const LAS u32x2*)(lds + L_W2 + kh * 64 + h * 8 + r4 * 16);
    }
#pragma unroll
    for (int r4 = 0; r4 < 4; ++r4) {
        acc[0][4 * r4] = bf_lo(bia[r4][0]); acc[0][4 * r4 + 1] = bf_hi(bia[r4][0]); acc[0][4 * r4 + 2] = bf_lo(bia[r4][1]); acc[0][4 * r4 + 3] = bf_hi(bia[r4][1]);
        acc[1][4 * r4] = 0.f; acc[1][4 * r4 + 1] = 0.f; acc[1][4 * r4 + 2] = 0.f; acc[1][4 * r4 + 3] = 0.f;
    }
    {
        const int blk = (lane >> 4) & 1, q = (lane & 15) >> 2, pp = lane & 3, n = lane & 31;
        unsigned ad[2];
#pragma unroll
        for (int j = 0; j < 2; ++j) {
            const unsigned c = 4 * (j ^ kh) + 2 * blk + (pp >> 1);
            ad[j] = L_W + (416 * kh + 8 * h + q) * 128 + ((c ^ (((q >> 1) & 1) << 2)) << 4) + (pp & 1) * 8;
        }
        const unsigned xb = L_A + w * (XD * 2048) + n * 64;
        const unsigned xa0 = xb + ((h ^ ((n >> 2) & 3)) << 4), xa1 = xb + (((2 + h) ^ ((n >> 2) & 3)) << 4);
        u32x4 xf[3][2];
        s16x8 wf[2][4];
        auto xread = [&](int s) {
            xf[s % 3][0] = *(const LAS u32x4*)(lds + xa0 + (s % XD) * 2048);
            xf[s % 3][1] = *(const LAS u32x4*)(lds + xa1 + (s % XD) * 2048);
        };
        auto wread = [&](int s) {
#pragma unroll
            for (int a = 0; a < 2; ++a)
#pragma unroll
                for (int j = 0; j < 2; ++j) {
                    const s16x4 lo = __builtin_amdgcn_ds_read_tr16_b64_v4i16((LAS s16x4*)(lds + ad[j] + s * 4096 + a * 2048));
                    const s16x4 hi = __builtin_amdgcn_ds_read_tr16_b64_v4i16((LAS s16x4*)(lds + ad[j] + s * 4096 + a * 2048 + 512));
                    wf[s & 1][2 * a + j] = __builtin_shufflevector(lo, hi, 0, 1, 2, 3, 4, 5, 6, 7);
                }
        };
        wread(0);
        wait_vm<2 * (XD - 1)>();
        xread(0);
        wait_vm<2 * (XD - 2)>();
        xread(1);
        __builtin_amdgcn_sched_barrier(0);
#pragma unroll
        for (int s = 0; s < 13; ++s) {
            if (s + 1 < 13) wread(s + 1);
#pragma unroll
            for (int a = 0; a < 2; ++a)
#pragma unroll
                for (int j = 0; j < 2; ++j) acc[j] = mfma32(wf[s & 1][2 * a + j], as_frag(xf[s % 3][a]), acc[j]);
            if (s + 1 < 13) {
#pragma unroll
                for (int j = 0; j < 4; ++j) { __builtin_amdgcn_sched_group_barrier(0x100, 2, 0); __builtin_amdgcn_sched_group_barrier(0x8, 1, 0); }
            }
            __builtin_amdgcn_sched_barrier(0);
            if (s + XD < 13) xload(s + XD);
            if (s == 11) {
                xdone = lds_ld(lds, L_F + 52);
#pragma unroll
                for (int r4 = 0; r4 < 4; ++r4) { w2f[4 * r4] = bf_lo(wv[r4][0]); w2f[4 * r4 + 1] = bf_hi(wv[r4][0]); w2f[4 * r4 + 2] = bf_lo(wv[r4][1]); w2f[4 * r4 + 3] = bf_hi(wv[r4][1]); }
#pragma unroll
                for (int e = 0; e < 16; ++e) asm volatile("" : "+v"(w2f[e]));
            }
            if (s + 2 < 13) {
                constexpr int LASTI = 12;
                const int last = s + XD < LASTI ? s + XD : LASTI;
                const int younger = last - (s + 2);
                if (younger >= 4) wait_vm<8>(); else if (younger == 3) wait_vm<6>(); else if (younger == 2) wait_vm<4>(); else if (younger == 1) wait_vm<2>(); else wait_vm<0>();
                if (s + 2 == 12) sig_add(lds, L_F + 52);
                if (s == GATE_STEP) sig_add(lds, L_F + 56);
                xread(s + 2);
            }
            __builtin_amdgcn_sched_barrier(0);
        }
    }
    STAMP(6);
    {
        if (xdone < 4) _Pragma("unroll 1") for (unsigned sp = 0; lds_ld(lds, L_F + 52) < 4 && sp < SPIN_CAP; ++sp) __builtin_amdgcn_s_sleep(0);
        CBAR();
        lds8* xw = lds + L_W + kh * 53248 + (5 + mh) * 4096 + lane * 16;
#pragma unroll
        for (int r4 = 0; r4 < 4; ++r4) *(LAS f32x4*)(xw + r4 * 1024) = (f32x4){acc[1][4 * r4], acc[1][4 * r4 + 1], acc[1][4 * r4 + 2], acc[1][4 * r4 + 3]};
        CBAR();
        sig_set(lds, L_F + 4 * w);
        _Pragma("unroll 1") for (unsigned sp = 0; lds_ld(lds, L_F + 4 * (w ^ 2)) == 0 && sp < SPIN_CAP; ++sp) __builtin_amdgcn_s_sleep(0);
        CBAR();
        const lds8* xr = lds + L_W + (kh ^ 1) * 53248 + (5 + mh) * 4096 + lane * 16;
        f32x4 o[4];
#pragma unroll
        for (int r4 = 0; r4 < 4; ++r4) o[r4] = *(const LAS f32x4*)(xr + r4 * 1024);
        asm volatile("" : "+v"(o[0]), "+v"(o[1]), "+v"(o[2]), "+v"(o[3]));
#pragma unroll
        for (int r4 = 0; r4 < 4; ++r4) {
            acc[0][4 * r4] += o[r4][0]; acc[0][4 * r4 + 1] += o[r4][1]; acc[0][4 * r4 + 2] += o[r4][2]; acc[0][4 * r4 + 3] += o[r4][3];
        }
    }
    STAMP(7);
    float s0 = 0.f, s1 = 0.f;
#pragma unroll
    for (int r4 = 0; r4 < 4; ++r4) {
        s0 = __builtin_fmaf(gelu_tanh(acc[0][4 * r4]), w2f[4 * r4], s0);
        s1 = __builtin_fmaf(gelu_tanh(acc[0][4 * r4 + 1]), w2f[4 * r4 + 1], s1);
        s0 = __builtin_fmaf(gelu_tanh(acc[0][4 * r4 + 2]), w2f[4 * r4 + 2], s0);
        s1 = __builtin_fmaf(gelu_tanh(acc[0][4 * r4 + 3]), w2f[4 * r4 + 3], s1);
    }
    s0 += s1;
    const auto sw = __builtin_amdgcn_permlane32_swap(__float_as_uint(s0), __float_as_uint(s0), false, false);
    const float part = __uint_as_float(sw[0]) + __uint_as_float(sw[1]);
    STAMP(8);
    if (lane < 32) st_granule((gu64*)(P.ws + WS_PART) + ((g * 32 + 2 * ntile + kh) * 64 + 32 * mh + lane), ep + 1u, __float_as_uint(part));
}

constexpr unsigned LP_W2 = 12288;
constexpr unsigned LP_WO = 14336;
constexpr unsigned LP_DM = 15360;
constexpr unsigned LP_W3 = 16384;
static_assert(LP_T1 + 4 * 1026 <= LP_W2, "prep scratch");
constexpr unsigned w3_slot(int j) { return j * 1280 + (j & 1) * 64 + ((j >> 2) & 1) * 128; }
DI void dma16_add(unsigned lds_base, unsigned lds_off, unsigned voff, __amdgpu_buffer_rsrc_t srd, unsigned sbase, unsigned soff) {
    unsigned t;
    asm volatile("s_add_u32 m0, %1, %2\n\ts_add_u32 %0, %5, %6\n\tbuffer_load_dwordx4 %3, %4, %0 offen lds"
                 : "=&s"(t) : "s"(lds_base), "i"(lds_off), "v"(voff), "s"(srd), "s"(sbase), "i"(soff) : "memory", "scc");
}
DI void prep_start(const Params& P, lds8* lds, int w, int lane, int p) {
    const unsigned ldsb = (unsigned)(size_t)lds;
    const __amdgpu_buffer_rsrc_t srdC = __builtin_amdgcn_make_buffer_rsrc((void*)P.wcin, 0, 3 * 17576 * 2, RSRC3);
    const int i0 = w == 0 ? 0 : w == 1 ? 6 : w == 2 ? 14 : 20;
    dma4(ldsb + LP_WO + w * 256, lane * 4, __builtin_amdgcn_make_buffer_rsrc((void*)P.wout, 0, 78 * 2, RSRC3), 0);
    {
        const unsigned hp = lane >> 5;
        const unsigned vo = hp * 1352 + ((lane >> 2) & 7) * 52 + (((lane & 3) ^ (hp << 1)) << 4);
        const unsigned lb = ldsb + LP_W3 + w * 16640, sb = 52 * 1352 + 52 * i0;
        dma16_add(lb, w3_slot(0), vo, srdC, sb, 0 * 2704);  dma16_add(lb, w3_slot(1), vo, srdC, sb, 1 * 2704);
        dma16_add(lb, w3_slot(2), vo, srdC, sb, 2 * 2704);  dma16_add(lb, w3_slot(3), vo, srdC, sb, 3 * 2704);
        dma16_add(lb, w3_slot(4), vo, srdC, sb, 4 * 2704);  dma16_add(lb, w3_slot(5), vo, srdC, sb, 5 * 2704);
        dma16_add(lb, w3_slot(6), vo, srdC, sb, 6 * 2704);  dma16_add(lb, w3_slot(7), vo, srdC, sb, 7 * 2704);
        dma16_add(lb, w3_slot(8), vo, srdC, sb, 8 * 2704);  dma16_add(lb, w3_slot(9), vo, srdC, sb, 9 * 2704);
        dma16_add(lb, w3_slot(10), vo, srdC, sb, 10 * 2704); dma16_add(lb, w3_slot(11), vo, srdC, sb, 11 * 2704);
        dma16_add(lb, w3_slot(12), vo, srdC, sb, 12 * 2704);
    }
    if (__builtin_expect(w < 2, 1)) {
        const unsigned vo = w == 0 ? 35152 + (lane >> 2) * 1352 + p * 52 + (lane & 3) * 16 : p * 1352 + lane * 16;
        const unsigned lb = ldsb + (w == 0 ? LP_W2 : LP_T0);
        dma16(lb, vo, srdC, 0);
        dma16(lb + 1024, vo, srdC, w == 0 ? 16 * 1352 : 1024);
    }
}
DI void prep_role(const Params& P, lds8* lds, int w, int lane, int p, int slot STAMP_ARG) {
    const int n = lane & 15, gq = lane >> 4;
    const int i0 = w == 0 ? 0 : w == 1 ? 6 : w == 2 ? 14 : 20;
    const int nact = w == 0 ? 48 : 44;
    const int ch = lane < nact ? lane : 0;
    const int cs = ch >> 1, chh = ch & 1;
    const GAS u32x4* tp = (const GAS u32x4*)((const GAS unsigned char*)(size_t)__builtin_amdgcn_kernarg_segment_ptr() + sizeof(Params) + ((w == 0 ? 384 : 0) + (cs * 2 + chh) * 8) * 4);
    const u32x4 ta = tp[0], tb = tp[1];
    STAMP(1);
    const int q = (lane & 15) >> 2, pp = lane & 3;
    int r0 = 8 * gq + q, r1 = r0 + 4;
    r0 = r0 < 26 ? r0 : 25; r1 = r1 < 26 ? r1 : 25;
    unsigned b0a, b0b, b1a, b1b;
    {
        const unsigned wb = LP_W3 + w * 16640 + pp * 8;
        const unsigned j0 = r0 >> 1, j1 = r1 >> 1;
        const unsigned s0 = wb + j0 * 1280 + (j0 & 1) * 64 + ((j0 >> 2) & 1) * 128 + (r0 & 1) * 512;
        const unsigned s1 = wb + j1 * 1280 + (j1 & 1) * 64 + ((j1 >> 2) & 1) * 128 + (r1 & 1) * 512;
        b0a = s0 + (r0 & 1) * 32; b0b = s0 + 32 - (r0 & 1) * 32; b1a = s1 + (r1 & 1) * 32; b1b = s1 + 32 - (r1 & 1) * 32;
    }
    unsigned va = LP_VT + n * 64 + 2 * i0, vb = n < 10 ? va + 1024 : LP_DM;
    unsigned vl = w == 3 ? LP_DM : 0;
    unsigned ac = LP_T1 + 4 * (4 * gq * 32 + n);
    unsigned a20 = LP_W2 + r0 * 64 + pp * 8, a21 = LP_W2 + r1 * 64 + pp * 8, abv = LP_VT + n * 64 + gq * 16;
    asm volatile("" : "+v"(b0a), "+v"(b0b), "+v"(b1a), "+v"(b1b), "+v"(va), "+v"(vb), "+v"(ac), "+v"(a20), "+v"(a21), "+v"(abv));
    if (w == 3) {
        LAS unsigned* z = (LAS unsigned*)(lds + LP_VT + (lane < 27 ? lane : 31) * 64 + 52);
        z[0] = 0; z[1] = 0; z[2] = 0;
        *(LAS unsigned*)(lds + LP_T1 + 4 * T_ZERO) = 0;
    }
    if (w == 1) *(LAS unsigned*)(lds + LP_T0 + 2 * T_ZERO) = 0;
    asm volatile("s_waitcnt vmcnt(0) lgkmcnt(0)" ::: "memory");
    __builtin_amdgcn_s_barrier();
    STAMP(2);
    {
        const u32x4 wo3 = *(const LAS u32x4*)(lds + LP_WO + w * 256 + 104 + 16 * gq);
        s16x8 bfr[16];
#pragma unroll
        for (int t = 0; t < 16; ++t) {
            const int ii = t >> 1, u = t & 1;
            const s16x4 lo = __builtin_amdgcn_ds_read_tr16_b64_v4i16((LAS s16x4*)(lds + (u ? b0b : b0a) + ii * 64));
            const s16x4 hi = __builtin_amdgcn_ds_read_tr16_b64_v4i16((LAS s16x4*)(lds + (u ? b1b : b1a) + ii * 64));
            bfr[t] = __builtin_shufflevector(lo, hi, 0, 1, 2, 3, 4, 5, 6, 7);
        }
        if (w == 3) {
            const unsigned o2 = *(const LAS unsigned*)(lds + LP_WO + 768 + 52 + (lane < 13 ? lane : 0) * 4);
            const unsigned o1 = *(const LAS unsigned short*)(lds + LP_WO + 768 + p * 2);
            *(LAS unsigned*)(lds + (lane < 13 ? LP_VT + 26 * 64 + lane * 4 : LP_DM)) = o2;
            *(LAS float*)(lds + LP_T1 + 4 * T_CONST) = bf_lo(o1);
        }
        float res[16];
#pragma unroll
        for (int t = 0; t < 16; ++t) res[t] = mfma16(as_frag(wo3), bfr[t], (f32x4){0.f, 0.f, 0.f, 0.f})[0];
#pragma unroll
        for (int k2 = 0; k2 < 4; ++k2) {
            const unsigned pa0 = pk_bf16(res[4 * k2], res[4 * k2 + 2]), pb0 = pk_bf16(res[4 * k2 + 1], res[4 * k2 + 3]);
            if (k2 < 3) { *(LAS unsigned*)(lds + va + 4 * k2) = pa0; *(LAS unsigned*)(lds + vb + 4 * k2) = pb0; }
            else { *(LAS unsigned*)(lds + (vl ? vl : va + 12)) = pa0; *(LAS unsigned*)(lds + (vl ? vl : vb + 12)) = pb0; }
        }
    }
    CBAR();
    sig_add(lds, L_F + 28);
    if (w > 1) return;
    STAMP(3);
    const __amdgpu_buffer_rsrc_t srdA = __builtin_amdgcn_make_buffer_rsrc((void*)(P.ws + WS_A), 0, A_BYTES, RSRC3);
    u32x4 o;
    if (w == 1) {
        float pv[16];
#pragma unroll
        for (int e = 0; e < 8; ++e) {
            const unsigned x = e < 4 ? ta[e] : tb[e - 4];
            pv[2 * e] = bf_lo(*(const LAS unsigned short*)(lds + LP_T0 + 2 * (x & 0xffff)));
            pv[2 * e + 1] = bf_lo(*(const LAS unsigned short*)(lds + LP_T0 + 2 * (x >> 16)));
        }
#pragma unroll
        for (int j = 0; j < 4; ++j) o[j] = pk_bf16(pv[4 * j] + pv[4 * j + 1], pv[4 * j + 2] + pv[4 * j + 3]);
    } else {
        s16x8 a2[2];
#pragma unroll
        for (int mt = 0; mt < 2; ++mt) {
            const s16x4 lo = __builtin_amdgcn_ds_read_tr16_b64_v4i16((LAS s16x4*)(lds + a20 + mt * 32));
            const s16x4 hi = __builtin_amdgcn_ds_read_tr16_b64_v4i16((LAS s16x4*)(lds + a21 + mt * 32));
            a2[mt] = __builtin_shufflevector(lo, hi, 0, 1, 2, 3, 4, 5, 6, 7);
        }
        unsigned pa[16];
#pragma unroll
        for (int e = 0; e < 8; ++e) {
            const unsigned x = e < 4 ? ta[e] : tb[e - 4];
            pa[2 * e] = LP_T1 + 4 * (x & 0xffff); pa[2 * e + 1] = LP_T1 + 4 * (x >> 16);
        }
#pragma unroll
        for (int e = 0; e < 16; ++e) asm volatile("" : "+v"(pa[e]));
        _Pragma("unroll 1") for (unsigned sp = 0; lds_ld(lds, L_F + 28) < 4 && sp < SPIN_CAP; ++sp) __builtin_amdgcn_s_sleep(0);
        CBAR();
        const u32x4 bf0 = *(const LAS u32x4*)(lds + abv), bf1 = *(const LAS u32x4*)(lds + abv + 1024);
        const f32x4 z4 = {0.f, 0.f, 0.f, 0.f};
        const f32x4 d00 = mfma16(a2[0], as_frag(bf0), z4), d01 = mfma16(a2[1], as_frag(bf0), z4);
        const f32x4 d10 = mfma16(a2[0], as_frag(bf1), z4), d11 = mfma16(a2[1], as_frag(bf1), z4);
#pragma unroll
        for (int e = 0; e < 4; ++e) {
            *(LAS float*)(lds + ac + e * 128) = d00[e]; *(LAS float*)(lds + ac + 2048 + e * 128) = d01[e];
            *(LAS float*)(lds + ac + 64 + e * 128) = d10[e]; *(LAS float*)(lds + ac + 2048 + 64 + e * 128) = d11[e];
        }
        STAMP(4);
        float pv[16];
#pragma unroll
        for (int e = 0; e < 16; ++e) pv[e] = *(const LAS float*)(lds + pa[e]);
#pragma unroll
        for (int j = 0; j < 4; ++j) o[j] = pk_bf16(pv[4 * j] + pv[4 * j + 1], pv[4 * j + 2] + pv[4 * j + 3]);
    }
    {
        const int fr = w == 0 ? (cs < 22 ? 2 * cs : 22 + cs) : 2 * cs + 1;
        __builtin_amdgcn_raw_buffer_store_b128(o, srdA, lane < nact ? (unsigned)(fr * 832 + (26 * chh + p) * 16) : 0x7ffffff0u, 0, AUX_SC1);
    }
    STAMP(5);
    WAIT_VM0();
    STAMP(6);
    if (w == 1) { sig_set(lds, L_F + 24); return; }
    const unsigned tagv = *(const LAS unsigned*)(lds + L_EP + (lane & 15) * 4);
    _Pragma("unroll 1") for (unsigned sp = 0; lds_ld(lds, L_F + 24) == 0 && sp < SPIN_CAP; ++sp) __builtin_amdgcn_s_sleep(0);
    if (lane < 16) __hip_atomic_store((gu32*)(P.ws + WS_FL) + lane * 64 + slot, tagv + 1u, __ATOMIC_RELAXED, __HIP_MEMORY_SCOPE_AGENT);
}

DI bool rows_ready(bool match) {
    const unsigned long long mk = __ballot(match);
    return (((unsigned)mk | (unsigned)(mk >> 32)) & 0x3ffffffu) == 0x3ffffffu;
}
constexpr unsigned A_PER = A_BYTES / 4;
DI void cin_role(const Params& P, lds8* lds, int cw, int lane, int g, int m, bool has_dnn, int pf_slot STAMP_ARG) {
    const int d = lane & 31, h = lane >> 5;
    const int b = 64 * g + 4 * m + cw;
    const unsigned ldsb = (unsigned)(size_t)lds;
    {
        const __amdgpu_buffer_rsrc_t srdT = __builtin_amdgcn_make_buffer_rsrc((void*)P.table, 0, NVOC * 64, RSRC3);
        const unsigned lb = ldsb + L_X0 + cw * 1664;
        WAIT_VM0();
        const int q = lane >> 2;
        const unsigned j0 = *(const LAS unsigned*)(lds + L_X0 + cw * 1664 + q * 4);
        const unsigned j1 = *(const LAS unsigned*)(lds + L_X0 + cw * 1664 + (q < 10 ? q + 16 : 25) * 4);
        unsigned o0 = j0 * 64 + (lane & 3) * 16, o1 = j1 * 64 + (lane & 3) * 16;
        asm volatile("" : "+v"(o0), "+v"(o1));
        STAMP(1);
        dma16(lb, o0, srdT, 0);
        if (lane < 40) dma16(lb + 1024, o1, srdT, 0);
        if (cw == 0) dma4_sc1(ldsb + L_FL, g * 256 + lane * 4, __builtin_amdgcn_make_buffer_rsrc((void*)(P.ws + WS_FL), 0, 4096, RSRC3), 0);
    }
    const unsigned bo = P.bout[0];
    STAMP(2);
    __builtin_amdgcn_s_setprio(0);
    CBAR();
    __builtin_amdgcn_s_barrier();
    const unsigned epv = *(const LAS unsigned*)(lds + L_EP + g * 4);
    unsigned bov = bo;
    asm volatile("s_waitcnt vmcnt(0)" : "+v"(bov) :: "memory");
    STAMP(3);
    float x[26];
    {
        const lds8* xb = lds + L_X0 + cw * 1664 + h * 64 + d * 2;
        const lds8* xl = lds + L_X0 + cw * 1664 + (h ? 0 : 25 * 64) + d * 2;
#pragma unroll
        for (int a = 0; a < 13; ++a) {
            const unsigned lo = *(const LAS unsigned short*)(xb + 2 * a * 64);
            const unsigned hi = a < 12 ? *(const LAS unsigned short*)(xb + (2 * a + 1) * 64) : *(const LAS unsigned short*)xl;
            x[2 * a] = bf_lo(lo); x[2 * a + 1] = bf_lo(hi);
        }
    }
    u32x4 z[24];
#pragma unroll
    for (int s = 0; s < 22; ++s) {
        float fa[8], fb[8];
#pragma unroll
        for (int e = 0; e < 8; ++e) { const AB pe = slot_ab(8 * s + e); fa[e] = x[pe.a]; fb[e] = x[pe.b]; }
        float t0, t1, t2, t3, t4, t5;
        unsigned o0, o1, o2, o3;
        asm("v_mul_f32 %4, %10, %11\n\tv_mul_f32 %5, %12, %13\n\tv_mul_f32 %6, %14, %15\n\tv_mul_f32 %7, %16, %17\n\t"
            "v_cvt_pk_bf16_f32 %0, %4, %5\n\tv_mul_f32 %8, %18, %19\n\tv_mul_f32 %9, %20, %21\n\tv_cvt_pk_bf16_f32 %1, %6, %7\n\t"
            "v_mul_f32 %4, %22, %23\n\tv_mul_f32 %5, %24, %25\n\tv_cvt_pk_bf16_f32 %2, %8, %9\n\tv_cvt_pk_bf16_f32 %3, %4, %5"
            : "=&v"(o0), "=&v"(o1), "=&v"(o2), "=v"(o3), "=&v"(t0), "=&v"(t1), "=&v"(t2), "=&v"(t3), "=&v"(t4), "=&v"(t5)
            : "v"(fa[0]), "v"(fb[0]), "v"(fa[1]), "v"(fb[1]), "v"(fa[2]), "v"(fb[2]), "v"(fa[3]), "v"(fb[3]),
              "v"(fa[4]), "v"(fb[4]), "v"(fa[5]), "v"(fb[5]), "v"(fa[6]), "v"(fb[6]), "v"(fa[7]), "v"(fb[7]));
        z[s] = (u32x4){o0, o1, o2, o3};
    }
#pragma unroll
    for (int s = 22; s < 24; ++s) {
#pragma unroll
        for (int j = 0; j < 4; ++j) {
            const AB p0 = slot_ab(8 * s + 2 * j), p1 = slot_ab(8 * s + 2 * j + 1);
            const float a0 = p0.a < 26 ? x[p0.a] : (p0.a == 26 ? 1.f : 0.f);
            const float a1 = p1.a < 26 ? x[p1.a] : (p1.a == 26 ? 1.f : 0.f);
            z[s][j] = pk_bf16(a0, a1);
        }
    }
#pragma unroll
    for (int s = 0; s < 24; ++s) asm volatile("" : "+v"(z[s]));
    STAMP(4);
    const unsigned tag = epv + 1u;
    {
        const __amdgpu_buffer_rsrc_t srdA = __builtin_amdgcn_make_buffer_rsrc((void*)(P.ws + WS_A), 0, A_BYTES, RSRC3);
        if (cw == 0) {
            if (has_dnn) _Pragma("unroll 1") for (unsigned sp = 0; lds_ld(lds, L_F + 56) < 4 && sp < SPIN_CAP; ++sp) __builtin_amdgcn_s_sleep(0);
            const gu32* fl = (const gu32*)(P.ws + WS_FL) + g * 64 + lane;
            if (!rows_ready(*(const LAS unsigned*)(lds + L_FL + lane * 4) == tag))
            _Pragma("unroll 1") for (unsigned sp = 0; sp < SPIN_CAP; ++sp) { if (rows_ready(ld_agent(fl) == tag)) break; __builtin_amdgcn_s_sleep(1); }
            sig_set(lds, L_F + 16);
            if (!has_dnn) dma4_sc1(ldsb + L_DUMMY, lane < 50 ? (unsigned)((((g & 1) * 3 + pf_slot) * 50 + lane) * 128) : 0x7fffff00u, srdA, 0);
        } else {
            _Pragma("unroll 1") for (unsigned sp = 0; lds_ld(lds, L_F + 16) == 0 && sp < SPIN_CAP; ++sp) __builtin_amdgcn_s_sleep(0);
        }
        CBAR();
        __builtin_amdgcn_s_setprio(CIN_PRIO);
        STAMP(5);
        {
            const unsigned l1 = ldsb + L_W + cw * 4096, vo = lane * 16;
            asm volatile("s_mov_b32 m0, %0\n\ts_nop 0\n\t"
                         "buffer_load_dwordx4 %1, %2, %3 offen sc1 lds\n\tbuffer_load_dwordx4 %1, %2, %3 offen offset:1024 sc1 lds\n\t"
                         "buffer_load_dwordx4 %1, %2, %3 offen offset:2048 sc1 lds\n\tbuffer_load_dwordx4 %1, %2, %3 offen offset:3072 sc1 lds"
                         :: "s"(l1), "v"(vo), "s"(srdA), "s"(cw * 4096) : "memory");
#pragma unroll
            for (int i = 0; i < 6; ++i) {
                const int q = 6 * cw + i;
                const unsigned go = q < 4 ? 16384 + 1024 * q : 19968 + 1024 * (q - 4), lo = q < 4 ? 16384 + 1024 * q : 53248 + 1024 * (q - 4);
                dma16_sc1(ldsb + L_W + lo, vo, srdA, go);
            }
        }
        wait_vm<6>();
        sig_add(lds, L_F + 20);
        _Pragma("unroll 1") for (unsigned sp = 0; lds_ld(lds, L_F + 20) < 4 && sp < SPIN_CAP; ++sp) __builtin_amdgcn_s_sleep(0);
        CBAR();
    }
    STAMP(6);
    const gu64* pp = (const gu64*)(P.ws + WS_PART) + ((g * 32 + (lane < 26 ? lane : 0)) * 64 + 4 * m + cw);
    unsigned long long pv = 0;
    f32x16 acc0, acc1;
#pragma unroll
    for (int i = 0; i < 16; ++i) { acc0[i] = 0.f; acc1[i] = 0.f; }
    {
        unsigned abo = L_W + (26 * h + d) * 16;
        asm volatile("" : "+v"(abo));
        const lds8* ab = lds + abo;
        constexpr int PF = 6;
        u32x4 af[46];
        unsigned cnt2 = 0;
        auto frag_of = [](int t) { return t < 24 ? t : t + 40; };
#pragma unroll
        for (int t = 0; t < PF; ++t) af[t] = *(const LAS u32x4*)(ab + frag_of(t) * 832);
        __builtin_amdgcn_sched_barrier(0);
#pragma unroll
        for (int t = 0; t < 46; ++t) {
            if (t == 9) { wait_vm<0>(); sig_add(lds, L_F + 44); }
            if (t + PF == 19 - 2) cnt2 = lds_ld(lds, L_F + 44);
            if (t + PF == 19) {
                if (cnt2 < 4) _Pragma("unroll 1") for (unsigned sp = 0; lds_ld(lds, L_F + 44) < 4 && sp < SPIN_CAP; ++sp) __builtin_amdgcn_s_sleep(0);
                CBAR();
            }
            if (t + PF < 46) af[t + PF] = *(const LAS u32x4*)(ab + frag_of(t + PF) * 832);
            if (t < 44) {
                if (t & 1) acc0 = mfma32(as_frag(af[t]), as_frag(z[t >> 1]), acc0);
                else acc1 = mfma32(as_frag(af[t]), as_frag(z[t >> 1]), acc1);
            } else acc1 = mfma32(as_frag(af[t]), as_frag(z[t - 22]), acc1);
            if (t == 30) pv = __hip_atomic_load(pp, __ATOMIC_RELAXED, __HIP_MEMORY_SCOPE_AGENT);
            __builtin_amdgcn_sched_barrier(0);
        }
    }
    float sa = 0.f;
#pragma unroll
    for (int i = 0; i < 12; ++i) sa = __builtin_fmaf(acc0[i], acc1[i], sa);
    const float sb = __builtin_fmaf(acc0[12], acc1[12], acc0[13] * acc1[13]);
    float tot = h == 0 ? sa + sb : sa;
    asm volatile("" : "+v"(tot));
    STAMP(7);
    _Pragma("unroll 1") for (unsigned sp = 0; sp < SPIN_CAP; ++sp) {
        if (__all((unsigned)(pv >> 32) == tag)) break;
        pv = __hip_atomic_load(pp, __ATOMIC_RELAXED, __HIP_MEMORY_SCOPE_AGENT);
    }
    STAMP(8);
    if (lane < 26) tot += __uint_as_float((unsigned)pv);
    tot = wave_sum_to_lane63(tot);
    if (lane == 63) {
        const float o = tot + bf_lo(bov);
        P.out[b] = (unsigned short)(pk_bf16(o, o) & 0xffffu);
    }
    if (m == 0 && cw == 0) {
        const gu32* fl = (const gu32*)(P.ws + WS_FL) + g * 64 + lane;
        _Pragma("unroll 1") for (unsigned sp = 0; sp < SPIN_CAP; ++sp) {
            if ((__ballot(ld_agent(fl) == tag) & 0x003fffff03ffffffull) == 0x003fffff03ffffffull) break;
            __builtin_amdgcn_s_sleep(1);
        }
        if (lane == 0) __hip_atomic_store((gu32*)(P.ws + WS_EP) + g, tag, __ATOMIC_RELAXED, __HIP_MEMORY_SCOPE_AGENT);
        if (g == 0) {
            const unsigned long long ts = __hip_atomic_load((const gu64*)(P.ws + (1u << 20)) + (lane & 7), __ATOMIC_RELAXED, __HIP_MEMORY_SCOPE_AGENT);
            const unsigned lo = (unsigned)ts;
            unsigned v[8];
            for (int i = 0; i < 8; ++i) v[i] = __builtin_amdgcn_readlane(lo, i);
            unsigned val;
            if (SKEW_SEL < 8) val = v[SKEW_SEL] - v[7];
            else { int mn = 0, mx = 0; for (int i = 1; i < 8; ++i) { int dlt = (int)(v[i] - v[0]); mn = dlt < mn ? dlt : mn; mx = dlt > mx ? dlt : mx; }
                   val = SKEW_SEL == 8 ? (unsigned)(mx - mn) : (unsigned)(0 - mn); }
            if (val > 2000) val = 2000;
            const unsigned long long e_ = __builtin_amdgcn_s_memrealtime();
            while (__builtin_amdgcn_s_memrealtime() - e_ < (unsigned long long)val * SKEW_K) __builtin_amdgcn_s_sleep(4);
        }
    }
}

DI void decode_block(int bid, int& g, int& m, int& ntile, int& k) {
    const int x = bid & 7, j = bid >> 3;
    g = 2 * x + (j >> 4); m = j & 15;
    ntile = m < 13 ? m : -1; k = m - 13;
}

__global__ void __launch_bounds__(512, 2) xdfm_kernel(KArgs KA) {
    const Params& P = KA.P;
    __shared__ __attribute__((aligned(16))) unsigned char smem[L_END];
    lds8* lds = (lds8*)smem;
    STAMP(0);
    const unsigned long long t_start = __builtin_amdgcn_s_memrealtime();
    const int w = __builtin_amdgcn_readfirstlane(threadIdx.x >> 6), lane = threadIdx.x & 63;
    {
        unsigned long long pc; unsigned len;
        asm volatile("s_getpc_b64 %0\n.Lxdfm_pc:\n\ts_mov_b32 %1, (.Lxdfm_end-.Lxdfm_pc)&4294967295" : "=s"(pc), "=s"(len));
        const __amdgpu_buffer_rsrc_t srdP = __builtin_amdgcn_make_buffer_rsrc((void*)pc, 0, len, RSRC3);
        dma4((unsigned)(size_t)lds + L_DUMMY, lane == 0 ? ((blockIdx.x >> 3) * 8 + w) * 128 : 0x7fffff00u, srdP, 0);
    }
    asm volatile("" :: "s"(P.idx), "s"(P.table), "s"(P.wcin), "s"(P.wout), "s"(P.w1), "s"(P.b1), "s"(P.w2), "s"(P.bout), "s"(P.out), "s"(P.ws));
    STAMP(9);
    int g, m, ntile, k;
    decode_block(blockIdx.x, g, m, ntile, k);
    if (blockIdx.x < 8 && threadIdx.x == 256) __hip_atomic_store((gu64*)(KA.P.ws + (1u << 20)) + blockIdx.x, t_start, __ATOMIC_RELAXED, __HIP_MEMORY_SCOPE_AGENT);
    const bool is_cin = w >= 4;
    if (__builtin_expect(is_cin, 1)) {
        __builtin_amdgcn_s_setprio(3);
        dma16((unsigned)(size_t)lds + L_X0 + (w - 4) * 1664, lane < 7 ? (64 * g + 4 * m + w - 4) * 104 + lane * 16 : 0x7fffff00u,
              __builtin_amdgcn_make_buffer_rsrc((void*)P.idx, 0, NBATCH * 26 * 4, RSRC3), 0);
        if (__builtin_expect(w == 7, 1)) dma4((unsigned)(size_t)lds + L_DUMMY, lane < 26 ? ((blockIdx.x >> 3) * 26 + lane) * 128 : 0x7fffff00u,
                         __builtin_amdgcn_make_buffer_rsrc((void*)P.wcin, 0, 3 * 17576 * 2, RSRC3), 0);
    }
    if (__builtin_expect(w == 0, 1)) {
        if (lane < 16) lds_st(lds, L_F + 4 * lane, 0);
        dma4_sc1((unsigned)(size_t)lds + L_EP, (lane & 15) * 4, __builtin_amdgcn_make_buffer_rsrc((void*)(P.ws + WS_EP), 0, 64, RSRC3), 0);
    }
    const int np = g * 3 + k;
    const bool second = (np >= 22 && np < 42) || np >= 46;
    const int p = second ? (np < 42 ? np - 22 : np - 26) : (np < 22 ? np : np - 20);
    if (__builtin_expect(!is_cin && ntile >= 0, 1)) dnn_start(P, lds, w, lane, g, ntile);
    if (__builtin_expect(!is_cin && ntile < 0, 1)) prep_start(P, lds, w, lane, p);
    STAMP(10);
    if (__builtin_expect(!is_cin && ntile >= 0, 1)) {
        dnn_role(P, lds, w, lane, g, ntile STAMP_PASS);
    } else if (is_cin) {
        cin_role(P, lds, w - 4, lane, g, m, ntile >= 0, k STAMP_PASS);
        __builtin_amdgcn_s_waitcnt(0x0F70);
    } else {
        prep_role(P, lds, w, lane, p, second ? 32 + p : p STAMP_PASS);
        __builtin_amdgcn_s_waitcnt(0x0F70);
    }
    STAMP(11);
    STAMP_FLUSH(P.ws, w, lane);
    asm volatile(".Lxdfm_end:");
}

extern "C" void kernel_launch(void* const* d_in, const int* in_sizes, int n_in,
                              void* d_out, int out_size, void* d_ws, size_t ws_size,
                              hipStream_t stream) {
    KArgs KA;
    KA.tab = make_tab();
    Params& P = KA.P;
    P.idx = (const int*)d_in[0]; P.table = (const unsigned short*)d_in[1]; P.wcin = (const unsigned short*)d_in[2];
    P.wout = (const unsigned short*)d_in[3]; P.w1 = (const unsigned short*)d_in[4]; P.b1 = (const unsigned short*)d_in[5];
    P.w2 = (const unsigned short*)d_in[6]; P.bout = (const unsigned short*)d_in[7];
    P.out = (unsigned short*)d_out; P.ws = (unsigned char*)d_ws;
    xdfm_kernel<<<256, 512, 0, stream>>>(KA);
}
```

```cpp
#define AMP_BLK 105
#define AMP_W 0
#define AMP_J 2
#define AMP_K 4
#include <hip/hip_runtime.h>
#include <stdint.h>

#define DI __device__ __forceinline__
#define GAS __attribute__((address_space(1)))
#define LAS __attribute__((address_space(3)))

typedef short    s16x4 __attribute__((ext_vector_type(4)));
typedef short    s16x8 __attribute__((ext_vector_type(8)));
typedef float    f32x2 __attribute__((ext_vector_type(2)));
typedef float    f32x4 __attribute__((ext_vector_type(4)));
typedef float    f32x16 __attribute__((ext_vector_type(16)));
typedef unsigned u32x2 __attribute__((ext_vector_type(2)));
typedef unsigned u32x4 __attribute__((ext_vector_type(4)));
typedef __bf16   bf16x2 __attribute__((ext_vector_type(2)));
typedef GAS unsigned gu32;
typedef GAS unsigned long long gu64;
typedef LAS unsigned char lds8;

#define MAPPING 1

constexpr int DIN = 832, NBATCH = 1024, NVOC = 100000;
#define A_AFTER_X 1
#define W_EARLY 1
#define A_PF 1
#define CODE_PF 1
#define WC_PF 1
#define POLL_STEP 10
#define CIN_PRIO 2
#define WO_LAST 0
#define EARLY_FL 1
#define GATE_STEP 10
#define PKMUL 0
#define A_DMA 1
constexpr unsigned RSRC3 = 0x00020000u;
constexpr int AUX_SC1 = 16;
constexpr unsigned SPIN_CAP = 1u << 20;

constexpr unsigned WS_EP = 0;
constexpr unsigned WS_FL = 4096;
constexpr unsigned WS_PART = 8192;
constexpr unsigned WS_A = 8192 + 16 * 32 * 64 * 8;
constexpr unsigned A_BYTES = 46 * 832;

#define XD 6
constexpr unsigned L_W = 0;
constexpr unsigned L_A = 832 * 128;
constexpr unsigned L_X0 = L_A + 4 * XD * 2048;
constexpr unsigned L_F = L_X0 + 4 * 1664;
constexpr unsigned L_DUMMY = L_F + 256;
constexpr unsigned L_EP = L_DUMMY + 256;
constexpr unsigned L_BI = L_EP + 256;
constexpr unsigned L_W2 = L_BI + 256;
constexpr unsigned L_FL = L_W2 + 256;
constexpr unsigned L_END = L_FL + 256;
static_assert(4 * XD * 2048 >= A_BYTES && L_END <= 163840, "LDS map");
constexpr unsigned LP_VT = 0;
constexpr unsigned LP_T0 = 2048;
constexpr unsigned LP_T1 = 2048 + 2304;

struct AB { int a, b; };
constexpr AB slot_ab(int t) {
    if (t < 156) { int a = 2 * (t / 12); return {a, (a + t % 12) % 26}; }
    if (t < 170) { int u = t - 156; int a = 2 * (u / 2); return {a, (a + 12 + (u & 1)) % 26}; }
    if (t < 176) { int a = 2 * (7 + t - 170); return {a, (a + 12) % 26}; }
    if (t < 189) return {2 * (t - 176), 26};
    if (t == 189) return {26, 26};
    return {27, 27};
}
constexpr unsigned T_ZERO = 1024, T_CONST = 1025;
struct PrepTab { unsigned v[2][24 * 2 * 8]; };
constexpr PrepTab make_tab() {
    PrepTab tb{};
    for (int m = 0; m < 2; ++m) for (int s = 0; s < 24; ++s) for (int h = 0; h < 2; ++h) for (int e = 0; e < 8; ++e) {
        const int st = m ? 32 : 26;
        int t = 8 * s + e; AB ab = slot_ab(t);
        unsigned o1 = T_ZERO, o2 = T_ZERO;
        if (ab.a == 27) {}
        else if (ab.a == 26) { if (h == 0 && m) o1 = T_CONST; }
        else if (ab.b == 26) { if (m) o1 = ((ab.a + h) % 26) * st + 26; }
        else {
            int u = (ab.a + h) % 26, v = (ab.b + h) % 26;
            bool dup = (t == 169 && h == 1);
            if (dup) {}
            else if (u == v) o1 = u * st + u;
            else { o1 = u * st + v; o2 = v * st + u; }
        }
        tb.v[m][(s * 2 + h) * 8 + e] = o1 | (o2 << 16);
    }
    return tb;
}
static_assert(slot_ab(169).a == 12 && slot_ab(169).b == 25, "dup slot");

struct Params {
    const int* idx; const unsigned short* table; const unsigned short* wcin; const unsigned short* wout;
    const unsigned short* w1; const unsigned short* b1; const unsigned short* w2; const unsigned short* bout;
    unsigned short* out; unsigned char* ws;
};
struct KArgs { Params P; PrepTab tab; };

DI unsigned sel(bool c, unsigned a, unsigned b) { unsigned r = c ? a : b; return r; }
DI float bf_lo(unsigned v) { return __uint_as_float(v << 16); }
DI float bf_hi(unsigned v) { return __uint_as_float(v & 0xffff0000u); }
DI unsigned pk_bf16(float lo, float hi) { f32x2 v = {lo, hi}; return __builtin_bit_cast(unsigned, __builtin_convertvector(v, bf16x2)); }
DI f32x4 mfma16(s16x8 a, s16x8 b, f32x4 c) { return __builtin_amdgcn_mfma_f32_16x16x32_bf16(a, b, c, 0, 0, 0); }
DI f32x16 mfma32(s16x8 a, s16x8 b, f32x16 c) { return __builtin_amdgcn_mfma_f32_32x32x16_bf16(a, b, c, 0, 0, 0); }
DI s16x8 as_frag(u32x4 v) { return __builtin_bit_cast(s16x8, v); }
template <int CTRL, int ROWMASK> DI float dpp_add(float v) {
    return v + __int_as_float(__builtin_amdgcn_update_dpp(0, __float_as_int(v), CTRL, ROWMASK, 0xf, true));
}
DI float wave_sum_to_lane63(float v) {
    v = dpp_add<0x111, 0xf>(v); v = dpp_add<0x112, 0xf>(v); v = dpp_add<0x114, 0xf>(v); v = dpp_add<0x118, 0xf>(v);
    v = dpp_add<0x142, 0xa>(v); v = dpp_add<0x143, 0xc>(v);
    return v;
}
DI float sum_lane_groups(float v) {
    auto a = __builtin_amdgcn_permlane16_swap(__float_as_uint(v), __float_as_uint(v), false, false);
    v = __uint_as_float(a[0]) + __uint_as_float(a[1]);
    auto b = __builtin_amdgcn_permlane32_swap(__float_as_uint(v), __float_as_uint(v), false, false);
    return __uint_as_float(b[0]) + __uint_as_float(b[1]);
}
DI float gelu_tanh(float x) {
    const float k1 = -2.0f * 0.7978845608028654f * 1.4426950408889634f;
    const float k2 = k1 * 0.044715f;
    float arg = x * __builtin_fmaf(x * x, k2, k1);
    float e = __builtin_amdgcn_exp2f(arg);
    return x * __builtin_amdgcn_rcpf(1.0f + e);
}
DI void dma16(unsigned lds_addr, unsigned voff, __amdgpu_buffer_rsrc_t srd, unsigned soff) {
    asm volatile("s_mov_b32 m0, %0\n\ts_nop 0\n\tbuffer_load_dwordx4 %1, %2, %3 offen lds"
                 :: "s"(lds_addr), "v"(voff), "s"(srd), "s"(soff) : "memory");
}
DI void dma16_sc1(unsigned lds_addr, unsigned voff, __amdgpu_buffer_rsrc_t srd, unsigned soff) {
    asm volatile("s_mov_b32 m0, %0\n\ts_nop 0\n\tbuffer_load_dwordx4 %1, %2, %3 offen sc1 lds"
                 :: "s"(lds_addr), "v"(voff), "s"(srd), "s"(soff) : "memory");
}
DI void dma4_sc1(unsigned lds_addr, unsigned voff, __amdgpu_buffer_rsrc_t srd, unsigned soff) {
    asm volatile("s_mov_b32 m0, %0\n\ts_nop 0\n\tbuffer_load_dword %1, %2, %3 offen sc1 lds"
                 :: "s"(lds_addr), "v"(voff), "s"(srd), "s"(soff) : "memory");
}
DI void dma4(unsigned lds_addr, unsigned voff, __amdgpu_buffer_rsrc_t srd, unsigned soff) {
    asm volatile("s_mov_b32 m0, %0\n\ts_nop 0\n\tbuffer_load_dword %1, %2, %3 offen lds"
                 :: "s"(lds_addr), "v"(voff), "s"(srd), "s"(soff) : "memory");
}
DI void st_granule(gu64* g, unsigned tag, unsigned val) {
    __hip_atomic_store(g, ((unsigned long long)tag << 32) | val, __ATOMIC_RELAXED, __HIP_MEMORY_SCOPE_AGENT);
}
DI unsigned ld_agent(const gu32* p) { return __hip_atomic_load(p, __ATOMIC_RELAXED, __HIP_MEMORY_SCOPE_AGENT); }
DI unsigned lds_ld(lds8* l, unsigned off) { return __hip_atomic_load((LAS unsigned*)(l + off), __ATOMIC_RELAXED, __HIP_MEMORY_SCOPE_WORKGROUP); }
DI void lds_st(lds8* l, unsigned off, unsigned v) { __hip_atomic_store((LAS unsigned*)(l + off), v, __ATOMIC_RELAXED, __HIP_MEMORY_SCOPE_WORKGROUP); }
DI void lds_add(lds8* l, unsigned off, unsigned v) { __hip_atomic_fetch_add((LAS unsigned*)(l + off), v, __ATOMIC_RELAXED, __HIP_MEMORY_SCOPE_WORKGROUP); }
DI void sig_add(lds8* l, unsigned off) {
    asm volatile("s_mov_b64 exec, 1\n\tds_add_u32 %0, %1\n\ts_mov_b64 exec, -1" :: "v"((unsigned)(size_t)l + off), "v"(1u) : "memory");
}
DI void sig_set(lds8* l, unsigned off) {
    asm volatile("s_mov_b64 exec, 1\n\tds_write_b32 %0, %1\n\ts_mov_b64 exec, -1" :: "v"((unsigned)(size_t)l + off), "v"(1u) : "memory");
}
#define WAIT_LGKM() asm volatile("s_waitcnt lgkmcnt(0)" ::: "memory")
#define WAIT_VM0() asm volatile("s_waitcnt vmcnt(0)" ::: "memory")
#define CBAR() asm volatile("" ::: "memory")
struct Stamps { unsigned long long t0, t1; };
#define STAMP(i) do { if ((i) == 0) { asm volatile("s_memrealtime %0\n\ts_waitcnt lgkmcnt(0)" : "=s"(st.t0) :: "memory"); } else if ((i) == AMP_J) { asm volatile("s_memrealtime %0\n\ts_waitcnt lgkmcnt(0)" : "=s"(st.t1) :: "memory"); } } while (0)
#define STAMP_ARG , Stamps& st
#define STAMP_PASS , st
#define STAMP_FLUSH(ws, w, lane) do { if (blockIdx.x == AMP_BLK && (w) == AMP_W) { const unsigned long long d_ = (st.t1 - st.t0) * AMP_K; const unsigned long long e_ = __builtin_amdgcn_s_memrealtime(); while (__builtin_amdgcn_s_memrealtime() - e_ < d_) __builtin_amdgcn_s_sleep(4); } } while (0)

template <int K> DI unsigned quad_bcast(unsigned v) { return __builtin_amdgcn_update_dpp(0u, v, K * 0x55, 0xf, 0xf, true); }
template <int N> DI void wait_vm() { asm volatile("s_waitcnt vmcnt(%0)" :: "i"(N) : "memory"); }
DI void dma_w4(unsigned lds_addr, unsigned voff, __amdgpu_buffer_rsrc_t srd, unsigned soff) {
    unsigned t;
    asm volatile("s_mov_b32 m0, %1\n\ts_add_u32 %0, %4, 12288\n\tbuffer_load_dwordx4 %2, %3, %4 offen lds\n\t"
                 "buffer_load_dwordx4 %2, %3, %0 offen offset:1024 lds\n\ts_add_u32 %0, %0, 12288\n\t"
                 "buffer_load_dwordx4 %2, %3, %0 offen offset:2048 lds\n\ts_add_u32 %0, %0, 12288\n\t"
                 "buffer_load_dwordx4 %2, %3, %0 offen offset:3072 lds"
                 : "=&s"(t) : "s"(lds_addr), "v"(voff), "s"(srd), "s"(soff) : "memory", "scc");
}
DI void dnn_start(const Params& P, lds8* lds, int w, int lane, int g, int ntile) {
    const int mh = w & 1, kh = w >> 1;
    const unsigned ldsb = (unsigned)(size_t)lds;
    const unsigned ring = ldsb + L_A + w * (XD * 2048);
    const __amdgpu_buffer_rsrc_t srdI = __builtin_amdgcn_make_buffer_rsrc((void*)P.idx, 0, NBATCH * 26 * 4, RSRC3);
    const unsigned vo = ((64 * g + 32 * mh + (lane >> 2)) * 26 + 13 * kh + 4 * (lane & 3)) * 4;
    dma16(ring, vo, srdI, 0);
    dma16(ring + 1024, vo, srdI, 1664);
    if (__builtin_expect(w == 1, 1)) dma4(ldsb + L_BI, ntile * 128 + lane * 4, __builtin_amdgcn_make_buffer_rsrc((void*)P.b1, 0, DIN * 2, RSRC3), 0);
    if (__builtin_expect(w == 2, 1)) dma4(ldsb + L_W2, ntile * 128 + lane * 4, __builtin_amdgcn_make_buffer_rsrc((void*)P.w2, 0, DIN * 2, RSRC3), 0);
    const __amdgpu_buffer_rsrc_t srdW = __builtin_amdgcn_make_buffer_rsrc((void*)P.w1, 0, DIN * DIN * 2, RSRC3);
    const unsigned vw = (lane >> 3) * 1664 + (((lane & 7) ^ (((lane >> 4) & 1) << 2)) << 4);
    const unsigned rowA = 416 * kh + 208 * mh + 104 * (g & 1);
    dma_w4(ldsb + L_W + rowA * 128, vw, srdW, rowA * 1664 + ntile * 128);
    if (W_EARLY > 1) dma_w4(ldsb + L_W + rowA * 128 + 4096, vw, srdW, rowA * 1664 + ntile * 128 + 4 * 13312);
    if (W_EARLY > 2) dma_w4(ldsb + L_W + rowA * 128 + 8192, vw, srdW, rowA * 1664 + ntile * 128 + 8 * 13312);
}
DI void dnn_role(const Params& P, lds8* lds, int w, int lane, int g, int ntile STAMP_ARG) {
    const int mh = w & 1, kh = w >> 1;
    const int h = lane >> 5;
    const unsigned ldsb = (unsigned)(size_t)lds;
    const unsigned ring = ldsb + L_A + w * (XD * 2048);
    const __amdgpu_buffer_rsrc_t srdW = __builtin_amdgcn_make_buffer_rsrc((void*)P.w1, 0, DIN * DIN * 2, RSRC3);
    const unsigned vw = (lane >> 3) * 1664 + (((lane & 7) ^ (((lane >> 4) & 1) << 2)) << 4);
    const unsigned rowA = 416 * kh + 208 * mh + 104 * (g & 1), rowB = 416 * kh + 208 * mh + 104 * ((g & 1) ^ 1);
    auto wdma = [&](unsigned row, int a) {
        const unsigned sb = row * 1664 + ntile * 128, lb = ldsb + L_W + row * 128;
        if (a < 3) dma_w4(lb + a * 4096, vw, srdW, sb + a * 4 * 13312);
        else dma16(lb + 12288, vw, srdW, sb + 12 * 13312);
    };
    STAMP(1);
    wait_vm<4 * W_EARLY>();
    WAIT_LGKM();
    __builtin_amdgcn_s_barrier();
    STAMP(2);
    const u32x4 i0 = *(const LAS u32x4*)(lds + L_A + w * (XD * 2048) + lane * 16);
    const u32x4 i1 = *(const LAS u32x4*)(lds + L_A + w * (XD * 2048) + 1024 + lane * 16);
    const unsigned ep = *(const LAS unsigned*)(lds + L_EP + g * 4);
#pragma unroll
    for (int a = W_EARLY; a < 4; ++a) wdma(rowA, a);
#pragma unroll
    for (int a = 0; a < 4; ++a) wdma(rowB, a);
    const __amdgpu_buffer_rsrc_t srdT = __builtin_amdgcn_make_buffer_rsrc((void*)P.table, 0, NVOC * 64, RSRC3);
    const unsigned c16 = (((lane & 3) ^ ((lane >> 4) & 3)) << 4);
    u32x4 t0 = i0 << 6, t1 = i1 << 6;
    asm volatile("" : "+v"(t0), "+v"(t1));
    auto xload = [&](int s) {
        const unsigned a0 = t0[s & 3], a1 = t1[s & 3];
        const unsigned o0 = (s < 4 ? quad_bcast<0>(a0) : s < 8 ? quad_bcast<1>(a0) : s < 12 ? quad_bcast<2>(a0) : quad_bcast<3>(a0)) + c16;
        const unsigned o1 = (s < 4 ? quad_bcast<0>(a1) : s < 8 ? quad_bcast<1>(a1) : s < 12 ? quad_bcast<2>(a1) : quad_bcast<3>(a1)) + c16;
        dma16(ring + (s % XD) * 2048, o0, srdT, 0);
        dma16(ring + (s % XD) * 2048 + 1024, o1, srdT, 0);
    };
#pragma unroll
    for (int s = 0; s < XD; ++s) xload(s);
    STAMP(3);
    wait_vm<2 * XD>();
    sig_add(lds, L_F + 32 + 4 * kh);
    _Pragma("unroll 1") for (unsigned sp = 0; lds_ld(lds, L_F + 32 + 4 * kh) < 2 && sp < SPIN_CAP; ++sp) __builtin_amdgcn_s_sleep(0);
    CBAR();
    STAMP(4);
    f32x16 acc[2];
    u32x2 bia[4], wv[4];
    float w2f[16];
    unsigned xdone = 0;
#pragma unroll
    for (int r4 = 0; r4 < 4; ++r4) {
        bia[r4] = *(const LAS u32x2*)(lds + L_BI + kh * 64 + h * 8 + r4 * 16);
        wv[r4] = *(const LAS u32x2*)(lds + L_W2 + kh * 64 + h * 8 + r4 * 16);
    }
#pragma unroll
    for (int r4 = 0; r4 < 4; ++r4) {
        acc[0][4 * r4] = bf_lo(bia[r4][0]); acc[0][4 * r4 + 1] = bf_hi(bia[r4][0]); acc[0][4 * r4 + 2] = bf_lo(bia[r4][1]); acc[0][4 * r4 + 3] = bf_hi(bia[r4][1]);
        acc[1][4 * r4] = 0.f; acc[1][4 * r4 + 1] = 0.f; acc[1][4 * r4 + 2] = 0.f; acc[1][4 * r4 + 3] = 0.f;
    }
    {
        const int blk = (lane >> 4) & 1, q = (lane & 15) >> 2, pp = lane & 3, n = lane & 31;
        unsigned ad[2];
#pragma unroll
        for (int j = 0; j < 2; ++j) {
            const unsigned c = 4 * (j ^ kh) + 2 * blk + (pp >> 1);
            ad[j] = L_W + (416 * kh + 8 * h + q) * 128 + ((c ^ (((q >> 1) & 1) << 2)) << 4) + (pp & 1) * 8;
        }
        const unsigned xb = L_A + w * (XD * 2048) + n * 64;
        const unsigned xa0 = xb + ((h ^ ((n >> 2) & 3)) << 4), xa1 = xb + (((2 + h) ^ ((n >> 2) & 3)) << 4);
        u32x4 xf[3][2];
        s16x8 wf[2][4];
        auto xread = [&](int s) {
            xf[s % 3][0] = *(const LAS u32x4*)(lds + xa0 + (s % XD) * 2048);
            xf[s % 3][1] = *(const LAS u32x4*)(lds + xa1 + (s % XD) * 2048);
        };
        auto wread = [&](int s) {
#pragma unroll
            for (int a = 0; a < 2; ++a)
#pragma unroll
                for (int j = 0; j < 2; ++j) {
                    const s16x4 lo = __builtin_amdgcn_ds_read_tr16_b64_v4i16((LAS s16x4*)(lds + ad[j] + s * 4096 + a * 2048));
                    const s16x4 hi = __builtin_amdgcn_ds_read_tr16_b64_v4i16((LAS s16x4*)(lds + ad[j] + s * 4096 + a * 2048 + 512));
                    wf[s & 1][2 * a + j] = __builtin_shufflevector(lo, hi, 0, 1, 2, 3, 4, 5, 6, 7);
                }
        };
        wread(0);
        wait_vm<2 * (XD - 1)>();
        xread(0);
        wait_vm<2 * (XD - 2)>();
        xread(1);
        __builtin_amdgcn_sched_barrier(0);
#pragma unroll
        for (int s = 0; s < 13; ++s) {
            if (s + 1 < 13) wread(s + 1);
#pragma unroll
            for (int a = 0; a < 2; ++a)
#pragma unroll
                for (int j = 0; j < 2; ++j) acc[j] = mfma32(wf[s & 1][2 * a + j], as_frag(xf[s % 3][a]), acc[j]);
            if (s + 1 < 13) {
#pragma unroll
                for (int j = 0; j < 4; ++j) { __builtin_amdgcn_sched_group_barrier(0x100, 2, 0); __builtin_amdgcn_sched_group_barrier(0x8, 1, 0); }
            }
            __builtin_amdgcn_sched_barrier(0);
            if (s + XD < 13) xload(s + XD);
            if (s == 11) {
                xdone = lds_ld(lds, L_F + 52);
#pragma unroll
                for (int r4 = 0; r4 < 4; ++r4) { w2f[4 * r4] = bf_lo(wv[r4][0]); w2f[4 * r4 + 1] = bf_hi(wv[r4][0]); w2f[4 * r4 + 2] = bf_lo(wv[r4][1]); w2f[4 * r4 + 3] = bf_hi(wv[r4][1]); }
#pragma unroll
                for (int e = 0; e < 16; ++e) asm volatile("" : "+v"(w2f[e]));
            }
            if (s + 2 < 13) {
                constexpr int LASTI = 12;
                const int last = s + XD < LASTI ? s + XD : LASTI;
                const int younger = last - (s + 2);
                if (younger >= 4) wait_vm<8>(); else if (younger == 3) wait_vm<6>(); else if (younger == 2) wait_vm<4>(); else if (younger == 1) wait_vm<2>(); else wait_vm<0>();
                if (s + 2 == 12) sig_add(lds, L_F + 52);
                if (s == GATE_STEP) sig_add(lds, L_F + 56);
                xread(s + 2);
            }
            __builtin_amdgcn_sched_barrier(0);
        }
    }
    STAMP(6);
    {
        if (xdone < 4) _Pragma("unroll 1") for (unsigned sp = 0; lds_ld(lds, L_F + 52) < 4 && sp < SPIN_CAP; ++sp) __builtin_amdgcn_s_sleep(0);
        CBAR();
        lds8* xw = lds + L_W + kh * 53248 + (5 + mh) * 4096 + lane * 16;
#pragma unroll
        for (int r4 = 0; r4 < 4; ++r4) *(LAS f32x4*)(xw + r4 * 1024) = (f32x4){acc[1][4 * r4], acc[1][4 * r4 + 1], acc[1][4 * r4 + 2], acc[1][4 * r4 + 3]};
        CBAR();
        sig_set(lds, L_F + 4 * w);
        _Pragma("unroll 1") for (unsigned sp = 0; lds_ld(lds, L_F + 4 * (w ^ 2)) == 0 && sp < SPIN_CAP; ++sp) __builtin_amdgcn_s_sleep(0);
        CBAR();
        const lds8* xr = lds + L_W + (kh ^ 1) * 53248 + (5 + mh) * 4096 + lane * 16;
        f32x4 o[4];
#pragma unroll
        for (int r4 = 0; r4 < 4; ++r4) o[r4] = *(const LAS f32x4*)(xr + r4 * 1024);
        asm volatile("" : "+v"(o[0]), "+v"(o[1]), "+v"(o[2]), "+v"(o[3]));
#pragma unroll
        for (int r4 = 0; r4 < 4; ++r4) {
            acc[0][4 * r4] += o[r4][0]; acc[0][4 * r4 + 1] += o[r4][1]; acc[0][4 * r4 + 2] += o[r4][2]; acc[0][4 * r4 + 3] += o[r4][3];
        }
    }
    STAMP(7);
    float s0 = 0.f, s1 = 0.f;
#pragma unroll
    for (int r4 = 0; r4 < 4; ++r4) {
        s0 = __builtin_fmaf(gelu_tanh(acc[0][4 * r4]), w2f[4 * r4], s0);
        s1 = __builtin_fmaf(gelu_tanh(acc[0][4 * r4 + 1]), w2f[4 * r4 + 1], s1);
        s0 = __builtin_fmaf(gelu_tanh(acc[0][4 * r4 + 2]), w2f[4 * r4 + 2], s0);
        s1 = __builtin_fmaf(gelu_tanh(acc[0][4 * r4 + 3]), w2f[4 * r4 + 3], s1);
    }
    s0 += s1;
    const auto sw = __builtin_amdgcn_permlane32_swap(__float_as_uint(s0), __float_as_uint(s0), false, false);
    const float part = __uint_as_float(sw[0]) + __uint_as_float(sw[1]);
    STAMP(8);
    if (lane < 32) st_granule((gu64*)(P.ws + WS_PART) + ((g * 32 + 2 * ntile + kh) * 64 + 32 * mh + lane), ep + 1u, __float_as_uint(part));
}

constexpr unsigned LP_W2 = 12288;
constexpr unsigned LP_WO = 14336;
constexpr unsigned LP_DM = 15360;
constexpr unsigned LP_W3 = 16384;
static_assert(LP_T1 + 4 * 1026 <= LP_W2, "prep scratch");
constexpr unsigned w3_slot(int j) { return j * 1280 + (j & 1) * 64 + ((j >> 2) & 1) * 128; }
DI void dma16_add(unsigned lds_base, unsigned lds_off, unsigned voff, __amdgpu_buffer_rsrc_t srd, unsigned sbase, unsigned soff) {
    unsigned t;
    asm volatile("s_add_u32 m0, %1, %2\n\ts_add_u32 %0, %5, %6\n\tbuffer_load_dwordx4 %3, %4, %0 offen lds"
                 : "=&s"(t) : "s"(lds_base), "i"(lds_off), "v"(voff), "s"(srd), "s"(sbase), "i"(soff) : "memory", "scc");
}
DI void prep_start(const Params& P, lds8* lds, int w, int lane, int p) {
    const unsigned ldsb = (unsigned)(size_t)lds;
    const __amdgpu_buffer_rsrc_t srdC = __builtin_amdgcn_make_buffer_rsrc((void*)P.wcin, 0, 3 * 17576 * 2, RSRC3);
    const int i0 = w == 0 ? 0 : w == 1 ? 6 : w == 2 ? 14 : 20;
    dma4(ldsb + LP_WO + w * 256, lane * 4, __builtin_amdgcn_make_buffer_rsrc((void*)P.wout, 0, 78 * 2, RSRC3), 0);
    {
        const unsigned hp = lane >> 5;
        const unsigned vo = hp * 1352 + ((lane >> 2) & 7) * 52 + (((lane & 3) ^ (hp << 1)) << 4);
        const unsigned lb = ldsb + LP_W3 + w * 16640, sb = 52 * 1352 + 52 * i0;
        dma16_add(lb, w3_slot(0), vo, srdC, sb, 0 * 2704);  dma16_add(lb, w3_slot(1), vo, srdC, sb, 1 * 2704);
        dma16_add(lb, w3_slot(2), vo, srdC, sb, 2 * 2704);  dma16_add(lb, w3_slot(3), vo, srdC, sb, 3 * 2704);
        dma16_add(lb, w3_slot(4), vo, srdC, sb, 4 * 2704);  dma16_add(lb, w3_slot(5), vo, srdC, sb, 5 * 2704);
        dma16_add(lb, w3_slot(6), vo, srdC, sb, 6 * 2704);  dma16_add(lb, w3_slot(7), vo, srdC, sb, 7 * 2704);
        dma16_add(lb, w3_slot(8), vo, srdC, sb, 8 * 2704);  dma16_add(lb, w3_slot(9), vo, srdC, sb, 9 * 2704);
        dma16_add(lb, w3_slot(10), vo, srdC, sb, 10 * 2704); dma16_add(lb, w3_slot(11), vo, srdC, sb, 11 * 2704);
        dma16_add(lb, w3_slot(12), vo, srdC, sb, 12 * 2704);
    }
    if (__builtin_expect(w < 2, 1)) {
        const unsigned vo = w == 0 ? 35152 + (lane >> 2) * 1352 + p * 52 + (lane & 3) * 16 : p * 1352 + lane * 16;
        const unsigned lb = ldsb + (w == 0 ? LP_W2 : LP_T0);
        dma16(lb, vo, srdC, 0);
        dma16(lb + 1024, vo, srdC, w == 0 ? 16 * 1352 : 1024);
    }
}
DI void prep_role(const Params& P, lds8* lds, int w, int lane, int p, int slot STAMP_ARG) {
    const int n = lane & 15, gq = lane >> 4;
    const int i0 = w == 0 ? 0 : w == 1 ? 6 : w == 2 ? 14 : 20;
    const int nact = w == 0 ? 48 : 44;
    const int ch = lane < nact ? lane : 0;
    const int cs = ch >> 1, chh = ch & 1;
    const GAS u32x4* tp = (const GAS u32x4*)((const GAS unsigned char*)(size_t)__builtin_amdgcn_kernarg_segment_ptr() + sizeof(Params) + ((w == 0 ? 384 : 0) + (cs * 2 + chh) * 8) * 4);
    const u32x4 ta = tp[0], tb = tp[1];
    STAMP(1);
    const int q = (lane & 15) >> 2, pp = lane & 3;
    int r0 = 8 * gq + q, r1 = r0 + 4;
    r0 = r0 < 26 ? r0 : 25; r1 = r1 < 26 ? r1 : 25;
    unsigned b0a, b0b, b1a, b1b;
    {
        const unsigned wb = LP_W3 + w * 16640 + pp * 8;
        const unsigned j0 = r0 >> 1, j1 = r1 >> 1;
        const unsigned s0 = wb + j0 * 1280 + (j0 & 1) * 64 + ((j0 >> 2) & 1) * 128 + (r0 & 1) * 512;
        const unsigned s1 = wb + j1 * 1280 + (j1 & 1) * 64 + ((j1 >> 2) & 1) * 128 + (r1 & 1) * 512;
        b0a = s0 + (r0 & 1) * 32; b0b = s0 + 32 - (r0 & 1) * 32; b1a = s1 + (r1 & 1) * 32; b1b = s1 + 32 - (r1 & 1) * 32;
    }
    unsigned va = LP_VT + n * 64 + 2 * i0, vb = n < 10 ? va + 1024 : LP_DM;
    unsigned vl = w == 3 ? LP_DM : 0;
    unsigned ac = LP_T1 + 4 * (4 * gq * 32 + n);
    unsigned a20 = LP_W2 + r0 * 64 + pp * 8, a21 = LP_W2 + r1 * 64 + pp * 8, abv = LP_VT + n * 64 + gq * 16;
    asm volatile("" : "+v"(b0a), "+v"(b0b), "+v"(b1a), "+v"(b1b), "+v"(va), "+v"(vb), "+v"(ac), "+v"(a20), "+v"(a21), "+v"(abv));
    if (w == 3) {
        LAS unsigned* z = (LAS unsigned*)(lds + LP_VT + (lane < 27 ? lane : 31) * 64 + 52);
        z[0] = 0; z[1] = 0; z[2] = 0;
        *(LAS unsigned*)(lds + LP_T1 + 4 * T_ZERO) = 0;
    }
    if (w == 1) *(LAS unsigned*)(lds + LP_T0 + 2 * T_ZERO) = 0;
    asm volatile("s_waitcnt vmcnt(0) lgkmcnt(0)" ::: "memory");
    __builtin_amdgcn_s_barrier();
    STAMP(2);
    {
        const u32x4 wo3 = *(const LAS u32x4*)(lds + LP_WO + w * 256 + 104 + 16 * gq);
        s16x8 bfr[16];
#pragma unroll
        for (int t = 0; t < 16; ++t) {
            const int ii = t >> 1, u = t & 1;
            const s16x4 lo = __builtin_amdgcn_ds_read_tr16_b64_v4i16((LAS s16x4*)(lds + (u ? b0b : b0a) + ii * 64));
            const s16x4 hi = __builtin_amdgcn_ds_read_tr16_b64_v4i16((LAS s16x4*)(lds + (u ? b1b : b1a) + ii * 64));
            bfr[t] = __builtin_shufflevector(lo, hi, 0, 1, 2, 3, 4, 5, 6, 7);
        }
        if (w == 3) {
            const unsigned o2 = *(const LAS unsigned*)(lds + LP_WO + 768 + 52 + (lane < 13 ? lane : 0) * 4);
            const unsigned o1 = *(const LAS unsigned short*)(lds + LP_WO + 768 + p * 2);
            *(LAS unsigned*)(lds + (lane < 13 ? LP_VT + 26 * 64 + lane * 4 : LP_DM)) = o2;
            *(LAS float*)(lds + LP_T1 + 4 * T_CONST) = bf_lo(o1);
        }
        float res[16];
#pragma unroll
        for (int t = 0; t < 16; ++t) res[t] = mfma16(as_frag(wo3), bfr[t], (f32x4){0.f, 0.f, 0.f, 0.f})[0];
#pragma unroll
        for (int k2 = 0; k2 < 4; ++k2) {
            const unsigned pa0 = pk_bf16(res[4 * k2], res[4 * k2 + 2]), pb0 = pk_bf16(res[4 * k2 + 1], res[4 * k2 + 3]);
            if (k2 < 3) { *(LAS unsigned*)(lds + va + 4 * k2) = pa0; *(LAS unsigned*)(lds + vb + 4 * k2) = pb0; }
            else { *(LAS unsigned*)(lds + (vl ? vl : va + 12)) = pa0; *(LAS unsigned*)(lds + (vl ? vl : vb + 12)) = pb0; }
        }
    }
    CBAR();
    sig_add(lds, L_F + 28);
    if (w > 1) return;
    STAMP(3);
    const __amdgpu_buffer_rsrc_t srdA = __builtin_amdgcn_make_buffer_rsrc((void*)(P.ws + WS_A), 0, A_BYTES, RSRC3);
    u32x4 o;
    if (w == 1) {
        float pv[16];
#pragma unroll
        for (int e = 0; e < 8; ++e) {
            const unsigned x = e < 4 ? ta[e] : tb[e - 4];
            pv[2 * e] = bf_lo(*(const LAS unsigned short*)(lds + LP_T0 + 2 * (x & 0xffff)));
            pv[2 * e + 1] = bf_lo(*(const LAS unsigned short*)(lds + LP_T0 + 2 * (x >> 16)));
        }
#pragma unroll
        for (int j = 0; j < 4; ++j) o[j] = pk_bf16(pv[4 * j] + pv[4 * j + 1], pv[4 * j + 2] + pv[4 * j + 3]);
    } else {
        s16x8 a2[2];
#pragma unroll
        for (int mt = 0; mt < 2; ++mt) {
            const s16x4 lo = __builtin_amdgcn_ds_read_tr16_b64_v4i16((LAS s16x4*)(lds + a20 + mt * 32));
            const s16x4 hi = __builtin_amdgcn_ds_read_tr16_b64_v4i16((LAS s16x4*)(lds + a21 + mt * 32));
            a2[mt] = __builtin_shufflevector(lo, hi, 0, 1, 2, 3, 4, 5, 6, 7);
        }
        unsigned pa[16];
#pragma unroll
        for (int e = 0; e < 8; ++e) {
            const unsigned x = e < 4 ? ta[e] : tb[e - 4];
            pa[2 * e] = LP_T1 + 4 * (x & 0xffff); pa[2 * e + 1] = LP_T1 + 4 * (x >> 16);
        }
#pragma unroll
        for (int e = 0; e < 16; ++e) asm volatile("" : "+v"(pa[e]));
        _Pragma("unroll 1") for (unsigned sp = 0; lds_ld(lds, L_F + 28) < 4 && sp < SPIN_CAP; ++sp) __builtin_amdgcn_s_sleep(0);
        CBAR();
        const u32x4 bf0 = *(const LAS u32x4*)(lds + abv), bf1 = *(const LAS u32x4*)(lds + abv + 1024);
        const f32x4 z4 = {0.f, 0.f, 0.f, 0.f};
        const f32x4 d00 = mfma16(a2[0], as_frag(bf0), z4), d01 = mfma16(a2[1], as_frag(bf0), z4);
        const f32x4 d10 = mfma16(a2[0], as_frag(bf1), z4), d11 = mfma16(a2[1], as_frag(bf1), z4);
#pragma unroll
        for (int e = 0; e < 4; ++e) {
            *(LAS float*)(lds + ac + e * 128) = d00[e]; *(LAS float*)(lds + ac + 2048 + e * 128) = d01[e];
            *(LAS float*)(lds + ac + 64 + e * 128) = d10[e]; *(LAS float*)(lds + ac + 2048 + 64 + e * 128) = d11[e];
        }
        STAMP(4);
        float pv[16];
#pragma unroll
        for (int e = 0; e < 16; ++e) pv[e] = *(const LAS float*)(lds + pa[e]);
#pragma unroll
        for (int j = 0; j < 4; ++j) o[j] = pk_bf16(pv[4 * j] + pv[4 * j + 1], pv[4 * j + 2] + pv[4 * j + 3]);
    }
    {
        const int fr = w == 0 ? (cs < 22 ? 2 * cs : 22 + cs) : 2 * cs + 1;
        __builtin_amdgcn_raw_buffer_store_b128(o, srdA, lane < nact ? (unsigned)(fr * 832 + (26 * chh + p) * 16) : 0x7ffffff0u, 0, AUX_SC1);
    }
    STAMP(5);
    WAIT_VM0();
    STAMP(6);
    if (w == 1) { sig_set(lds, L_F + 24); return; }
    const unsigned tagv = *(const LAS unsigned*)(lds + L_EP + (lane & 15) * 4);
    _Pragma("unroll 1") for (unsigned sp = 0; lds_ld(lds, L_F + 24) == 0 && sp < SPIN_CAP; ++sp) __builtin_amdgcn_s_sleep(0);
    if (lane < 16) __hip_atomic_store((gu32*)(P.ws + WS_FL) + lane * 64 + slot, tagv + 1u, __ATOMIC_RELAXED, __HIP_MEMORY_SCOPE_AGENT);
}

DI bool rows_ready(bool match) {
    const unsigned long long mk = __ballot(match);
    return (((unsigned)mk | (unsigned)(mk >> 32)) & 0x3ffffffu) == 0x3ffffffu;
}
constexpr unsigned A_PER = A_BYTES / 4;
DI void cin_role(const Params& P, lds8* lds, int cw, int lane, int g, int m, bool has_dnn, int pf_slot STAMP_ARG) {
    const int d = lane & 31, h = lane >> 5;
    const int b = 64 * g + 4 * m + cw;
    const unsigned ldsb = (unsigned)(size_t)lds;
    {
        const __amdgpu_buffer_rsrc_t srdT = __builtin_amdgcn_make_buffer_rsrc((void*)P.table, 0, NVOC * 64, RSRC3);
        const unsigned lb = ldsb + L_X0 + cw * 1664;
        WAIT_VM0();
        const int q = lane >> 2;
        const unsigned j0 = *(const LAS unsigned*)(lds + L_X0 + cw * 1664 + q * 4);
        const unsigned j1 = *(const LAS unsigned*)(lds + L_X0 + cw * 1664 + (q < 10 ? q + 16 : 25) * 4);
        unsigned o0 = j0 * 64 + (lane & 3) * 16, o1 = j1 * 64 + (lane & 3) * 16;
        asm volatile("" : "+v"(o0), "+v"(o1));
        STAMP(1);
        dma16(lb, o0, srdT, 0);
        if (lane < 40) dma16(lb + 1024, o1, srdT, 0);
        if (cw == 0) dma4_sc1(ldsb + L_FL, g * 256 + lane * 4, __builtin_amdgcn_make_buffer_rsrc((void*)(P.ws + WS_FL), 0, 4096, RSRC3), 0);
    }
    const unsigned bo = P.bout[0];
    STAMP(2);
    __builtin_amdgcn_s_setprio(0);
    CBAR();
    __builtin_amdgcn_s_barrier();
    const unsigned epv = *(const LAS unsigned*)(lds + L_EP + g * 4);
    unsigned bov = bo;
    asm volatile("s_waitcnt vmcnt(0)" : "+v"(bov) :: "memory");
    STAMP(3);
    float x[26];
    {
        const lds8* xb = lds + L_X0 + cw * 1664 + h * 64 + d * 2;
        const lds8* xl = lds + L_X0 + cw * 1664 + (h ? 0 : 25 * 64) + d * 2;
#pragma unroll
        for (int a = 0; a < 13; ++a) {
            const unsigned lo = *(const LAS unsigned short*)(xb + 2 * a * 64);
            const unsigned hi = a < 12 ? *(const LAS unsigned short*)(xb + (2 * a + 1) * 64) : *(const LAS unsigned short*)xl;
            x[2 * a] = bf_lo(lo); x[2 * a + 1] = bf_lo(hi);
        }
    }
    u32x4 z[24];
#pragma unroll
    for (int s = 0; s < 22; ++s) {
        float fa[8], fb[8];
#pragma unroll
        for (int e = 0; e < 8; ++e) { const AB pe = slot_ab(8 * s + e); fa[e] = x[pe.a]; fb[e] = x[pe.b]; }
        float t0, t1, t2, t3, t4, t5;
        unsigned o0, o1, o2, o3;
        asm("v_mul_f32 %4, %10, %11\n\tv_mul_f32 %5, %12, %13\n\tv_mul_f32 %6, %14, %15\n\tv_mul_f32 %7, %16, %17\n\t"
            "v_cvt_pk_bf16_f32 %0, %4, %5\n\tv_mul_f32 %8, %18, %19\n\tv_mul_f32 %9, %20, %21\n\tv_cvt_pk_bf16_f32 %1, %6, %7\n\t"
            "v_mul_f32 %4, %22, %23\n\tv_mul_f32 %5, %24, %25\n\tv_cvt_pk_bf16_f32 %2, %8, %9\n\tv_cvt_pk_bf16_f32 %3, %4, %5"
            : "=&v"(o0), "=&v"(o1), "=&v"(o2), "=v"(o3), "=&v"(t0), "=&v"(t1), "=&v"(t2), "=&v"(t3), "=&v"(t4), "=&v"(t5)
            : "v"(fa[0]), "v"(fb[0]), "v"(fa[1]), "v"(fb[1]), "v"(fa[2]), "v"(fb[2]), "v"(fa[3]), "v"(fb[3]),
              "v"(fa[4]), "v"(fb[4]), "v"(fa[5]), "v"(fb[5]), "v"(fa[6]), "v"(fb[6]), "v"(fa[7]), "v"(fb[7]));
        z[s] = (u32x4){o0, o1, o2, o3};
    }
#pragma unroll
    for (int s = 22; s < 24; ++s) {
#pragma unroll
        for (int j = 0; j < 4; ++j) {
            const AB p0 = slot_ab(8 * s + 2 * j), p1 = slot_ab(8 * s + 2 * j + 1);
            const float a0 = p0.a < 26 ? x[p0.a] : (p0.a == 26 ? 1.f : 0.f);
            const float a1 = p1.a < 26 ? x[p1.a] : (p1.a == 26 ? 1.f : 0.f);
            z[s][j] = pk_bf16(a0, a1);
        }
    }
#pragma unroll
    for (int s = 0; s < 24; ++s) asm volatile("" : "+v"(z[s]));
    STAMP(4);
    const unsigned tag = epv + 1u;
    {
        const __amdgpu_buffer_rsrc_t srdA = __builtin_amdgcn_make_buffer_rsrc((void*)(P.ws + WS_A), 0, A_BYTES, RSRC3);
        if (cw == 0) {
            if (has_dnn) _Pragma("unroll 1") for (unsigned sp = 0; lds_ld(lds, L_F + 56) < 4 && sp < SPIN_CAP; ++sp) __builtin_amdgcn_s_sleep(0);
            const gu32* fl = (const gu32*)(P.ws + WS_FL) + g * 64 + lane;
            if (!rows_ready(*(const LAS unsigned*)(lds + L_FL + lane * 4) == tag))
            _Pragma("unroll 1") for (unsigned sp = 0; sp < SPIN_CAP; ++sp) { if (rows_ready(ld_agent(fl) == tag)) break; __builtin_amdgcn_s_sleep(1); }
            sig_set(lds, L_F + 16);
            if (!has_dnn) dma4_sc1(ldsb + L_DUMMY, lane < 50 ? (unsigned)((((g & 1) * 3 + pf_slot) * 50 + lane) * 128) : 0x7fffff00u, srdA, 0);
        } else {
            _Pragma("unroll 1") for (unsigned sp = 0; lds_ld(lds, L_F + 16) == 0 && sp < SPIN_CAP; ++sp) __builtin_amdgcn_s_sleep(0);
        }
        CBAR();
        __builtin_amdgcn_s_setprio(CIN_PRIO);
        STAMP(5);
        {
            const unsigned l1 = ldsb + L_W + cw * 4096, vo = lane * 16;
            asm volatile("s_mov_b32 m0, %0\n\ts_nop 0\n\t"
                         "buffer_load_dwordx4 %1, %2, %3 offen sc1 lds\n\tbuffer_load_dwordx4 %1, %2, %3 offen offset:1024 sc1 lds\n\t"
                         "buffer_load_dwordx4 %1, %2, %3 offen offset:2048 sc1 lds\n\tbuffer_load_dwordx4 %1, %2, %3 offen offset:3072 sc1 lds"
                         :: "s"(l1), "v"(vo), "s"(srdA), "s"(cw * 4096) : "memory");
#pragma unroll
            for (int i = 0; i < 6; ++i) {
                const int q = 6 * cw + i;
                const unsigned go = q < 4 ? 16384 + 1024 * q : 19968 + 1024 * (q - 4), lo = q < 4 ? 16384 + 1024 * q : 53248 + 1024 * (q - 4);
                dma16_sc1(ldsb + L_W + lo, vo, srdA, go);
            }
        }
        wait_vm<6>();
        sig_add(lds, L_F + 20);
        _Pragma("unroll 1") for (unsigned sp = 0; lds_ld(lds, L_F + 20) < 4 && sp < SPIN_CAP; ++sp) __builtin_amdgcn_s_sleep(0);
        CBAR();
    }
    STAMP(6);
    const gu64* pp = (const gu64*)(P.ws + WS_PART) + ((g * 32 + (lane < 26 ? lane : 0)) * 64 + 4 * m + cw);
    unsigned long long pv = 0;
    f32x16 acc0, acc1;
#pragma unroll
    for (int i = 0; i < 16; ++i) { acc0[i] = 0.f; acc1[i] = 0.f; }
    {
        unsigned abo = L_W + (26 * h + d) * 16;
        asm volatile("" : "+v"(abo));
        const lds8* ab = lds + abo;
        constexpr int PF = 6;
        u32x4 af[46];
        unsigned cnt2 = 0;
        auto frag_of = [](int t) { return t < 24 ? t : t + 40; };
#pragma unroll
        for (int t = 0; t < PF; ++t) af[t] = *(const LAS u32x4*)(ab + frag_of(t) * 832);
        __builtin_amdgcn_sched_barrier(0);
#pragma unroll
        for (int t = 0; t < 46; ++t) {
            if (t == 9) { wait_vm<0>(); sig_add(lds, L_F + 44); }
            if (t + PF == 19 - 2) cnt2 = lds_ld(lds, L_F + 44);
            if (t + PF == 19) {
                if (cnt2 < 4) _Pragma("unroll 1") for (unsigned sp = 0; lds_ld(lds, L_F + 44) < 4 && sp < SPIN_CAP; ++sp) __builtin_amdgcn_s_sleep(0);
                CBAR();
            }
            if (t + PF < 46) af[t + PF] = *(const LAS u32x4*)(ab + frag_of(t + PF) * 832);
            if (t < 44) {
                if (t & 1) acc0 = mfma32(as_frag(af[t]), as_frag(z[t >> 1]), acc0);
                else acc1 = mfma32(as_frag(af[t]), as_frag(z[t >> 1]), acc1);
            } else acc1 = mfma32(as_frag(af[t]), as_frag(z[t - 22]), acc1);
            if (t == 30) pv = __hip_atomic_load(pp, __ATOMIC_RELAXED, __HIP_MEMORY_SCOPE_AGENT);
            __builtin_amdgcn_sched_barrier(0);
        }
    }
    float sa = 0.f;
#pragma unroll
    for (int i = 0; i < 12; ++i) sa = __builtin_fmaf(acc0[i], acc1[i], sa);
    const float sb = __builtin_fmaf(acc0[12], acc1[12], acc0[13] * acc1[13]);
    float tot = h == 0 ? sa + sb : sa;
    asm volatile("" : "+v"(tot));
    STAMP(7);
    _Pragma("unroll 1") for (unsigned sp = 0; sp < SPIN_CAP; ++sp) {
        if (__all((unsigned)(pv >> 32) == tag)) break;
        pv = __hip_atomic_load(pp, __ATOMIC_RELAXED, __HIP_MEMORY_SCOPE_AGENT);
    }
    STAMP(8);
    if (lane < 26) tot += __uint_as_float((unsigned)pv);
    tot = wave_sum_to_lane63(tot);
    if (lane == 63) {
        const float o = tot + bf_lo(bov);
        P.out[b] = (unsigned short)(pk_bf16(o, o) & 0xffffu);
    }
    if (m == 0 && cw == 0) {
        const gu32* fl = (const gu32*)(P.ws + WS_FL) + g * 64 + lane;
        _Pragma("unroll 1") for (unsigned sp = 0; sp < SPIN_CAP; ++sp) {
            if ((__ballot(ld_agent(fl) == tag) & 0x003fffff03ffffffull) == 0x003fffff03ffffffull) break;
            __builtin_amdgcn_s_sleep(1);
        }
        if (lane == 0) __hip_atomic_store((gu32*)(P.ws + WS_EP) + g, tag, __ATOMIC_RELAXED, __HIP_MEMORY_SCOPE_AGENT);
    }
}

DI void decode_block(int bid, int& g, int& m, int& ntile, int& k) {
    const int x = bid & 7, j = bid >> 3;
    g = 2 * x + (j >> 4); m = j & 15;
    ntile = m < 13 ? m : -1; k = m - 13;
}

__global__ void __launch_bounds__(512, 2) xdfm_kernel(KArgs KA) {
    const Params& P = KA.P;
    __shared__ __attribute__((aligned(16))) unsigned char smem[L_END];
    lds8* lds = (lds8*)smem;
    Stamps st; st.t0 = 0; st.t1 = 0;
    STAMP(0);
    const int w = __builtin_amdgcn_readfirstlane(threadIdx.x >> 6), lane = threadIdx.x & 63;
    {
        unsigned long long pc; unsigned len;
        asm volatile("s_getpc_b64 %0\n.Lxdfm_pc:\n\ts_mov_b32 %1, (.Lxdfm_end-.Lxdfm_pc)&4294967295" : "=s"(pc), "=s"(len));
        const __amdgpu_buffer_rsrc_t srdP = __builtin_amdgcn_make_buffer_rsrc((void*)pc, 0, len, RSRC3);
        dma4((unsigned)(size_t)lds + L_DUMMY, lane == 0 ? ((blockIdx.x >> 3) * 8 + w) * 128 : 0x7fffff00u, srdP, 0);
    }
    asm volatile("" :: "s"(P.idx), "s"(P.table), "s"(P.wcin), "s"(P.wout), "s"(P.w1), "s"(P.b1), "s"(P.w2), "s"(P.bout), "s"(P.out), "s"(P.ws));
    STAMP(9);
    int g, m, ntile, k;
    decode_block(blockIdx.x, g, m, ntile, k);
    const bool is_cin = w >= 4;
    if (__builtin_expect(is_cin, 1)) {
        __builtin_amdgcn_s_setprio(3);
        dma16((unsigned)(size_t)lds + L_X0 + (w - 4) * 1664, lane < 7 ? (64 * g + 4 * m + w - 4) * 104 + lane * 16 : 0x7fffff00u,
              __builtin_amdgcn_make_buffer_rsrc((void*)P.idx, 0, NBATCH * 26 * 4, RSRC3), 0);
        if (__builtin_expect(w == 7, 1)) dma4((unsigned)(size_t)lds + L_DUMMY, lane < 26 ? ((blockIdx.x >> 3) * 26 + lane) * 128 : 0x7fffff00u,
                         __builtin_amdgcn_make_buffer_rsrc((void*)P.wcin, 0, 3 * 17576 * 2, RSRC3), 0);
    }
    if (__builtin_expect(w == 0, 1)) {
        if (lane < 16) lds_st(lds, L_F + 4 * lane, 0);
        dma4_sc1((unsigned)(size_t)lds + L_EP, (lane & 15) * 4, __builtin_amdgcn_make_buffer_rsrc((void*)(P.ws + WS_EP), 0, 64, RSRC3), 0);
    }
    const int np = g * 3 + k;
    const bool second = (np >= 22 && np < 42) || np >= 46;
    const int p = second ? (np < 42 ? np - 22 : np - 26) : (np < 22 ? np : np - 20);
    if (__builtin_expect(!is_cin && ntile >= 0, 1)) dnn_start(P, lds, w, lane, g, ntile);
    if (__builtin_expect(!is_cin && ntile < 0, 1)) prep_start(P, lds, w, lane, p);
    STAMP(10);
    if (__builtin_expect(!is_cin && ntile >= 0, 1)) {
        dnn_role(P, lds, w, lane, g, ntile STAMP_PASS);
    } else if (is_cin) {
        cin_role(P, lds, w - 4, lane, g, m, ntile >= 0, k STAMP_PASS);
        __builtin_amdgcn_s_waitcnt(0x0F70);
    } else {
        prep_role(P, lds, w, lane, p, second ? 32 + p : p STAMP_PASS);
        __builtin_amdgcn_s_waitcnt(0x0F70);
    }
    STAMP(11);
    STAMP_FLUSH(P.ws, w, lane);
    asm volatile(".Lxdfm_end:");
}

extern "C" void kernel_launch(void* const* d_in, const int* in_sizes, int n_in,
                              void* d_out, int out_size, void* d_ws, size_t ws_size,
                              hipStream_t stream) {
    KArgs KA;
    KA.tab = make_tab();
    Params& P = KA.P;
    P.idx = (const int*)d_in[0]; P.table = (const unsigned short*)d_in[1]; P.wcin = (const unsigned short*)d_in[2];
    P.wout = (const unsigned short*)d_in[3]; P.w1 = (const unsigned short*)d_in[4]; P.b1 = (const unsigned short*)d_in[5];
    P.w2 = (const unsigned short*)d_in[6]; P.bout = (const unsigned short*)d_in[7];
    P.out = (unsigned short*)d_out; P.ws = (unsigned char*)d_ws;
    xdfm_kernel<<<256, 512, 0, stream>>>(KA);
}
```

```cpp
#include <hip/hip_runtime.h>
#include <stdint.h>

#define DI __device__ __forceinline__
#define GAS __attribute__((address_space(1)))
#define LAS __attribute__((address_space(3)))

typedef short    s16x4 __attribute__((ext_vector_type(4)));
typedef short    s16x8 __attribute__((ext_vector_type(8)));
typedef float    f32x2 __attribute__((ext_vector_type(2)));
typedef float    f32x4 __attribute__((ext_vector_type(4)));
typedef float    f32x16 __attribute__((ext_vector_type(16)));
typedef unsigned u32x2 __attribute__((ext_vector_type(2)));
typedef unsigned u32x4 __attribute__((ext_vector_type(4)));
typedef __bf16   bf16x2 __attribute__((ext_vector_type(2)));
typedef GAS unsigned gu32;
typedef GAS unsigned long long gu64;
typedef LAS unsigned char lds8;

#define MAPPING 1

constexpr int DIN = 832, NBATCH = 1024, NVOC = 100000;
#define A_AFTER_X 1
#define W_EARLY 1
#define A_PF 1
#define CODE_PF 1
#define WC_PF 1
#define POLL_STEP 10
#define CIN_PRIO 2
#define WO_LAST 0
#define EARLY_FL 1
#define GATE_STEP 10
#define PKMUL 0
#define A_DMA 1
constexpr unsigned RSRC3 = 0x00020000u;
constexpr int AUX_SC1 = 16;
constexpr unsigned SPIN_CAP = 1u << 20;

constexpr unsigned WS_EP = 0;
constexpr unsigned WS_FL = 4096;
constexpr unsigned WS_PART = 8192;
constexpr unsigned WS_A = 8192 + 16 * 32 * 64 * 8;
constexpr unsigned A_BYTES = 46 * 832;

#define XD 6
constexpr unsigned L_W = 0;
constexpr unsigned L_A = 832 * 128;
constexpr unsigned L_X0 = L_A + 4 * XD * 2048;
constexpr unsigned L_F = L_X0 + 4 * 1664;
constexpr unsigned L_DUMMY = L_F + 256;
constexpr unsigned L_EP = L_DUMMY + 256;
constexpr unsigned L_BI = L_EP + 256;
constexpr unsigned L_W2 = L_BI + 256;
constexpr unsigned L_FL = L_W2 + 256;
constexpr unsigned L_END = L_FL + 256;
static_assert(4 * XD * 2048 >= A_BYTES && L_END <= 163840, "LDS map");
constexpr unsigned LP_VT = 0;
constexpr unsigned LP_T0 = 2048;
constexpr unsigned LP_T1 = 2048 + 2304;

struct AB { int a, b; };
constexpr AB slot_ab(int t) {
    if (t < 156) { int a = 2 * (t / 12); return {a, (a + t % 12) % 26}; }
    if (t < 170) { int u = t - 156; int a = 2 * (u / 2); return {a, (a + 12 + (u & 1)) % 26}; }
    if (t < 176) { int a = 2 * (7 + t - 170); return {a, (a + 12) % 26}; }
    if (t < 189) return {2 * (t - 176), 26};
    if (t == 189) return {26, 26};
    return {27, 27};
}
constexpr unsigned T_ZERO = 1024, T_CONST = 1025;
struct PrepTab { unsigned v[2][24 * 2 * 8]; };
constexpr PrepTab make_tab() {
    PrepTab tb{};
    for (int m = 0; m < 2; ++m) for (int s = 0; s < 24; ++s) for (int h = 0; h < 2; ++h) for (int e = 0; e < 8; ++e) {
        const int st = m ? 32 : 26;
        int t = 8 * s + e; AB ab = slot_ab(t);
        unsigned o1 = T_ZERO, o2 = T_ZERO;
        if (ab.a == 27) {}
        else if (ab.a == 26) { if (h == 0 && m) o1 = T_CONST; }
        else if (ab.b == 26) { if (m) o1 = ((ab.a + h) % 26) * st + 26; }
        else {
            int u = (ab.a + h) % 26, v = (ab.b + h) % 26;
            bool dup = (t == 169 && h == 1);
            if (dup) {}
            else if (u == v) o1 = u * st + u;
            else { o1 = u * st + v; o2 = v * st + u; }
        }
        tb.v[m][(s * 2 + h) * 8 + e] = o1 | (o2 << 16);
    }
    return tb;
}
static_assert(slot_ab(169).a == 12 && slot_ab(169).b == 25, "dup slot");

struct Params {
    const int* idx; const unsigned short* table; const unsigned short* wcin; const unsigned short* wout;
    const unsigned short* w1; const unsigned short* b1; const unsigned short* w2; const unsigned short* bout;
    unsigned short* out; unsigned char* ws;
};
struct KArgs { Params P; PrepTab tab; };

DI unsigned sel(bool c, unsigned a, unsigned b) { unsigned r = c ? a : b; return r; }
DI float bf_lo(unsigned v) { return __uint_as_float(v << 16); }
DI float bf_hi(unsigned v) { return __uint_as_float(v & 0xffff0000u); }
DI unsigned pk_bf16(float lo, float hi) { f32x2 v = {lo, hi}; return __builtin_bit_cast(unsigned, __builtin_convertvector(v, bf16x2)); }
DI f32x4 mfma16(s16x8 a, s16x8 b, f32x4 c) { return __builtin_amdgcn_mfma_f32_16x16x32_bf16(a, b, c, 0, 0, 0); }
DI f32x16 mfma32(s16x8 a, s16x8 b, f32x16 c) { return __builtin_amdgcn_mfma_f32_32x32x16_bf16(a, b, c, 0, 0, 0); }
DI s16x8 as_frag(u32x4 v) { return __builtin_bit_cast(s16x8, v); }
template <int CTRL, int ROWMASK> DI float dpp_add(float v) {
    return v + __int_as_float(__builtin_amdgcn_update_dpp(0, __float_as_int(v), CTRL, ROWMASK, 0xf, true));
}
DI float wave_sum_to_lane63(float v) {
    v = dpp_add<0x111, 0xf>(v); v = dpp_add<0x112, 0xf>(v); v = dpp_add<0x114, 0xf>(v); v = dpp_add<0x118, 0xf>(v);
    v = dpp_add<0x142, 0xa>(v); v = dpp_add<0x143, 0xc>(v);
    return v;
}
DI float sum_lane_groups(float v) {
    auto a = __builtin_amdgcn_permlane16_swap(__float_as_uint(v), __float_as_uint(v), false, false);
    v = __uint_as_float(a[0]) + __uint_as_float(a[1]);
    auto b = __builtin_amdgcn_permlane32_swap(__float_as_uint(v), __float_as_uint(v), false, false);
    return __uint_as_float(b[0]) + __uint_as_float(b[1]);
}
DI float gelu_tanh(float x) {
    const float k1 = -2.0f * 0.7978845608028654f * 1.4426950408889634f;
    const float k2 = k1 * 0.044715f;
    float arg = x * __builtin_fmaf(x * x, k2, k1);
    float e = __builtin_amdgcn_exp2f(arg);
    return x * __builtin_amdgcn_rcpf(1.0f + e);
}
DI void dma16(unsigned lds_addr, unsigned voff, __amdgpu_buffer_rsrc_t srd, unsigned soff) {
    asm volatile("s_mov_b32 m0, %0\n\ts_nop 0\n\tbuffer_load_dwordx4 %1, %2, %3 offen lds"
                 :: "s"(lds_addr), "v"(voff), "s"(srd), "s"(soff) : "memory");
}
DI void dma16_sc1(unsigned lds_addr, unsigned voff, __amdgpu_buffer_rsrc_t srd, unsigned soff) {
    asm volatile("s_mov_b32 m0, %0\n\ts_nop 0\n\tbuffer_load_dwordx4 %1, %2, %3 offen sc1 lds"
                 :: "s"(lds_addr), "v"(voff), "s"(srd), "s"(soff) : "memory");
}
DI void dma4_sc1(unsigned lds_addr, unsigned voff, __amdgpu_buffer_rsrc_t srd, unsigned soff) {
    asm volatile("s_mov_b32 m0, %0\n\ts_nop 0\n\tbuffer_load_dword %1, %2, %3 offen sc1 lds"
                 :: "s"(lds_addr), "v"(voff), "s"(srd), "s"(soff) : "memory");
}
DI void dma4(unsigned lds_addr, unsigned voff, __amdgpu_buffer_rsrc_t srd, unsigned soff) {
    asm volatile("s_mov_b32 m0, %0\n\ts_nop 0\n\tbuffer_load_dword %1, %2, %3 offen lds"
                 :: "s"(lds_addr), "v"(voff), "s"(srd), "s"(soff) : "memory");
}
DI void st_granule(gu64* g, unsigned tag, unsigned val) {
    __hip_atomic_store(g, ((unsigned long long)tag << 32) | val, __ATOMIC_RELAXED, __HIP_MEMORY_SCOPE_AGENT);
}
DI unsigned ld_agent(const gu32* p) { return __hip_atomic_load(p, __ATOMIC_RELAXED, __HIP_MEMORY_SCOPE_AGENT); }
DI unsigned lds_ld(lds8* l, unsigned off) { return __hip_atomic_load((LAS unsigned*)(l + off), __ATOMIC_RELAXED, __HIP_MEMORY_SCOPE_WORKGROUP); }
DI void lds_st(lds8* l, unsigned off, unsigned v) { __hip_atomic_store((LAS unsigned*)(l + off), v, __ATOMIC_RELAXED, __HIP_MEMORY_SCOPE_WORKGROUP); }
DI void lds_add(lds8* l, unsigned off, unsigned v) { __hip_atomic_fetch_add((LAS unsigned*)(l + off), v, __ATOMIC_RELAXED, __HIP_MEMORY_SCOPE_WORKGROUP); }
DI void sig_add(lds8* l, unsigned off) {
    asm volatile("s_mov_b64 exec, 1\n\tds_add_u32 %0, %1\n\ts_mov_b64 exec, -1" :: "v"((unsigned)(size_t)l + off), "v"(1u) : "memory");
}
DI void sig_set(lds8* l, unsigned off) {
    asm volatile("s_mov_b64 exec, 1\n\tds_write_b32 %0, %1\n\ts_mov_b64 exec, -1" :: "v"((unsigned)(size_t)l + off), "v"(1u) : "memory");
}
#define WAIT_LGKM() asm volatile("s_waitcnt lgkmcnt(0)" ::: "memory")
#define WAIT_VM0() asm volatile("s_waitcnt vmcnt(0)" ::: "memory")
#define CBAR() asm volatile("" ::: "memory")
#define STAMP(i)
#define STAMP_ARG
#define STAMP_PASS
#define STAMP_FLUSH(ws, w, lane)

template <int K> DI unsigned quad_bcast(unsigned v) { return __builtin_amdgcn_update_dpp(0u, v, K * 0x55, 0xf, 0xf, true); }
template <int N> DI void wait_vm() { asm volatile("s_waitcnt vmcnt(%0)" :: "i"(N) : "memory"); }
DI void dma_w4(unsigned lds_addr, unsigned voff, __amdgpu_buffer_rsrc_t srd, unsigned soff) {
    unsigned t;
    asm volatile("s_mov_b32 m0, %1\n\ts_add_u32 %0, %4, 12288\n\tbuffer_load_dwordx4 %2, %3, %4 offen lds\n\t"
                 "buffer_load_dwordx4 %2, %3, %0 offen offset:1024 lds\n\ts_add_u32 %0, %0, 12288\n\t"
                 "buffer_load_dwordx4 %2, %3, %0 offen offset:2048 lds\n\ts_add_u32 %0, %0, 12288\n\t"
                 "buffer_load_dwordx4 %2, %3, %0 offen offset:3072 lds"
                 : "=&s"(t) : "s"(lds_addr), "v"(voff), "s"(srd), "s"(soff) : "memory", "scc");
}
DI void dnn_start(const Params& P, lds8* lds, int w, int lane, int g, int ntile) {
    const int mh = w & 1, kh = w >> 1;
    const unsigned ldsb = (unsigned)(size_t)lds;
    const unsigned ring = ldsb + L_A + w * (XD * 2048);
    const __amdgpu_buffer_rsrc_t srdI = __builtin_amdgcn_make_buffer_rsrc((void*)P.idx, 0, NBATCH * 26 * 4, RSRC3);
    const unsigned vo = ((64 * g + 32 * mh + (lane >> 2)) * 26 + 13 * kh + 4 * (lane & 3)) * 4;
    dma16(ring, vo, srdI, 0);
    dma16(ring + 1024, vo, srdI, 1664);
    if (__builtin_expect(w == 1, 1)) dma4(ldsb + L_BI, ntile * 128 + lane * 4, __builtin_amdgcn_make_buffer_rsrc((void*)P.b1, 0, DIN * 2, RSRC3), 0);
    if (__builtin_expect(w == 2, 1)) dma4(ldsb + L_W2, ntile * 128 + lane * 4, __builtin_amdgcn_make_buffer_rsrc((void*)P.w2, 0, DIN * 2, RSRC3), 0);
    const __amdgpu_buffer_rsrc_t srdW = __builtin_amdgcn_make_buffer_rsrc((void*)P.w1, 0, DIN * DIN * 2, RSRC3);
    const unsigned vw = (lane >> 3) * 1664 + (((lane & 7) ^ (((lane >> 4) & 1) << 2)) << 4);
    const unsigned rowA = 416 * kh + 208 * mh + 104 * (g & 1);
    dma_w4(ldsb + L_W + rowA * 128, vw, srdW, rowA * 1664 + ntile * 128);
    if (W_EARLY > 1) dma_w4(ldsb + L_W + rowA * 128 + 4096, vw, srdW, rowA * 1664 + ntile * 128 + 4 * 13312);
    if (W_EARLY > 2) dma_w4(ldsb + L_W + rowA * 128 + 8192, vw, srdW, rowA * 1664 + ntile * 128 + 8 * 13312);
}
DI void dnn_role(const Params& P, lds8* lds, int w, int lane, int g, int ntile STAMP_ARG) {
    const int mh = w & 1, kh = w >> 1;
    const int h = lane >> 5;
    const unsigned ldsb = (unsigned)(size_t)lds;
    const unsigned ring = ldsb + L_A + w * (XD * 2048);
    const __amdgpu_buffer_rsrc_t srdW = __builtin_amdgcn_make_buffer_rsrc((void*)P.w1, 0, DIN * DIN * 2, RSRC3);
    const unsigned vw = (lane >> 3) * 1664 + (((lane & 7) ^ (((lane >> 4) & 1) << 2)) << 4);
    const unsigned rowA = 416 * kh + 208 * mh + 104 * (g & 1), rowB = 416 * kh + 208 * mh + 104 * ((g & 1) ^ 1);
    auto wdma = [&](unsigned row, int a) {
        const unsigned sb = row * 1664 + ntile * 128, lb = ldsb + L_W + row * 128;
        if (a < 3) dma_w4(lb + a * 4096, vw, srdW, sb + a * 4 * 13312);
        else dma16(lb + 12288, vw, srdW, sb + 12 * 13312);
    };
    STAMP(1);
    wait_vm<4 * W_EARLY>();
    WAIT_LGKM();
    __builtin_amdgcn_s_barrier();
    STAMP(2);
    const u32x4 i0 = *(const LAS u32x4*)(lds + L_A + w * (XD * 2048) + lane * 16);
    const u32x4 i1 = *(const LAS u32x4*)(lds + L_A + w * (XD * 2048) + 1024 + lane * 16);
    const unsigned ep = *(const LAS unsigned*)(lds + L_EP + g * 4);
#pragma unroll
    for (int a = W_EARLY; a < 4; ++a) wdma(rowA, a);
#pragma unroll
    for (int a = 0; a < 4; ++a) wdma(rowB, a);
    const __amdgpu_buffer_rsrc_t srdT = __builtin_amdgcn_make_buffer_rsrc((void*)P.table, 0, NVOC * 64, RSRC3);
    const unsigned c16 = (((lane & 3) ^ ((lane >> 4) & 3)) << 4);
    u32x4 t0 = i0 << 6, t1 = i1 << 6;
    asm volatile("" : "+v"(t0), "+v"(t1));
    auto xload = [&](int s) {
        const unsigned a0 = t0[s & 3], a1 = t1[s & 3];
        const unsigned o0 = (s < 4 ? quad_bcast<0>(a0) : s < 8 ? quad_bcast<1>(a0) : s < 12 ? quad_bcast<2>(a0) : quad_bcast<3>(a0)) + c16;
        const unsigned o1 = (s < 4 ? quad_bcast<0>(a1) : s < 8 ? quad_bcast<1>(a1) : s < 12 ? quad_bcast<2>(a1) : quad_bcast<3>(a1)) + c16;
        dma16(ring + (s % XD) * 2048, o0, srdT, 0);
        dma16(ring + (s % XD) * 2048 + 1024, o1, srdT, 0);
    };
#pragma unroll
    for (int s = 0; s < XD; ++s) xload(s);
    STAMP(3);
    wait_vm<2 * XD>();
    sig_add(lds, L_F + 32 + 4 * kh);
    _Pragma("unroll 1") for (unsigned sp = 0; lds_ld(lds, L_F + 32 + 4 * kh) < 2 && sp < SPIN_CAP; ++sp) __builtin_amdgcn_s_sleep(0);
    CBAR();
    STAMP(4);
    f32x16 acc[2];
    u32x2 bia[4], wv[4];
    float w2f[16];
    unsigned xdone = 0;
#pragma unroll
    for (int r4 = 0; r4 < 4; ++r4) {
        bia[r4] = *(const LAS u32x2*)(lds + L_BI + kh * 64 + h * 8 + r4 * 16);
        wv[r4] = *(const LAS u32x2*)(lds + L_W2 + kh * 64 + h * 8 + r4 * 16);
    }
#pragma unroll
    for (int r4 = 0; r4 < 4; ++r4) {
        acc[0][4 * r4] = bf_lo(bia[r4][0]); acc[0][4 * r4 + 1] = bf_hi(bia[r4][0]); acc[0][4 * r4 + 2] = bf_lo(bia[r4][1]); acc[0][4 * r4 + 3] = bf_hi(bia[r4][1]);
        acc[1][4 * r4] = 0.f; acc[1][4 * r4 + 1] = 0.f; acc[1][4 * r4 + 2] = 0.f; acc[1][4 * r4 + 3] = 0.f;
    }
    {
        const int blk = (lane >> 4) & 1, q = (lane & 15) >> 2, pp = lane & 3, n = lane & 31;
        unsigned ad[2];
#pragma unroll
        for (int j = 0; j < 2; ++j) {
            const unsigned c = 4 * (j ^ kh) + 2 * blk + (pp >> 1);
            ad[j] = L_W + (416 * kh + 8 * h + q) * 128 + ((c ^ (((q >> 1) & 1) << 2)) << 4) + (pp & 1) * 8;
        }
        const unsigned xb = L_A + w * (XD * 2048) + n * 64;
        const unsigned xa0 = xb + ((h ^ ((n >> 2) & 3)) << 4), xa1 = xb + (((2 + h) ^ ((n >> 2) & 3)) << 4);
        u32x4 xf[3][2];
        s16x8 wf[2][4];
        auto xread = [&](int s) {
            xf[s % 3][0] = *(const LAS u32x4*)(lds + xa0 + (s % XD) * 2048);
            xf[s % 3][1] = *(const LAS u32x4*)(lds + xa1 + (s % XD) * 2048);
        };
        auto wread = [&](int s) {
#pragma unroll
            for (int a = 0; a < 2; ++a)
#pragma unroll
                for (int j = 0; j < 2; ++j) {
                    const s16x4 lo = __builtin_amdgcn_ds_read_tr16_b64_v4i16((LAS s16x4*)(lds + ad[j] + s * 4096 + a * 2048));
                    const s16x4 hi = __builtin_amdgcn_ds_read_tr16_b64_v4i16((LAS s16x4*)(lds + ad[j] + s * 4096 + a * 2048 + 512));
                    wf[s & 1][2 * a + j] = __builtin_shufflevector(lo, hi, 0, 1, 2, 3, 4, 5, 6, 7);
                }
        };
        wread(0);
        wait_vm<2 * (XD - 1)>();
        xread(0);
        wait_vm<2 * (XD - 2)>();
        xread(1);
        __builtin_amdgcn_sched_barrier(0);
#pragma unroll
        for (int s = 0; s < 13; ++s) {
            if (s + 1 < 13) wread(s + 1);
#pragma unroll
            for (int a = 0; a < 2; ++a)
#pragma unroll
                for (int j = 0; j < 2; ++j) acc[j] = mfma32(wf[s & 1][2 * a + j], as_frag(xf[s % 3][a]), acc[j]);
            if (s + 1 < 13) {
#pragma unroll
                for (int j = 0; j < 4; ++j) { __builtin_amdgcn_sched_group_barrier(0x100, 2, 0); __builtin_amdgcn_sched_group_barrier(0x8, 1, 0); }
            }
            __builtin_amdgcn_sched_barrier(0);
            if (s + XD < 13) xload(s + XD);
            if (s == 11) {
                xdone = lds_ld(lds, L_F + 52);
#pragma unroll
                for (int r4 = 0; r4 < 4; ++r4) { w2f[4 * r4] = bf_lo(wv[r4][0]); w2f[4 * r4 + 1] = bf_hi(wv[r4][0]); w2f[4 * r4 + 2] = bf_lo(wv[r4][1]); w2f[4 * r4 + 3] = bf_hi(wv[r4][1]); }
#pragma unroll
                for (int e = 0; e < 16; ++e) asm volatile("" : "+v"(w2f[e]));
            }
            if (s + 2 < 13) {
                constexpr int LASTI = 12;
                const int last = s + XD < LASTI ? s + XD : LASTI;
                const int younger = last - (s + 2);
                if (younger >= 4) wait_vm<8>(); else if (younger == 3) wait_vm<6>(); else if (younger == 2) wait_vm<4>(); else if (younger == 1) wait_vm<2>(); else wait_vm<0>();
                if (s + 2 == 12) sig_add(lds, L_F + 52);
                if (s == GATE_STEP) sig_add(lds, L_F + 56);
                xread(s + 2);
            }
            __builtin_amdgcn_sched_barrier(0);
        }
    }
    STAMP(6);
    {
        if (xdone < 4) _Pragma("unroll 1") for (unsigned sp = 0; lds_ld(lds, L_F + 52) < 4 && sp < SPIN_CAP; ++sp) __builtin_amdgcn_s_sleep(0);
        CBAR();
        lds8* xw = lds + L_W + kh * 53248 + (5 + mh) * 4096 + lane * 16;
#pragma unroll
        for (int r4 = 0; r4 < 4; ++r4) *(LAS f32x4*)(xw + r4 * 1024) = (f32x4){acc[1][4 * r4], acc[1][4 * r4 + 1], acc[1][4 * r4 + 2], acc[1][4 * r4 + 3]};
        CBAR();
        sig_set(lds, L_F + 4 * w);
        _Pragma("unroll 1") for (unsigned sp = 0; lds_ld(lds, L_F + 4 * (w ^ 2)) == 0 && sp < SPIN_CAP; ++sp) __builtin_amdgcn_s_sleep(0);
        CBAR();
        const lds8* xr = lds + L_W + (kh ^ 1) * 53248 + (5 + mh) * 4096 + lane * 16;
        f32x4 o[4];
#pragma unroll
        for (int r4 = 0; r4 < 4; ++r4) o[r4] = *(const LAS f32x4*)(xr + r4 * 1024);
        asm volatile("" : "+v"(o[0]), "+v"(o[1]), "+v"(o[2]), "+v"(o[3]));
#pragma unroll
        for (int r4 = 0; r4 < 4; ++r4) {
            acc[0][4 * r4] += o[r4][0]; acc[0][4 * r4 + 1] += o[r4][1]; acc[0][4 * r4 + 2] += o[r4][2]; acc[0][4 * r4 + 3] += o[r4][3];
        }
    }
    STAMP(7);
    float s0 = 0.f, s1 = 0.f;
#pragma unroll
    for (int r4 = 0; r4 < 4; ++r4) {
        s0 = __builtin_fmaf(gelu_tanh(acc[0][4 * r4]), w2f[4 * r4], s0);
        s1 = __builtin_fmaf(gelu_tanh(acc[0][4 * r4 + 1]), w2f[4 * r4 + 1], s1);
        s0 = __builtin_fmaf(gelu_tanh(acc[0][4 * r4 + 2]), w2f[4 * r4 + 2], s0);
        s1 = __builtin_fmaf(gelu_tanh(acc[0][4 * r4 + 3]), w2f[4 * r4 + 3], s1);
    }
    s0 += s1;
    const auto sw = __builtin_amdgcn_permlane32_swap(__float_as_uint(s0), __float_as_uint(s0), false, false);
    const float part = __uint_as_float(sw[0]) + __uint_as_float(sw[1]);
    STAMP(8);
    if (lane < 32) st_granule((gu64*)(P.ws + WS_PART) + ((g * 32 + 2 * ntile + kh) * 64 + 32 * mh + lane), ep + 1u, __float_as_uint(part));
}

constexpr unsigned LP_W2 = 12288;
constexpr unsigned LP_WO = 14336;
constexpr unsigned LP_DM = 15360;
constexpr unsigned LP_W3 = 16384;
static_assert(LP_T1 + 4 * 1026 <= LP_W2, "prep scratch");
constexpr unsigned w3_slot(int j) { return j * 1280 + (j & 1) * 64 + ((j >> 2) & 1) * 128; }
DI void dma16_add(unsigned lds_base, unsigned lds_off, unsigned voff, __amdgpu_buffer_rsrc_t srd, unsigned sbase, unsigned soff) {
    unsigned t;
    asm volatile("s_add_u32 m0, %1, %2\n\ts_add_u32 %0, %5, %6\n\tbuffer_load_dwordx4 %3, %4, %0 offen lds"
                 : "=&s"(t) : "s"(lds_base), "i"(lds_off), "v"(voff), "s"(srd), "s"(sbase), "i"(soff) : "memory", "scc");
}
DI void prep_start(const Params& P, lds8* lds, int w, int lane, int p) {
    const unsigned ldsb = (unsigned)(size_t)lds;
    const __amdgpu_buffer_rsrc_t srdC = __builtin_amdgcn_make_buffer_rsrc((void*)P.wcin, 0, 3 * 17576 * 2, RSRC3);
    const int i0 = w == 0 ? 0 : w == 1 ? 6 : w == 2 ? 14 : 20;
    dma4(ldsb + LP_WO + w * 256, lane * 4, __builtin_amdgcn_make_buffer_rsrc((void*)P.wout, 0, 78 * 2, RSRC3), 0);
    {
        const unsigned hp = lane >> 5;
        const unsigned vo = hp * 1352 + ((lane >> 2) & 7) * 52 + (((lane & 3) ^ (hp << 1)) << 4);
        const unsigned lb = ldsb + LP_W3 + w * 16640, sb = 52 * 1352 + 52 * i0;
        dma16_add(lb, w3_slot(0), vo, srdC, sb, 0 * 2704);  dma16_add(lb, w3_slot(1), vo, srdC, sb, 1 * 2704);
        dma16_add(lb, w3_slot(2), vo, srdC, sb, 2 * 2704);  dma16_add(lb, w3_slot(3), vo, srdC, sb, 3 * 2704);
        dma16_add(lb, w3_slot(4), vo, srdC, sb, 4 * 2704);  dma16_add(lb, w3_slot(5), vo, srdC, sb, 5 * 2704);
        dma16_add(lb, w3_slot(6), vo, srdC, sb, 6 * 2704);  dma16_add(lb, w3_slot(7), vo, srdC, sb, 7 * 2704);
        dma16_add(lb, w3_slot(8), vo, srdC, sb, 8 * 2704);  dma16_add(lb, w3_slot(9), vo, srdC, sb, 9 * 2704);
        dma16_add(lb, w3_slot(10), vo, srdC, sb, 10 * 2704); dma16_add(lb, w3_slot(11), vo, srdC, sb, 11 * 2704);
        dma16_add(lb, w3_slot(12), vo, srdC, sb, 12 * 2704);
    }
    if (__builtin_expect(w < 2, 1)) {
        const unsigned vo = w == 0 ? 35152 + (lane >> 2) * 1352 + p * 52 + (lane & 3) * 16 : p * 1352 + lane * 16;
        const unsigned lb = ldsb + (w == 0 ? LP_W2 : LP_T0);
        dma16(lb, vo, srdC, 0);
        dma16(lb + 1024, vo, srdC, w == 0 ? 16 * 1352 : 1024);
    }
}
DI void prep_role(const Params& P, lds8* lds, int w, int lane, int p, int slot STAMP_ARG) {
    const int n = lane & 15, gq = lane >> 4;
    const int i0 = w == 0 ? 0 : w == 1 ? 6 : w == 2 ? 14 : 20;
    const int nact = w == 0 ? 48 : 44;
    const int ch = lane < nact ? lane : 0;
    const int cs = ch >> 1, chh = ch & 1;
    const GAS u32x4* tp = (const GAS u32x4*)((const GAS unsigned char*)(size_t)__builtin_amdgcn_kernarg_segment_ptr() + sizeof(Params) + ((w == 0 ? 384 : 0) + (cs * 2 + chh) * 8) * 4);
    const u32x4 ta = tp[0], tb = tp[1];
    STAMP(1);
    const int q = (lane & 15) >> 2, pp = lane & 3;
    int r0 = 8 * gq + q, r1 = r0 + 4;
    r0 = r0 < 26 ? r0 : 25; r1 = r1 < 26 ? r1 : 25;
    unsigned b0a, b0b, b1a, b1b;
    {
        const unsigned wb = LP_W3 + w * 16640 + pp * 8;
        const unsigned j0 = r0 >> 1, j1 = r1 >> 1;
        const unsigned s0 = wb + j0 * 1280 + (j0 & 1) * 64 + ((j0 >> 2) & 1) * 128 + (r0 & 1) * 512;
        const unsigned s1 = wb + j1 * 1280 + (j1 & 1) * 64 + ((j1 >> 2) & 1) * 128 + (r1 & 1) * 512;
        b0a = s0 + (r0 & 1) * 32; b0b = s0 + 32 - (r0 & 1) * 32; b1a = s1 + (r1 & 1) * 32; b1b = s1 + 32 - (r1 & 1) * 32;
    }
    unsigned va = LP_VT + n * 64 + 2 * i0, vb = n < 10 ? va + 1024 : LP_DM;
    unsigned vl = w == 3 ? LP_DM : 0;
    unsigned ac = LP_T1 + 4 * (4 * gq * 32 + n);
    unsigned a20 = LP_W2 + r0 * 64 + pp * 8, a21 = LP_W2 + r1 * 64 + pp * 8, abv = LP_VT + n * 64 + gq * 16;
    asm volatile("" : "+v"(b0a), "+v"(b0b), "+v"(b1a), "+v"(b1b), "+v"(va), "+v"(vb), "+v"(ac), "+v"(a20), "+v"(a21), "+v"(abv));
    if (w == 3) {
        LAS unsigned* z = (LAS unsigned*)(lds + LP_VT + (lane < 27 ? lane : 31) * 64 + 52);
        z[0] = 0; z[1] = 0; z[2] = 0;
        *(LAS unsigned*)(lds + LP_T1 + 4 * T_ZERO) = 0;
    }
    if (w == 1) *(LAS unsigned*)(lds + LP_T0 + 2 * T_ZERO) = 0;
    asm volatile("s_waitcnt vmcnt(0) lgkmcnt(0)" ::: "memory");
    __builtin_amdgcn_s_barrier();
    STAMP(2);
    {
        const u32x4 wo3 = *(const LAS u32x4*)(lds + LP_WO + w * 256 + 104 + 16 * gq);
        s16x8 bfr[16];
#pragma unroll
        for (int t = 0; t < 16; ++t) {
            const int ii = t >> 1, u = t & 1;
            const s16x4 lo = __builtin_amdgcn_ds_read_tr16_b64_v4i16((LAS s16x4*)(lds + (u ? b0b : b0a) + ii * 64));
            const s16x4 hi = __builtin_amdgcn_ds_read_tr16_b64_v4i16((LAS s16x4*)(lds + (u ? b1b : b1a) + ii * 64));
            bfr[t] = __builtin_shufflevector(lo, hi, 0, 1, 2, 3, 4, 5, 6, 7);
        }
        if (w == 3) {
            const unsigned o2 = *(const LAS unsigned*)(lds + LP_WO + 768 + 52 + (lane < 13 ? lane : 0) * 4);
            const unsigned o1 = *(const LAS unsigned short*)(lds + LP_WO + 768 + p * 2);
            *(LAS unsigned*)(lds + (lane < 13 ? LP_VT + 26 * 64 + lane * 4 : LP_DM)) = o2;
            *(LAS float*)(lds + LP_T1 + 4 * T_CONST) = bf_lo(o1);
        }
        float res[16];
#pragma unroll
        for (int t = 0; t < 16; ++t) res[t] = mfma16(as_frag(wo3), bfr[t], (f32x4){0.f, 0.f, 0.f, 0.f})[0];
#pragma unroll
        for (int k2 = 0; k2 < 4; ++k2) {
            const unsigned pa0 = pk_bf16(res[4 * k2], res[4 * k2 + 2]), pb0 = pk_bf16(res[4 * k2 + 1], res[4 * k2 + 3]);
            if (k2 < 3) { *(LAS unsigned*)(lds + va + 4 * k2) = pa0; *(LAS unsigned*)(lds + vb + 4 * k2) = pb0; }
            else { *(LAS unsigned*)(lds + (vl ? vl : va + 12)) = pa0; *(LAS unsigned*)(lds + (vl ? vl : vb + 12)) = pb0; }
        }
    }
    CBAR();
    sig_add(lds, L_F + 28);
    if (w > 1) return;
    STAMP(3);
    const __amdgpu_buffer_rsrc_t srdA = __builtin_amdgcn_make_buffer_rsrc((void*)(P.ws + WS_A), 0, A_BYTES, RSRC3);
    u32x4 o;
    if (w == 1) {
        float pv[16];
#pragma unroll
        for (int e = 0; e < 8; ++e) {
            const unsigned x = e < 4 ? ta[e] : tb[e - 4];
            pv[2 * e] = bf_lo(*(const LAS unsigned short*)(lds + LP_T0 + 2 * (x & 0xffff)));
            pv[2 * e + 1] = bf_lo(*(const LAS unsigned short*)(lds + LP_T0 + 2 * (x >> 16)));
        }
#pragma unroll
        for (int j = 0; j < 4; ++j) o[j] = pk_bf16(pv[4 * j] + pv[4 * j + 1], pv[4 * j + 2] + pv[4 * j + 3]);
    } else {
        s16x8 a2[2];
#pragma unroll
        for (int mt = 0; mt < 2; ++mt) {
            const s16x4 lo = __builtin_amdgcn_ds_read_tr16_b64_v4i16((LAS s16x4*)(lds + a20 + mt * 32));
            const s16x4 hi = __builtin_amdgcn_ds_read_tr16_b64_v4i16((LAS s16x4*)(lds + a21 + mt * 32));
            a2[mt] = __builtin_shufflevector(lo, hi, 0, 1, 2, 3, 4, 5, 6, 7);
        }
        unsigned pa[16];
#pragma unroll
        for (int e = 0; e < 8; ++e) {
            const unsigned x = e < 4 ? ta[e] : tb[e - 4];
            pa[2 * e] = LP_T1 + 4 * (x & 0xffff); pa[2 * e + 1] = LP_T1 + 4 * (x >> 16);
        }
#pragma unroll
        for (int e = 0; e < 16; ++e) asm volatile("" : "+v"(pa[e]));
        _Pragma("unroll 1") for (unsigned sp = 0; lds_ld(lds, L_F + 28) < 4 && sp < SPIN_CAP; ++sp) __builtin_amdgcn_s_sleep(0);
        CBAR();
        const u32x4 bf0 = *(const LAS u32x4*)(lds + abv), bf1 = *(const LAS u32x4*)(lds + abv + 1024);
        const f32x4 z4 = {0.f, 0.f, 0.f, 0.f};
        const f32x4 d00 = mfma16(a2[0], as_frag(bf0), z4), d01 = mfma16(a2[1], as_frag(bf0), z4);
        const f32x4 d10 = mfma16(a2[0], as_frag(bf1), z4), d11 = mfma16(a2[1], as_frag(bf1), z4);
#pragma unroll
        for (int e = 0; e < 4; ++e) {
            *(LAS float*)(lds + ac + e * 128) = d00[e]; *(LAS float*)(lds + ac + 2048 + e * 128) = d01[e];
            *(LAS float*)(lds + ac + 64 + e * 128) = d10[e]; *(LAS float*)(lds + ac + 2048 + 64 + e * 128) = d11[e];
        }
        STAMP(4);
        float pv[16];
#pragma unroll
        for (int e = 0; e < 16; ++e) pv[e] = *(const LAS float*)(lds + pa[e]);
#pragma unroll
        for (int j = 0; j < 4; ++j) o[j] = pk_bf16(pv[4 * j] + pv[4 * j + 1], pv[4 * j + 2] + pv[4 * j + 3]);
    }
    {
        const int fr = w == 0 ? (cs < 22 ? 2 * cs : 22 + cs) : 2 * cs + 1;
        __builtin_amdgcn_raw_buffer_store_b128(o, srdA, lane < nact ? (unsigned)(fr * 832 + (26 * chh + p) * 16) : 0x7ffffff0u, 0, AUX_SC1);
    }
    STAMP(5);
    WAIT_VM0();
    STAMP(6);
    if (w == 1) { sig_set(lds, L_F + 24); return; }
    const unsigned tagv = *(const LAS unsigned*)(lds + L_EP + (lane & 15) * 4);
    _Pragma("unroll 1") for (unsigned sp = 0; lds_ld(lds, L_F + 24) == 0 && sp < SPIN_CAP; ++sp) __builtin_amdgcn_s_sleep(0);
    if (lane < 16) __hip_atomic_store((gu32*)(P.ws + WS_FL) + lane * 64 + slot, tagv + 1u, __ATOMIC_RELAXED, __HIP_MEMORY_SCOPE_AGENT);
}

DI bool rows_ready(bool match) {
    const unsigned long long mk = __ballot(match);
    return (((unsigned)mk | (unsigned)(mk >> 32)) & 0x3ffffffu) == 0x3ffffffu;
}
constexpr unsigned A_PER = A_BYTES / 4;
DI void cin_role(const Params& P, lds8* lds, int cw, int lane, int g, int m, bool has_dnn, int pf_slot STAMP_ARG) {
    const int d = lane & 31, h = lane >> 5;
    const int b = 64 * g + 4 * m + cw;
    const unsigned ldsb = (unsigned)(size_t)lds;
    {
        const __amdgpu_buffer_rsrc_t srdT = __builtin_amdgcn_make_buffer_rsrc((void*)P.table, 0, NVOC * 64, RSRC3);
        const unsigned lb = ldsb + L_X0 + cw * 1664;
        WAIT_VM0();
        const int q = lane >> 2;
        const unsigned j0 = *(const LAS unsigned*)(lds + L_X0 + cw * 1664 + q * 4);
        const unsigned j1 = *(const LAS unsigned*)(lds + L_X0 + cw * 1664 + (q < 10 ? q + 16 : 25) * 4);
        unsigned o0 = j0 * 64 + (lane & 3) * 16, o1 = j1 * 64 + (lane & 3) * 16;
        asm volatile("" : "+v"(o0), "+v"(o1));
        STAMP(1);
        dma16(lb, o0, srdT, 0);
        if (lane < 40) dma16(lb + 1024, o1, srdT, 0);
        if (cw == 0) dma4_sc1(ldsb + L_FL, g * 256 + lane * 4, __builtin_amdgcn_make_buffer_rsrc((void*)(P.ws + WS_FL), 0, 4096, RSRC3), 0);
    }
    const unsigned bo = P.bout[0];
    STAMP(2);
    __builtin_amdgcn_s_setprio(0);
    CBAR();
    __builtin_amdgcn_s_barrier();
    const unsigned epv = *(const LAS unsigned*)(lds + L_EP + g * 4);
    unsigned bov = bo;
    asm volatile("s_waitcnt vmcnt(0)" : "+v"(bov) :: "memory");
    STAMP(3);
    float x[26];
    {
        const lds8* xb = lds + L_X0 + cw * 1664 + h * 64 + d * 2;
        const lds8* xl = lds + L_X0 + cw * 1664 + (h ? 0 : 25 * 64) + d * 2;
#pragma unroll
        for (int a = 0; a < 13; ++a) {
            const unsigned lo = *(const LAS unsigned short*)(xb + 2 * a * 64);
            const unsigned hi = a < 12 ? *(const LAS unsigned short*)(xb + (2 * a + 1) * 64) : *(const LAS unsigned short*)xl;
            x[2 * a] = bf_lo(lo); x[2 * a + 1] = bf_lo(hi);
        }
    }
    u32x4 z[24];
#pragma unroll
    for (int s = 0; s < 22; ++s) {
        float fa[8], fb[8];
#pragma unroll
        for (int e = 0; e < 8; ++e) { const AB pe = slot_ab(8 * s + e); fa[e] = x[pe.a]; fb[e] = x[pe.b]; }
        float t0, t1, t2, t3, t4, t5;
        unsigned o0, o1, o2, o3;
        asm("v_mul_f32 %4, %10, %11\n\tv_mul_f32 %5, %12, %13\n\tv_mul_f32 %6, %14, %15\n\tv_mul_f32 %7, %16, %17\n\t"
            "v_cvt_pk_bf16_f32 %0, %4, %5\n\tv_mul_f32 %8, %18, %19\n\tv_mul_f32 %9, %20, %21\n\tv_cvt_pk_bf16_f32 %1, %6, %7\n\t"
            "v_mul_f32 %4, %22, %23\n\tv_mul_f32 %5, %24, %25\n\tv_cvt_pk_bf16_f32 %2, %8, %9\n\tv_cvt_pk_bf16_f32 %3, %4, %5"
            : "=&v"(o0), "=&v"(o1), "=&v"(o2), "=v"(o3), "=&v"(t0), "=&v"(t1), "=&v"(t2), "=&v"(t3), "=&v"(t4), "=&v"(t5)
            : "v"(fa[0]), "v"(fb[0]), "v"(fa[1]), "v"(fb[1]), "v"(fa[2]), "v"(fb[2]), "v"(fa[3]), "v"(fb[3]),
              "v"(fa[4]), "v"(fb[4]), "v"(fa[5]), "v"(fb[5]), "v"(fa[6]), "v"(fb[6]), "v"(fa[7]), "v"(fb[7]));
        z[s] = (u32x4){o0, o1, o2, o3};
    }
#pragma unroll
    for (int s = 22; s < 24; ++s) {
#pragma unroll
        for (int j = 0; j < 4; ++j) {
            const AB p0 = slot_ab(8 * s + 2 * j), p1 = slot_ab(8 * s + 2 * j + 1);
            const float a0 = p0.a < 26 ? x[p0.a] : (p0.a == 26 ? 1.f : 0.f);
            const float a1 = p1.a < 26 ? x[p1.a] : (p1.a == 26 ? 1.f : 0.f);
            z[s][j] = pk_bf16(a0, a1);
        }
    }
#pragma unroll
    for (int s = 0; s < 24; ++s) asm volatile("" : "+v"(z[s]));
    STAMP(4);
    const unsigned tag = epv + 1u;
    {
        const __amdgpu_buffer_rsrc_t srdA = __builtin_amdgcn_make_buffer_rsrc((void*)(P.ws + WS_A), 0, A_BYTES, RSRC3);
        if (cw == 0) {
            if (has_dnn) _Pragma("unroll 1") for (unsigned sp = 0; lds_ld(lds, L_F + 56) < 4 && sp < SPIN_CAP; ++sp) __builtin_amdgcn_s_sleep(0);
            const gu32* fl = (const gu32*)(P.ws + WS_FL) + g * 64 + lane;
            if (!rows_ready(*(const LAS unsigned*)(lds + L_FL + lane * 4) == tag))
            _Pragma("unroll 1") for (unsigned sp = 0; sp < SPIN_CAP; ++sp) { if (rows_ready(ld_agent(fl) == tag)) break; __builtin_amdgcn_s_sleep(1); }
            sig_set(lds, L_F + 16);
            if (!has_dnn) dma4_sc1(ldsb + L_DUMMY, lane < 50 ? (unsigned)((((g & 1) * 3 + pf_slot) * 50 + lane) * 128) : 0x7fffff00u, srdA, 0);
        } else {
            _Pragma("unroll 1") for (unsigned sp = 0; lds_ld(lds, L_F + 16) == 0 && sp < SPIN_CAP; ++sp) __builtin_amdgcn_s_sleep(0);
        }
        CBAR();
        __builtin_amdgcn_s_setprio(CIN_PRIO);
        STAMP(5);
        {
            const unsigned l1 = ldsb + L_W + cw * 4096, vo = lane * 16;
            asm volatile("s_mov_b32 m0, %0\n\ts_nop 0\n\t"
                         "buffer_load_dwordx4 %1, %2, %3 offen sc1 lds\n\tbuffer_load_dwordx4 %1, %2, %3 offen offset:1024 sc1 lds\n\t"
                         "buffer_load_dwordx4 %1, %2, %3 offen offset:2048 sc1 lds\n\tbuffer_load_dwordx4 %1, %2, %3 offen offset:3072 sc1 lds"
                         :: "s"(l1), "v"(vo), "s"(srdA), "s"(cw * 4096) : "memory");
#pragma unroll
            for (int i = 0; i < 6; ++i) {
                const int q = 6 * cw + i;
                const unsigned go = q < 4 ? 16384 + 1024 * q : 19968 + 1024 * (q - 4), lo = q < 4 ? 16384 + 1024 * q : 53248 + 1024 * (q - 4);
                dma16_sc1(ldsb + L_W + lo, vo, srdA, go);
            }
        }
        wait_vm<6>();
        sig_add(lds, L_F + 20);
        _Pragma("unroll 1") for (unsigned sp = 0; lds_ld(lds, L_F + 20) < 4 && sp < SPIN_CAP; ++sp) __builtin_amdgcn_s_sleep(0);
        CBAR();
    }
    STAMP(6);
    const gu64* pp = (const gu64*)(P.ws + WS_PART) + ((g * 32 + (lane < 26 ? lane : 0)) * 64 + 4 * m + cw);
    unsigned long long pv = 0;
    f32x16 acc0, acc1;
#pragma unroll
    for (int i = 0; i < 16; ++i) { acc0[i] = 0.f; acc1[i] = 0.f; }
    {
        unsigned abo = L_W + (26 * h + d) * 16;
        asm volatile("" : "+v"(abo));
        const lds8* ab = lds + abo;
        constexpr int PF = 6;
        u32x4 af[46];
        unsigned cnt2 = 0;
        auto frag_of = [](int t) { return t < 24 ? t : t + 40; };
#pragma unroll
        for (int t = 0; t < PF; ++t) af[t] = *(const LAS u32x4*)(ab + frag_of(t) * 832);
        __builtin_amdgcn_sched_barrier(0);
#pragma unroll
        for (int t = 0; t < 46; ++t) {
            if (t == 9) { wait_vm<0>(); sig_add(lds, L_F + 44); }
            if (t + PF == 19 - 2) cnt2 = lds_ld(lds, L_F + 44);
            if (t + PF == 19) {
                if (cnt2 < 4) _Pragma("unroll 1") for (unsigned sp = 0; lds_ld(lds, L_F + 44) < 4 && sp < SPIN_CAP; ++sp) __builtin_amdgcn_s_sleep(0);
                CBAR();
            }
            if (t + PF < 46) af[t + PF] = *(const LAS u32x4*)(ab + frag_of(t + PF) * 832);
            if (t < 44) {
                if (t & 1) acc0 = mfma32(as_frag(af[t]), as_frag(z[t >> 1]), acc0);
                else acc1 = mfma32(as_frag(af[t]), as_frag(z[t >> 1]), acc1);
            } else acc1 = mfma32(as_frag(af[t]), as_frag(z[t - 22]), acc1);
            if (t == 30) pv = __hip_atomic_load(pp, __ATOMIC_RELAXED, __HIP_MEMORY_SCOPE_AGENT);
            __builtin_amdgcn_sched_barrier(0);
        }
    }
    float sa = 0.f;
#pragma unroll
    for (int i = 0; i < 12; ++i) sa = __builtin_fmaf(acc0[i], acc1[i], sa);
    const float sb = __builtin_fmaf(acc0[12], acc1[12], acc0[13] * acc1[13]);
    float tot = h == 0 ? sa + sb : sa;
    asm volatile("" : "+v"(tot));
    STAMP(7);
    _Pragma("unroll 1") for (unsigned sp = 0; sp < SPIN_CAP; ++sp) {
        if (__all((unsigned)(pv >> 32) == tag)) break;
        pv = __hip_atomic_load(pp, __ATOMIC_RELAXED, __HIP_MEMORY_SCOPE_AGENT);
    }
    STAMP(8);
    if (lane < 26) tot += __uint_as_float((unsigned)pv);
    tot = wave_sum_to_lane63(tot);
    if (lane == 63) {
        const float o = tot + bf_lo(bov);
        P.out[b] = (unsigned short)(pk_bf16(o, o) & 0xffffu);
    }
    if (m == 0 && cw == 0) {
        const gu32* fl = (const gu32*)(P.ws + WS_FL) + g * 64 + lane;
        _Pragma("unroll 1") for (unsigned sp = 0; sp < SPIN_CAP; ++sp) {
            if ((__ballot(ld_agent(fl) == tag) & 0x003fffff03ffffffull) == 0x003fffff03ffffffull) break;
            __builtin_amdgcn_s_sleep(1);
        }
        if (lane == 0) __hip_atomic_store((gu32*)(P.ws + WS_EP) + g, tag, __ATOMIC_RELAXED, __HIP_MEMORY_SCOPE_AGENT);
    }
}

DI void decode_block(int bid, int& g, int& m, int& ntile, int& k) {
    const int x = bid & 7, j = bid >> 3;
    g = 2 * x + (j >> 4); m = j & 15;
    ntile = m < 13 ? m : -1; k = m - 13;
}

__global__ void __launch_bounds__(512, 2) xdfm_kernel(KArgs KA) {
    const Params& P = KA.P;
    __shared__ __attribute__((aligned(16))) unsigned char smem[L_END];
    lds8* lds = (lds8*)smem;
    STAMP(0);
    const int w = __builtin_amdgcn_readfirstlane(threadIdx.x >> 6), lane = threadIdx.x & 63;
    {
        unsigned long long pc; unsigned len;
        asm volatile("s_getpc_b64 %0\n.Lxdfm_pc:\n\ts_mov_b32 %1, (.Lxdfm_end-.Lxdfm_pc)&4294967295" : "=s"(pc), "=s"(len));
        const __amdgpu_buffer_rsrc_t srdP = __builtin_amdgcn_make_buffer_rsrc((void*)pc, 0, len, RSRC3);
        dma4((unsigned)(size_t)lds + L_DUMMY, lane == 0 ? ((blockIdx.x >> 3) * 8 + w) * 128 : 0x7fffff00u, srdP, 0);
    }
    asm volatile("" :: "s"(P.idx), "s"(P.table), "s"(P.wcin), "s"(P.wout), "s"(P.w1), "s"(P.b1), "s"(P.w2), "s"(P.bout), "s"(P.out), "s"(P.ws));
    STAMP(9);
    int g, m, ntile, k;
    decode_block(blockIdx.x, g, m, ntile, k);
    const bool is_cin = w >= 4;
    if (__builtin_expect(is_cin, 1)) {
        __builtin_amdgcn_s_setprio(3);
        dma16((unsigned)(size_t)lds + L_X0 + (w - 4) * 1664, lane < 7 ? (64 * g + 4 * m + w - 4) * 104 + lane * 16 : 0x7fffff00u,
              __builtin_amdgcn_make_buffer_rsrc((void*)P.idx, 0, NBATCH * 26 * 4, RSRC3), 0);
        if (__builtin_expect(w == 7, 1)) dma4((unsigned)(size_t)lds + L_DUMMY, lane < 26 ? ((blockIdx.x >> 3) * 26 + lane) * 128 : 0x7fffff00u,
                         __builtin_amdgcn_make_buffer_rsrc((void*)P.wcin, 0, 3 * 17576 * 2, RSRC3), 0);
    }
    if (__builtin_expect(w == 0, 1)) {
        if (lane < 16) lds_st(lds, L_F + 4 * lane, 0);
        dma4_sc1((unsigned)(size_t)lds + L_EP, (lane & 15) * 4, __builtin_amdgcn_make_buffer_rsrc((void*)(P.ws + WS_EP), 0, 64, RSRC3), 0);
    }
    const int np = g * 3 + k;
    const bool second = (np >= 22 && np < 42) || np >= 46;
    const int p = second ? (np < 42 ? np - 22 : np - 26) : (np < 22 ? np : np - 20);
    if (__builtin_expect(!is_cin && ntile >= 0, 1)) dnn_start(P, lds, w, lane, g, ntile);
    if (__builtin_expect(!is_cin && ntile < 0, 1)) prep_start(P, lds, w, lane, p);
    STAMP(10);
    if (__builtin_expect(!is_cin && ntile >= 0, 1)) {
        dnn_role(P, lds, w, lane, g, ntile STAMP_PASS);
    } else if (is_cin) {
        cin_role(P, lds, w - 4, lane, g, m, ntile >= 0, k STAMP_PASS);
        __builtin_amdgcn_s_waitcnt(0x0F70);
    } else {
        prep_role(P, lds, w, lane, p, second ? 32 + p : p STAMP_PASS);
        __builtin_amdgcn_s_waitcnt(0x0F70);
    }
    STAMP(11);
    STAMP_FLUSH(P.ws, w, lane);
    asm volatile(".Lxdfm_end:");
}

extern "C" void kernel_launch(void* const* d_in, const int* in_sizes, int n_in,
                              void* d_out, int out_size, void* d_ws, size_t ws_size,
                              hipStream_t stream) {
    KArgs KA;
    KA.tab = make_tab();
    Params& P = KA.P;
    P.idx = (const int*)d_in[0]; P.table = (const unsigned short*)d_in[1]; P.wcin = (const unsigned short*)d_in[2];
    P.wout = (const unsigned short*)d_in[3]; P.w1 = (const unsigned short*)d_in[4]; P.b1 = (const unsigned short*)d_in[5];
    P.w2 = (const unsigned short*)d_in[6]; P.bout = (const unsigned short*)d_in[7];
    P.out = (unsigned short*)d_out; P.ws = (unsigned char*)d_ws;
    xdfm_kernel<<<256, 512, 0, stream>>>(KA);
}
```

```cpp
#include <hip/hip_runtime.h>
#include <stdint.h>

#define DI __device__ __forceinline__
#define GAS __attribute__((address_space(1)))
#define LAS __attribute__((address_space(3)))

typedef short    s16x4 __attribute__((ext_vector_type(4)));
typedef short    s16x8 __attribute__((ext_vector_type(8)));
typedef float    f32x2 __attribute__((ext_vector_type(2)));
typedef float    f32x4 __attribute__((ext_vector_type(4)));
typedef float    f32x16 __attribute__((ext_vector_type(16)));
typedef unsigned u32x2 __attribute__((ext_vector_type(2)));
typedef unsigned u32x4 __attribute__((ext_vector_type(4)));
typedef __bf16   bf16x2 __attribute__((ext_vector_type(2)));
typedef GAS unsigned gu32;
typedef GAS unsigned long long gu64;
typedef LAS unsigned char lds8;

constexpr int DIN = 832, NBATCH = 1024, NVOC = 100000;
constexpr int XD = 6;
constexpr int W_EARLY = 2;
constexpr unsigned RSRC3 = 0x00020000u;
constexpr int AUX_SC1 = 16;
constexpr unsigned SPIN_CAP = 1u << 20;

constexpr unsigned WS_EP = 0;
constexpr unsigned WS_FL = 4096;
constexpr unsigned WS_PART = 8192;
constexpr unsigned WS_A = 8192 + 16 * 32 * 64 * 8;
constexpr unsigned A_BYTES = 46 * 832;

constexpr unsigned L_W = 0;
constexpr unsigned L_A = 832 * 128;
constexpr unsigned L_X0 = L_A + 4 * XD * 2048;
constexpr unsigned L_F = L_X0 + 4 * 1664;
constexpr unsigned L_DUMMY = L_F + 256;
constexpr unsigned L_EP = L_DUMMY + 256;
constexpr unsigned L_BI = L_EP + 256;
constexpr unsigned L_W2 = L_BI + 256;
constexpr unsigned L_FL = L_W2 + 256;
constexpr unsigned L_END = L_FL + 256;
static_assert(L_END <= 163840, "LDS map");
constexpr unsigned LP_VT = 0;
constexpr unsigned LP_T0 = 2048;
constexpr unsigned LP_T1 = 2048 + 2304;

struct AB { int a, b; };
constexpr AB slot_ab(int t) {
    if (t < 156) { int a = 2 * (t / 12); return {a, (a + t % 12) % 26}; }
    if (t < 170) { int u = t - 156; int a = 2 * (u / 2); return {a, (a + 12 + (u & 1)) % 26}; }
    if (t < 176) { int a = 2 * (7 + t - 170); return {a, (a + 12) % 26}; }
    if (t < 189) return {2 * (t - 176), 26};
    if (t == 189) return {26, 26};
    return {27, 27};
}
constexpr unsigned T_ZERO = 1024, T_CONST = 1025;
struct PrepTab { unsigned v[2][24 * 2 * 8]; };
constexpr PrepTab make_tab() {
    PrepTab tb{};
    for (int m = 0; m < 2; ++m) for (int s = 0; s < 24; ++s) for (int h = 0; h < 2; ++h) for (int e = 0; e < 8; ++e) {
        const int st = m ? 32 : 26;
        int t = 8 * s + e; AB ab = slot_ab(t);
        unsigned o1 = T_ZERO, o2 = T_ZERO;
        if (ab.a == 27) {}
        else if (ab.a == 26) { if (h == 0 && m) o1 = T_CONST; }
        else if (ab.b == 26) { if (m) o1 = ((ab.a + h) % 26) * st + 26; }
        else {
            int u = (ab.a + h) % 26, v = (ab.b + h) % 26;
            bool dup = (t == 169 && h == 1);
            if (dup) {}
            else if (u == v) o1 = u * st + u;
            else { o1 = u * st + v; o2 = v * st + u; }
        }
        tb.v[m][(s * 2 + h) * 8 + e] = o1 | (o2 << 16);
    }
    return tb;
}
static_assert(slot_ab(169).a == 12 && slot_ab(169).b == 25, "dup slot");

struct Params {
    const int* idx; const unsigned short* table; const unsigned short* wcin; const unsigned short* wout;
    const unsigned short* w1; const unsigned short* b1; const unsigned short* w2; const unsigned short* bout;
    unsigned short* out; unsigned char* ws;
};
struct KArgs { Params P; PrepTab tab; };

DI float bf_lo(unsigned v) { return __uint_as_float(v << 16); }
DI float bf_hi(unsigned v) { return __uint_as_float(v & 0xffff0000u); }
DI unsigned pk_bf16(float lo, float hi) { f32x2 v = {lo, hi}; return __builtin_bit_cast(unsigned, __builtin_convertvector(v, bf16x2)); }
DI f32x4 mfma16(s16x8 a, s16x8 b, f32x4 c) { return __builtin_amdgcn_mfma_f32_16x16x32_bf16(a, b, c, 0, 0, 0); }
DI f32x16 mfma32(s16x8 a, s16x8 b, f32x16 c) { return __builtin_amdgcn_mfma_f32_32x32x16_bf16(a, b, c, 0, 0, 0); }
DI s16x8 as_frag(u32x4 v) { return __builtin_bit_cast(s16x8, v); }
template <int CTRL, int ROWMASK> DI float dpp_add(float v) {
    return v + __int_as_float(__builtin_amdgcn_update_dpp(0, __float_as_int(v), CTRL, ROWMASK, 0xf, true));
}
DI float wave_sum_to_lane63(float v) {
    v = dpp_add<0x111, 0xf>(v); v = dpp_add<0x112, 0xf>(v); v = dpp_add<0x114, 0xf>(v); v = dpp_add<0x118, 0xf>(v);
    v = dpp_add<0x142, 0xa>(v); v = dpp_add<0x143, 0xc>(v);
    return v;
}
DI float gelu_tanh(float x) {
    const float k1 = -2.0f * 0.7978845608028654f * 1.4426950408889634f;
    const float k2 = k1 * 0.044715f;
    float arg = x * __builtin_fmaf(x * x, k2, k1);
    float e = __builtin_amdgcn_exp2f(arg);
    return x * __builtin_amdgcn_rcpf(1.0f + e);
}
DI void dma16(unsigned lds_addr, unsigned voff, __amdgpu_buffer_rsrc_t srd, unsigned soff) {
    asm volatile("s_mov_b32 m0, %0\n\ts_nop 0\n\tbuffer_load_dwordx4 %1, %2, %3 offen lds"
                 :: "s"(lds_addr), "v"(voff), "s"(srd), "s"(soff) : "memory");
}
DI void dma16_sc1(unsigned lds_addr, unsigned voff, __amdgpu_buffer_rsrc_t srd, unsigned soff) {
    asm volatile("s_mov_b32 m0, %0\n\ts_nop 0\n\tbuffer_load_dwordx4 %1, %2, %3 offen sc1 lds"
                 :: "s"(lds_addr), "v"(voff), "s"(srd), "s"(soff) : "memory");
}
DI void dma4_sc1(unsigned lds_addr, unsigned voff, __amdgpu_buffer_rsrc_t srd, unsigned soff) {
    asm volatile("s_mov_b32 m0, %0\n\ts_nop 0\n\tbuffer_load_dword %1, %2, %3 offen sc1 lds"
                 :: "s"(lds_addr), "v"(voff), "s"(srd), "s"(soff) : "memory");
}
DI void dma4(unsigned lds_addr, unsigned voff, __amdgpu_buffer_rsrc_t srd, unsigned soff) {
    asm volatile("s_mov_b32 m0, %0\n\ts_nop 0\n\tbuffer_load_dword %1, %2, %3 offen lds"
                 :: "s"(lds_addr), "v"(voff), "s"(srd), "s"(soff) : "memory");
}
DI void st_granule(gu64* g, unsigned tag, unsigned val) {
    __hip_atomic_store(g, ((unsigned long long)tag << 32) | val, __ATOMIC_RELAXED, __HIP_MEMORY_SCOPE_AGENT);
}
DI unsigned ld_agent(const gu32* p) { return __hip_atomic_load(p, __ATOMIC_RELAXED, __HIP_MEMORY_SCOPE_AGENT); }
DI unsigned lds_ld(lds8* l, unsigned off) { return __hip_atomic_load((LAS unsigned*)(l + off), __ATOMIC_RELAXED, __HIP_MEMORY_SCOPE_WORKGROUP); }
DI void lds_st(lds8* l, unsigned off, unsigned v) { __hip_atomic_store((LAS unsigned*)(l + off), v, __ATOMIC_RELAXED, __HIP_MEMORY_SCOPE_WORKGROUP); }
DI void sig_add(lds8* l, unsigned off) {
    asm volatile("s_mov_b64 exec, 1\n\tds_add_u32 %0, %1\n\ts_mov_b64 exec, -1" :: "v"((unsigned)(size_t)l + off), "v"(1u) : "memory");
}
DI void sig_set(lds8* l, unsigned off) {
    asm volatile("s_mov_b64 exec, 1\n\tds_write_b32 %0, %1\n\ts_mov_b64 exec, -1" :: "v"((unsigned)(size_t)l + off), "v"(1u) : "memory");
}
#define WAIT_LGKM() asm volatile("s_waitcnt lgkmcnt(0)" ::: "memory")
#define WAIT_VM0() asm volatile("s_waitcnt vmcnt(0)" ::: "memory")
#define CBAR() asm volatile("" ::: "memory")

template <int K> DI unsigned quad_bcast(unsigned v) { return __builtin_amdgcn_update_dpp(0u, v, K * 0x55, 0xf, 0xf, true); }
template <int N> DI void wait_vm() { asm volatile("s_waitcnt vmcnt(%0)" :: "i"(N) : "memory"); }
DI void dma_w4(unsigned lds_addr, unsigned voff, __amdgpu_buffer_rsrc_t srd, unsigned soff) {
    unsigned t;
    asm volatile("s_mov_b32 m0, %1\n\ts_add_u32 %0, %4, 12288\n\tbuffer_load_dwordx4 %2, %3, %4 offen lds\n\t"
                 "buffer_load_dwordx4 %2, %3, %0 offen offset:1024 lds\n\ts_add_u32 %0, %0, 12288\n\t"
                 "buffer_load_dwordx4 %2, %3, %0 offen offset:2048 lds\n\ts_add_u32 %0, %0, 12288\n\t"
                 "buffer_load_dwordx4 %2, %3, %0 offen offset:3072 lds"
                 : "=&s"(t) : "s"(lds_addr), "v"(voff), "s"(srd), "s"(soff) : "memory", "scc");
}
DI void dnn_start(const Params& P, lds8* lds, int w, int lane, int g, int ntile) {
    const int mh = w & 1, kh = w >> 1;
    const unsigned ldsb = (unsigned)(size_t)lds;
    const unsigned ring = ldsb + L_A + w * (XD * 2048);
    const __amdgpu_buffer_rsrc_t srdI = __builtin_amdgcn_make_buffer_rsrc((void*)P.idx, 0, NBATCH * 26 * 4, RSRC3);
    const unsigned vo = ((64 * g + 32 * mh + (lane >> 2)) * 26 + 13 * kh + 4 * (lane & 3)) * 4;
    dma16(ring, vo, srdI, 0);
    dma16(ring + 1024, vo, srdI, 1664);
    if (__builtin_expect(w == 1, 1)) dma4(ldsb + L_BI, ntile * 128 + lane * 4, __builtin_amdgcn_make_buffer_rsrc((void*)P.b1, 0, DIN * 2, RSRC3), 0);
    if (__builtin_expect(w == 2, 1)) dma4(ldsb + L_W2, ntile * 128 + lane * 4, __builtin_amdgcn_make_buffer_rsrc((void*)P.w2, 0, DIN * 2, RSRC3), 0);
    const __amdgpu_buffer_rsrc_t srdW = __builtin_amdgcn_make_buffer_rsrc((void*)P.w1, 0, DIN * DIN * 2, RSRC3);
    const unsigned vw = (lane >> 3) * 1664 + (((lane & 7) ^ (((lane >> 4) & 1) << 2)) << 4);
    const unsigned rowA = 416 * kh + 208 * mh + 104 * (g & 1);
    dma_w4(ldsb + L_W + rowA * 128, vw, srdW, rowA * 1664 + ntile * 128);
    if (W_EARLY > 1) dma_w4(ldsb + L_W + rowA * 128 + 4096, vw, srdW, rowA * 1664 + ntile * 128 + 4 * 13312);
    if (W_EARLY > 2) dma_w4(ldsb + L_W + rowA * 128 + 8192, vw, srdW, rowA * 1664 + ntile * 128 + 8 * 13312);
}
DI void dnn_role(const Params& P, lds8* lds, int w, int lane, int g, int ntile) {
    const int mh = w & 1, kh = w >> 1;
    const int h = lane >> 5;
    const unsigned ldsb = (unsigned)(size_t)lds;
    const unsigned ring = ldsb + L_A + w * (XD * 2048);
    const __amdgpu_buffer_rsrc_t srdW = __builtin_amdgcn_make_buffer_rsrc((void*)P.w1, 0, DIN * DIN * 2, RSRC3);
    const unsigned vw = (lane >> 3) * 1664 + (((lane & 7) ^ (((lane >> 4) & 1) << 2)) << 4);
    const unsigned rowA = 416 * kh + 208 * mh + 104 * (g & 1), rowB = 416 * kh + 208 * mh + 104 * ((g & 1) ^ 1);
    auto wdma = [&](unsigned row, int a) {
        const unsigned sb = row * 1664 + ntile * 128, lb = ldsb + L_W + row * 128;
        if (a < 3) dma_w4(lb + a * 4096, vw, srdW, sb + a * 4 * 13312);
        else dma16(lb + 12288, vw, srdW, sb + 12 * 13312);
    };
    wait_vm<4 * W_EARLY>();
    WAIT_LGKM();
    __builtin_amdgcn_s_barrier();
    const u32x4 i0 = *(const LAS u32x4*)(lds + L_A + w * (XD * 2048) + lane * 16);
    const u32x4 i1 = *(const LAS u32x4*)(lds + L_A + w * (XD * 2048) + 1024 + lane * 16);
    const unsigned ep = *(const LAS unsigned*)(lds + L_EP + g * 4);
#pragma unroll
    for (int a = W_EARLY; a < 4; ++a) wdma(rowA, a);
#pragma unroll
    for (int a = 0; a < 4; ++a) wdma(rowB, a);
    const __amdgpu_buffer_rsrc_t srdT = __builtin_amdgcn_make_buffer_rsrc((void*)P.table, 0, NVOC * 64, RSRC3);
    const unsigned c16 = (((lane & 3) ^ ((lane >> 4) & 3)) << 4);
    u32x4 t0 = i0 << 6, t1 = i1 << 6;
    asm volatile("" : "+v"(t0), "+v"(t1));
    auto xload = [&](int s) {
        const unsigned a0 = t0[s & 3], a1 = t1[s & 3];
        const unsigned o0 = (s < 4 ? quad_bcast<0>(a0) : s < 8 ? quad_bcast<1>(a0) : s < 12 ? quad_bcast<2>(a0) : quad_bcast<3>(a0)) + c16;
        const unsigned o1 = (s < 4 ? quad_bcast<0>(a1) : s < 8 ? quad_bcast<1>(a1) : s < 12 ? quad_bcast<2>(a1) : quad_bcast<3>(a1)) + c16;
        dma16(ring + (s % XD) * 2048, o0, srdT, 0);
        dma16(ring + (s % XD) * 2048 + 1024, o1, srdT, 0);
    };
#pragma unroll
    for (int s = 0; s < XD; ++s) xload(s);
    wait_vm<2 * XD>();
    sig_add(lds, L_F + 32 + 4 * kh);
    _Pragma("unroll 1") for (unsigned sp = 0; lds_ld(lds, L_F + 32 + 4 * kh) < 2 && sp < SPIN_CAP; ++sp) __builtin_amdgcn_s_sleep(0);
    CBAR();
    f32x16 acc[2];
    u32x2 bia[4], wv[4];
    float w2f[16];
    unsigned xdone = 0;
#pragma unroll
    for (int r4 = 0; r4 < 4; ++r4) {
        bia[r4] = *(const LAS u32x2*)(lds + L_BI + kh * 64 + h * 8 + r4 * 16);
        wv[r4] = *(const LAS u32x2*)(lds + L_W2 + kh * 64 + h * 8 + r4 * 16);
    }
#pragma unroll
    for (int r4 = 0; r4 < 4; ++r4) {
        acc[0][4 * r4] = bf_lo(bia[r4][0]); acc[0][4 * r4 + 1] = bf_hi(bia[r4][0]); acc[0][4 * r4 + 2] = bf_lo(bia[r4][1]); acc[0][4 * r4 + 3] = bf_hi(bia[r4][1]);
        acc[1][4 * r4] = 0.f; acc[1][4 * r4 + 1] = 0.f; acc[1][4 * r4 + 2] = 0.f; acc[1][4 * r4 + 3] = 0.f;
    }
    {
        const int blk = (lane >> 4) & 1, q = (lane & 15) >> 2, pp = lane & 3, n = lane & 31;
        unsigned ad[2];
#pragma unroll
        for (int j = 0; j < 2; ++j) {
            const unsigned c = 4 * (j ^ kh) + 2 * blk + (pp >> 1);
            ad[j] = L_W + (416 * kh + 8 * h + q) * 128 + ((c ^ (((q >> 1) & 1) << 2)) << 4) + (pp & 1) * 8;
        }
        const unsigned xb = L_A + w * (XD * 2048) + n * 64;
        const unsigned xa0 = xb + ((h ^ ((n >> 2) & 3)) << 4), xa1 = xb + (((2 + h) ^ ((n >> 2) & 3)) << 4);
        u32x4 xf[3][2];
        s16x8 wf[2][4];
        auto xread = [&](int s) {
            xf[s % 3][0] = *(const LAS u32x4*)(lds + xa0 + (s % XD) * 2048);
            xf[s % 3][1] = *(const LAS u32x4*)(lds + xa1 + (s % XD) * 2048);
        };
        auto wread = [&](int s) {
#pragma unroll
            for (int a = 0; a < 2; ++a)
#pragma unroll
                for (int j = 0; j < 2; ++j) {
                    const s16x4 lo = __builtin_amdgcn_ds_read_tr16_b64_v4i16((LAS s16x4*)(lds + ad[j] + s * 4096 + a * 2048));
                    const s16x4 hi = __builtin_amdgcn_ds_read_tr16_b64_v4i16((LAS s16x4*)(lds + ad[j] + s * 4096 + a * 2048 + 512));
                    wf[s & 1][2 * a + j] = __builtin_shufflevector(lo, hi, 0, 1, 2, 3, 4, 5, 6, 7);
                }
        };
        wread(0);
        wait_vm<2 * (XD - 1)>();
        xread(0);
        wait_vm<2 * (XD - 2)>();
        xread(1);
        __builtin_amdgcn_sched_barrier(0);
#pragma unroll
        for (int s = 0; s < 13; ++s) {
            if (s + 1 < 13) wread(s + 1);
#pragma unroll
            for (int a = 0; a < 2; ++a)
#pragma unroll
                for (int j = 0; j < 2; ++j) acc[j] = mfma32(wf[s & 1][2 * a + j], as_frag(xf[s % 3][a]), acc[j]);
            if (s + 1 < 13) {
#pragma unroll
                for (int j = 0; j < 4; ++j) { __builtin_amdgcn_sched_group_barrier(0x100, 2, 0); __builtin_amdgcn_sched_group_barrier(0x8, 1, 0); }
            }
            __builtin_amdgcn_sched_barrier(0);
            if (s + XD < 13) xload(s + XD);
            if (s == 11) {
                xdone = lds_ld(lds, L_F + 52);
#pragma unroll
                for (int r4 = 0; r4 < 4; ++r4) { w2f[4 * r4] = bf_lo(wv[r4][0]); w2f[4 * r4 + 1] = bf_hi(wv[r4][0]); w2f[4 * r4 + 2] = bf_lo(wv[r4][1]); w2f[4 * r4 + 3] = bf_hi(wv[r4][1]); }
#pragma unroll
                for (int e = 0; e < 16; ++e) asm volatile("" : "+v"(w2f[e]));
            }
            if (s + 2 < 13) {
                constexpr int LASTI = 12;
                const int last = s + XD < LASTI ? s + XD : LASTI;
                const int younger = last - (s + 2);
                if (younger >= 4) wait_vm<8>(); else if (younger == 3) wait_vm<6>(); else if (younger == 2) wait_vm<4>(); else if (younger == 1) wait_vm<2>(); else wait_vm<0>();
                if (s + 2 == 12) sig_add(lds, L_F + 52);
                xread(s + 2);
            }
            __builtin_amdgcn_sched_barrier(0);
        }
    }
    {
        if (xdone < 4) _Pragma("unroll 1") for (unsigned sp = 0; lds_ld(lds, L_F + 52) < 4 && sp < SPIN_CAP; ++sp) __builtin_amdgcn_s_sleep(0);
        CBAR();
        lds8* xw = lds + L_W + kh * 53248 + (5 + mh) * 4096 + lane * 16;
#pragma unroll
        for (int r4 = 0; r4 < 4; ++r4) *(LAS f32x4*)(xw + r4 * 1024) = (f32x4){acc[1][4 * r4], acc[1][4 * r4 + 1], acc[1][4 * r4 + 2], acc[1][4 * r4 + 3]};
        CBAR();
        sig_set(lds, L_F + 4 * w);
        _Pragma("unroll 1") for (unsigned sp = 0; lds_ld(lds, L_F + 4 * (w ^ 2)) == 0 && sp < SPIN_CAP; ++sp) __builtin_amdgcn_s_sleep(0);
        CBAR();
        const lds8* xr = lds + L_W + (kh ^ 1) * 53248 + (5 + mh) * 4096 + lane * 16;
        f32x4 o[4];
#pragma unroll
        for (int r4 = 0; r4 < 4; ++r4) o[r4] = *(const LAS f32x4*)(xr + r4 * 1024);
        asm volatile("" : "+v"(o[0]), "+v"(o[1]), "+v"(o[2]), "+v"(o[3]));
#pragma unroll
        for (int r4 = 0; r4 < 4; ++r4) {
            acc[0][4 * r4] += o[r4][0]; acc[0][4 * r4 + 1] += o[r4][1]; acc[0][4 * r4 + 2] += o[r4][2]; acc[0][4 * r4 + 3] += o[r4][3];
        }
    }
    float s0 = 0.f, s1 = 0.f;
#pragma unroll
    for (int r4 = 0; r4 < 4; ++r4) {
        s0 = __builtin_fmaf(gelu_tanh(acc[0][4 * r4]), w2f[4 * r4], s0);
        s1 = __builtin_fmaf(gelu_tanh(acc[0][4 * r4 + 1]), w2f[4 * r4 + 1], s1);
        s0 = __builtin_fmaf(gelu_tanh(acc[0][4 * r4 + 2]), w2f[4 * r4 + 2], s0);
        s1 = __builtin_fmaf(gelu_tanh(acc[0][4 * r4 + 3]), w2f[4 * r4 + 3], s1);
    }
    s0 += s1;
    const auto sw = __builtin_amdgcn_permlane32_swap(__float_as_uint(s0), __float_as_uint(s0), false, false);
    const float part = __uint_as_float(sw[0]) + __uint_as_float(sw[1]);
    if (lane < 32) st_granule((gu64*)(P.ws + WS_PART) + ((g * 32 + 2 * ntile + kh) * 64 + 32 * mh + lane), ep + 1u, __float_as_uint(part));
}

constexpr unsigned LP_W2 = 12288;
constexpr unsigned LP_WO = 14336;
constexpr unsigned LP_DM = 15360;
constexpr unsigned LP_W3 = 16384;
static_assert(LP_T1 + 4 * 1026 <= LP_W2, "prep scratch");
constexpr unsigned w3_slot(int j) { return j * 1280 + (j & 1) * 64 + ((j >> 2) & 1) * 128; }
DI void dma16_add(unsigned lds_base, unsigned lds_off, unsigned voff, __amdgpu_buffer_rsrc_t srd, unsigned sbase, unsigned soff) {
    unsigned t;
    asm volatile("s_add_u32 m0, %1, %2\n\ts_add_u32 %0, %5, %6\n\tbuffer_load_dwordx4 %3, %4, %0 offen lds"
                 : "=&s"(t) : "s"(lds_base), "i"(lds_off), "v"(voff), "s"(srd), "s"(sbase), "i"(soff) : "memory", "scc");
}
DI void prep_start(const Params& P, lds8* lds, int w, int lane, int p) {
    const unsigned ldsb = (unsigned)(size_t)lds;
    const __amdgpu_buffer_rsrc_t srdC = __builtin_amdgcn_make_buffer_rsrc((void*)P.wcin, 0, 3 * 17576 * 2, RSRC3);
    const int i0 = w == 0 ? 0 : w == 1 ? 6 : w == 2 ? 14 : 20;
    dma4(ldsb + LP_WO + w * 256, lane * 4, __builtin_amdgcn_make_buffer_rsrc((void*)P.wout, 0, 78 * 2, RSRC3), 0);
    {
        const unsigned hp = lane >> 5;
        const unsigned vo = hp * 1352 + ((lane >> 2) & 7) * 52 + (((lane & 3) ^ (hp << 1)) << 4);
        const unsigned lb = ldsb + LP_W3 + w * 16640, sb = 52 * 1352 + 52 * i0;
        dma16_add(lb, w3_slot(0), vo, srdC, sb, 0 * 2704);  dma16_add(lb, w3_slot(1), vo, srdC, sb, 1 * 2704);
        dma16_add(lb, w3_slot(2), vo, srdC, sb, 2 * 2704);  dma16_add(lb, w3_slot(3), vo, srdC, sb, 3 * 2704);
        dma16_add(lb, w3_slot(4), vo, srdC, sb, 4 * 2704);  dma16_add(lb, w3_slot(5), vo, srdC, sb, 5 * 2704);
        dma16_add(lb, w3_slot(6), vo, srdC, sb, 6 * 2704);  dma16_add(lb, w3_slot(7), vo, srdC, sb, 7 * 2704);
        dma16_add(lb, w3_slot(8), vo, srdC, sb, 8 * 2704);  dma16_add(lb, w3_slot(9), vo, srdC, sb, 9 * 2704);
        dma16_add(lb, w3_slot(10), vo, srdC, sb, 10 * 2704); dma16_add(lb, w3_slot(11), vo, srdC, sb, 11 * 2704);
        dma16_add(lb, w3_slot(12), vo, srdC, sb, 12 * 2704);
    }
    if (__builtin_expect(w < 2, 1)) {
        const unsigned vo = w == 0 ? 35152 + (lane >> 2) * 1352 + p * 52 + (lane & 3) * 16 : p * 1352 + lane * 16;
        const unsigned lb = ldsb + (w == 0 ? LP_W2 : LP_T0);
        dma16(lb, vo, srdC, 0);
        dma16(lb + 1024, vo, srdC, w == 0 ? 16 * 1352 : 1024);
    }
}
DI void prep_role(const Params& P, lds8* lds, int w, int lane, int p, int slot) {
    const int n = lane & 15, gq = lane >> 4;
    const int i0 = w == 0 ? 0 : w == 1 ? 6 : w == 2 ? 14 : 20;
    const int nact = w == 0 ? 48 : 44;
    const int ch = lane < nact ? lane : 0;
    const int cs = ch >> 1, chh = ch & 1;
    const GAS u32x4* tp = (const GAS u32x4*)((const GAS unsigned char*)(size_t)__builtin_amdgcn_kernarg_segment_ptr() + sizeof(Params) + ((w == 0 ? 384 : 0) + (cs * 2 + chh) * 8) * 4);
    const u32x4 ta = tp[0], tb = tp[1];
    const int q = (lane & 15) >> 2, pp = lane & 3;
    int r0 = 8 * gq + q, r1 = r0 + 4;
    r0 = r0 < 26 ? r0 : 25; r1 = r1 < 26 ? r1 : 25;
    unsigned b0a, b0b, b1a, b1b;
    {
        const unsigned wb = LP_W3 + w * 16640 + pp * 8;
        const unsigned j0 = r0 >> 1, j1 = r1 >> 1;
        const unsigned s0 = wb + j0 * 1280 + (j0 & 1) * 64 + ((j0 >> 2) & 1) * 128 + (r0 & 1) * 512;
        const unsigned s1 = wb + j1 * 1280 + (j1 & 1) * 64 + ((j1 >> 2) & 1) * 128 + (r1 & 1) * 512;
        b0a = s0 + (r0 & 1) * 32; b0b = s0 + 32 - (r0 & 1) * 32; b1a = s1 + (r1 & 1) * 32; b1b = s1 + 32 - (r1 & 1) * 32;
    }
    unsigned va = LP_VT + n * 64 + 2 * i0, vb = n < 10 ? va + 1024 : LP_DM;
    unsigned vl = w == 3 ? LP_DM : 0;
    unsigned ac = LP_T1 + 4 * (4 * gq * 32 + n);
    unsigned a20 = LP_W2 + r0 * 64 + pp * 8, a21 = LP_W2 + r1 * 64 + pp * 8, abv = LP_VT + n * 64 + gq * 16;
    asm volatile("" : "+v"(b0a), "+v"(b0b), "+v"(b1a), "+v"(b1b), "+v"(va), "+v"(vb), "+v"(ac), "+v"(a20), "+v"(a21), "+v"(abv));
    if (w == 3) {
        LAS unsigned* z = (LAS unsigned*)(lds + LP_VT + (lane < 27 ? lane : 31) * 64 + 52);
        z[0] = 0; z[1] = 0; z[2] = 0;
        *(LAS unsigned*)(lds + LP_T1 + 4 * T_ZERO) = 0;
    }
    if (w == 1) *(LAS unsigned*)(lds + LP_T0 + 2 * T_ZERO) = 0;
    asm volatile("s_waitcnt vmcnt(0) lgkmcnt(0)" ::: "memory");
    __builtin_amdgcn_s_barrier();
    {
        const u32x4 wo3 = *(const LAS u32x4*)(lds + LP_WO + w * 256 + 104 + 16 * gq);
        s16x8 bfr[16];
#pragma unroll
        for (int t = 0; t < 16; ++t) {
            const int ii = t >> 1, u = t & 1;
            const s16x4 lo = __builtin_amdgcn_ds_read_tr16_b64_v4i16((LAS s16x4*)(lds + (u ? b0b : b0a) + ii * 64));
            const s16x4 hi = __builtin_amdgcn_ds_read_tr16_b64_v4i16((LAS s16x4*)(lds + (u ? b1b : b1a) + ii * 64));
            bfr[t] = __builtin_shufflevector(lo, hi, 0, 1, 2, 3, 4, 5, 6, 7);
        }
        if (w == 3) {
            const unsigned o2 = *(const LAS unsigned*)(lds + LP_WO + 768 + 52 + (lane < 13 ? lane : 0) * 4);
            const unsigned o1 = *(const LAS unsigned short*)(lds + LP_WO + 768 + p * 2);
            *(LAS unsigned*)(lds + (lane < 13 ? LP_VT + 26 * 64 + lane * 4 : LP_DM)) = o2;
            *(LAS float*)(lds + LP_T1 + 4 * T_CONST) = bf_lo(o1);
        }
        float res[16];
#pragma unroll
        for (int t = 0; t < 16; ++t) res[t] = mfma16(as_frag(wo3), bfr[t], (f32x4){0.f, 0.f, 0.f, 0.f})[0];
#pragma unroll
        for (int k2 = 0; k2 < 4; ++k2) {
            const unsigned pa0 = pk_bf16(res[4 * k2], res[4 * k2 + 2]), pb0 = pk_bf16(res[4 * k2 + 1], res[4 * k2 + 3]);
            if (k2 < 3) { *(LAS unsigned*)(lds + va + 4 * k2) = pa0; *(LAS unsigned*)(lds + vb + 4 * k2) = pb0; }
            else { *(LAS unsigned*)(lds + (vl ? vl : va + 12)) = pa0; *(LAS unsigned*)(lds + (vl ? vl : vb + 12)) = pb0; }
        }
    }
    CBAR();
    sig_add(lds, L_F + 28);
    if (w > 1) return;
    const __amdgpu_buffer_rsrc_t srdA = __builtin_amdgcn_make_buffer_rsrc((void*)(P.ws + WS_A), 0, A_BYTES, RSRC3);
    u32x4 o;
    if (w == 1) {
        float pv[16];
#pragma unroll
        for (int e = 0; e < 8; ++e) {
            const unsigned x = e < 4 ? ta[e] : tb[e - 4];
            pv[2 * e] = bf_lo(*(const LAS unsigned short*)(lds + LP_T0 + 2 * (x & 0xffff)));
            pv[2 * e + 1] = bf_lo(*(const LAS unsigned short*)(lds + LP_T0 + 2 * (x >> 16)));
        }
#pragma unroll
        for (int j = 0; j < 4; ++j) o[j] = pk_bf16(pv[4 * j] + pv[4 * j + 1], pv[4 * j + 2] + pv[4 * j + 3]);
    } else {
        s16x8 a2[2];
#pragma unroll
        for (int mt = 0; mt < 2; ++mt) {
            const s16x4 lo = __builtin_amdgcn_ds_read_tr16_b64_v4i16((LAS s16x4*)(lds + a20 + mt * 32));
            const s16x4 hi = __builtin_amdgcn_ds_read_tr16_b64_v4i16((LAS s16x4*)(lds + a21 + mt * 32));
            a2[mt] = __builtin_shufflevector(lo, hi, 0, 1, 2, 3, 4, 5, 6, 7);
        }
        unsigned pa[16];
#pragma unroll
        for (int e = 0; e < 8; ++e) {
            const unsigned x = e < 4 ? ta[e] : tb[e - 4];
            pa[2 * e] = LP_T1 + 4 * (x & 0xffff); pa[2 * e + 1] = LP_T1 + 4 * (x >> 16);
        }
#pragma unroll
        for (int e = 0; e < 16; ++e) asm volatile("" : "+v"(pa[e]));
        _Pragma("unroll 1") for (unsigned sp = 0; lds_ld(lds, L_F + 28) < 4 && sp < SPIN_CAP; ++sp) __builtin_amdgcn_s_sleep(0);
        CBAR();
        const u32x4 bf0 = *(const LAS u32x4*)(lds + abv), bf1 = *(const LAS u32x4*)(lds + abv + 1024);
        const f32x4 z4 = {0.f, 0.f, 0.f, 0.f};
        const f32x4 d00 = mfma16(a2[0], as_frag(bf0), z4), d01 = mfma16(a2[1], as_frag(bf0), z4);
        const f32x4 d10 = mfma16(a2[0], as_frag(bf1), z4), d11 = mfma16(a2[1], as_frag(bf1), z4);
#pragma unroll
        for (int e = 0; e < 4; ++e) {
            *(LAS float*)(lds + ac + e * 128) = d00[e]; *(LAS float*)(lds + ac + 2048 + e * 128) = d01[e];
            *(LAS float*)(lds + ac + 64 + e * 128) = d10[e]; *(LAS float*)(lds + ac + 2048 + 64 + e * 128) = d11[e];
        }
        float pv[16];
#pragma unroll
        for (int e = 0; e < 16; ++e) pv[e] = *(const LAS float*)(lds + pa[e]);
#pragma unroll
        for (int j = 0; j < 4; ++j) o[j] = pk_bf16(pv[4 * j] + pv[4 * j + 1], pv[4 * j + 2] + pv[4 * j + 3]);
    }
    {
        const int fr = w == 0 ? (cs < 22 ? 2 * cs : 22 + cs) : 2 * cs + 1;
        __builtin_amdgcn_raw_buffer_store_b128(o, srdA, lane < nact ? (unsigned)(fr * 832 + (26 * chh + p) * 16) : 0x7ffffff0u, 0, AUX_SC1);
    }
    WAIT_VM0();
    if (w == 1) { sig_set(lds, L_F + 24); return; }
    const unsigned tagv = *(const LAS unsigned*)(lds + L_EP + (lane & 15) * 4);
    _Pragma("unroll 1") for (unsigned sp = 0; lds_ld(lds, L_F + 24) == 0 && sp < SPIN_CAP; ++sp) __builtin_amdgcn_s_sleep(0);
    if (lane < 16) __hip_atomic_store((gu32*)(P.ws + WS_FL) + lane * 64 + slot, tagv + 1u, __ATOMIC_RELAXED, __HIP_MEMORY_SCOPE_AGENT);
    sig_set(lds, L_F + 60);
}

DI bool rows_ready(bool match) {
    const unsigned long long mk = __ballot(match);
    return (((unsigned)mk | (unsigned)(mk >> 32)) & 0x3ffffffu) == 0x3ffffffu;
}
DI void cin_role(const Params& P, lds8* lds, int cw, int lane, int g, int m, bool has_dnn, int pf_slot) {
    const int d = lane & 31, h = lane >> 5;
    const int b = 64 * g + 4 * m + cw;
    const unsigned ldsb = (unsigned)(size_t)lds;
    {
        const __amdgpu_buffer_rsrc_t srdT = __builtin_amdgcn_make_buffer_rsrc((void*)P.table, 0, NVOC * 64, RSRC3);
        const unsigned lb = ldsb + L_X0 + cw * 1664;
        WAIT_VM0();
        const int q = lane >> 2;
        const unsigned j0 = *(const LAS unsigned*)(lds + L_X0 + cw * 1664 + q * 4);
        const unsigned j1 = *(const LAS unsigned*)(lds + L_X0 + cw * 1664 + (q < 10 ? q + 16 : 25) * 4);
        unsigned o0 = j0 * 64 + (lane & 3) * 16, o1 = j1 * 64 + (lane & 3) * 16;
        asm volatile("" : "+v"(o0), "+v"(o1));
        dma16(lb, o0, srdT, 0);
        if (lane < 40) dma16(lb + 1024, o1, srdT, 0);
        if (cw == 0) dma4_sc1(ldsb + L_FL, g * 256 + lane * 4, __builtin_amdgcn_make_buffer_rsrc((void*)(P.ws + WS_FL), 0, 4096, RSRC3), 0);
    }
    const unsigned bo = P.bout[0];
    __builtin_amdgcn_s_setprio(0);
    CBAR();
    __builtin_amdgcn_s_barrier();
    const unsigned epv = *(const LAS unsigned*)(lds + L_EP + g * 4);
    unsigned bov = bo;
    asm volatile("s_waitcnt vmcnt(0)" : "+v"(bov) :: "memory");
    float x[26];
    {
        const lds8* xb = lds + L_X0 + cw * 1664 + h * 64 + d * 2;
        const lds8* xl = lds + L_X0 + cw * 1664 + (h ? 0 : 25 * 64) + d * 2;
#pragma unroll
        for (int a = 0; a < 13; ++a) {
            const unsigned lo = *(const LAS unsigned short*)(xb + 2 * a * 64);
            const unsigned hi = a < 12 ? *(const LAS unsigned short*)(xb + (2 * a + 1) * 64) : *(const LAS unsigned short*)xl;
            x[2 * a] = bf_lo(lo); x[2 * a + 1] = bf_lo(hi);
        }
    }
    u32x4 z[24];
#pragma unroll
    for (int s = 0; s < 22; ++s) {
        float fa[8], fb[8];
#pragma unroll
        for (int e = 0; e < 8; ++e) { const AB pe = slot_ab(8 * s + e); fa[e] = x[pe.a]; fb[e] = x[pe.b]; }
        float t0, t1, t2, t3, t4, t5;
        unsigned o0, o1, o2, o3;
        asm("v_mul_f32 %4, %10, %11\n\tv_mul_f32 %5, %12, %13\n\tv_mul_f32 %6, %14, %15\n\tv_mul_f32 %7, %16, %17\n\t"
            "v_cvt_pk_bf16_f32 %0, %4, %5\n\tv_mul_f32 %8, %18, %19\n\tv_mul_f32 %9, %20, %21\n\tv_cvt_pk_bf16_f32 %1, %6, %7\n\t"
            "v_mul_f32 %4, %22, %23\n\tv_mul_f32 %5, %24, %25\n\tv_cvt_pk_bf16_f32 %2, %8, %9\n\tv_cvt_pk_bf16_f32 %3, %4, %5"
            : "=&v"(o0), "=&v"(o1), "=&v"(o2), "=v"(o3), "=&v"(t0), "=&v"(t1), "=&v"(t2), "=&v"(t3), "=&v"(t4), "=&v"(t5)
            : "v"(fa[0]), "v"(fb[0]), "v"(fa[1]), "v"(fb[1]), "v"(fa[2]), "v"(fb[2]), "v"(fa[3]), "v"(fb[3]),
              "v"(fa[4]), "v"(fb[4]), "v"(fa[5]), "v"(fb[5]), "v"(fa[6]), "v"(fb[6]), "v"(fa[7]), "v"(fb[7]));
        z[s] = (u32x4){o0, o1, o2, o3};
    }
#pragma unroll
    for (int s = 22; s < 24; ++s) {
#pragma unroll
        for (int j = 0; j < 4; ++j) {
            const AB p0 = slot_ab(8 * s + 2 * j), p1 = slot_ab(8 * s + 2 * j + 1);
            const float a0 = p0.a < 26 ? x[p0.a] : (p0.a == 26 ? 1.f : 0.f);
            const float a1 = p1.a < 26 ? x[p1.a] : (p1.a == 26 ? 1.f : 0.f);
            z[s][j] = pk_bf16(a0, a1);
        }
    }
#pragma unroll
    for (int s = 0; s < 24; ++s) asm volatile("" : "+v"(z[s]));
    const unsigned tag = epv + 1u;
    {
        const __amdgpu_buffer_rsrc_t srdA = __builtin_amdgcn_make_buffer_rsrc((void*)(P.ws + WS_A), 0, A_BYTES, RSRC3);
        if (cw == 0) {
            if (has_dnn) _Pragma("unroll 1") for (unsigned sp = 0; lds_ld(lds, L_F + 52) < 4 && sp < SPIN_CAP; ++sp) __builtin_amdgcn_s_sleep(0);
            else _Pragma("unroll 1") for (unsigned sp = 0; lds_ld(lds, L_F + 60) == 0 && sp < SPIN_CAP; ++sp) __builtin_amdgcn_s_sleep(1);
            const gu32* fl = (const gu32*)(P.ws + WS_FL) + g * 64 + lane;
            if (!rows_ready(*(const LAS unsigned*)(lds + L_FL + lane * 4) == tag))
            _Pragma("unroll 1") for (unsigned sp = 0; sp < SPIN_CAP; ++sp) { if (rows_ready(ld_agent(fl) == tag)) break; __builtin_amdgcn_s_sleep(1); }
            sig_set(lds, L_F + 16);
            if (!has_dnn) dma4_sc1(ldsb + L_DUMMY, lane < 50 ? (unsigned)((((g & 1) * 3 + pf_slot) * 50 + lane) * 128) : 0x7fffff00u, srdA, 0);
        } else {
            _Pragma("unroll 1") for (unsigned sp = 0; lds_ld(lds, L_F + 16) == 0 && sp < SPIN_CAP; ++sp) __builtin_amdgcn_s_sleep(0);
        }
        CBAR();
        __builtin_amdgcn_s_setprio(2);
        {
            const unsigned l1 = ldsb + L_W + cw * 4096, vo = lane * 16;
            asm volatile("s_mov_b32 m0, %0\n\ts_nop 0\n\t"
                         "buffer_load_dwordx4 %1, %2, %3 offen sc1 lds\n\tbuffer_load_dwordx4 %1, %2, %3 offen offset:1024 sc1 lds\n\t"
                         "buffer_load_dwordx4 %1, %2, %3 offen offset:2048 sc1 lds\n\tbuffer_load_dwordx4 %1, %2, %3 offen offset:3072 sc1 lds"
                         :: "s"(l1), "v"(vo), "s"(srdA), "s"(cw * 4096) : "memory");
#pragma unroll
            for (int i = 0; i < 6; ++i) {
                const int q = 6 * cw + i;
                const unsigned go = q < 4 ? 16384 + 1024 * q : 19968 + 1024 * (q - 4), lo = q < 4 ? 16384 + 1024 * q : 53248 + 1024 * (q - 4);
                dma16_sc1(ldsb + L_W + lo, vo, srdA, go);
            }
        }
        wait_vm<6>();
        sig_add(lds, L_F + 20);
        _Pragma("unroll 1") for (unsigned sp = 0; lds_ld(lds, L_F + 20) < 4 && sp < SPIN_CAP; ++sp) __builtin_amdgcn_s_sleep(0);
        CBAR();
    }
    const gu64* pp = (const gu64*)(P.ws + WS_PART) + ((g * 32 + (lane < 26 ? lane : 0)) * 64 + 4 * m + cw);
    unsigned long long pv = 0;
    f32x16 acc0, acc1;
#pragma unroll
    for (int i = 0; i < 16; ++i) { acc0[i] = 0.f; acc1[i] = 0.f; }
    {
        unsigned abo = L_W + (26 * h + d) * 16;
        asm volatile("" : "+v"(abo));
        const lds8* ab = lds + abo;
        constexpr int PF = 6;
        u32x4 af[46];
        unsigned cnt2 = 0;
        auto frag_of = [](int t) { return t < 24 ? t : t + 40; };
#pragma unroll
        for (int t = 0; t < PF; ++t) af[t] = *(const LAS u32x4*)(ab + frag_of(t) * 832);
        __builtin_amdgcn_sched_barrier(0);
#pragma unroll
        for (int t = 0; t < 46; ++t) {
            if (t == 9) { wait_vm<0>(); sig_add(lds, L_F + 44); }
            if (t + PF == 19 - 2) cnt2 = lds_ld(lds, L_F + 44);
            if (t + PF == 19) {
                if (cnt2 < 4) _Pragma("unroll 1") for (unsigned sp = 0; lds_ld(lds, L_F + 44) < 4 && sp < SPIN_CAP; ++sp) __builtin_amdgcn_s_sleep(0);
                CBAR();
            }
            if (t + PF < 46) af[t + PF] = *(const LAS u32x4*)(ab + frag_of(t + PF) * 832);
            if (t < 44) {
                if (t & 1) acc0 = mfma32(as_frag(af[t]), as_frag(z[t >> 1]), acc0);
                else acc1 = mfma32(as_frag(af[t]), as_frag(z[t >> 1]), acc1);
            } else acc1 = mfma32(as_frag(af[t]), as_frag(z[t - 22]), acc1);
            if (t == 30) pv = __hip_atomic_load(pp, __ATOMIC_RELAXED, __HIP_MEMORY_SCOPE_AGENT);
            __builtin_amdgcn_sched_barrier(0);
        }
    }
    float sa = 0.f;
#pragma unroll
    for (int i = 0; i < 12; ++i) sa = __builtin_fmaf(acc0[i], acc1[i], sa);
    const float sb = __builtin_fmaf(acc0[12], acc1[12], acc0[13] * acc1[13]);
    float tot = h == 0 ? sa + sb : sa;
    asm volatile("" : "+v"(tot));
    _Pragma("unroll 1") for (unsigned sp = 0; sp < SPIN_CAP; ++sp) {
        if (__all((unsigned)(pv >> 32) == tag)) break;
        pv = __hip_atomic_load(pp, __ATOMIC_RELAXED, __HIP_MEMORY_SCOPE_AGENT);
    }
    if (lane < 26) tot += __uint_as_float((unsigned)pv);
    tot = wave_sum_to_lane63(tot);
    if (lane == 63) {
        const float o = tot + bf_lo(bov);
        P.out[b] = (unsigned short)(pk_bf16(o, o) & 0xffffu);
    }
    if (m == 0 && cw == 0) {
        const gu32* fl = (const gu32*)(P.ws + WS_FL) + g * 64 + lane;
        _Pragma("unroll 1") for (unsigned sp = 0; sp < SPIN_CAP; ++sp) {
            if ((__ballot(ld_agent(fl) == tag) & 0x003fffff03ffffffull) == 0x003fffff03ffffffull) break;
            __builtin_amdgcn_s_sleep(1);
        }
        if (lane == 0) __hip_atomic_store((gu32*)(P.ws + WS_EP) + g, tag, __ATOMIC_RELAXED, __HIP_MEMORY_SCOPE_AGENT);
    }
}

DI void decode_block(int bid, int& g, int& m, int& ntile, int& k) {
    const int x = bid & 7, j = bid >> 3;
    g = 2 * x + (j >> 4); m = j & 15;
    ntile = m < 13 ? m : -1; k = m - 13;
}

__global__ void __launch_bounds__(512, 2) xdfm_kernel(KArgs KA) {
    const Params& P = KA.P;
    __shared__ __attribute__((aligned(16))) unsigned char smem[L_END];
    lds8* lds = (lds8*)smem;
    const int w = __builtin_amdgcn_readfirstlane(threadIdx.x >> 6), lane = threadIdx.x & 63;
    {
        unsigned long long pc; unsigned len;
        asm volatile("s_getpc_b64 %0\n.Lxdfm_pc:\n\ts_mov_b32 %1, (.Lxdfm_end-.Lxdfm_pc)&4294967295" : "=s"(pc), "=s"(len));
        const __amdgpu_buffer_rsrc_t srdP = __builtin_amdgcn_make_buffer_rsrc((void*)pc, 0, len, RSRC3);
        dma4((unsigned)(size_t)lds + L_DUMMY, lane == 0 ? ((blockIdx.x >> 3) * 8 + w) * 128 : 0x7fffff00u, srdP, 0);
    }
    asm volatile("" :: "s"(P.idx), "s"(P.table), "s"(P.wcin), "s"(P.wout), "s"(P.w1), "s"(P.b1), "s"(P.w2), "s"(P.bout), "s"(P.out), "s"(P.ws));
    int g, m, ntile, k;
    decode_block(blockIdx.x, g, m, ntile, k);
    const bool is_cin = w >= 4;
    if (__builtin_expect(is_cin, 1)) {
        __builtin_amdgcn_s_setprio(3);
        dma16((unsigned)(size_t)lds + L_X0 + (w - 4) * 1664, lane < 7 ? (64 * g + 4 * m + w - 4) * 104 + lane * 16 : 0x7fffff00u,
              __builtin_amdgcn_make_buffer_rsrc((void*)P.idx, 0, NBATCH * 26 * 4, RSRC3), 0);
        if (__builtin_expect(w == 7, 1)) dma4((unsigned)(size_t)lds + L_DUMMY, lane < 26 ? ((blockIdx.x >> 3) * 26 + lane) * 128 : 0x7fffff00u,
                         __builtin_amdgcn_make_buffer_rsrc((void*)P.wcin, 0, 3 * 17576 * 2, RSRC3), 0);
    }
    if (__builtin_expect(w == 0, 1)) {
        if (lane < 16) lds_st(lds, L_F + 4 * lane, 0);
        dma4_sc1((unsigned)(size_t)lds + L_EP, (lane & 15) * 4, __builtin_amdgcn_make_buffer_rsrc((void*)(P.ws + WS_EP), 0, 64, RSRC3), 0);
    }
    const int np = g * 3 + k;
    const bool second = (np >= 22 && np < 42) || np >= 46;
    const int p = second ? (np < 42 ? np - 22 : np - 26) : (np < 22 ? np : np - 20);
    if (__builtin_expect(!is_cin && ntile >= 0, 1)) dnn_start(P, lds, w, lane, g, ntile);
    if (__builtin_expect(!is_cin && ntile < 0, 1)) prep_start(P, lds, w, lane, p);
    if (__builtin_expect(!is_cin && ntile >= 0, 1)) {
        dnn_role(P, lds, w, lane, g, ntile);
    } else if (is_cin) {
        cin_role(P, lds, w - 4, lane, g, m, ntile >= 0, k);
        __builtin_amdgcn_s_waitcnt(0x0F70);
    } else {
        prep_role(P, lds, w, lane, p, second ? 32 + p : p);
        __builtin_amdgcn_s_waitcnt(0x0F70);
    }
    asm volatile(".Lxdfm_end:");
}

extern "C" void kernel_launch(void* const* d_in, const int* in_sizes, int n_in,
                              void* d_out, int out_size, void* d_ws, size_t ws_size,
                              hipStream_t stream) {
    KArgs KA;
    KA.tab = make_tab();
    Params& P = KA.P;
    P.idx = (const int*)d_in[0]; P.table = (const unsigned short*)d_in[1]; P.wcin = (const unsigned short*)d_in[2];
    P.wout = (const unsigned short*)d_in[3]; P.w1 = (const unsigned short*)d_in[4]; P.b1 = (const unsigned short*)d_in[5];
    P.w2 = (const unsigned short*)d_in[6]; P.bout = (const unsigned short*)d_in[7];
    P.out = (unsigned short*)d_out; P.ws = (unsigned char*)d_ws;
    xdfm_kernel<<<256, 512, 0, stream>>>(KA);
}
```

```cpp
#include <hip/hip_runtime.h>
#include <stdint.h>

#define DI __device__ __forceinline__
#define GAS __attribute__((address_space(1)))
#define LAS __attribute__((address_space(3)))

typedef short    s16x4 __attribute__((ext_vector_type(4)));
typedef short    s16x8 __attribute__((ext_vector_type(8)));
typedef float    f32x2 __attribute__((ext_vector_type(2)));
typedef float    f32x4 __attribute__((ext_vector_type(4)));
typedef float    f32x16 __attribute__((ext_vector_type(16)));
typedef unsigned u32x2 __attribute__((ext_vector_type(2)));
typedef unsigned u32x4 __attribute__((ext_vector_type(4)));
typedef __bf16   bf16x2 __attribute__((ext_vector_type(2)));
typedef GAS unsigned gu32;
typedef GAS unsigned long long gu64;
typedef LAS unsigned char lds8;

constexpr int DIN = 832, NBATCH = 1024, NVOC = 100000;
constexpr int XD = 6;
constexpr int W_EARLY = 2;
constexpr unsigned RSRC3 = 0x00020000u;
constexpr int AUX_SC1 = 16;
constexpr unsigned SPIN_CAP = 1u << 20;

constexpr unsigned WS_EP = 0;
constexpr unsigned WS_FL = 4096;
constexpr unsigned WS_PART = 8192;
constexpr unsigned WS_A = 8192 + 16 * 32 * 64 * 8;
constexpr unsigned A_BYTES = 46 * 832;

constexpr unsigned L_W = 0;
constexpr unsigned L_A = 832 * 128;
constexpr unsigned L_X0 = L_A + 4 * XD * 2048;
constexpr unsigned L_F = L_X0 + 4 * 1664;
constexpr unsigned L_DUMMY = L_F + 256;
constexpr unsigned L_EP = L_DUMMY + 256;
constexpr unsigned L_BI = L_EP + 256;
constexpr unsigned L_W2 = L_BI + 256;
constexpr unsigned L_FL = L_W2 + 256;
constexpr unsigned L_END = L_FL + 256;
static_assert(L_END <= 163840, "LDS map");
constexpr unsigned LP_VT = 0;
constexpr unsigned LP_T0 = 2048;
constexpr unsigned LP_T1 = 2048 + 2304;

struct AB { int a, b; };
constexpr AB slot_ab(int t) {
    if (t < 156) { int a = 2 * (t / 12); return {a, (a + t % 12) % 26}; }
    if (t < 170) { int u = t - 156; int a = 2 * (u / 2); return {a, (a + 12 + (u & 1)) % 26}; }
    if (t < 176) { int a = 2 * (7 + t - 170); return {a, (a + 12) % 26}; }
    if (t < 189) return {2 * (t - 176), 26};
    if (t == 189) return {26, 26};
    return {27, 27};
}
constexpr unsigned T_ZERO = 1024, T_CONST = 1025;
struct PrepTab { unsigned v[2][24 * 2 * 8]; };
constexpr PrepTab make_tab() {
    PrepTab tb{};
    for (int m = 0; m < 2; ++m) for (int s = 0; s < 24; ++s) for (int h = 0; h < 2; ++h) for (int e = 0; e < 8; ++e) {
        const int st = m ? 32 : 26;
        int t = 8 * s + e; AB ab = slot_ab(t);
        unsigned o1 = T_ZERO, o2 = T_ZERO;
        if (ab.a == 27) {}
        else if (ab.a == 26) { if (h == 0 && m) o1 = T_CONST; }
        else if (ab.b == 26) { if (m) o1 = ((ab.a + h) % 26) * st + 26; }
        else {
            int u = (ab.a + h) % 26, v = (ab.b + h) % 26;
            bool dup = (t == 169 && h == 1);
            if (dup) {}
            else if (u == v) o1 = u * st + u;
            else { o1 = u * st + v; o2 = v * st + u; }
        }
        tb.v[m][(s * 2 + h) * 8 + e] = o1 | (o2 << 16);
    }
    return tb;
}
static_assert(slot_ab(169).a == 12 && slot_ab(169).b == 25, "dup slot");

struct Params {
    const int* idx; const unsigned short* table; const unsigned short* wcin; const unsigned short* wout;
    const unsigned short* w1; const unsigned short* b1; const unsigned short* w2; const unsigned short* bout;
    unsigned short* out; unsigned char* ws;
};
struct KArgs { Params P; PrepTab tab; };

DI float bf_lo(unsigned v) { return __uint_as_float(v << 16); }
DI float bf_hi(unsigned v) { return __uint_as_float(v & 0xffff0000u); }
DI unsigned pk_bf16(float lo, float hi) { f32x2 v = {lo, hi}; return __builtin_bit_cast(unsigned, __builtin_convertvector(v, bf16x2)); }
DI f32x4 mfma16(s16x8 a, s16x8 b, f32x4 c) { return __builtin_amdgcn_mfma_f32_16x16x32_bf16(a, b, c, 0, 0, 0); }
DI f32x16 mfma32(s16x8 a, s16x8 b, f32x16 c) { return __builtin_amdgcn_mfma_f32_32x32x16_bf16(a, b, c, 0, 0, 0); }
DI s16x8 as_frag(u32x4 v) { return __builtin_bit_cast(s16x8, v); }
template <int CTRL, int ROWMASK> DI float dpp_add(float v) {
    return v + __int_as_float(__builtin_amdgcn_update_dpp(0, __float_as_int(v), CTRL, ROWMASK, 0xf, true));
}
DI float wave_sum_to_lane63(float v) {
    v = dpp_add<0x111, 0xf>(v); v = dpp_add<0x112, 0xf>(v); v = dpp_add<0x114, 0xf>(v); v = dpp_add<0x118, 0xf>(v);
    v = dpp_add<0x142, 0xa>(v); v = dpp_add<0x143, 0xc>(v);
    return v;
}
DI float gelu_tanh(float x) {
    const float k1 = -2.0f * 0.7978845608028654f * 1.4426950408889634f;
    const float k2 = k1 * 0.044715f;
    float arg = x * __builtin_fmaf(x * x, k2, k1);
    float e = __builtin_amdgcn_exp2f(arg);
    return x * __builtin_amdgcn_rcpf(1.0f + e);
}
DI void dma16(unsigned lds_addr, unsigned voff, __amdgpu_buffer_rsrc_t srd, unsigned soff) {
    asm volatile("s_mov_b32 m0, %0\n\ts_nop 0\n\tbuffer_load_dwordx4 %1, %2, %3 offen lds"
                 :: "s"(lds_addr), "v"(voff), "s"(srd), "s"(soff) : "memory");
}
DI void dma16_sc1(unsigned lds_addr, unsigned voff, __amdgpu_buffer_rsrc_t srd, unsigned soff) {
    asm volatile("s_mov_b32 m0, %0\n\ts_nop 0\n\tbuffer_load_dwordx4 %1, %2, %3 offen sc1 lds"
                 :: "s"(lds_addr), "v"(voff), "s"(srd), "s"(soff) : "memory");
}
DI void dma4_sc1(unsigned lds_addr, unsigned voff, __amdgpu_buffer_rsrc_t srd, unsigned soff) {
    asm volatile("s_mov_b32 m0, %0\n\ts_nop 0\n\tbuffer_load_dword %1, %2, %3 offen sc1 lds"
                 :: "s"(lds_addr), "v"(voff), "s"(srd), "s"(soff) : "memory");
}
DI void dma4(unsigned lds_addr, unsigned voff, __amdgpu_buffer_rsrc_t srd, unsigned soff) {
    asm volatile("s_mov_b32 m0, %0\n\ts_nop 0\n\tbuffer_load_dword %1, %2, %3 offen lds"
                 :: "s"(lds_addr), "v"(voff), "s"(srd), "s"(soff) : "memory");
}
DI void st_granule(gu64* g, unsigned tag, unsigned val) {
    __hip_atomic_store(g, ((unsigned long long)tag << 32) | val, __ATOMIC_RELAXED, __HIP_MEMORY_SCOPE_AGENT);
}
DI unsigned ld_agent(const gu32* p) { return __hip_atomic_load(p, __ATOMIC_RELAXED, __HIP_MEMORY_SCOPE_AGENT); }
DI unsigned lds_ld(lds8* l, unsigned off) { return __hip_atomic_load((LAS unsigned*)(l + off), __ATOMIC_RELAXED, __HIP_MEMORY_SCOPE_WORKGROUP); }
DI void lds_st(lds8* l, unsigned off, unsigned v) { __hip_atomic_store((LAS unsigned*)(l + off), v, __ATOMIC_RELAXED, __HIP_MEMORY_SCOPE_WORKGROUP); }
DI void sig_add(lds8* l, unsigned off) {
    asm volatile("s_mov_b64 exec, 1\n\tds_add_u32 %0, %1\n\ts_mov_b64 exec, -1" :: "v"((unsigned)(size_t)l + off), "v"(1u) : "memory");
}
DI void sig_set(lds8* l, unsigned off) {
    asm volatile("s_mov_b64 exec, 1\n\tds_write_b32 %0, %1\n\ts_mov_b64 exec, -1" :: "v"((unsigned)(size_t)l + off), "v"(1u) : "memory");
}
#define WAIT_LGKM() asm volatile("s_waitcnt lgkmcnt(0)" ::: "memory")
#define WAIT_VM0() asm volatile("s_waitcnt vmcnt(0)" ::: "memory")
#define CBAR() asm volatile("" ::: "memory")

template <int K> DI unsigned quad_bcast(unsigned v) { return __builtin_amdgcn_update_dpp(0u, v, K * 0x55, 0xf, 0xf, true); }
template <int N> DI void wait_vm() { asm volatile("s_waitcnt vmcnt(%0)" :: "i"(N) : "memory"); }
DI void dma_w4(unsigned lds_addr, unsigned voff, __amdgpu_buffer_rsrc_t srd, unsigned soff) {
    unsigned t;
    asm volatile("s_mov_b32 m0, %1\n\ts_add_u32 %0, %4, 12288\n\tbuffer_load_dwordx4 %2, %3, %4 offen lds\n\t"
                 "buffer_load_dwordx4 %2, %3, %0 offen offset:1024 lds\n\ts_add_u32 %0, %0, 12288\n\t"
                 "buffer_load_dwordx4 %2, %3, %0 offen offset:2048 lds\n\ts_add_u32 %0, %0, 12288\n\t"
                 "buffer_load_dwordx4 %2, %3, %0 offen offset:3072 lds"
                 : "=&s"(t) : "s"(lds_addr), "v"(voff), "s"(srd), "s"(soff) : "memory", "scc");
}
DI void dnn_start(const Params& P, lds8* lds, int w, int lane, int g, int ntile) {
    const int mh = w & 1, kh = w >> 1;
    const unsigned ldsb = (unsigned)(size_t)lds;
    const unsigned ring = ldsb + L_A + w * (XD * 2048);
    const __amdgpu_buffer_rsrc_t srdI = __builtin_amdgcn_make_buffer_rsrc((void*)P.idx, 0, NBATCH * 26 * 4, RSRC3);
    const unsigned vo = ((64 * g + 32 * mh + (lane >> 2)) * 26 + 13 * kh + 4 * (lane & 3)) * 4;
    dma16(ring, vo, srdI, 0);
    dma16(ring + 1024, vo, srdI, 1664);
    if (__builtin_expect(w == 1, 1)) dma4(ldsb + L_BI, ntile * 128 + lane * 4, __builtin_amdgcn_make_buffer_rsrc((void*)P.b1, 0, DIN * 2, RSRC3), 0);
    if (__builtin_expect(w == 2, 1)) dma4(ldsb + L_W2, ntile * 128 + lane * 4, __builtin_amdgcn_make_buffer_rsrc((void*)P.w2, 0, DIN * 2, RSRC3), 0);
    const __amdgpu_buffer_rsrc_t srdW = __builtin_amdgcn_make_buffer_rsrc((void*)P.w1, 0, DIN * DIN * 2, RSRC3);
    const unsigned vw = (lane >> 3) * 1664 + (((lane & 7) ^ (((lane >> 4) & 1) << 2)) << 4);
    const unsigned rowA = 416 * kh + 208 * mh + 104 * (g & 1);
    dma_w4(ldsb + L_W + rowA * 128, vw, srdW, rowA * 1664 + ntile * 128);
    if (W_EARLY > 1) dma_w4(ldsb + L_W + rowA * 128 + 4096, vw, srdW, rowA * 1664 + ntile * 128 + 4 * 13312);
    if (W_EARLY > 2) dma_w4(ldsb + L_W + rowA * 128 + 8192, vw, srdW, rowA * 1664 + ntile * 128 + 8 * 13312);
}
DI void dnn_role(const Params& P, lds8* lds, int w, int lane, int g, int ntile) {
    const int mh = w & 1, kh = w >> 1;
    const int h = lane >> 5;
    const unsigned ldsb = (unsigned)(size_t)lds;
    const unsigned ring = ldsb + L_A + w * (XD * 2048);
    const __amdgpu_buffer_rsrc_t srdW = __builtin_amdgcn_make_buffer_rsrc((void*)P.w1, 0, DIN * DIN * 2, RSRC3);
    const unsigned vw = (lane >> 3) * 1664 + (((lane & 7) ^ (((lane >> 4) & 1) << 2)) << 4);
    const unsigned rowA = 416 * kh + 208 * mh + 104 * (g & 1), rowB = 416 * kh + 208 * mh + 104 * ((g & 1) ^ 1);
    auto wdma = [&](unsigned row, int a) {
        const unsigned sb = row * 1664 + ntile * 128, lb = ldsb + L_W + row * 128;
        if (a < 3) dma_w4(lb + a * 4096, vw, srdW, sb + a * 4 * 13312);
        else dma16(lb + 12288, vw, srdW, sb + 12 * 13312);
    };
    wait_vm<4 * W_EARLY>();
    WAIT_LGKM();
    __builtin_amdgcn_s_barrier();
    const u32x4 i0 = *(const LAS u32x4*)(lds + L_A + w * (XD * 2048) + lane * 16);
    const u32x4 i1 = *(const LAS u32x4*)(lds + L_A + w * (XD * 2048) + 1024 + lane * 16);
    const unsigned ep = *(const LAS unsigned*)(lds + L_EP + g * 4);
#pragma unroll
    for (int a = W_EARLY; a < 4; ++a) wdma(rowA, a);
#pragma unroll
    for (int a = 0; a < 4; ++a) wdma(rowB, a);
    const __amdgpu_buffer_rsrc_t srdT = __builtin_amdgcn_make_buffer_rsrc((void*)P.table, 0, NVOC * 64, RSRC3);
    const unsigned c16 = (((lane & 3) ^ ((lane >> 4) & 3)) << 4);
    u32x4 t0 = i0 << 6, t1 = i1 << 6;
    asm volatile("" : "+v"(t0), "+v"(t1));
    auto xload = [&](int s) {
        const unsigned a0 = t0[s & 3], a1 = t1[s & 3];
        const unsigned o0 = (s < 4 ? quad_bcast<0>(a0) : s < 8 ? quad_bcast<1>(a0) : s < 12 ? quad_bcast<2>(a0) : quad_bcast<3>(a0)) + c16;
        const unsigned o1 = (s < 4 ? quad_bcast<0>(a1) : s < 8 ? quad_bcast<1>(a1) : s < 12 ? quad_bcast<2>(a1) : quad_bcast<3>(a1)) + c16;
        dma16(ring + (s % XD) * 2048, o0, srdT, 0);
        dma16(ring + (s % XD) * 2048 + 1024, o1, srdT, 0);
    };
#pragma unroll
    for (int s = 0; s < XD; ++s) xload(s);
    wait_vm<2 * XD>();
    sig_add(lds, L_F + 32 + 4 * kh);
    _Pragma("unroll 1") for (unsigned sp = 0; lds_ld(lds, L_F + 32 + 4 * kh) < 2 && sp < SPIN_CAP; ++sp) __builtin_amdgcn_s_sleep(0);
    CBAR();
    f32x16 acc[2];
    u32x2 bia[4], wv[4];
    float w2f[16];
    unsigned xdone = 0;
#pragma unroll
    for (int r4 = 0; r4 < 4; ++r4) {
        bia[r4] = *(const LAS u32x2*)(lds + L_BI + kh * 64 + h * 8 + r4 * 16);
        wv[r4] = *(const LAS u32x2*)(lds + L_W2 + kh * 64 + h * 8 + r4 * 16);
    }
#pragma unroll
    for (int r4 = 0; r4 < 4; ++r4) {
        acc[0][4 * r4] = bf_lo(bia[r4][0]); acc[0][4 * r4 + 1] = bf_hi(bia[r4][0]); acc[0][4 * r4 + 2] = bf_lo(bia[r4][1]); acc[0][4 * r4 + 3] = bf_hi(bia[r4][1]);
        acc[1][4 * r4] = 0.f; acc[1][4 * r4 + 1] = 0.f; acc[1][4 * r4 + 2] = 0.f; acc[1][4 * r4 + 3] = 0.f;
    }
    {
        const int blk = (lane >> 4) & 1, q = (lane & 15) >> 2, pp = lane & 3, n = lane & 31;
        unsigned ad[2];
#pragma unroll
        for (int j = 0; j < 2; ++j) {
            const unsigned c = 4 * (j ^ kh) + 2 * blk + (pp >> 1);
            ad[j] = L_W + (416 * kh + 8 * h + q) * 128 + ((c ^ (((q >> 1) & 1) << 2)) << 4) + (pp & 1) * 8;
        }
        const unsigned xb = L_A + w * (XD * 2048) + n * 64;
        const unsigned xa0 = xb + ((h ^ ((n >> 2) & 3)) << 4), xa1 = xb + (((2 + h) ^ ((n >> 2) & 3)) << 4);
        u32x4 xf[3][2];
        s16x8 wf[2][4];
        auto xread = [&](int s) {
            xf[s % 3][0] = *(const LAS u32x4*)(lds + xa0 + (s % XD) * 2048);
            xf[s % 3][1] = *(const LAS u32x4*)(lds + xa1 + (s % XD) * 2048);
        };
        auto wread = [&](int s) {
#pragma unroll
            for (int a = 0; a < 2; ++a)
#pragma unroll
                for (int j = 0; j < 2; ++j) {
                    const s16x4 lo = __builtin_amdgcn_ds_read_tr16_b64_v4i16((LAS s16x4*)(lds + ad[j] + s * 4096 + a * 2048));
                    const s16x4 hi = __builtin_amdgcn_ds_read_tr16_b64_v4i16((LAS s16x4*)(lds + ad[j] + s * 4096 + a * 2048 + 512));
                    wf[s & 1][2 * a + j] = __builtin_shufflevector(lo, hi, 0, 1, 2, 3, 4, 5, 6, 7);
                }
        };
        wread(0);
        wait_vm<2 * (XD - 1)>();
        xread(0);
        wait_vm<2 * (XD - 2)>();
        xread(1);
        __builtin_amdgcn_sched_barrier(0);
#pragma unroll
        for (int s = 0; s < 13; ++s) {
            if (s + 1 < 13) wread(s + 1);
#pragma unroll
            for (int a = 0; a < 2; ++a)
#pragma unroll
                for (int j = 0; j < 2; ++j) acc[j] = mfma32(wf[s & 1][2 * a + j], as_frag(xf[s % 3][a]), acc[j]);
            if (s + 1 < 13) {
#pragma unroll
                for (int j = 0; j < 4; ++j) { __builtin_amdgcn_sched_group_barrier(0x100, 2, 0); __builtin_amdgcn_sched_group_barrier(0x8, 1, 0); }
            }
            __builtin_amdgcn_sched_barrier(0);
            if (s + XD < 13) xload(s + XD);
            if (s == 11) {
                xdone = lds_ld(lds, L_F + 52);
#pragma unroll
                for (int r4 = 0; r4 < 4; ++r4) { w2f[4 * r4] = bf_lo(wv[r4][0]); w2f[4 * r4 + 1] = bf_hi(wv[r4][0]); w2f[4 * r4 + 2] = bf_lo(wv[r4][1]); w2f[4 * r4 + 3] = bf_hi(wv[r4][1]); }
#pragma unroll
                for (int e = 0; e < 16; ++e) asm volatile("" : "+v"(w2f[e]));
            }
            if (s + 2 < 13) {
                constexpr int LASTI = 12;
                const int last = s + XD < LASTI ? s + XD : LASTI;
                const int younger = last - (s + 2);
                if (younger >= 4) wait_vm<8>(); else if (younger == 3) wait_vm<6>(); else if (younger == 2) wait_vm<4>(); else if (younger == 1) wait_vm<2>(); else wait_vm<0>();
                if (s + 2 == 12) sig_add(lds, L_F + 52);
                xread(s + 2);
            }
            __builtin_amdgcn_sched_barrier(0);
        }
    }
    {
        if (xdone < 4) _Pragma("unroll 1") for (unsigned sp = 0; lds_ld(lds, L_F + 52) < 4 && sp < SPIN_CAP; ++sp) __builtin_amdgcn_s_sleep(0);
        CBAR();
        lds8* xw = lds + L_W + kh * 53248 + (5 + mh) * 4096 + lane * 16;
#pragma unroll
        for (int r4 = 0; r4 < 4; ++r4) *(LAS f32x4*)(xw + r4 * 1024) = (f32x4){acc[1][4 * r4], acc[1][4 * r4 + 1], acc[1][4 * r4 + 2], acc[1][4 * r4 + 3]};
        CBAR();
        sig_set(lds, L_F + 4 * w);
        _Pragma("unroll 1") for (unsigned sp = 0; lds_ld(lds, L_F + 4 * (w ^ 2)) == 0 && sp < SPIN_CAP; ++sp) __builtin_amdgcn_s_sleep(0);
        CBAR();
        const lds8* xr = lds + L_W + (kh ^ 1) * 53248 + (5 + mh) * 4096 + lane * 16;
        f32x4 o[4];
#pragma unroll
        for (int r4 = 0; r4 < 4; ++r4) o[r4] = *(const LAS f32x4*)(xr + r4 * 1024);
        asm volatile("" : "+v"(o[0]), "+v"(o[1]), "+v"(o[2]), "+v"(o[3]));
#pragma unroll
        for (int r4 = 0; r4 < 4; ++r4) {
            acc[0][4 * r4] += o[r4][0]; acc[0][4 * r4 + 1] += o[r4][1]; acc[0][4 * r4 + 2] += o[r4][2]; acc[0][4 * r4 + 3] += o[r4][3];
        }
    }
    float s0 = 0.f, s1 = 0.f;
#pragma unroll
    for (int r4 = 0; r4 < 4; ++r4) {
        s0 = __builtin_fmaf(gelu_tanh(acc[0][4 * r4]), w2f[4 * r4], s0);
        s1 = __builtin_fmaf(gelu_tanh(acc[0][4 * r4 + 1]), w2f[4 * r4 + 1], s1);
        s0 = __builtin_fmaf(gelu_tanh(acc[0][4 * r4 + 2]), w2f[4 * r4 + 2], s0);
        s1 = __builtin_fmaf(gelu_tanh(acc[0][4 * r4 + 3]), w2f[4 * r4 + 3], s1);
    }
    s0 += s1;
    const auto sw = __builtin_amdgcn_permlane32_swap(__float_as_uint(s0), __float_as_uint(s0), false, false);
    const float part = __uint_as_float(sw[0]) + __uint_as_float(sw[1]);
    if (lane < 32) st_granule((gu64*)(P.ws + WS_PART) + ((g * 32 + 2 * ntile + kh) * 64 + 32 * mh + lane), ep + 1u, __float_as_uint(part));
}

constexpr unsigned LP_W2 = 12288;
constexpr unsigned LP_WO = 14336;
constexpr unsigned LP_DM = 15360;
constexpr unsigned LP_W3 = 16384;
static_assert(LP_T1 + 4 * 1026 <= LP_W2, "prep scratch");
constexpr unsigned w3_slot(int j) { return j * 1280 + (j & 1) * 64 + ((j >> 2) & 1) * 128; }
DI void dma16_add(unsigned lds_base, unsigned lds_off, unsigned voff, __amdgpu_buffer_rsrc_t srd, unsigned sbase, unsigned soff) {
    unsigned t;
    asm volatile("s_add_u32 m0, %1, %2\n\ts_add_u32 %0, %5, %6\n\tbuffer_load_dwordx4 %3, %4, %0 offen lds"
                 : "=&s"(t) : "s"(lds_base), "i"(lds_off), "v"(voff), "s"(srd), "s"(sbase), "i"(soff) : "memory", "scc");
}
DI void prep_start(const Params& P, lds8* lds, int w, int lane, int p) {
    const unsigned ldsb = (unsigned)(size_t)lds;
    const __amdgpu_buffer_rsrc_t srdC = __builtin_amdgcn_make_buffer_rsrc((void*)P.wcin, 0, 3 * 17576 * 2, RSRC3);
    const int i0 = w == 0 ? 0 : w == 1 ? 6 : w == 2 ? 14 : 20;
    dma4(ldsb + LP_WO + w * 256, lane * 4, __builtin_amdgcn_make_buffer_rsrc((void*)P.wout, 0, 78 * 2, RSRC3), 0);
    {
        const unsigned hp = lane >> 5;
        const unsigned vo = hp * 1352 + ((lane >> 2) & 7) * 52 + (((lane & 3) ^ (hp << 1)) << 4);
        const unsigned lb = ldsb + LP_W3 + w * 16640, sb = 52 * 1352 + 52 * i0;
        dma16_add(lb, w3_slot(0), vo, srdC, sb, 0 * 2704);  dma16_add(lb, w3_slot(1), vo, srdC, sb, 1 * 2704);
        dma16_add(lb, w3_slot(2), vo, srdC, sb, 2 * 2704);  dma16_add(lb, w3_slot(3), vo, srdC, sb, 3 * 2704);
        dma16_add(lb, w3_slot(4), vo, srdC, sb, 4 * 2704);  dma16_add(lb, w3_slot(5), vo, srdC, sb, 5 * 2704);
        dma16_add(lb, w3_slot(6), vo, srdC, sb, 6 * 2704);  dma16_add(lb, w3_slot(7), vo, srdC, sb, 7 * 2704);
        dma16_add(lb, w3_slot(8), vo, srdC, sb, 8 * 2704);  dma16_add(lb, w3_slot(9), vo, srdC, sb, 9 * 2704);
        dma16_add(lb, w3_slot(10), vo, srdC, sb, 10 * 2704); dma16_add(lb, w3_slot(11), vo, srdC, sb, 11 * 2704);
        dma16_add(lb, w3_slot(12), vo, srdC, sb, 12 * 2704);
    }
    if (__builtin_expect(w < 2, 1)) {
        const unsigned vo = w == 0 ? 35152 + (lane >> 2) * 1352 + p * 52 + (lane & 3) * 16 : p * 1352 + lane * 16;
        const unsigned lb = ldsb + (w == 0 ? LP_W2 : LP_T0);
        dma16(lb, vo, srdC, 0);
        dma16(lb + 1024, vo, srdC, w == 0 ? 16 * 1352 : 1024);
    }
}
DI void prep_role(const Params& P, lds8* lds, int w, int lane, int p, int slot, int g, bool updater) {
    const int n = lane & 15, gq = lane >> 4;
    const int i0 = w == 0 ? 0 : w == 1 ? 6 : w == 2 ? 14 : 20;
    const int nact = w == 0 ? 48 : 44;
    const int ch = lane < nact ? lane : 0;
    const int cs = ch >> 1, chh = ch & 1;
    const GAS u32x4* tp = (const GAS u32x4*)((const GAS unsigned char*)(size_t)__builtin_amdgcn_kernarg_segment_ptr() + sizeof(Params) + ((w == 0 ? 384 : 0) + (cs * 2 + chh) * 8) * 4);
    const u32x4 ta = tp[0], tb = tp[1];
    const int q = (lane & 15) >> 2, pp = lane & 3;
    int r0 = 8 * gq + q, r1 = r0 + 4;
    r0 = r0 < 26 ? r0 : 25; r1 = r1 < 26 ? r1 : 25;
    unsigned b0a, b0b, b1a, b1b;
    {
        const unsigned wb = LP_W3 + w * 16640 + pp * 8;
        const unsigned j0 = r0 >> 1, j1 = r1 >> 1;
        const unsigned s0 = wb + j0 * 1280 + (j0 & 1) * 64 + ((j0 >> 2) & 1) * 128 + (r0 & 1) * 512;
        const unsigned s1 = wb + j1 * 1280 + (j1 & 1) * 64 + ((j1 >> 2) & 1) * 128 + (r1 & 1) * 512;
        b0a = s0 + (r0 & 1) * 32; b0b = s0 + 32 - (r0 & 1) * 32; b1a = s1 + (r1 & 1) * 32; b1b = s1 + 32 - (r1 & 1) * 32;
    }
    unsigned va = LP_VT + n * 64 + 2 * i0, vb = n < 10 ? va + 1024 : LP_DM;
    unsigned vl = w == 3 ? LP_DM : 0;
    unsigned ac = LP_T1 + 4 * (4 * gq * 32 + n);
    unsigned a20 = LP_W2 + r0 * 64 + pp * 8, a21 = LP_W2 + r1 * 64 + pp * 8, abv = LP_VT + n * 64 + gq * 16;
    asm volatile("" : "+v"(b0a), "+v"(b0b), "+v"(b1a), "+v"(b1b), "+v"(va), "+v"(vb), "+v"(ac), "+v"(a20), "+v"(a21), "+v"(abv));
    if (w == 3) {
        LAS unsigned* z = (LAS unsigned*)(lds + LP_VT + (lane < 27 ? lane : 31) * 64 + 52);
        z[0] = 0; z[1] = 0; z[2] = 0;
        *(LAS unsigned*)(lds + LP_T1 + 4 * T_ZERO) = 0;
    }
    if (w == 1) *(LAS unsigned*)(lds + LP_T0 + 2 * T_ZERO) = 0;
    asm volatile("s_waitcnt vmcnt(0) lgkmcnt(0)" ::: "memory");
    __builtin_amdgcn_s_barrier();
    {
        const u32x4 wo3 = *(const LAS u32x4*)(lds + LP_WO + w * 256 + 104 + 16 * gq);
        s16x8 bfr[16];
#pragma unroll
        for (int t = 0; t < 16; ++t) {
            const int ii = t >> 1, u = t & 1;
            const s16x4 lo = __builtin_amdgcn_ds_read_tr16_b64_v4i16((LAS s16x4*)(lds + (u ? b0b : b0a) + ii * 64));
            const s16x4 hi = __builtin_amdgcn_ds_read_tr16_b64_v4i16((LAS s16x4*)(lds + (u ? b1b : b1a) + ii * 64));
            bfr[t] = __builtin_shufflevector(lo, hi, 0, 1, 2, 3, 4, 5, 6, 7);
        }
        if (w == 3) {
            const unsigned o2 = *(const LAS unsigned*)(lds + LP_WO + 768 + 52 + (lane < 13 ? lane : 0) * 4);
            const unsigned o1 = *(const LAS unsigned short*)(lds + LP_WO + 768 + p * 2);
            *(LAS unsigned*)(lds + (lane < 13 ? LP_VT + 26 * 64 + lane * 4 : LP_DM)) = o2;
            *(LAS float*)(lds + LP_T1 + 4 * T_CONST) = bf_lo(o1);
        }
        float res[16];
#pragma unroll
        for (int t = 0; t < 16; ++t) res[t] = mfma16(as_frag(wo3), bfr[t], (f32x4){0.f, 0.f, 0.f, 0.f})[0];
#pragma unroll
        for (int k2 = 0; k2 < 4; ++k2) {
            const unsigned pa0 = pk_bf16(res[4 * k2], res[4 * k2 + 2]), pb0 = pk_bf16(res[4 * k2 + 1], res[4 * k2 + 3]);
            if (k2 < 3) { *(LAS unsigned*)(lds + va + 4 * k2) = pa0; *(LAS unsigned*)(lds + vb + 4 * k2) = pb0; }
            else { *(LAS unsigned*)(lds + (vl ? vl : va + 12)) = pa0; *(LAS unsigned*)(lds + (vl ? vl : vb + 12)) = pb0; }
        }
    }
    CBAR();
    sig_add(lds, L_F + 28);
    if (w > 1) return;
    const __amdgpu_buffer_rsrc_t srdA = __builtin_amdgcn_make_buffer_rsrc((void*)(P.ws + WS_A), 0, A_BYTES, RSRC3);
    u32x4 o;
    if (w == 1) {
        float pv[16];
#pragma unroll
        for (int e = 0; e < 8; ++e) {
            const unsigned x = e < 4 ? ta[e] : tb[e - 4];
            pv[2 * e] = bf_lo(*(const LAS unsigned short*)(lds + LP_T0 + 2 * (x & 0xffff)));
            pv[2 * e + 1] = bf_lo(*(const LAS unsigned short*)(lds + LP_T0 + 2 * (x >> 16)));
        }
#pragma unroll
        for (int j = 0; j < 4; ++j) o[j] = pk_bf16(pv[4 * j] + pv[4 * j + 1], pv[4 * j + 2] + pv[4 * j + 3]);
    } else {
        s16x8 a2[2];
#pragma unroll
        for (int mt = 0; mt < 2; ++mt) {
            const s16x4 lo = __builtin_amdgcn_ds_read_tr16_b64_v4i16((LAS s16x4*)(lds + a20 + mt * 32));
            const s16x4 hi = __builtin_amdgcn_ds_read_tr16_b64_v4i16((LAS s16x4*)(lds + a21 + mt * 32));
            a2[mt] = __builtin_shufflevector(lo, hi, 0, 1, 2, 3, 4, 5, 6, 7);
        }
        unsigned pa[16];
#pragma unroll
        for (int e = 0; e < 8; ++e) {
            const unsigned x = e < 4 ? ta[e] : tb[e - 4];
            pa[2 * e] = LP_T1 + 4 * (x & 0xffff); pa[2 * e + 1] = LP_T1 + 4 * (x >> 16);
        }
#pragma unroll
        for (int e = 0; e < 16; ++e) asm volatile("" : "+v"(pa[e]));
        _Pragma("unroll 1") for (unsigned sp = 0; lds_ld(lds, L_F + 28) < 4 && sp < SPIN_CAP; ++sp) __builtin_amdgcn_s_sleep(0);
        CBAR();
        const u32x4 bf0 = *(const LAS u32x4*)(lds + abv), bf1 = *(const LAS u32x4*)(lds + abv + 1024);
        const f32x4 z4 = {0.f, 0.f, 0.f, 0.f};
        const f32x4 d00 = mfma16(a2[0], as_frag(bf0), z4), d01 = mfma16(a2[1], as_frag(bf0), z4);
        const f32x4 d10 = mfma16(a2[0], as_frag(bf1), z4), d11 = mfma16(a2[1], as_frag(bf1), z4);
#pragma unroll
        for (int e = 0; e < 4; ++e) {
            *(LAS float*)(lds + ac + e * 128) = d00[e]; *(LAS float*)(lds + ac + 2048 + e * 128) = d01[e];
            *(LAS float*)(lds + ac + 64 + e * 128) = d10[e]; *(LAS float*)(lds + ac + 2048 + 64 + e * 128) = d11[e];
        }
        float pv[16];
#pragma unroll
        for (int e = 0; e < 16; ++e) pv[e] = *(const LAS float*)(lds + pa[e]);
#pragma unroll
        for (int j = 0; j < 4; ++j) o[j] = pk_bf16(pv[4 * j] + pv[4 * j + 1], pv[4 * j + 2] + pv[4 * j + 3]);
    }
    {
        const int fr = w == 0 ? (cs < 22 ? 2 * cs : 22 + cs) : 2 * cs + 1;
        __builtin_amdgcn_raw_buffer_store_b128(o, srdA, lane < nact ? (unsigned)(fr * 832 + (26 * chh + p) * 16) : 0x7ffffff0u, 0, AUX_SC1);
    }
    WAIT_VM0();
    if (w == 1) { sig_set(lds, L_F + 24); return; }
    const unsigned tagv = *(const LAS unsigned*)(lds + L_EP + (lane & 15) * 4);
    _Pragma("unroll 1") for (unsigned sp = 0; lds_ld(lds, L_F + 24) == 0 && sp < SPIN_CAP; ++sp) __builtin_amdgcn_s_sleep(0);
    if (lane < 16) __hip_atomic_store((gu32*)(P.ws + WS_FL) + lane * 64 + slot, tagv + 1u, __ATOMIC_RELAXED, __HIP_MEMORY_SCOPE_AGENT);
    sig_set(lds, L_F + 60);
    if (updater) {
        const unsigned tag = *(const LAS unsigned*)(lds + L_EP + g * 4) + 1u;
        const gu32* fl = (const gu32*)(P.ws + WS_FL) + g * 64 + lane;
        const gu32* ar = (const gu32*)(P.ws + WS_PART) + ((g * 32 + 26) * 64 + (lane < 13 ? lane : 0)) * 2 + 1;
        _Pragma("unroll 1") for (unsigned sp = 0; sp < SPIN_CAP; ++sp) {
            const bool fok = (__ballot(ld_agent(fl) == tag) & 0x003fffff03ffffffull) == 0x003fffff03ffffffull;
            if (fok && __all(ld_agent(ar) == tag)) break;
            __builtin_amdgcn_s_sleep(2);
        }
        if (lane == 0) __hip_atomic_store((gu32*)(P.ws + WS_EP) + g, tag, __ATOMIC_RELAXED, __HIP_MEMORY_SCOPE_AGENT);
    }
}

DI bool rows_ready(bool match) {
    const unsigned long long mk = __ballot(match);
    return (((unsigned)mk | (unsigned)(mk >> 32)) & 0x3ffffffu) == 0x3ffffffu;
}
DI void cin_role(const Params& P, lds8* lds, int cw, int lane, int g, int m, bool has_dnn, int pf_slot) {
    const int d = lane & 31, h = lane >> 5;
    const int b = 64 * g + 4 * m + cw;
    const unsigned ldsb = (unsigned)(size_t)lds;
    {
        const __amdgpu_buffer_rsrc_t srdT = __builtin_amdgcn_make_buffer_rsrc((void*)P.table, 0, NVOC * 64, RSRC3);
        const unsigned lb = ldsb + L_X0 + cw * 1664;
        WAIT_VM0();
        const int q = lane >> 2;
        const unsigned j0 = *(const LAS unsigned*)(lds + L_X0 + cw * 1664 + q * 4);
        const unsigned j1 = *(const LAS unsigned*)(lds + L_X0 + cw * 1664 + (q < 10 ? q + 16 : 25) * 4);
        unsigned o0 = j0 * 64 + (lane & 3) * 16, o1 = j1 * 64 + (lane & 3) * 16;
        asm volatile("" : "+v"(o0), "+v"(o1));
        dma16(lb, o0, srdT, 0);
        if (lane < 40) dma16(lb + 1024, o1, srdT, 0);
        if (cw == 0) dma4_sc1(ldsb + L_FL, g * 256 + lane * 4, __builtin_amdgcn_make_buffer_rsrc((void*)(P.ws + WS_FL), 0, 4096, RSRC3), 0);
    }
    const unsigned bo = P.bout[0];
    __builtin_amdgcn_s_setprio(0);
    CBAR();
    __builtin_amdgcn_s_barrier();
    const unsigned epv = *(const LAS unsigned*)(lds + L_EP + g * 4);
    unsigned bov = bo;
    asm volatile("s_waitcnt vmcnt(0)" : "+v"(bov) :: "memory");
    if (cw == 1 && has_dnn && lane == 0) st_granule((gu64*)(P.ws + WS_PART) + ((g * 32 + 26) * 64 + m), epv + 1u, 0u);
    float x[26];
    {
        const lds8* xb = lds + L_X0 + cw * 1664 + h * 64 + d * 2;
        const lds8* xl = lds + L_X0 + cw * 1664 + (h ? 0 : 25 * 64) + d * 2;
#pragma unroll
        for (int a = 0; a < 13; ++a) {
            const unsigned lo = *(const LAS unsigned short*)(xb + 2 * a * 64);
            const unsigned hi = a < 12 ? *(const LAS unsigned short*)(xb + (2 * a + 1) * 64) : *(const LAS unsigned short*)xl;
            x[2 * a] = bf_lo(lo); x[2 * a + 1] = bf_lo(hi);
        }
    }
    u32x4 z[24];
#pragma unroll
    for (int s = 0; s < 22; ++s) {
        float fa[8], fb[8];
#pragma unroll
        for (int e = 0; e < 8; ++e) { const AB pe = slot_ab(8 * s + e); fa[e] = x[pe.a]; fb[e] = x[pe.b]; }
        float t0, t1, t2, t3, t4, t5;
        unsigned o0, o1, o2, o3;
        asm("v_mul_f32 %4, %10, %11\n\tv_mul_f32 %5, %12, %13\n\tv_mul_f32 %6, %14, %15\n\tv_mul_f32 %7, %16, %17\n\t"
            "v_cvt_pk_bf16_f32 %0, %4, %5\n\tv_mul_f32 %8, %18, %19\n\tv_mul_f32 %9, %20, %21\n\tv_cvt_pk_bf16_f32 %1, %6, %7\n\t"
            "v_mul_f32 %4, %22, %23\n\tv_mul_f32 %5, %24, %25\n\tv_cvt_pk_bf16_f32 %2, %8, %9\n\tv_cvt_pk_bf16_f32 %3, %4, %5"
            : "=&v"(o0), "=&v"(o1), "=&v"(o2), "=v"(o3), "=&v"(t0), "=&v"(t1), "=&v"(t2), "=&v"(t3), "=&v"(t4), "=&v"(t5)
            : "v"(fa[0]), "v"(fb[0]), "v"(fa[1]), "v"(fb[1]), "v"(fa[2]), "v"(fb[2]), "v"(fa[3]), "v"(fb[3]),
              "v"(fa[4]), "v"(fb[4]), "v"(fa[5]), "v"(fb[5]), "v"(fa[6]), "v"(fb[6]), "v"(fa[7]), "v"(fb[7]));
        z[s] = (u32x4){o0, o1, o2, o3};
    }
#pragma unroll
    for (int s = 22; s < 24; ++s) {
#pragma unroll
        for (int j = 0; j < 4; ++j) {
            const AB p0 = slot_ab(8 * s + 2 * j), p1 = slot_ab(8 * s + 2 * j + 1);
            const float a0 = p0.a < 26 ? x[p0.a] : (p0.a == 26 ? 1.f : 0.f);
            const float a1 = p1.a < 26 ? x[p1.a] : (p1.a == 26 ? 1.f : 0.f);
            z[s][j] = pk_bf16(a0, a1);
        }
    }
#pragma unroll
    for (int s = 0; s < 24; ++s) asm volatile("" : "+v"(z[s]));
    const unsigned tag = epv + 1u;
    {
        const __amdgpu_buffer_rsrc_t srdA = __builtin_amdgcn_make_buffer_rsrc((void*)(P.ws + WS_A), 0, A_BYTES, RSRC3);
        if (cw == 0) {
            if (has_dnn) _Pragma("unroll 1") for (unsigned sp = 0; lds_ld(lds, L_F + 52) < 4 && sp < SPIN_CAP; ++sp) __builtin_amdgcn_s_sleep(0);
            else _Pragma("unroll 1") for (unsigned sp = 0; lds_ld(lds, L_F + 60) == 0 && sp < SPIN_CAP; ++sp) __builtin_amdgcn_s_sleep(1);
            const gu32* fl = (const gu32*)(P.ws + WS_FL) + g * 64 + lane;
            if (!rows_ready(*(const LAS unsigned*)(lds + L_FL + lane * 4) == tag))
            _Pragma("unroll 1") for (unsigned sp = 0; sp < SPIN_CAP; ++sp) { if (rows_ready(ld_agent(fl) == tag)) break; __builtin_amdgcn_s_sleep(1); }
            sig_set(lds, L_F + 16);
            if (!has_dnn) dma4_sc1(ldsb + L_DUMMY, lane < 50 ? (unsigned)((((g & 1) * 3 + pf_slot) * 50 + lane) * 128) : 0x7fffff00u, srdA, 0);
        } else {
            _Pragma("unroll 1") for (unsigned sp = 0; lds_ld(lds, L_F + 16) == 0 && sp < SPIN_CAP; ++sp) __builtin_amdgcn_s_sleep(0);
        }
        CBAR();
        __builtin_amdgcn_s_setprio(2);
        {
            const unsigned l1 = ldsb + L_W + cw * 4096, vo = lane * 16;
            asm volatile("s_mov_b32 m0, %0\n\ts_nop 0\n\t"
                         "buffer_load_dwordx4 %1, %2, %3 offen sc1 lds\n\tbuffer_load_dwordx4 %1, %2, %3 offen offset:1024 sc1 lds\n\t"
                         "buffer_load_dwordx4 %1, %2, %3 offen offset:2048 sc1 lds\n\tbuffer_load_dwordx4 %1, %2, %3 offen offset:3072 sc1 lds"
                         :: "s"(l1), "v"(vo), "s"(srdA), "s"(cw * 4096) : "memory");
#pragma unroll
            for (int i = 0; i < 6; ++i) {
                const int q = 6 * cw + i;
                const unsigned go = q < 4 ? 16384 + 1024 * q : 19968 + 1024 * (q - 4), lo = q < 4 ? 16384 + 1024 * q : 53248 + 1024 * (q - 4);
                dma16_sc1(ldsb + L_W + lo, vo, srdA, go);
            }
        }
        wait_vm<6>();
        sig_add(lds, L_F + 20);
        _Pragma("unroll 1") for (unsigned sp = 0; lds_ld(lds, L_F + 20) < 4 && sp < SPIN_CAP; ++sp) __builtin_amdgcn_s_sleep(0);
        CBAR();
    }
    const gu64* pp = (const gu64*)(P.ws + WS_PART) + ((g * 32 + (lane < 26 ? lane : 0)) * 64 + 4 * m + cw);
    unsigned long long pv = 0;
    f32x16 acc0, acc1;
#pragma unroll
    for (int i = 0; i < 16; ++i) { acc0[i] = 0.f; acc1[i] = 0.f; }
    {
        unsigned abo = L_W + (26 * h + d) * 16;
        asm volatile("" : "+v"(abo));
        const lds8* ab = lds + abo;
        constexpr int PF = 6;
        u32x4 af[46];
        unsigned cnt2 = 0;
        auto frag_of = [](int t) { return t < 24 ? t : t + 40; };
#pragma unroll
        for (int t = 0; t < PF; ++t) af[t] = *(const LAS u32x4*)(ab + frag_of(t) * 832);
        __builtin_amdgcn_sched_barrier(0);
#pragma unroll
        for (int t = 0; t < 46; ++t) {
            if (t == 9) { wait_vm<0>(); sig_add(lds, L_F + 44); }
            if (t + PF == 19 - 2) cnt2 = lds_ld(lds, L_F + 44);
            if (t + PF == 19) {
                if (cnt2 < 4) _Pragma("unroll 1") for (unsigned sp = 0; lds_ld(lds, L_F + 44) < 4 && sp < SPIN_CAP; ++sp) __builtin_amdgcn_s_sleep(0);
                CBAR();
            }
            if (t + PF < 46) af[t + PF] = *(const LAS u32x4*)(ab + frag_of(t + PF) * 832);
            if (t < 44) {
                if (t & 1) acc0 = mfma32(as_frag(af[t]), as_frag(z[t >> 1]), acc0);
                else acc1 = mfma32(as_frag(af[t]), as_frag(z[t >> 1]), acc1);
            } else acc1 = mfma32(as_frag(af[t]), as_frag(z[t - 22]), acc1);
            if (t == 30) pv = __hip_atomic_load(pp, __ATOMIC_RELAXED, __HIP_MEMORY_SCOPE_AGENT);
            __builtin_amdgcn_sched_barrier(0);
        }
    }
    float sa = 0.f;
#pragma unroll
    for (int i = 0; i < 12; ++i) sa = __builtin_fmaf(acc0[i], acc1[i], sa);
    const float sb = __builtin_fmaf(acc0[12], acc1[12], acc0[13] * acc1[13]);
    float tot = h == 0 ? sa + sb : sa;
    asm volatile("" : "+v"(tot));
    _Pragma("unroll 1") for (unsigned sp = 0; sp < SPIN_CAP; ++sp) {
        if (__all((unsigned)(pv >> 32) == tag)) break;
        pv = __hip_atomic_load(pp, __ATOMIC_RELAXED, __HIP_MEMORY_SCOPE_AGENT);
    }
    if (lane < 26) tot += __uint_as_float((unsigned)pv);
    tot = wave_sum_to_lane63(tot);
    if (lane == 63) {
        const float o = tot + bf_lo(bov);
        P.out[b] = (unsigned short)(pk_bf16(o, o) & 0xffffu);
    }
}

DI void decode_block(int bid, int& g, int& m, int& ntile, int& k) {
    const int x = bid & 7, j = bid >> 3;
    g = 2 * x + (j >> 4); m = j & 15;
    ntile = m < 13 ? m : -1; k = m - 13;
}

__global__ void __launch_bounds__(512, 2) xdfm_kernel(KArgs KA) {
    const Params& P = KA.P;
    __shared__ __attribute__((aligned(16))) unsigned char smem[L_END];
    lds8* lds = (lds8*)smem;
    const int w = __builtin_amdgcn_readfirstlane(threadIdx.x >> 6), lane = threadIdx.x & 63;
    {
        unsigned long long pc; unsigned len;
        asm volatile("s_getpc_b64 %0\n.Lxdfm_pc:\n\ts_mov_b32 %1, (.Lxdfm_end-.Lxdfm_pc)&4294967295" : "=s"(pc), "=s"(len));
        const __amdgpu_buffer_rsrc_t srdP = __builtin_amdgcn_make_buffer_rsrc((void*)pc, 0, len, RSRC3);
        dma4((unsigned)(size_t)lds + L_DUMMY, lane == 0 ? ((blockIdx.x >> 3) * 8 + w) * 128 : 0x7fffff00u, srdP, 0);
    }
    asm volatile("" :: "s"(P.idx), "s"(P.table), "s"(P.wcin), "s"(P.wout), "s"(P.w1), "s"(P.b1), "s"(P.w2), "s"(P.bout), "s"(P.out), "s"(P.ws));
    int g, m, ntile, k;
    decode_block(blockIdx.x, g, m, ntile, k);
    const bool is_cin = w >= 4;
    if (__builtin_expect(is_cin, 1)) {
        __builtin_amdgcn_s_setprio(3);
        dma16((unsigned)(size_t)lds + L_X0 + (w - 4) * 1664, lane < 7 ? (64 * g + 4 * m + w - 4) * 104 + lane * 16 : 0x7fffff00u,
              __builtin_amdgcn_make_buffer_rsrc((void*)P.idx, 0, NBATCH * 26 * 4, RSRC3), 0);
        if (__builtin_expect(w == 7, 1)) dma4((unsigned)(size_t)lds + L_DUMMY, lane < 26 ? ((blockIdx.x >> 3) * 26 + lane) * 128 : 0x7fffff00u,
                         __builtin_amdgcn_make_buffer_rsrc((void*)P.wcin, 0, 3 * 17576 * 2, RSRC3), 0);
    }
    if (__builtin_expect(w == 0, 1)) {
        if (lane < 16) lds_st(lds, L_F + 4 * lane, 0);
        dma4_sc1((unsigned)(size_t)lds + L_EP, (lane & 15) * 4, __builtin_amdgcn_make_buffer_rsrc((void*)(P.ws + WS_EP), 0, 64, RSRC3), 0);
    }
    const int np = g * 3 + k;
    const bool second = (np >= 22 && np < 42) || np >= 46;
    const int p = second ? (np < 42 ? np - 22 : np - 26) : (np < 22 ? np : np - 20);
    if (__builtin_expect(!is_cin && ntile >= 0, 1)) dnn_start(P, lds, w, lane, g, ntile);
    if (__builtin_expect(!is_cin && ntile < 0, 1)) prep_start(P, lds, w, lane, p);
    if (__builtin_expect(!is_cin && ntile >= 0, 1)) {
        dnn_role(P, lds, w, lane, g, ntile);
    } else if (is_cin) {
        cin_role(P, lds, w - 4, lane, g, m, ntile >= 0, k);
        __builtin_amdgcn_s_waitcnt(0x0F70);
    } else {
        prep_role(P, lds, w, lane, p, second ? 32 + p : p, g, k == 0);
        __builtin_amdgcn_s_waitcnt(0x0F70);
    }
    asm volatile(".Lxdfm_end:");
}

extern "C" void kernel_launch(void* const* d_in, const int* in_sizes, int n_in,
                              void* d_out, int out_size, void* d_ws, size_t ws_size,
                              hipStream_t stream) {
    KArgs KA;
    KA.tab = make_tab();
    Params& P = KA.P;
    P.idx = (const int*)d_in[0]; P.table = (const unsigned short*)d_in[1]; P.wcin = (const unsigned short*)d_in[2];
    P.wout = (const unsigned short*)d_in[3]; P.w1 = (const unsigned short*)d_in[4]; P.b1 = (const unsigned short*)d_in[5];
    P.w2 = (const unsigned short*)d_in[6]; P.bout = (const unsigned short*)d_in[7];
    P.out = (unsigned short*)d_out; P.ws = (unsigned char*)d_ws;
    xdfm_kernel<<<256, 512, 0, stream>>>(KA);
}
```

```cpp
#define AMP_BLK 43
#define AMP_W 4
#define AMP_J 3
#define AMP_K 5
#include <hip/hip_runtime.h>
#include <stdint.h>

#define DI __device__ __forceinline__
#define GAS __attribute__((address_space(1)))
#define LAS __attribute__((address_space(3)))

typedef short    s16x4 __attribute__((ext_vector_type(4)));
typedef short    s16x8 __attribute__((ext_vector_type(8)));
typedef float    f32x2 __attribute__((ext_vector_type(2)));
typedef float    f32x4 __attribute__((ext_vector_type(4)));
typedef float    f32x16 __attribute__((ext_vector_type(16)));
typedef unsigned u32x2 __attribute__((ext_vector_type(2)));
typedef unsigned u32x4 __attribute__((ext_vector_type(4)));
typedef __bf16   bf16x2 __attribute__((ext_vector_type(2)));
typedef GAS unsigned gu32;
typedef GAS unsigned long long gu64;
typedef LAS unsigned char lds8;

#define MAPPING 1

constexpr int DIN = 832, NBATCH = 1024, NVOC = 100000;
#define A_AFTER_X 1
#define W_EARLY 2
#define A_PF 1
#define CODE_PF 1
#define WC_PF 1
#define POLL_STEP 10
#define CIN_PRIO 2
#define WO_LAST 0
#define EARLY_FL 1
#define GATE_STEP 10
#define PKMUL 0
#define A_DMA 1
constexpr unsigned RSRC3 = 0x00020000u;
constexpr int AUX_SC1 = 16;
constexpr unsigned SPIN_CAP = 1u << 20;

constexpr unsigned WS_EP = 0;
constexpr unsigned WS_FL = 4096;
constexpr unsigned WS_PART = 8192;
constexpr unsigned WS_A = 8192 + 16 * 32 * 64 * 8;
constexpr unsigned A_BYTES = 46 * 832;

#define XD 6
constexpr unsigned L_W = 0;
constexpr unsigned L_A = 832 * 128;
constexpr unsigned L_X0 = L_A + 4 * XD * 2048;
constexpr unsigned L_F = L_X0 + 4 * 1664;
constexpr unsigned L_DUMMY = L_F + 256;
constexpr unsigned L_EP = L_DUMMY + 256;
constexpr unsigned L_BI = L_EP + 256;
constexpr unsigned L_W2 = L_BI + 256;
constexpr unsigned L_FL = L_W2 + 256;
constexpr unsigned L_END = L_FL + 256;
static_assert(4 * XD * 2048 >= A_BYTES && L_END <= 163840, "LDS map");
constexpr unsigned LP_VT = 0;
constexpr unsigned LP_T0 = 2048;
constexpr unsigned LP_T1 = 2048 + 2304;

struct AB { int a, b; };
constexpr AB slot_ab(int t) {
    if (t < 156) { int a = 2 * (t / 12); return {a, (a + t % 12) % 26}; }
    if (t < 170) { int u = t - 156; int a = 2 * (u / 2); return {a, (a + 12 + (u & 1)) % 26}; }
    if (t < 176) { int a = 2 * (7 + t - 170); return {a, (a + 12) % 26}; }
    if (t < 189) return {2 * (t - 176), 26};
    if (t == 189) return {26, 26};
    return {27, 27};
}
constexpr unsigned T_ZERO = 1024, T_CONST = 1025;
struct PrepTab { unsigned v[2][24 * 2 * 8]; };
constexpr PrepTab make_tab() {
    PrepTab tb{};
    for (int m = 0; m < 2; ++m) for (int s = 0; s < 24; ++s) for (int h = 0; h < 2; ++h) for (int e = 0; e < 8; ++e) {
        const int st = m ? 32 : 26;
        int t = 8 * s + e; AB ab = slot_ab(t);
        unsigned o1 = T_ZERO, o2 = T_ZERO;
        if (ab.a == 27) {}
        else if (ab.a == 26) { if (h == 0 && m) o1 = T_CONST; }
        else if (ab.b == 26) { if (m) o1 = ((ab.a + h) % 26) * st + 26; }
        else {
            int u = (ab.a + h) % 26, v = (ab.b + h) % 26;
            bool dup = (t == 169 && h == 1);
            if (dup) {}
            else if (u == v) o1 = u * st + u;
            else { o1 = u * st + v; o2 = v * st + u; }
        }
        tb.v[m][(s * 2 + h) * 8 + e] = o1 | (o2 << 16);
    }
    return tb;
}
static_assert(slot_ab(169).a == 12 && slot_ab(169).b == 25, "dup slot");

struct Params {
    const int* idx; const unsigned short* table; const unsigned short* wcin; const unsigned short* wout;
    const unsigned short* w1; const unsigned short* b1; const unsigned short* w2; const unsigned short* bout;
    unsigned short* out; unsigned char* ws;
};
struct KArgs { Params P; PrepTab tab; };

DI unsigned sel(bool c, unsigned a, unsigned b) { unsigned r = c ? a : b; return r; }
DI float bf_lo(unsigned v) { return __uint_as_float(v << 16); }
DI float bf_hi(unsigned v) { return __uint_as_float(v & 0xffff0000u); }
DI unsigned pk_bf16(float lo, float hi) { f32x2 v = {lo, hi}; return __builtin_bit_cast(unsigned, __builtin_convertvector(v, bf16x2)); }
DI f32x4 mfma16(s16x8 a, s16x8 b, f32x4 c) { return __builtin_amdgcn_mfma_f32_16x16x32_bf16(a, b, c, 0, 0, 0); }
DI f32x16 mfma32(s16x8 a, s16x8 b, f32x16 c) { return __builtin_amdgcn_mfma_f32_32x32x16_bf16(a, b, c, 0, 0, 0); }
DI s16x8 as_frag(u32x4 v) { return __builtin_bit_cast(s16x8, v); }
template <int CTRL, int ROWMASK> DI float dpp_add(float v) {
    return v + __int_as_float(__builtin_amdgcn_update_dpp(0, __float_as_int(v), CTRL, ROWMASK, 0xf, true));
}
DI float wave_sum_to_lane63(float v) {
    v = dpp_add<0x111, 0xf>(v); v = dpp_add<0x112, 0xf>(v); v = dpp_add<0x114, 0xf>(v); v = dpp_add<0x118, 0xf>(v);
    v = dpp_add<0x142, 0xa>(v); v = dpp_add<0x143, 0xc>(v);
    return v;
}
DI float sum_lane_groups(float v) {
    auto a = __builtin_amdgcn_permlane16_swap(__float_as_uint(v), __float_as_uint(v), false, false);
    v = __uint_as_float(a[0]) + __uint_as_float(a[1]);
    auto b = __builtin_amdgcn_permlane32_swap(__float_as_uint(v), __float_as_uint(v), false, false);
    return __uint_as_float(b[0]) + __uint_as_float(b[1]);
}
DI float gelu_tanh(float x) {
    const float k1 = -2.0f * 0.7978845608028654f * 1.4426950408889634f;
    const float k2 = k1 * 0.044715f;
    float arg = x * __builtin_fmaf(x * x, k2, k1);
    float e = __builtin_amdgcn_exp2f(arg);
    return x * __builtin_amdgcn_rcpf(1.0f + e);
}
DI void dma16(unsigned lds_addr, unsigned voff, __amdgpu_buffer_rsrc_t srd, unsigned soff) {
    asm volatile("s_mov_b32 m0, %0\n\ts_nop 0\n\tbuffer_load_dwordx4 %1, %2, %3 offen lds"
                 :: "s"(lds_addr), "v"(voff), "s"(srd), "s"(soff) : "memory");
}
DI void dma16_sc1(unsigned lds_addr, unsigned voff, __amdgpu_buffer_rsrc_t srd, unsigned soff) {
    asm volatile("s_mov_b32 m0, %0\n\ts_nop 0\n\tbuffer_load_dwordx4 %1, %2, %3 offen sc1 lds"
                 :: "s"(lds_addr), "v"(voff), "s"(srd), "s"(soff) : "memory");
}
DI void dma4_sc1(unsigned lds_addr, unsigned voff, __amdgpu_buffer_rsrc_t srd, unsigned soff) {
    asm volatile("s_mov_b32 m0, %0\n\ts_nop 0\n\tbuffer_load_dword %1, %2, %3 offen sc1 lds"
                 :: "s"(lds_addr), "v"(voff), "s"(srd), "s"(soff) : "memory");
}
DI void dma4(unsigned lds_addr, unsigned voff, __amdgpu_buffer_rsrc_t srd, unsigned soff) {
    asm volatile("s_mov_b32 m0, %0\n\ts_nop 0\n\tbuffer_load_dword %1, %2, %3 offen lds"
                 :: "s"(lds_addr), "v"(voff), "s"(srd), "s"(soff) : "memory");
}
DI void st_granule(gu64* g, unsigned tag, unsigned val) {
    __hip_atomic_store(g, ((unsigned long long)tag << 32) | val, __ATOMIC_RELAXED, __HIP_MEMORY_SCOPE_AGENT);
}
DI unsigned ld_agent(const gu32* p) { return __hip_atomic_load(p, __ATOMIC_RELAXED, __HIP_MEMORY_SCOPE_AGENT); }
DI unsigned lds_ld(lds8* l, unsigned off) { return __hip_atomic_load((LAS unsigned*)(l + off), __ATOMIC_RELAXED, __HIP_MEMORY_SCOPE_WORKGROUP); }
DI void lds_st(lds8* l, unsigned off, unsigned v) { __hip_atomic_store((LAS unsigned*)(l + off), v, __ATOMIC_RELAXED, __HIP_MEMORY_SCOPE_WORKGROUP); }
DI void lds_add(lds8* l, unsigned off, unsigned v) { __hip_atomic_fetch_add((LAS unsigned*)(l + off), v, __ATOMIC_RELAXED, __HIP_MEMORY_SCOPE_WORKGROUP); }
DI void sig_add(lds8* l, unsigned off) {
    asm volatile("s_mov_b64 exec, 1\n\tds_add_u32 %0, %1\n\ts_mov_b64 exec, -1" :: "v"((unsigned)(size_t)l + off), "v"(1u) : "memory");
}
DI void sig_set(lds8* l, unsigned off) {
    asm volatile("s_mov_b64 exec, 1\n\tds_write_b32 %0, %1\n\ts_mov_b64 exec, -1" :: "v"((unsigned)(size_t)l + off), "v"(1u) : "memory");
}
#define WAIT_LGKM() asm volatile("s_waitcnt lgkmcnt(0)" ::: "memory")
#define WAIT_VM0() asm volatile("s_waitcnt vmcnt(0)" ::: "memory")
#define CBAR() asm volatile("" ::: "memory")
struct Stamps { unsigned long long t0, t1; };
#define STAMP(i) do { if ((i) == 0) { asm volatile("s_memrealtime %0\n\ts_waitcnt lgkmcnt(0)" : "=s"(st.t0) :: "memory"); } else if ((i) == AMP_J) { asm volatile("s_memrealtime %0\n\ts_waitcnt lgkmcnt(0)" : "=s"(st.t1) :: "memory"); } } while (0)
#define STAMP_ARG , Stamps& st
#define STAMP_PASS , st
#define STAMP_FLUSH(ws, w, lane) do { if (blockIdx.x == AMP_BLK && (w) == AMP_W) { const unsigned long long d_ = (st.t1 - st.t0) * AMP_K; const unsigned long long e_ = __builtin_amdgcn_s_memrealtime(); while (__builtin_amdgcn_s_memrealtime() - e_ < d_) __builtin_amdgcn_s_sleep(4); } } while (0)

template <int K> DI unsigned quad_bcast(unsigned v) { return __builtin_amdgcn_update_dpp(0u, v, K * 0x55, 0xf, 0xf, true); }
template <int N> DI void wait_vm() { asm volatile("s_waitcnt vmcnt(%0)" :: "i"(N) : "memory"); }
DI void dma_w4(unsigned lds_addr, unsigned voff, __amdgpu_buffer_rsrc_t srd, unsigned soff) {
    unsigned t;
    asm volatile("s_mov_b32 m0, %1\n\ts_add_u32 %0, %4, 12288\n\tbuffer_load_dwordx4 %2, %3, %4 offen lds\n\t"
                 "buffer_load_dwordx4 %2, %3, %0 offen offset:1024 lds\n\ts_add_u32 %0, %0, 12288\n\t"
                 "buffer_load_dwordx4 %2, %3, %0 offen offset:2048 lds\n\ts_add_u32 %0, %0, 12288\n\t"
                 "buffer_load_dwordx4 %2, %3, %0 offen offset:3072 lds"
                 : "=&s"(t) : "s"(lds_addr), "v"(voff), "s"(srd), "s"(soff) : "memory", "scc");
}
DI void dnn_start(const Params& P, lds8* lds, int w, int lane, int g, int ntile) {
    const int mh = w & 1, kh = w >> 1;
    const unsigned ldsb = (unsigned)(size_t)lds;
    const unsigned ring = ldsb + L_A + w * (XD * 2048);
    const __amdgpu_buffer_rsrc_t srdI = __builtin_amdgcn_make_buffer_rsrc((void*)P.idx, 0, NBATCH * 26 * 4, RSRC3);
    const unsigned vo = ((64 * g + 32 * mh + (lane >> 2)) * 26 + 13 * kh + 4 * (lane & 3)) * 4;
    dma16(ring, vo, srdI, 0);
    dma16(ring + 1024, vo, srdI, 1664);
    if (__builtin_expect(w == 1, 1)) dma4(ldsb + L_BI, ntile * 128 + lane * 4, __builtin_amdgcn_make_buffer_rsrc((void*)P.b1, 0, DIN * 2, RSRC3), 0);
    if (__builtin_expect(w == 2, 1)) dma4(ldsb + L_W2, ntile * 128 + lane * 4, __builtin_amdgcn_make_buffer_rsrc((void*)P.w2, 0, DIN * 2, RSRC3), 0);
    const __amdgpu_buffer_rsrc_t srdW = __builtin_amdgcn_make_buffer_rsrc((void*)P.w1, 0, DIN * DIN * 2, RSRC3);
    const unsigned vw = (lane >> 3) * 1664 + (((lane & 7) ^ (((lane >> 4) & 1) << 2)) << 4);
    const unsigned rowA = 416 * kh + 208 * mh + 104 * (g & 1);
    dma_w4(ldsb + L_W + rowA * 128, vw, srdW, rowA * 1664 + ntile * 128);
    if (W_EARLY > 1) dma_w4(ldsb + L_W + rowA * 128 + 4096, vw, srdW, rowA * 1664 + ntile * 128 + 4 * 13312);
    if (W_EARLY > 2) dma_w4(ldsb + L_W + rowA * 128 + 8192, vw, srdW, rowA * 1664 + ntile * 128 + 8 * 13312);
}
DI void dnn_role(const Params& P, lds8* lds, int w, int lane, int g, int ntile STAMP_ARG) {
    const int mh = w & 1, kh = w >> 1;
    const int h = lane >> 5;
    const unsigned ldsb = (unsigned)(size_t)lds;
    const unsigned ring = ldsb + L_A + w * (XD * 2048);
    const __amdgpu_buffer_rsrc_t srdW = __builtin_amdgcn_make_buffer_rsrc((void*)P.w1, 0, DIN * DIN * 2, RSRC3);
    const unsigned vw = (lane >> 3) * 1664 + (((lane & 7) ^ (((lane >> 4) & 1) << 2)) << 4);
    const unsigned rowA = 416 * kh + 208 * mh + 104 * (g & 1), rowB = 416 * kh + 208 * mh + 104 * ((g & 1) ^ 1);
    auto wdma = [&](unsigned row, int a) {
        const unsigned sb = row * 1664 + ntile * 128, lb = ldsb + L_W + row * 128;
        if (a < 3) dma_w4(lb + a * 4096, vw, srdW, sb + a * 4 * 13312);
        else dma16(lb + 12288, vw, srdW, sb + 12 * 13312);
    };
    STAMP(1);
    wait_vm<4 * W_EARLY>();
    WAIT_LGKM();
    __builtin_amdgcn_s_barrier();
    STAMP(2);
    const u32x4 i0 = *(const LAS u32x4*)(lds + L_A + w * (XD * 2048) + lane * 16);
    const u32x4 i1 = *(const LAS u32x4*)(lds + L_A + w * (XD * 2048) + 1024 + lane * 16);
    const unsigned ep = *(const LAS unsigned*)(lds + L_EP + g * 4);
#pragma unroll
    for (int a = W_EARLY; a < 4; ++a) wdma(rowA, a);
#pragma unroll
    for (int a = 0; a < 4; ++a) wdma(rowB, a);
    const __amdgpu_buffer_rsrc_t srdT = __builtin_amdgcn_make_buffer_rsrc((void*)P.table, 0, NVOC * 64, RSRC3);
    const unsigned c16 = (((lane & 3) ^ ((lane >> 4) & 3)) << 4);
    u32x4 t0 = i0 << 6, t1 = i1 << 6;
    asm volatile("" : "+v"(t0), "+v"(t1));
    auto xload = [&](int s) {
        const unsigned a0 = t0[s & 3], a1 = t1[s & 3];
        const unsigned o0 = (s < 4 ? quad_bcast<0>(a0) : s < 8 ? quad_bcast<1>(a0) : s < 12 ? quad_bcast<2>(a0) : quad_bcast<3>(a0)) + c16;
        const unsigned o1 = (s < 4 ? quad_bcast<0>(a1) : s < 8 ? quad_bcast<1>(a1) : s < 12 ? quad_bcast<2>(a1) : quad_bcast<3>(a1)) + c16;
        dma16(ring + (s % XD) * 2048, o0, srdT, 0);
        dma16(ring + (s % XD) * 2048 + 1024, o1, srdT, 0);
    };
#pragma unroll
    for (int s = 0; s < XD; ++s) xload(s);
    STAMP(3);
    wait_vm<2 * XD>();
    sig_add(lds, L_F + 32 + 4 * kh);
    _Pragma("unroll 1") for (unsigned sp = 0; lds_ld(lds, L_F + 32 + 4 * kh) < 2 && sp < SPIN_CAP; ++sp) __builtin_amdgcn_s_sleep(0);
    CBAR();
    STAMP(4);
    f32x16 acc[2];
    u32x2 bia[4], wv[4];
    float w2f[16];
    unsigned xdone = 0;
#pragma unroll
    for (int r4 = 0; r4 < 4; ++r4) {
        bia[r4] = *(const LAS u32x2*)(lds + L_BI + kh * 64 + h * 8 + r4 * 16);
        wv[r4] = *(const LAS u32x2*)(lds + L_W2 + kh * 64 + h * 8 + r4 * 16);
    }
#pragma unroll
    for (int r4 = 0; r4 < 4; ++r4) {
        acc[0][4 * r4] = bf_lo(bia[r4][0]); acc[0][4 * r4 + 1] = bf_hi(bia[r4][0]); acc[0][4 * r4 + 2] = bf_lo(bia[r4][1]); acc[0][4 * r4 + 3] = bf_hi(bia[r4][1]);
        acc[1][4 * r4] = 0.f; acc[1][4 * r4 + 1] = 0.f; acc[1][4 * r4 + 2] = 0.f; acc[1][4 * r4 + 3] = 0.f;
    }
    {
        const int blk = (lane >> 4) & 1, q = (lane & 15) >> 2, pp = lane & 3, n = lane & 31;
        unsigned ad[2];
#pragma unroll
        for (int j = 0; j < 2; ++j) {
            const unsigned c = 4 * (j ^ kh) + 2 * blk + (pp >> 1);
            ad[j] = L_W + (416 * kh + 8 * h + q) * 128 + ((c ^ (((q >> 1) & 1) << 2)) << 4) + (pp & 1) * 8;
        }
        const unsigned xb = L_A + w * (XD * 2048) + n * 64;
        const unsigned xa0 = xb + ((h ^ ((n >> 2) & 3)) << 4), xa1 = xb + (((2 + h) ^ ((n >> 2) & 3)) << 4);
        u32x4 xf[3][2];
        s16x8 wf[2][4];
        auto xread = [&](int s) {
            xf[s % 3][0] = *(const LAS u32x4*)(lds + xa0 + (s % XD) * 2048);
            xf[s % 3][1] = *(const LAS u32x4*)(lds + xa1 + (s % XD) * 2048);
        };
        auto wread = [&](int s) {
#pragma unroll
            for (int a = 0; a < 2; ++a)
#pragma unroll
                for (int j = 0; j < 2; ++j) {
                    const s16x4 lo = __builtin_amdgcn_ds_read_tr16_b64_v4i16((LAS s16x4*)(lds + ad[j] + s * 4096 + a * 2048));
                    const s16x4 hi = __builtin_amdgcn_ds_read_tr16_b64_v4i16((LAS s16x4*)(lds + ad[j] + s * 4096 + a * 2048 + 512));
                    wf[s & 1][2 * a + j] = __builtin_shufflevector(lo, hi, 0, 1, 2, 3, 4, 5, 6, 7);
                }
        };
        wread(0);
        wait_vm<2 * (XD - 1)>();
        xread(0);
        wait_vm<2 * (XD - 2)>();
        xread(1);
        __builtin_amdgcn_sched_barrier(0);
#pragma unroll
        for (int s = 0; s < 13; ++s) {
            if (s + 1 < 13) wread(s + 1);
#pragma unroll
            for (int a = 0; a < 2; ++a)
#pragma unroll
                for (int j = 0; j < 2; ++j) acc[j] = mfma32(wf[s & 1][2 * a + j], as_frag(xf[s % 3][a]), acc[j]);
            if (s + 1 < 13) {
#pragma unroll
                for (int j = 0; j < 4; ++j) { __builtin_amdgcn_sched_group_barrier(0x100, 2, 0); __builtin_amdgcn_sched_group_barrier(0x8, 1, 0); }
            }
            __builtin_amdgcn_sched_barrier(0);
            if (s + XD < 13) xload(s + XD);
            if (s == 6) STAMP(20);
            if (s == 11) {
                xdone = lds_ld(lds, L_F + 52);
#pragma unroll
                for (int r4 = 0; r4 < 4; ++r4) { w2f[4 * r4] = bf_lo(wv[r4][0]); w2f[4 * r4 + 1] = bf_hi(wv[r4][0]); w2f[4 * r4 + 2] = bf_lo(wv[r4][1]); w2f[4 * r4 + 3] = bf_hi(wv[r4][1]); }
#pragma unroll
                for (int e = 0; e < 16; ++e) asm volatile("" : "+v"(w2f[e]));
            }
            if (s + 2 < 13) {
                constexpr int LASTI = 12;
                const int last = s + XD < LASTI ? s + XD : LASTI;
                const int younger = last - (s + 2);
                if (younger >= 4) wait_vm<8>(); else if (younger == 3) wait_vm<6>(); else if (younger == 2) wait_vm<4>(); else if (younger == 1) wait_vm<2>(); else wait_vm<0>();
                if (s + 2 == 12) STAMP(21);
                if (s + 2 == 12) sig_add(lds, L_F + 52);
                if (s == GATE_STEP) sig_add(lds, L_F + 56);
                xread(s + 2);
            }
            __builtin_amdgcn_sched_barrier(0);
        }
    }
    STAMP(6);
    {
        if (xdone < 4) _Pragma("unroll 1") for (unsigned sp = 0; lds_ld(lds, L_F + 52) < 4 && sp < SPIN_CAP; ++sp) __builtin_amdgcn_s_sleep(0);
        CBAR();
        lds8* xw = lds + L_W + kh * 53248 + (5 + mh) * 4096 + lane * 16;
#pragma unroll
        for (int r4 = 0; r4 < 4; ++r4) *(LAS f32x4*)(xw + r4 * 1024) = (f32x4){acc[1][4 * r4], acc[1][4 * r4 + 1], acc[1][4 * r4 + 2], acc[1][4 * r4 + 3]};
        CBAR();
        sig_set(lds, L_F + 4 * w);
        _Pragma("unroll 1") for (unsigned sp = 0; lds_ld(lds, L_F + 4 * (w ^ 2)) == 0 && sp < SPIN_CAP; ++sp) __builtin_amdgcn_s_sleep(0);
        CBAR();
        const lds8* xr = lds + L_W + (kh ^ 1) * 53248 + (5 + mh) * 4096 + lane * 16;
        f32x4 o[4];
#pragma unroll
        for (int r4 = 0; r4 < 4; ++r4) o[r4] = *(const LAS f32x4*)(xr + r4 * 1024);
        asm volatile("" : "+v"(o[0]), "+v"(o[1]), "+v"(o[2]), "+v"(o[3]));
#pragma unroll
        for (int r4 = 0; r4 < 4; ++r4) {
            acc[0][4 * r4] += o[r4][0]; acc[0][4 * r4 + 1] += o[r4][1]; acc[0][4 * r4 + 2] += o[r4][2]; acc[0][4 * r4 + 3] += o[r4][3];
        }
    }
    STAMP(7);
    float s0 = 0.f, s1 = 0.f;
#pragma unroll
    for (int r4 = 0; r4 < 4; ++r4) {
        s0 = __builtin_fmaf(gelu_tanh(acc[0][4 * r4]), w2f[4 * r4], s0);
        s1 = __builtin_fmaf(gelu_tanh(acc[0][4 * r4 + 1]), w2f[4 * r4 + 1], s1);
        s0 = __builtin_fmaf(gelu_tanh(acc[0][4 * r4 + 2]), w2f[4 * r4 + 2], s0);
        s1 = __builtin_fmaf(gelu_tanh(acc[0][4 * r4 + 3]), w2f[4 * r4 + 3], s1);
    }
    s0 += s1;
    const auto sw = __builtin_amdgcn_permlane32_swap(__float_as_uint(s0), __float_as_uint(s0), false, false);
    const float part = __uint_as_float(sw[0]) + __uint_as_float(sw[1]);
    STAMP(8);
    if (lane < 32) st_granule((gu64*)(P.ws + WS_PART) + ((g * 32 + 2 * ntile + kh) * 64 + 32 * mh + lane), ep + 1u, __float_as_uint(part));
}

constexpr unsigned LP_W2 = 12288;
constexpr unsigned LP_WO = 14336;
constexpr unsigned LP_DM = 15360;
constexpr unsigned LP_W3 = 16384;
static_assert(LP_T1 + 4 * 1026 <= LP_W2, "prep scratch");
constexpr unsigned w3_slot(int j) { return j * 1280 + (j & 1) * 64 + ((j >> 2) & 1) * 128; }
DI void dma16_add(unsigned lds_base, unsigned lds_off, unsigned voff, __amdgpu_buffer_rsrc_t srd, unsigned sbase, unsigned soff) {
    unsigned t;
    asm volatile("s_add_u32 m0, %1, %2\n\ts_add_u32 %0, %5, %6\n\tbuffer_load_dwordx4 %3, %4, %0 offen lds"
                 : "=&s"(t) : "s"(lds_base), "i"(lds_off), "v"(voff), "s"(srd), "s"(sbase), "i"(soff) : "memory", "scc");
}
DI void prep_start(const Params& P, lds8* lds, int w, int lane, int p) {
    const unsigned ldsb = (unsigned)(size_t)lds;
    const __amdgpu_buffer_rsrc_t srdC = __builtin_amdgcn_make_buffer_rsrc((void*)P.wcin, 0, 3 * 17576 * 2, RSRC3);
    const int i0 = w == 0 ? 0 : w == 1 ? 6 : w == 2 ? 14 : 20;
    dma4(ldsb + LP_WO + w * 256, lane * 4, __builtin_amdgcn_make_buffer_rsrc((void*)P.wout, 0, 78 * 2, RSRC3), 0);
    {
        const unsigned hp = lane >> 5;
        const unsigned vo = hp * 1352 + ((lane >> 2) & 7) * 52 + (((lane & 3) ^ (hp << 1)) << 4);
        const unsigned lb = ldsb + LP_W3 + w * 16640, sb = 52 * 1352 + 52 * i0;
        dma16_add(lb, w3_slot(0), vo, srdC, sb, 0 * 2704);  dma16_add(lb, w3_slot(1), vo, srdC, sb, 1 * 2704);
        dma16_add(lb, w3_slot(2), vo, srdC, sb, 2 * 2704);  dma16_add(lb, w3_slot(3), vo, srdC, sb, 3 * 2704);
        dma16_add(lb, w3_slot(4), vo, srdC, sb, 4 * 2704);  dma16_add(lb, w3_slot(5), vo, srdC, sb, 5 * 2704);
        dma16_add(lb, w3_slot(6), vo, srdC, sb, 6 * 2704);  dma16_add(lb, w3_slot(7), vo, srdC, sb, 7 * 2704);
        dma16_add(lb, w3_slot(8), vo, srdC, sb, 8 * 2704);  dma16_add(lb, w3_slot(9), vo, srdC, sb, 9 * 2704);
        dma16_add(lb, w3_slot(10), vo, srdC, sb, 10 * 2704); dma16_add(lb, w3_slot(11), vo, srdC, sb, 11 * 2704);
        dma16_add(lb, w3_slot(12), vo, srdC, sb, 12 * 2704);
    }
    if (__builtin_expect(w < 2, 1)) {
        const unsigned vo = w == 0 ? 35152 + (lane >> 2) * 1352 + p * 52 + (lane & 3) * 16 : p * 1352 + lane * 16;
        const unsigned lb = ldsb + (w == 0 ? LP_W2 : LP_T0);
        dma16(lb, vo, srdC, 0);
        dma16(lb + 1024, vo, srdC, w == 0 ? 16 * 1352 : 1024);
    }
}
DI void prep_role(const Params& P, lds8* lds, int w, int lane, int p, int slot STAMP_ARG) {
    const int n = lane & 15, gq = lane >> 4;
    const int i0 = w == 0 ? 0 : w == 1 ? 6 : w == 2 ? 14 : 20;
    const int nact = w == 0 ? 48 : 44;
    const int ch = lane < nact ? lane : 0;
    const int cs = ch >> 1, chh = ch & 1;
    const GAS u32x4* tp = (const GAS u32x4*)((const GAS unsigned char*)(size_t)__builtin_amdgcn_kernarg_segment_ptr() + sizeof(Params) + ((w == 0 ? 384 : 0) + (cs * 2 + chh) * 8) * 4);
    const u32x4 ta = tp[0], tb = tp[1];
    STAMP(1);
    const int q = (lane & 15) >> 2, pp = lane & 3;
    int r0 = 8 * gq + q, r1 = r0 + 4;
    r0 = r0 < 26 ? r0 : 25; r1 = r1 < 26 ? r1 : 25;
    unsigned b0a, b0b, b1a, b1b;
    {
        const unsigned wb = LP_W3 + w * 16640 + pp * 8;
        const unsigned j0 = r0 >> 1, j1 = r1 >> 1;
        const unsigned s0 = wb + j0 * 1280 + (j0 & 1) * 64 + ((j0 >> 2) & 1) * 128 + (r0 & 1) * 512;
        const unsigned s1 = wb + j1 * 1280 + (j1 & 1) * 64 + ((j1 >> 2) & 1) * 128 + (r1 & 1) * 512;
        b0a = s0 + (r0 & 1) * 32; b0b = s0 + 32 - (r0 & 1) * 32; b1a = s1 + (r1 & 1) * 32; b1b = s1 + 32 - (r1 & 1) * 32;
    }
    unsigned va = LP_VT + n * 64 + 2 * i0, vb = n < 10 ? va + 1024 : LP_DM;
    unsigned vl = w == 3 ? LP_DM : 0;
    unsigned ac = LP_T1 + 4 * (4 * gq * 32 + n);
    unsigned a20 = LP_W2 + r0 * 64 + pp * 8, a21 = LP_W2 + r1 * 64 + pp * 8, abv = LP_VT + n * 64 + gq * 16;
    asm volatile("" : "+v"(b0a), "+v"(b0b), "+v"(b1a), "+v"(b1b), "+v"(va), "+v"(vb), "+v"(ac), "+v"(a20), "+v"(a21), "+v"(abv));
    if (w == 3) {
        LAS unsigned* z = (LAS unsigned*)(lds + LP_VT + (lane < 27 ? lane : 31) * 64 + 52);
        z[0] = 0; z[1] = 0; z[2] = 0;
        *(LAS unsigned*)(lds + LP_T1 + 4 * T_ZERO) = 0;
    }
    if (w == 1) *(LAS unsigned*)(lds + LP_T0 + 2 * T_ZERO) = 0;
    asm volatile("s_waitcnt vmcnt(0) lgkmcnt(0)" ::: "memory");
    __builtin_amdgcn_s_barrier();
    STAMP(2);
    {
        const u32x4 wo3 = *(const LAS u32x4*)(lds + LP_WO + w * 256 + 104 + 16 * gq);
        s16x8 bfr[16];
#pragma unroll
        for (int t = 0; t < 16; ++t) {
            const int ii = t >> 1, u = t & 1;
            const s16x4 lo = __builtin_amdgcn_ds_read_tr16_b64_v4i16((LAS s16x4*)(lds + (u ? b0b : b0a) + ii * 64));
            const s16x4 hi = __builtin_amdgcn_ds_read_tr16_b64_v4i16((LAS s16x4*)(lds + (u ? b1b : b1a) + ii * 64));
            bfr[t] = __builtin_shufflevector(lo, hi, 0, 1, 2, 3, 4, 5, 6, 7);
        }
        if (w == 3) {
            const unsigned o2 = *(const LAS unsigned*)(lds + LP_WO + 768 + 52 + (lane < 13 ? lane : 0) * 4);
            const unsigned o1 = *(const LAS unsigned short*)(lds + LP_WO + 768 + p * 2);
            *(LAS unsigned*)(lds + (lane < 13 ? LP_VT + 26 * 64 + lane * 4 : LP_DM)) = o2;
            *(LAS float*)(lds + LP_T1 + 4 * T_CONST) = bf_lo(o1);
        }
        float res[16];
#pragma unroll
        for (int t = 0; t < 16; ++t) res[t] = mfma16(as_frag(wo3), bfr[t], (f32x4){0.f, 0.f, 0.f, 0.f})[0];
#pragma unroll
        for (int k2 = 0; k2 < 4; ++k2) {
            const unsigned pa0 = pk_bf16(res[4 * k2], res[4 * k2 + 2]), pb0 = pk_bf16(res[4 * k2 + 1], res[4 * k2 + 3]);
            if (k2 < 3) { *(LAS unsigned*)(lds + va + 4 * k2) = pa0; *(LAS unsigned*)(lds + vb + 4 * k2) = pb0; }
            else { *(LAS unsigned*)(lds + (vl ? vl : va + 12)) = pa0; *(LAS unsigned*)(lds + (vl ? vl : vb + 12)) = pb0; }
        }
    }
    CBAR();
    sig_add(lds, L_F + 28);
    if (w > 1) return;
    STAMP(3);
    const __amdgpu_buffer_rsrc_t srdA = __builtin_amdgcn_make_buffer_rsrc((void*)(P.ws + WS_A), 0, A_BYTES, RSRC3);
    u32x4 o;
    if (w == 1) {
        float pv[16];
#pragma unroll
        for (int e = 0; e < 8; ++e) {
            const unsigned x = e < 4 ? ta[e] : tb[e - 4];
            pv[2 * e] = bf_lo(*(const LAS unsigned short*)(lds + LP_T0 + 2 * (x & 0xffff)));
            pv[2 * e + 1] = bf_lo(*(const LAS unsigned short*)(lds + LP_T0 + 2 * (x >> 16)));
        }
#pragma unroll
        for (int j = 0; j < 4; ++j) o[j] = pk_bf16(pv[4 * j] + pv[4 * j + 1], pv[4 * j + 2] + pv[4 * j + 3]);
    } else {
        s16x8 a2[2];
#pragma unroll
        for (int mt = 0; mt < 2; ++mt) {
            const s16x4 lo = __builtin_amdgcn_ds_read_tr16_b64_v4i16((LAS s16x4*)(lds + a20 + mt * 32));
            const s16x4 hi = __builtin_amdgcn_ds_read_tr16_b64_v4i16((LAS s16x4*)(lds + a21 + mt * 32));
            a2[mt] = __builtin_shufflevector(lo, hi, 0, 1, 2, 3, 4, 5, 6, 7);
        }
        unsigned pa[16];
#pragma unroll
        for (int e = 0; e < 8; ++e) {
            const unsigned x = e < 4 ? ta[e] : tb[e - 4];
            pa[2 * e] = LP_T1 + 4 * (x & 0xffff); pa[2 * e + 1] = LP_T1 + 4 * (x >> 16);
        }
#pragma unroll
        for (int e = 0; e < 16; ++e) asm volatile("" : "+v"(pa[e]));
        _Pragma("unroll 1") for (unsigned sp = 0; lds_ld(lds, L_F + 28) < 4 && sp < SPIN_CAP; ++sp) __builtin_amdgcn_s_sleep(0);
        CBAR();
        const u32x4 bf0 = *(const LAS u32x4*)(lds + abv), bf1 = *(const LAS u32x4*)(lds + abv + 1024);
        const f32x4 z4 = {0.f, 0.f, 0.f, 0.f};
        const f32x4 d00 = mfma16(a2[0], as_frag(bf0), z4), d01 = mfma16(a2[1], as_frag(bf0), z4);
        const f32x4 d10 = mfma16(a2[0], as_frag(bf1), z4), d11 = mfma16(a2[1], as_frag(bf1), z4);
#pragma unroll
        for (int e = 0; e < 4; ++e) {
            *(LAS float*)(lds + ac + e * 128) = d00[e]; *(LAS float*)(lds + ac + 2048 + e * 128) = d01[e];
            *(LAS float*)(lds + ac + 64 + e * 128) = d10[e]; *(LAS float*)(lds + ac + 2048 + 64 + e * 128) = d11[e];
        }
        STAMP(4);
        float pv[16];
#pragma unroll
        for (int e = 0; e < 16; ++e) pv[e] = *(const LAS float*)(lds + pa[e]);
#pragma unroll
        for (int j = 0; j < 4; ++j) o[j] = pk_bf16(pv[4 * j] + pv[4 * j + 1], pv[4 * j + 2] + pv[4 * j + 3]);
    }
    {
        const int fr = w == 0 ? (cs < 22 ? 2 * cs : 22 + cs) : 2 * cs + 1;
        __builtin_amdgcn_raw_buffer_store_b128(o, srdA, lane < nact ? (unsigned)(fr * 832 + (26 * chh + p) * 16) : 0x7ffffff0u, 0, AUX_SC1);
    }
    STAMP(5);
    WAIT_VM0();
    STAMP(6);
    if (w == 1) { sig_set(lds, L_F + 24); return; }
    const unsigned tagv = *(const LAS unsigned*)(lds + L_EP + (lane & 15) * 4);
    _Pragma("unroll 1") for (unsigned sp = 0; lds_ld(lds, L_F + 24) == 0 && sp < SPIN_CAP; ++sp) __builtin_amdgcn_s_sleep(0);
    if (lane < 16) __hip_atomic_store((gu32*)(P.ws + WS_FL) + lane * 64 + slot, tagv + 1u, __ATOMIC_RELAXED, __HIP_MEMORY_SCOPE_AGENT);
    sig_set(lds, L_F + 60);
}

DI bool rows_ready(bool match) {
    const unsigned long long mk = __ballot(match);
    return (((unsigned)mk | (unsigned)(mk >> 32)) & 0x3ffffffu) == 0x3ffffffu;
}
constexpr unsigned A_PER = A_BYTES / 4;
DI void cin_role(const Params& P, lds8* lds, int cw, int lane, int g, int m, bool has_dnn, int pf_slot STAMP_ARG) {
    const int d = lane & 31, h = lane >> 5;
    const int b = 64 * g + 4 * m + cw;
    const unsigned ldsb = (unsigned)(size_t)lds;
    {
        const __amdgpu_buffer_rsrc_t srdT = __builtin_amdgcn_make_buffer_rsrc((void*)P.table, 0, NVOC * 64, RSRC3);
        const unsigned lb = ldsb + L_X0 + cw * 1664;
        WAIT_VM0();
        const int q = lane >> 2;
        const unsigned j0 = *(const LAS unsigned*)(lds + L_X0 + cw * 1664 + q * 4);
        const unsigned j1 = *(const LAS unsigned*)(lds + L_X0 + cw * 1664 + (q < 10 ? q + 16 : 25) * 4);
        unsigned o0 = j0 * 64 + (lane & 3) * 16, o1 = j1 * 64 + (lane & 3) * 16;
        asm volatile("" : "+v"(o0), "+v"(o1));
        STAMP(1);
        dma16(lb, o0, srdT, 0);
        if (lane < 40) dma16(lb + 1024, o1, srdT, 0);
        if (cw == 0) dma4_sc1(ldsb + L_FL, g * 256 + lane * 4, __builtin_amdgcn_make_buffer_rsrc((void*)(P.ws + WS_FL), 0, 4096, RSRC3), 0);
    }
    const unsigned bo = P.bout[0];
    STAMP(2);
    __builtin_amdgcn_s_setprio(0);
    CBAR();
    __builtin_amdgcn_s_barrier();
    const unsigned epv = *(const LAS unsigned*)(lds + L_EP + g * 4);
    unsigned bov = bo;
    asm volatile("s_waitcnt vmcnt(0)" : "+v"(bov) :: "memory");
    STAMP(3);
    float x[26];
    {
        const lds8* xb = lds + L_X0 + cw * 1664 + h * 64 + d * 2;
        const lds8* xl = lds + L_X0 + cw * 1664 + (h ? 0 : 25 * 64) + d * 2;
#pragma unroll
        for (int a = 0; a < 13; ++a) {
            const unsigned lo = *(const LAS unsigned short*)(xb + 2 * a * 64);
            const unsigned hi = a < 12 ? *(const LAS unsigned short*)(xb + (2 * a + 1) * 64) : *(const LAS unsigned short*)xl;
            x[2 * a] = bf_lo(lo); x[2 * a + 1] = bf_lo(hi);
        }
    }
    u32x4 z[24];
#pragma unroll
    for (int s = 0; s < 22; ++s) {
        float fa[8], fb[8];
#pragma unroll
        for (int e = 0; e < 8; ++e) { const AB pe = slot_ab(8 * s + e); fa[e] = x[pe.a]; fb[e] = x[pe.b]; }
        float t0, t1, t2, t3, t4, t5;
        unsigned o0, o1, o2, o3;
        asm("v_mul_f32 %4, %10, %11\n\tv_mul_f32 %5, %12, %13\n\tv_mul_f32 %6, %14, %15\n\tv_mul_f32 %7, %16, %17\n\t"
            "v_cvt_pk_bf16_f32 %0, %4, %5\n\tv_mul_f32 %8, %18, %19\n\tv_mul_f32 %9, %20, %21\n\tv_cvt_pk_bf16_f32 %1, %6, %7\n\t"
            "v_mul_f32 %4, %22, %23\n\tv_mul_f32 %5, %24, %25\n\tv_cvt_pk_bf16_f32 %2, %8, %9\n\tv_cvt_pk_bf16_f32 %3, %4, %5"
            : "=&v"(o0), "=&v"(o1), "=&v"(o2), "=v"(o3), "=&v"(t0), "=&v"(t1), "=&v"(t2), "=&v"(t3), "=&v"(t4), "=&v"(t5)
            : "v"(fa[0]), "v"(fb[0]), "v"(fa[1]), "v"(fb[1]), "v"(fa[2]), "v"(fb[2]), "v"(fa[3]), "v"(fb[3]),
              "v"(fa[4]), "v"(fb[4]), "v"(fa[5]), "v"(fb[5]), "v"(fa[6]), "v"(fb[6]), "v"(fa[7]), "v"(fb[7]));
        z[s] = (u32x4){o0, o1, o2, o3};
    }
#pragma unroll
    for (int s = 22; s < 24; ++s) {
#pragma unroll
        for (int j = 0; j < 4; ++j) {
            const AB p0 = slot_ab(8 * s + 2 * j), p1 = slot_ab(8 * s + 2 * j + 1);
            const float a0 = p0.a < 26 ? x[p0.a] : (p0.a == 26 ? 1.f : 0.f);
            const float a1 = p1.a < 26 ? x[p1.a] : (p1.a == 26 ? 1.f : 0.f);
            z[s][j] = pk_bf16(a0, a1);
        }
    }
#pragma unroll
    for (int s = 0; s < 24; ++s) asm volatile("" : "+v"(z[s]));
    STAMP(4);
    const unsigned tag = epv + 1u;
    {
        const __amdgpu_buffer_rsrc_t srdA = __builtin_amdgcn_make_buffer_rsrc((void*)(P.ws + WS_A), 0, A_BYTES, RSRC3);
        if (cw == 0) {
            if (has_dnn) _Pragma("unroll 1") for (unsigned sp = 0; lds_ld(lds, L_F + 56) < 4 && sp < SPIN_CAP; ++sp) __builtin_amdgcn_s_sleep(0);
            else _Pragma("unroll 1") for (unsigned sp = 0; lds_ld(lds, L_F + 60) == 0 && sp < SPIN_CAP; ++sp) __builtin_amdgcn_s_sleep(1);
            const gu32* fl = (const gu32*)(P.ws + WS_FL) + g * 64 + lane;
            if (!rows_ready(*(const LAS unsigned*)(lds + L_FL + lane * 4) == tag))
            _Pragma("unroll 1") for (unsigned sp = 0; sp < SPIN_CAP; ++sp) { if (rows_ready(ld_agent(fl) == tag)) break; __builtin_amdgcn_s_sleep(1); }
            sig_set(lds, L_F + 16);
            if (!has_dnn) dma4_sc1(ldsb + L_DUMMY, lane < 50 ? (unsigned)((((g & 1) * 3 + pf_slot) * 50 + lane) * 128) : 0x7fffff00u, srdA, 0);
        } else {
            _Pragma("unroll 1") for (unsigned sp = 0; lds_ld(lds, L_F + 16) == 0 && sp < SPIN_CAP; ++sp) __builtin_amdgcn_s_sleep(0);
        }
        CBAR();
        __builtin_amdgcn_s_setprio(CIN_PRIO);
        STAMP(5);
        {
            const unsigned l1 = ldsb + L_W + cw * 4096, vo = lane * 16;
            asm volatile("s_mov_b32 m0, %0\n\ts_nop 0\n\t"
                         "buffer_load_dwordx4 %1, %2, %3 offen sc1 lds\n\tbuffer_load_dwordx4 %1, %2, %3 offen offset:1024 sc1 lds\n\t"
                         "buffer_load_dwordx4 %1, %2, %3 offen offset:2048 sc1 lds\n\tbuffer_load_dwordx4 %1, %2, %3 offen offset:3072 sc1 lds"
                         :: "s"(l1), "v"(vo), "s"(srdA), "s"(cw * 4096) : "memory");
#pragma unroll
            for (int i = 0; i < 6; ++i) {
                const int q = 6 * cw + i;
                const unsigned go = q < 4 ? 16384 + 1024 * q : 19968 + 1024 * (q - 4), lo = q < 4 ? 16384 + 1024 * q : 53248 + 1024 * (q - 4);
                dma16_sc1(ldsb + L_W + lo, vo, srdA, go);
            }
        }
        wait_vm<6>();
        sig_add(lds, L_F + 20);
        _Pragma("unroll 1") for (unsigned sp = 0; lds_ld(lds, L_F + 20) < 4 && sp < SPIN_CAP; ++sp) __builtin_amdgcn_s_sleep(0);
        CBAR();
    }
    STAMP(6);
    const gu64* pp = (const gu64*)(P.ws + WS_PART) + ((g * 32 + (lane < 26 ? lane : 0)) * 64 + 4 * m + cw);
    unsigned long long pv = 0;
    f32x16 acc0, acc1;
#pragma unroll
    for (int i = 0; i < 16; ++i) { acc0[i] = 0.f; acc1[i] = 0.f; }
    {
        unsigned abo = L_W + (26 * h + d) * 16;
        asm volatile("" : "+v"(abo));
        const lds8* ab = lds + abo;
        constexpr int PF = 6;
        u32x4 af[46];
        unsigned cnt2 = 0;
        auto frag_of = [](int t) { return t < 24 ? t : t + 40; };
#pragma unroll
        for (int t = 0; t < PF; ++t) af[t] = *(const LAS u32x4*)(ab + frag_of(t) * 832);
        __builtin_amdgcn_sched_barrier(0);
#pragma unroll
        for (int t = 0; t < 46; ++t) {
            if (t == 9) { wait_vm<0>(); sig_add(lds, L_F + 44); }
            if (t + PF == 19 - 2) cnt2 = lds_ld(lds, L_F + 44);
            if (t + PF == 19) {
                if (cnt2 < 4) _Pragma("unroll 1") for (unsigned sp = 0; lds_ld(lds, L_F + 44) < 4 && sp < SPIN_CAP; ++sp) __builtin_amdgcn_s_sleep(0);
                CBAR();
            }
            if (t + PF < 46) af[t + PF] = *(const LAS u32x4*)(ab + frag_of(t + PF) * 832);
            if (t < 44) {
                if (t & 1) acc0 = mfma32(as_frag(af[t]), as_frag(z[t >> 1]), acc0);
                else acc1 = mfma32(as_frag(af[t]), as_frag(z[t >> 1]), acc1);
            } else acc1 = mfma32(as_frag(af[t]), as_frag(z[t - 22]), acc1);
            if (t == 30) pv = __hip_atomic_load(pp, __ATOMIC_RELAXED, __HIP_MEMORY_SCOPE_AGENT);
            __builtin_amdgcn_sched_barrier(0);
        }
    }
    float sa = 0.f;
#pragma unroll
    for (int i = 0; i < 12; ++i) sa = __builtin_fmaf(acc0[i], acc1[i], sa);
    const float sb = __builtin_fmaf(acc0[12], acc1[12], acc0[13] * acc1[13]);
    float tot = h == 0 ? sa + sb : sa;
    asm volatile("" : "+v"(tot));
    STAMP(7);
    _Pragma("unroll 1") for (unsigned sp = 0; sp < SPIN_CAP; ++sp) {
        if (__all((unsigned)(pv >> 32) == tag)) break;
        pv = __hip_atomic_load(pp, __ATOMIC_RELAXED, __HIP_MEMORY_SCOPE_AGENT);
    }
    STAMP(8);
    if (lane < 26) tot += __uint_as_float((unsigned)pv);
    tot = wave_sum_to_lane63(tot);
    if (lane == 63) {
        const float o = tot + bf_lo(bov);
        P.out[b] = (unsigned short)(pk_bf16(o, o) & 0xffffu);
    }
    if (m == 0 && cw == 0) {
        const gu32* fl = (const gu32*)(P.ws + WS_FL) + g * 64 + lane;
        _Pragma("unroll 1") for (unsigned sp = 0; sp < SPIN_CAP; ++sp) {
            if ((__ballot(ld_agent(fl) == tag) & 0x003fffff03ffffffull) == 0x003fffff03ffffffull) break;
            __builtin_amdgcn_s_sleep(1);
        }
        if (lane == 0) __hip_atomic_store((gu32*)(P.ws + WS_EP) + g, tag, __ATOMIC_RELAXED, __HIP_MEMORY_SCOPE_AGENT);
    }
}

DI void decode_block(int bid, int& g, int& m, int& ntile, int& k) {
    const int x = bid & 7, j = bid >> 3;
    g = 2 * x + (j >> 4); m = j & 15;
    ntile = m < 13 ? m : -1; k = m - 13;
}

__global__ void __launch_bounds__(512, 2) xdfm_kernel(KArgs KA) {
    const Params& P = KA.P;
    __shared__ __attribute__((aligned(16))) unsigned char smem[L_END];
    lds8* lds = (lds8*)smem;
    Stamps st; st.t0 = 0; st.t1 = 0;
    STAMP(0);
    const int w = __builtin_amdgcn_readfirstlane(threadIdx.x >> 6), lane = threadIdx.x & 63;
    {
        unsigned long long pc; unsigned len;
        asm volatile("s_getpc_b64 %0\n.Lxdfm_pc:\n\ts_mov_b32 %1, (.Lxdfm_end-.Lxdfm_pc)&4294967295" : "=s"(pc), "=s"(len));
        const __amdgpu_buffer_rsrc_t srdP = __builtin_amdgcn_make_buffer_rsrc((void*)pc, 0, len, RSRC3);
        dma4((unsigned)(size_t)lds + L_DUMMY, lane == 0 ? ((blockIdx.x >> 3) * 8 + w) * 128 : 0x7fffff00u, srdP, 0);
    }
    asm volatile("" :: "s"(P.idx), "s"(P.table), "s"(P.wcin), "s"(P.wout), "s"(P.w1), "s"(P.b1), "s"(P.w2), "s"(P.bout), "s"(P.out), "s"(P.ws));
    STAMP(9);
    int g, m, ntile, k;
    decode_block(blockIdx.x, g, m, ntile, k);
    const bool is_cin = w >= 4;
    if (__builtin_expect(is_cin, 1)) {
        __builtin_amdgcn_s_setprio(3);
        dma16((unsigned)(size_t)lds + L_X0 + (w - 4) * 1664, lane < 7 ? (64 * g + 4 * m + w - 4) * 104 + lane * 16 : 0x7fffff00u,
              __builtin_amdgcn_make_buffer_rsrc((void*)P.idx, 0, NBATCH * 26 * 4, RSRC3), 0);
        if (__builtin_expect(w == 7, 1)) dma4((unsigned)(size_t)lds + L_DUMMY, lane < 26 ? ((blockIdx.x >> 3) * 26 + lane) * 128 : 0x7fffff00u,
                         __builtin_amdgcn_make_buffer_rsrc((void*)P.wcin, 0, 3 * 17576 * 2, RSRC3), 0);
    }
    if (__builtin_expect(w == 0, 1)) {
        if (lane < 16) lds_st(lds, L_F + 4 * lane, 0);
        dma4_sc1((unsigned)(size_t)lds + L_EP, (lane & 15) * 4, __builtin_amdgcn_make_buffer_rsrc((void*)(P.ws + WS_EP), 0, 64, RSRC3), 0);
    }
    const int np = g * 3 + k;
    const bool second = (np >= 22 && np < 42) || np >= 46;
    const int p = second ? (np < 42 ? np - 22 : np - 26) : (np < 22 ? np : np - 20);
    if (__builtin_expect(!is_cin && ntile >= 0, 1)) dnn_start(P, lds, w, lane, g, ntile);
    if (__builtin_expect(!is_cin && ntile < 0, 1)) prep_start(P, lds, w, lane, p);
    STAMP(10);
    if (__builtin_expect(!is_cin && ntile >= 0, 1)) {
        dnn_role(P, lds, w, lane, g, ntile STAMP_PASS);
    } else if (is_cin) {
        cin_role(P, lds, w - 4, lane, g, m, ntile >= 0, k STAMP_PASS);
        __builtin_amdgcn_s_waitcnt(0x0F70);
    } else {
        prep_role(P, lds, w, lane, p, second ? 32 + p : p STAMP_PASS);
        __builtin_amdgcn_s_waitcnt(0x0F70);
    }
    STAMP(11);
    STAMP_FLUSH(P.ws, w, lane);
    asm volatile(".Lxdfm_end:");
}

extern "C" void kernel_launch(void* const* d_in, const int* in_sizes, int n_in,
                              void* d_out, int out_size, void* d_ws, size_t ws_size,
                              hipStream_t stream) {
    KArgs KA;
    KA.tab = make_tab();
    Params& P = KA.P;
    P.idx = (const int*)d_in[0]; P.table = (const unsigned short*)d_in[1]; P.wcin = (const unsigned short*)d_in[2];
    P.wout = (const unsigned short*)d_in[3]; P.w1 = (const unsigned short*)d_in[4]; P.b1 = (const unsigned short*)d_in[5];
    P.w2 = (const unsigned short*)d_in[6]; P.bout = (const unsigned short*)d_in[7];
    P.out = (unsigned short*)d_out; P.ws = (unsigned char*)d_ws;
    xdfm_kernel<<<256, 512, 0, stream>>>(KA);
}
```

```cpp
#include <hip/hip_runtime.h>
#include <stdint.h>

#define DI __device__ __forceinline__
#define GAS __attribute__((address_space(1)))
#define LAS __attribute__((address_space(3)))

typedef short    s16x4 __attribute__((ext_vector_type(4)));
typedef short    s16x8 __attribute__((ext_vector_type(8)));
typedef float    f32x2 __attribute__((ext_vector_type(2)));
typedef float    f32x4 __attribute__((ext_vector_type(4)));
typedef float    f32x16 __attribute__((ext_vector_type(16)));
typedef unsigned u32x2 __attribute__((ext_vector_type(2)));
typedef unsigned u32x4 __attribute__((ext_vector_type(4)));
typedef __bf16   bf16x2 __attribute__((ext_vector_type(2)));
typedef GAS unsigned gu32;
typedef GAS unsigned long long gu64;
typedef LAS unsigned char lds8;

constexpr int DIN = 832, NBATCH = 1024, NVOC = 100000;
#define PAD_PREP 1
constexpr int XD = 6;
constexpr int W_EARLY = 2;
constexpr unsigned RSRC3 = 0x00020000u;
constexpr int AUX_SC1 = 16;
constexpr unsigned SPIN_CAP = 1u << 20;

constexpr unsigned WS_EP = 0;
constexpr unsigned WS_FL = 4096;
constexpr unsigned WS_PART = 8192;
constexpr unsigned WS_A = 8192 + 16 * 32 * 64 * 8;
constexpr unsigned A_BYTES = 46 * 832;

constexpr unsigned L_W = 0;
constexpr unsigned L_A = 832 * 128;
constexpr unsigned L_X0 = L_A + 4 * XD * 2048;
constexpr unsigned L_F = L_X0 + 4 * 1664;
constexpr unsigned L_DUMMY = L_F + 256;
constexpr unsigned L_EP = L_DUMMY + 256;
constexpr unsigned L_BI = L_EP + 256;
constexpr unsigned L_W2 = L_BI + 256;
constexpr unsigned L_FL = L_W2 + 256;
constexpr unsigned L_END = L_FL + 256;
static_assert(L_END <= 163840, "LDS map");
constexpr unsigned LP_VT = 0;
constexpr unsigned LP_T0 = 2048;
constexpr unsigned LP_T1 = 2048 + 2304;

struct AB { int a, b; };
constexpr AB slot_ab(int t) {
    if (t < 156) { int a = 2 * (t / 12); return {a, (a + t % 12) % 26}; }
    if (t < 170) { int u = t - 156; int a = 2 * (u / 2); return {a, (a + 12 + (u & 1)) % 26}; }
    if (t < 176) { int a = 2 * (7 + t - 170); return {a, (a + 12) % 26}; }
    if (t < 189) return {2 * (t - 176), 26};
    if (t == 189) return {26, 26};
    return {27, 27};
}
constexpr unsigned T_ZERO = 1024, T_CONST = 1025;
struct PrepTab { unsigned v[2][24 * 2 * 8]; };
constexpr PrepTab make_tab() {
    PrepTab tb{};
    for (int m = 0; m < 2; ++m) for (int s = 0; s < 24; ++s) for (int h = 0; h < 2; ++h) for (int e = 0; e < 8; ++e) {
        const int st = m ? 32 : 26;
        int t = 8 * s + e; AB ab = slot_ab(t);
        unsigned o1 = T_ZERO, o2 = T_ZERO;
        if (ab.a == 27) {}
        else if (ab.a == 26) { if (h == 0 && m) o1 = T_CONST; }
        else if (ab.b == 26) { if (m) o1 = ((ab.a + h) % 26) * st + 26; }
        else {
            int u = (ab.a + h) % 26, v = (ab.b + h) % 26;
            bool dup = (t == 169 && h == 1);
            if (dup) {}
            else if (u == v) o1 = u * st + u;
            else { o1 = u * st + v; o2 = v * st + u; }
        }
        tb.v[m][(s * 2 + h) * 8 + e] = o1 | (o2 << 16);
    }
    return tb;
}
static_assert(slot_ab(169).a == 12 && slot_ab(169).b == 25, "dup slot");

struct Params {
    const int* idx; const unsigned short* table; const unsigned short* wcin; const unsigned short* wout;
    const unsigned short* w1; const unsigned short* b1; const unsigned short* w2; const unsigned short* bout;
    unsigned short* out; unsigned char* ws;
};
struct KArgs { Params P; PrepTab tab; };

DI float bf_lo(unsigned v) { return __uint_as_float(v << 16); }
DI float bf_hi(unsigned v) { return __uint_as_float(v & 0xffff0000u); }
DI unsigned pk_bf16(float lo, float hi) { f32x2 v = {lo, hi}; return __builtin_bit_cast(unsigned, __builtin_convertvector(v, bf16x2)); }
DI f32x4 mfma16(s16x8 a, s16x8 b, f32x4 c) { return __builtin_amdgcn_mfma_f32_16x16x32_bf16(a, b, c, 0, 0, 0); }
DI f32x16 mfma32(s16x8 a, s16x8 b, f32x16 c) { return __builtin_amdgcn_mfma_f32_32x32x16_bf16(a, b, c, 0, 0, 0); }
DI s16x8 as_frag(u32x4 v) { return __builtin_bit_cast(s16x8, v); }
template <int CTRL, int ROWMASK> DI float dpp_add(float v) {
    return v + __int_as_float(__builtin_amdgcn_update_dpp(0, __float_as_int(v), CTRL, ROWMASK, 0xf, true));
}
DI float wave_sum_to_lane63(float v) {
    v = dpp_add<0x111, 0xf>(v); v = dpp_add<0x112, 0xf>(v); v = dpp_add<0x114, 0xf>(v); v = dpp_add<0x118, 0xf>(v);
    v = dpp_add<0x142, 0xa>(v); v = dpp_add<0x143, 0xc>(v);
    return v;
}
DI float gelu_tanh(float x) {
    const float k1 = -2.0f * 0.7978845608028654f * 1.4426950408889634f;
    const float k2 = k1 * 0.044715f;
    float arg = x * __builtin_fmaf(x * x, k2, k1);
    float e = __builtin_amdgcn_exp2f(arg);
    return x * __builtin_amdgcn_rcpf(1.0f + e);
}
DI void dma16(unsigned lds_addr, unsigned voff, __amdgpu_buffer_rsrc_t srd, unsigned soff) {
    asm volatile("s_mov_b32 m0, %0\n\ts_nop 0\n\tbuffer_load_dwordx4 %1, %2, %3 offen lds"
                 :: "s"(lds_addr), "v"(voff), "s"(srd), "s"(soff) : "memory");
}
DI void dma16_sc1(unsigned lds_addr, unsigned voff, __amdgpu_buffer_rsrc_t srd, unsigned soff) {
    asm volatile("s_mov_b32 m0, %0\n\ts_nop 0\n\tbuffer_load_dwordx4 %1, %2, %3 offen sc1 lds"
                 :: "s"(lds_addr), "v"(voff), "s"(srd), "s"(soff) : "memory");
}
DI void dma4_sc1(unsigned lds_addr, unsigned voff, __amdgpu_buffer_rsrc_t srd, unsigned soff) {
    asm volatile("s_mov_b32 m0, %0\n\ts_nop 0\n\tbuffer_load_dword %1, %2, %3 offen sc1 lds"
                 :: "s"(lds_addr), "v"(voff), "s"(srd), "s"(soff) : "memory");
}
DI void dma4(unsigned lds_addr, unsigned voff, __amdgpu_buffer_rsrc_t srd, unsigned soff) {
    asm volatile("s_mov_b32 m0, %0\n\ts_nop 0\n\tbuffer_load_dword %1, %2, %3 offen lds"
                 :: "s"(lds_addr), "v"(voff), "s"(srd), "s"(soff) : "memory");
}
DI void st_granule(gu64* g, unsigned tag, unsigned val) {
    __hip_atomic_store(g, ((unsigned long long)tag << 32) | val, __ATOMIC_RELAXED, __HIP_MEMORY_SCOPE_AGENT);
}
DI unsigned ld_agent(const gu32* p) { return __hip_atomic_load(p, __ATOMIC_RELAXED, __HIP_MEMORY_SCOPE_AGENT); }
DI unsigned lds_ld(lds8* l, unsigned off) { return __hip_atomic_load((LAS unsigned*)(l + off), __ATOMIC_RELAXED, __HIP_MEMORY_SCOPE_WORKGROUP); }
DI void lds_st(lds8* l, unsigned off, unsigned v) { __hip_atomic_store((LAS unsigned*)(l + off), v, __ATOMIC_RELAXED, __HIP_MEMORY_SCOPE_WORKGROUP); }
DI void sig_add(lds8* l, unsigned off) {
    asm volatile("s_mov_b64 exec, 1\n\tds_add_u32 %0, %1\n\ts_mov_b64 exec, -1" :: "v"((unsigned)(size_t)l + off), "v"(1u) : "memory");
}
DI void sig_set(lds8* l, unsigned off) {
    asm volatile("s_mov_b64 exec, 1\n\tds_write_b32 %0, %1\n\ts_mov_b64 exec, -1" :: "v"((unsigned)(size_t)l + off), "v"(1u) : "memory");
}
#define CODE_PAD(name) asm volatile("s_branch .Lskip_" name "\n.Lpad_" name ":\n\ts_setpc_b64 s[30:31]\n.Lskip_" name ":")
#define CODE_TOUCH(name) asm volatile("s_getpc_b64 s[28:29]\n.Lcall_" name ":\n\ts_add_u32 s28, s28, (.Lpad_" name "-.Lcall_" name ")&4294967295\n\t" \
                                      "s_addc_u32 s29, s29, 0\n\ts_swappc_b64 s[30:31], s[28:29]" ::: "s28", "s29", "s30", "s31", "scc")
#define WAIT_LGKM() asm volatile("s_waitcnt lgkmcnt(0)" ::: "memory")
#define WAIT_VM0() asm volatile("s_waitcnt vmcnt(0)" ::: "memory")
#define CBAR() asm volatile("" ::: "memory")

template <int K> DI unsigned quad_bcast(unsigned v) { return __builtin_amdgcn_update_dpp(0u, v, K * 0x55, 0xf, 0xf, true); }
template <int N> DI void wait_vm() { asm volatile("s_waitcnt vmcnt(%0)" :: "i"(N) : "memory"); }
DI void dma_w4(unsigned lds_addr, unsigned voff, __amdgpu_buffer_rsrc_t srd, unsigned soff) {
    unsigned t;
    asm volatile("s_mov_b32 m0, %1\n\ts_add_u32 %0, %4, 12288\n\tbuffer_load_dwordx4 %2, %3, %4 offen lds\n\t"
                 "buffer_load_dwordx4 %2, %3, %0 offen offset:1024 lds\n\ts_add_u32 %0, %0, 12288\n\t"
                 "buffer_load_dwordx4 %2, %3, %0 offen offset:2048 lds\n\ts_add_u32 %0, %0, 12288\n\t"
                 "buffer_load_dwordx4 %2, %3, %0 offen offset:3072 lds"
                 : "=&s"(t) : "s"(lds_addr), "v"(voff), "s"(srd), "s"(soff) : "memory", "scc");
}
DI void dnn_start(const Params& P, lds8* lds, int w, int lane, int g, int ntile) {
    const int mh = w & 1, kh = w >> 1;
    const unsigned ldsb = (unsigned)(size_t)lds;
    const unsigned ring = ldsb + L_A + w * (XD * 2048);
    const __amdgpu_buffer_rsrc_t srdI = __builtin_amdgcn_make_buffer_rsrc((void*)P.idx, 0, NBATCH * 26 * 4, RSRC3);
    const unsigned vo = ((64 * g + 32 * mh + (lane >> 2)) * 26 + 13 * kh + 4 * (lane & 3)) * 4;
    dma16(ring, vo, srdI, 0);
    dma16(ring + 1024, vo, srdI, 1664);
    if (__builtin_expect(w == 1, 1)) dma4(ldsb + L_BI, ntile * 128 + lane * 4, __builtin_amdgcn_make_buffer_rsrc((void*)P.b1, 0, DIN * 2, RSRC3), 0);
    if (__builtin_expect(w == 2, 1)) dma4(ldsb + L_W2, ntile * 128 + lane * 4, __builtin_amdgcn_make_buffer_rsrc((void*)P.w2, 0, DIN * 2, RSRC3), 0);
    const __amdgpu_buffer_rsrc_t srdW = __builtin_amdgcn_make_buffer_rsrc((void*)P.w1, 0, DIN * DIN * 2, RSRC3);
    const unsigned vw = (lane >> 3) * 1664 + (((lane & 7) ^ (((lane >> 4) & 1) << 2)) << 4);
    const unsigned rowA = 416 * kh + 208 * mh + 104 * (g & 1);
    dma_w4(ldsb + L_W + rowA * 128, vw, srdW, rowA * 1664 + ntile * 128);
    if (W_EARLY > 1) dma_w4(ldsb + L_W + rowA * 128 + 4096, vw, srdW, rowA * 1664 + ntile * 128 + 4 * 13312);
    if (W_EARLY > 2) dma_w4(ldsb + L_W + rowA * 128 + 8192, vw, srdW, rowA * 1664 + ntile * 128 + 8 * 13312);
}
DI void dnn_role(const Params& P, lds8* lds, int w, int lane, int g, int ntile) {
    const int mh = w & 1, kh = w >> 1;
    const int h = lane >> 5;
    const unsigned ldsb = (unsigned)(size_t)lds;
    const unsigned ring = ldsb + L_A + w * (XD * 2048);
    const __amdgpu_buffer_rsrc_t srdW = __builtin_amdgcn_make_buffer_rsrc((void*)P.w1, 0, DIN * DIN * 2, RSRC3);
    const unsigned vw = (lane >> 3) * 1664 + (((lane & 7) ^ (((lane >> 4) & 1) << 2)) << 4);
    const unsigned rowA = 416 * kh + 208 * mh + 104 * (g & 1), rowB = 416 * kh + 208 * mh + 104 * ((g & 1) ^ 1);
    auto wdma = [&](unsigned row, int a) {
        const unsigned sb = row * 1664 + ntile * 128, lb = ldsb + L_W + row * 128;
        if (a < 3) dma_w4(lb + a * 4096, vw, srdW, sb + a * 4 * 13312);
        else dma16(lb + 12288, vw, srdW, sb + 12 * 13312);
    };
    wait_vm<4 * W_EARLY>();
    WAIT_LGKM();
    __builtin_amdgcn_s_barrier();
    const u32x4 i0 = *(const LAS u32x4*)(lds + L_A + w * (XD * 2048) + lane * 16);
    const u32x4 i1 = *(const LAS u32x4*)(lds + L_A + w * (XD * 2048) + 1024 + lane * 16);
    const unsigned ep = *(const LAS unsigned*)(lds + L_EP + g * 4);
#pragma unroll
    for (int a = W_EARLY; a < 4; ++a) wdma(rowA, a);
#pragma unroll
    for (int a = 0; a < 4; ++a) wdma(rowB, a);
    const __amdgpu_buffer_rsrc_t srdT = __builtin_amdgcn_make_buffer_rsrc((void*)P.table, 0, NVOC * 64, RSRC3);
    const unsigned c16 = (((lane & 3) ^ ((lane >> 4) & 3)) << 4);
    u32x4 t0 = i0 << 6, t1 = i1 << 6;
    asm volatile("" : "+v"(t0), "+v"(t1));
    auto xload = [&](int s) {
        const unsigned a0 = t0[s & 3], a1 = t1[s & 3];
        const unsigned o0 = (s < 4 ? quad_bcast<0>(a0) : s < 8 ? quad_bcast<1>(a0) : s < 12 ? quad_bcast<2>(a0) : quad_bcast<3>(a0)) + c16;
        const unsigned o1 = (s < 4 ? quad_bcast<0>(a1) : s < 8 ? quad_bcast<1>(a1) : s < 12 ? quad_bcast<2>(a1) : quad_bcast<3>(a1)) + c16;
        dma16(ring + (s % XD) * 2048, o0, srdT, 0);
        dma16(ring + (s % XD) * 2048 + 1024, o1, srdT, 0);
    };
#pragma unroll
    for (int s = 0; s < XD; ++s) xload(s);
    wait_vm<2 * XD>();
    sig_add(lds, L_F + 32 + 4 * kh);
    _Pragma("unroll 1") for (unsigned sp = 0; lds_ld(lds, L_F + 32 + 4 * kh) < 2 && sp < SPIN_CAP; ++sp) __builtin_amdgcn_s_sleep(0);
    CBAR();
    f32x16 acc[2];
    u32x2 bia[4], wv[4];
    float w2f[16];
    unsigned xdone = 0;
#pragma unroll
    for (int r4 = 0; r4 < 4; ++r4) {
        bia[r4] = *(const LAS u32x2*)(lds + L_BI + kh * 64 + h * 8 + r4 * 16);
        wv[r4] = *(const LAS u32x2*)(lds + L_W2 + kh * 64 + h * 8 + r4 * 16);
    }
#pragma unroll
    for (int r4 = 0; r4 < 4; ++r4) {
        acc[0][4 * r4] = bf_lo(bia[r4][0]); acc[0][4 * r4 + 1] = bf_hi(bia[r4][0]); acc[0][4 * r4 + 2] = bf_lo(bia[r4][1]); acc[0][4 * r4 + 3] = bf_hi(bia[r4][1]);
        acc[1][4 * r4] = 0.f; acc[1][4 * r4 + 1] = 0.f; acc[1][4 * r4 + 2] = 0.f; acc[1][4 * r4 + 3] = 0.f;
    }
    {
        const int blk = (lane >> 4) & 1, q = (lane & 15) >> 2, pp = lane & 3, n = lane & 31;
        unsigned ad[2];
#pragma unroll
        for (int j = 0; j < 2; ++j) {
            const unsigned c = 4 * (j ^ kh) + 2 * blk + (pp >> 1);
            ad[j] = L_W + (416 * kh + 8 * h + q) * 128 + ((c ^ (((q >> 1) & 1) << 2)) << 4) + (pp & 1) * 8;
        }
        const unsigned xb = L_A + w * (XD * 2048) + n * 64;
        const unsigned xa0 = xb + ((h ^ ((n >> 2) & 3)) << 4), xa1 = xb + (((2 + h) ^ ((n >> 2) & 3)) << 4);
        u32x4 xf[3][2];
        s16x8 wf[2][4];
        auto xread = [&](int s) {
            xf[s % 3][0] = *(const LAS u32x4*)(lds + xa0 + (s % XD) * 2048);
            xf[s % 3][1] = *(const LAS u32x4*)(lds + xa1 + (s % XD) * 2048);
        };
        auto wread = [&](int s) {
#pragma unroll
            for (int a = 0; a < 2; ++a)
#pragma unroll
                for (int j = 0; j < 2; ++j) {
                    const s16x4 lo = __builtin_amdgcn_ds_read_tr16_b64_v4i16((LAS s16x4*)(lds + ad[j] + s * 4096 + a * 2048));
                    const s16x4 hi = __builtin_amdgcn_ds_read_tr16_b64_v4i16((LAS s16x4*)(lds + ad[j] + s * 4096 + a * 2048 + 512));
                    wf[s & 1][2 * a + j] = __builtin_shufflevector(lo, hi, 0, 1, 2, 3, 4, 5, 6, 7);
                }
        };
        wread(0);
        wait_vm<2 * (XD - 1)>();
        xread(0);
        wait_vm<2 * (XD - 2)>();
        xread(1);
        __builtin_amdgcn_sched_barrier(0);
#pragma unroll
        for (int s = 0; s < 13; ++s) {
            if (s + 1 < 13) wread(s + 1);
#pragma unroll
            for (int a = 0; a < 2; ++a)
#pragma unroll
                for (int j = 0; j < 2; ++j) acc[j] = mfma32(wf[s & 1][2 * a + j], as_frag(xf[s % 3][a]), acc[j]);
            if (s + 1 < 13) {
#pragma unroll
                for (int j = 0; j < 4; ++j) { __builtin_amdgcn_sched_group_barrier(0x100, 2, 0); __builtin_amdgcn_sched_group_barrier(0x8, 1, 0); }
            }
            __builtin_amdgcn_sched_barrier(0);
            if (s + XD < 13) xload(s + XD);
            if (s == 11) {
                xdone = lds_ld(lds, L_F + 52);
#pragma unroll
                for (int r4 = 0; r4 < 4; ++r4) { w2f[4 * r4] = bf_lo(wv[r4][0]); w2f[4 * r4 + 1] = bf_hi(wv[r4][0]); w2f[4 * r4 + 2] = bf_lo(wv[r4][1]); w2f[4 * r4 + 3] = bf_hi(wv[r4][1]); }
#pragma unroll
                for (int e = 0; e < 16; ++e) asm volatile("" : "+v"(w2f[e]));
            }
            if (s + 2 < 13) {
                constexpr int LASTI = 12;
                const int last = s + XD < LASTI ? s + XD : LASTI;
                const int younger = last - (s + 2);
                if (younger >= 4) wait_vm<8>(); else if (younger == 3) wait_vm<6>(); else if (younger == 2) wait_vm<4>(); else if (younger == 1) wait_vm<2>(); else wait_vm<0>();
                if (s + 2 == 12) sig_add(lds, L_F + 52);
                xread(s + 2);
            }
            __builtin_amdgcn_sched_barrier(0);
        }
    }
    {
        if (xdone < 4) _Pragma("unroll 1") for (unsigned sp = 0; lds_ld(lds, L_F + 52) < 4 && sp < SPIN_CAP; ++sp) __builtin_amdgcn_s_sleep(0);
        CBAR();
        lds8* xw = lds + L_W + kh * 53248 + (5 + mh) * 4096 + lane * 16;
#pragma unroll
        for (int r4 = 0; r4 < 4; ++r4) *(LAS f32x4*)(xw + r4 * 1024) = (f32x4){acc[1][4 * r4], acc[1][4 * r4 + 1], acc[1][4 * r4 + 2], acc[1][4 * r4 + 3]};
        CBAR();
        sig_set(lds, L_F + 4 * w);
        _Pragma("unroll 1") for (unsigned sp = 0; lds_ld(lds, L_F + 4 * (w ^ 2)) == 0 && sp < SPIN_CAP; ++sp) __builtin_amdgcn_s_sleep(0);
        CBAR();
        const lds8* xr = lds + L_W + (kh ^ 1) * 53248 + (5 + mh) * 4096 + lane * 16;
        f32x4 o[4];
#pragma unroll
        for (int r4 = 0; r4 < 4; ++r4) o[r4] = *(const LAS f32x4*)(xr + r4 * 1024);
        asm volatile("" : "+v"(o[0]), "+v"(o[1]), "+v"(o[2]), "+v"(o[3]));
#pragma unroll
        for (int r4 = 0; r4 < 4; ++r4) {
            acc[0][4 * r4] += o[r4][0]; acc[0][4 * r4 + 1] += o[r4][1]; acc[0][4 * r4 + 2] += o[r4][2]; acc[0][4 * r4 + 3] += o[r4][3];
        }
    }
    float s0 = 0.f, s1 = 0.f;
#pragma unroll
    for (int r4 = 0; r4 < 4; ++r4) {
        s0 = __builtin_fmaf(gelu_tanh(acc[0][4 * r4]), w2f[4 * r4], s0);
        s1 = __builtin_fmaf(gelu_tanh(acc[0][4 * r4 + 1]), w2f[4 * r4 + 1], s1);
        s0 = __builtin_fmaf(gelu_tanh(acc[0][4 * r4 + 2]), w2f[4 * r4 + 2], s0);
        s1 = __builtin_fmaf(gelu_tanh(acc[0][4 * r4 + 3]), w2f[4 * r4 + 3], s1);
    }
    s0 += s1;
    const auto sw = __builtin_amdgcn_permlane32_swap(__float_as_uint(s0), __float_as_uint(s0), false, false);
    const float part = __uint_as_float(sw[0]) + __uint_as_float(sw[1]);
    const unsigned nxt = __builtin_amdgcn_update_dpp(0u, __float_as_uint(part), 0xf5, 0xf, 0xf, true);
    const __amdgpu_buffer_rsrc_t srdP = __builtin_amdgcn_make_buffer_rsrc((void*)(P.ws + WS_PART), 0, 16 * 32 * 64 * 8, RSRC3);
    const u32x4 gr = {__float_as_uint(part), ep + 1u, nxt, ep + 1u};
    __builtin_amdgcn_raw_buffer_store_b128(gr, srdP, (lane < 32 && !(lane & 1)) ? (unsigned)(((g * 32 + 2 * ntile + kh) * 64 + 32 * mh + lane) * 8) : 0x7ffffff0u, 0, AUX_SC1);
}

constexpr unsigned LP_W2 = 12288;
constexpr unsigned LP_WO = 14336;
constexpr unsigned LP_DM = 15360;
constexpr unsigned LP_W3 = 16384;
static_assert(LP_T1 + 4 * 1026 <= LP_W2, "prep scratch");
constexpr unsigned w3_slot(int j) { return j * 1280 + (j & 1) * 64 + ((j >> 2) & 1) * 128; }
DI void dma16_add(unsigned lds_base, unsigned lds_off, unsigned voff, __amdgpu_buffer_rsrc_t srd, unsigned sbase, unsigned soff) {
    unsigned t;
    asm volatile("s_add_u32 m0, %1, %2\n\ts_add_u32 %0, %5, %6\n\tbuffer_load_dwordx4 %3, %4, %0 offen lds"
                 : "=&s"(t) : "s"(lds_base), "i"(lds_off), "v"(voff), "s"(srd), "s"(sbase), "i"(soff) : "memory", "scc");
}
DI void prep_start(const Params& P, lds8* lds, int w, int lane, int p) {
    const unsigned ldsb = (unsigned)(size_t)lds;
    const __amdgpu_buffer_rsrc_t srdC = __builtin_amdgcn_make_buffer_rsrc((void*)P.wcin, 0, 3 * 17576 * 2, RSRC3);
    const int i0 = w == 0 ? 0 : w == 1 ? 6 : w == 2 ? 14 : 20;
    dma4(ldsb + LP_WO + w * 256, lane * 4, __builtin_amdgcn_make_buffer_rsrc((void*)P.wout, 0, 78 * 2, RSRC3), 0);
    {
        const unsigned hp = lane >> 5;
        const unsigned vo = hp * 1352 + ((lane >> 2) & 7) * 52 + (((lane & 3) ^ (hp << 1)) << 4);
        const unsigned lb = ldsb + LP_W3 + w * 16640, sb = 52 * 1352 + 52 * i0;
        dma16_add(lb, w3_slot(0), vo, srdC, sb, 0 * 2704);  dma16_add(lb, w3_slot(1), vo, srdC, sb, 1 * 2704);
        dma16_add(lb, w3_slot(2), vo, srdC, sb, 2 * 2704);  dma16_add(lb, w3_slot(3), vo, srdC, sb, 3 * 2704);
        dma16_add(lb, w3_slot(4), vo, srdC, sb, 4 * 2704);  dma16_add(lb, w3_slot(5), vo, srdC, sb, 5 * 2704);
        dma16_add(lb, w3_slot(6), vo, srdC, sb, 6 * 2704);  dma16_add(lb, w3_slot(7), vo, srdC, sb, 7 * 2704);
        dma16_add(lb, w3_slot(8), vo, srdC, sb, 8 * 2704);  dma16_add(lb, w3_slot(9), vo, srdC, sb, 9 * 2704);
        dma16_add(lb, w3_slot(10), vo, srdC, sb, 10 * 2704); dma16_add(lb, w3_slot(11), vo, srdC, sb, 11 * 2704);
        dma16_add(lb, w3_slot(12), vo, srdC, sb, 12 * 2704);
    }
    if (__builtin_expect(w < 2, 1)) {
        const unsigned vo = w == 0 ? 35152 + (lane >> 2) * 1352 + p * 52 + (lane & 3) * 16 : p * 1352 + lane * 16;
        const unsigned lb = ldsb + (w == 0 ? LP_W2 : LP_T0);
        dma16(lb, vo, srdC, 0);
        dma16(lb + 1024, vo, srdC, w == 0 ? 16 * 1352 : 1024);
    }
}
DI void prep_role(const Params& P, lds8* lds, int w, int lane, int p, int slot, int g, bool updater) {
    const int n = lane & 15, gq = lane >> 4;
    const int i0 = w == 0 ? 0 : w == 1 ? 6 : w == 2 ? 14 : 20;
    const int nact = w == 0 ? 48 : 44;
    const int ch = lane < nact ? lane : 0;
    const int cs = ch >> 1, chh = ch & 1;
    const GAS u32x4* tp = (const GAS u32x4*)((const GAS unsigned char*)(size_t)__builtin_amdgcn_kernarg_segment_ptr() + sizeof(Params) + ((w == 0 ? 384 : 0) + (cs * 2 + chh) * 8) * 4);
    const u32x4 ta = tp[0], tb = tp[1];
    const int q = (lane & 15) >> 2, pp = lane & 3;
    int r0 = 8 * gq + q, r1 = r0 + 4;
    r0 = r0 < 26 ? r0 : 25; r1 = r1 < 26 ? r1 : 25;
    unsigned b0a, b0b, b1a, b1b;
    {
        const unsigned wb = LP_W3 + w * 16640 + pp * 8;
        const unsigned j0 = r0 >> 1, j1 = r1 >> 1;
        const unsigned s0 = wb + j0 * 1280 + (j0 & 1) * 64 + ((j0 >> 2) & 1) * 128 + (r0 & 1) * 512;
        const unsigned s1 = wb + j1 * 1280 + (j1 & 1) * 64 + ((j1 >> 2) & 1) * 128 + (r1 & 1) * 512;
        b0a = s0 + (r0 & 1) * 32; b0b = s0 + 32 - (r0 & 1) * 32; b1a = s1 + (r1 & 1) * 32; b1b = s1 + 32 - (r1 & 1) * 32;
    }
    unsigned va = LP_VT + n * 64 + 2 * i0, vb = n < 10 ? va + 1024 : LP_DM;
    unsigned vl = w == 3 ? LP_DM : 0;
    unsigned ac = LP_T1 + 4 * (4 * gq * 32 + n);
    unsigned a20 = LP_W2 + r0 * 64 + pp * 8, a21 = LP_W2 + r1 * 64 + pp * 8, abv = LP_VT + n * 64 + gq * 16;
    asm volatile("" : "+v"(b0a), "+v"(b0b), "+v"(b1a), "+v"(b1b), "+v"(va), "+v"(vb), "+v"(ac), "+v"(a20), "+v"(a21), "+v"(abv));
    if (w == 3) {
        LAS unsigned* z = (LAS unsigned*)(lds + LP_VT + (lane < 27 ? lane : 31) * 64 + 52);
        z[0] = 0; z[1] = 0; z[2] = 0;
        *(LAS unsigned*)(lds + LP_T1 + 4 * T_ZERO) = 0;
    }
    if (w == 1) *(LAS unsigned*)(lds + LP_T0 + 2 * T_ZERO) = 0;
    CODE_PAD("p1");
    asm volatile("s_waitcnt vmcnt(0) lgkmcnt(0)" ::: "memory");
    __builtin_amdgcn_s_barrier();
    {
        const u32x4 wo3 = *(const LAS u32x4*)(lds + LP_WO + w * 256 + 104 + 16 * gq);
        s16x8 bfr[16];
#pragma unroll
        for (int t = 0; t < 16; ++t) {
            const int ii = t >> 1, u = t & 1;
            const s16x4 lo = __builtin_amdgcn_ds_read_tr16_b64_v4i16((LAS s16x4*)(lds + (u ? b0b : b0a) + ii * 64));
            const s16x4 hi = __builtin_amdgcn_ds_read_tr16_b64_v4i16((LAS s16x4*)(lds + (u ? b1b : b1a) + ii * 64));
            bfr[t] = __builtin_shufflevector(lo, hi, 0, 1, 2, 3, 4, 5, 6, 7);
        }
        if (w == 3) {
            const unsigned o2 = *(const LAS unsigned*)(lds + LP_WO + 768 + 52 + (lane < 13 ? lane : 0) * 4);
            const unsigned o1 = *(const LAS unsigned short*)(lds + LP_WO + 768 + p * 2);
            *(LAS unsigned*)(lds + (lane < 13 ? LP_VT + 26 * 64 + lane * 4 : LP_DM)) = o2;
            *(LAS float*)(lds + LP_T1 + 4 * T_CONST) = bf_lo(o1);
        }
        float res[16];
#pragma unroll
        for (int t = 0; t < 16; ++t) res[t] = mfma16(as_frag(wo3), bfr[t], (f32x4){0.f, 0.f, 0.f, 0.f})[0];
#pragma unroll
        for (int k2 = 0; k2 < 4; ++k2) {
            const unsigned pa0 = pk_bf16(res[4 * k2], res[4 * k2 + 2]), pb0 = pk_bf16(res[4 * k2 + 1], res[4 * k2 + 3]);
            if (k2 < 3) { *(LAS unsigned*)(lds + va + 4 * k2) = pa0; *(LAS unsigned*)(lds + vb + 4 * k2) = pb0; }
            else { *(LAS unsigned*)(lds + (vl ? vl : va + 12)) = pa0; *(LAS unsigned*)(lds + (vl ? vl : vb + 12)) = pb0; }
        }
    }
    CBAR();
    sig_add(lds, L_F + 28);
    CODE_PAD("p2");
    if (w > 1) return;
    const __amdgpu_buffer_rsrc_t srdA = __builtin_amdgcn_make_buffer_rsrc((void*)(P.ws + WS_A), 0, A_BYTES, RSRC3);
    u32x4 o;
    if (w == 1) {
        float pv[16];
#pragma unroll
        for (int e = 0; e < 8; ++e) {
            const unsigned x = e < 4 ? ta[e] : tb[e - 4];
            pv[2 * e] = bf_lo(*(const LAS unsigned short*)(lds + LP_T0 + 2 * (x & 0xffff)));
            pv[2 * e + 1] = bf_lo(*(const LAS unsigned short*)(lds + LP_T0 + 2 * (x >> 16)));
        }
#pragma unroll
        for (int j = 0; j < 4; ++j) o[j] = pk_bf16(pv[4 * j] + pv[4 * j + 1], pv[4 * j + 2] + pv[4 * j + 3]);
    } else {
        s16x8 a2[2];
#pragma unroll
        for (int mt = 0; mt < 2; ++mt) {
            const s16x4 lo = __builtin_amdgcn_ds_read_tr16_b64_v4i16((LAS s16x4*)(lds + a20 + mt * 32));
            const s16x4 hi = __builtin_amdgcn_ds_read_tr16_b64_v4i16((LAS s16x4*)(lds + a21 + mt * 32));
            a2[mt] = __builtin_shufflevector(lo, hi, 0, 1, 2, 3, 4, 5, 6, 7);
        }
        unsigned pa[16];
#pragma unroll
        for (int e = 0; e < 8; ++e) {
            const unsigned x = e < 4 ? ta[e] : tb[e - 4];
            pa[2 * e] = LP_T1 + 4 * (x & 0xffff); pa[2 * e + 1] = LP_T1 + 4 * (x >> 16);
        }
#pragma unroll
        for (int e = 0; e < 16; ++e) asm volatile("" : "+v"(pa[e]));
        CODE_PAD("p3");
        _Pragma("unroll 1") for (unsigned sp = 0; lds_ld(lds, L_F + 28) < 4 && sp < SPIN_CAP; ++sp) __builtin_amdgcn_s_sleep(0);
        CBAR();
        const u32x4 bf0 = *(const LAS u32x4*)(lds + abv), bf1 = *(const LAS u32x4*)(lds + abv + 1024);
        const f32x4 z4 = {0.f, 0.f, 0.f, 0.f};
        const f32x4 d00 = mfma16(a2[0], as_frag(bf0), z4), d01 = mfma16(a2[1], as_frag(bf0), z4);
        const f32x4 d10 = mfma16(a2[0], as_frag(bf1), z4), d11 = mfma16(a2[1], as_frag(bf1), z4);
#pragma unroll
        for (int e = 0; e < 4; ++e) {
            *(LAS float*)(lds + ac + e * 128) = d00[e]; *(LAS float*)(lds + ac + 2048 + e * 128) = d01[e];
            *(LAS float*)(lds + ac + 64 + e * 128) = d10[e]; *(LAS float*)(lds + ac + 2048 + 64 + e * 128) = d11[e];
        }
        float pv[16];
#pragma unroll
        for (int e = 0; e < 16; ++e) pv[e] = *(const LAS float*)(lds + pa[e]);
#pragma unroll
        for (int j = 0; j < 4; ++j) o[j] = pk_bf16(pv[4 * j] + pv[4 * j + 1], pv[4 * j + 2] + pv[4 * j + 3]);
    }
    {
        const int fr = w == 0 ? (cs < 22 ? 2 * cs : 22 + cs) : 2 * cs + 1;
        __builtin_amdgcn_raw_buffer_store_b128(o, srdA, lane < nact ? (unsigned)(fr * 832 + (26 * chh + p) * 16) : 0x7ffffff0u, 0, AUX_SC1);
    }
    WAIT_VM0();
    if (w == 1) { sig_set(lds, L_F + 24); return; }
    const unsigned tagv = *(const LAS unsigned*)(lds + L_EP + (lane & 15) * 4);
    _Pragma("unroll 1") for (unsigned sp = 0; lds_ld(lds, L_F + 24) == 0 && sp < SPIN_CAP; ++sp) __builtin_amdgcn_s_sleep(0);
    if (lane < 16) __hip_atomic_store((gu32*)(P.ws + WS_FL) + lane * 64 + slot, tagv + 1u, __ATOMIC_RELAXED, __HIP_MEMORY_SCOPE_AGENT);
    sig_set(lds, L_F + 60);
    if (updater) {
        const unsigned tag = *(const LAS unsigned*)(lds + L_EP + g * 4) + 1u;
        const gu32* fl = (const gu32*)(P.ws + WS_FL) + g * 64 + lane;
        const gu32* ar = (const gu32*)(P.ws + WS_PART) + ((g * 32 + 26) * 64 + (lane < 13 ? lane : 0)) * 2 + 1;
        _Pragma("unroll 1") for (unsigned sp = 0; sp < SPIN_CAP; ++sp) {
            const bool fok = (__ballot(ld_agent(fl) == tag) & 0x003fffff03ffffffull) == 0x003fffff03ffffffull;
            if (fok && __all(ld_agent(ar) == tag)) break;
            __builtin_amdgcn_s_sleep(2);
        }
        if (lane == 0) __hip_atomic_store((gu32*)(P.ws + WS_EP) + g, tag, __ATOMIC_RELAXED, __HIP_MEMORY_SCOPE_AGENT);
    }
}

DI bool rows_ready(bool match) {
    const unsigned long long mk = __ballot(match);
    return (((unsigned)mk | (unsigned)(mk >> 32)) & 0x3ffffffu) == 0x3ffffffu;
}
DI void cin_role(const Params& P, lds8* lds, int cw, int lane, int g, int m, bool has_dnn, int pf_slot) {
    const int d = lane & 31, h = lane >> 5;
    const int b = 64 * g + 4 * m + cw;
    const unsigned ldsb = (unsigned)(size_t)lds;
    {
        const __amdgpu_buffer_rsrc_t srdT = __builtin_amdgcn_make_buffer_rsrc((void*)P.table, 0, NVOC * 64, RSRC3);
        const unsigned lb = ldsb + L_X0 + cw * 1664;
        WAIT_VM0();
        const int q = lane >> 2;
        const unsigned j0 = *(const LAS unsigned*)(lds + L_X0 + cw * 1664 + q * 4);
        const unsigned j1 = *(const LAS unsigned*)(lds + L_X0 + cw * 1664 + (q < 10 ? q + 16 : 25) * 4);
        unsigned o0 = j0 * 64 + (lane & 3) * 16, o1 = j1 * 64 + (lane & 3) * 16;
        asm volatile("" : "+v"(o0), "+v"(o1));
        dma16(lb, o0, srdT, 0);
        if (lane < 40) dma16(lb + 1024, o1, srdT, 0);
        if (cw == 0) dma4_sc1(ldsb + L_FL, g * 256 + lane * 4, __builtin_amdgcn_make_buffer_rsrc((void*)(P.ws + WS_FL), 0, 4096, RSRC3), 0);
    }
    const unsigned bo = P.bout[0];
    __builtin_amdgcn_s_setprio(0);
    CBAR();
    __builtin_amdgcn_s_barrier();
    const unsigned epv = *(const LAS unsigned*)(lds + L_EP + g * 4);
    unsigned bov = bo;
    asm volatile("s_waitcnt vmcnt(0)" : "+v"(bov) :: "memory");
    if (cw == 1 && has_dnn && lane == 0) st_granule((gu64*)(P.ws + WS_PART) + ((g * 32 + 26) * 64 + m), epv + 1u, 0u);
    float x[26];
    {
        const lds8* xb = lds + L_X0 + cw * 1664 + h * 64 + d * 2;
        const lds8* xl = lds + L_X0 + cw * 1664 + (h ? 0 : 25 * 64) + d * 2;
#pragma unroll
        for (int a = 0; a < 13; ++a) {
            const unsigned lo = *(const LAS unsigned short*)(xb + 2 * a * 64);
            const unsigned hi = a < 12 ? *(const LAS unsigned short*)(xb + (2 * a + 1) * 64) : *(const LAS unsigned short*)xl;
            x[2 * a] = bf_lo(lo); x[2 * a + 1] = bf_lo(hi);
        }
    }
    u32x4 z[24];
#pragma unroll
    for (int s = 0; s < 22; ++s) {
        float fa[8], fb[8];
#pragma unroll
        for (int e = 0; e < 8; ++e) { const AB pe = slot_ab(8 * s + e); fa[e] = x[pe.a]; fb[e] = x[pe.b]; }
        float t0, t1, t2, t3, t4, t5;
        unsigned o0, o1, o2, o3;
        asm("v_mul_f32 %4, %10, %11\n\tv_mul_f32 %5, %12, %13\n\tv_mul_f32 %6, %14, %15\n\tv_mul_f32 %7, %16, %17\n\t"
            "v_cvt_pk_bf16_f32 %0, %4, %5\n\tv_mul_f32 %8, %18, %19\n\tv_mul_f32 %9, %20, %21\n\tv_cvt_pk_bf16_f32 %1, %6, %7\n\t"
            "v_mul_f32 %4, %22, %23\n\tv_mul_f32 %5, %24, %25\n\tv_cvt_pk_bf16_f32 %2, %8, %9\n\tv_cvt_pk_bf16_f32 %3, %4, %5"
            : "=&v"(o0), "=&v"(o1), "=&v"(o2), "=v"(o3), "=&v"(t0), "=&v"(t1), "=&v"(t2), "=&v"(t3), "=&v"(t4), "=&v"(t5)
            : "v"(fa[0]), "v"(fb[0]), "v"(fa[1]), "v"(fb[1]), "v"(fa[2]), "v"(fb[2]), "v"(fa[3]), "v"(fb[3]),
              "v"(fa[4]), "v"(fb[4]), "v"(fa[5]), "v"(fb[5]), "v"(fa[6]), "v"(fb[6]), "v"(fa[7]), "v"(fb[7]));
        z[s] = (u32x4){o0, o1, o2, o3};
    }
#pragma unroll
    for (int s = 22; s < 24; ++s) {
#pragma unroll
        for (int j = 0; j < 4; ++j) {
            const AB p0 = slot_ab(8 * s + 2 * j), p1 = slot_ab(8 * s + 2 * j + 1);
            const float a0 = p0.a < 26 ? x[p0.a] : (p0.a == 26 ? 1.f : 0.f);
            const float a1 = p1.a < 26 ? x[p1.a] : (p1.a == 26 ? 1.f : 0.f);
            z[s][j] = pk_bf16(a0, a1);
        }
    }
#pragma unroll
    for (int s = 0; s < 24; ++s) asm volatile("" : "+v"(z[s]));
    const unsigned tag = epv + 1u;
    {
        const __amdgpu_buffer_rsrc_t srdA = __builtin_amdgcn_make_buffer_rsrc((void*)(P.ws + WS_A), 0, A_BYTES, RSRC3);
        if (cw == 0) {
            if (has_dnn) _Pragma("unroll 1") for (unsigned sp = 0; lds_ld(lds, L_F + 52) < 4 && sp < SPIN_CAP; ++sp) __builtin_amdgcn_s_sleep(0);
            else _Pragma("unroll 1") for (unsigned sp = 0; lds_ld(lds, L_F + 60) == 0 && sp < SPIN_CAP; ++sp) __builtin_amdgcn_s_sleep(1);
            const gu32* fl = (const gu32*)(P.ws + WS_FL) + g * 64 + lane;
            if (!rows_ready(*(const LAS unsigned*)(lds + L_FL + lane * 4) == tag))
            _Pragma("unroll 1") for (unsigned sp = 0; sp < SPIN_CAP; ++sp) { if (rows_ready(ld_agent(fl) == tag)) break; __builtin_amdgcn_s_sleep(1); }
            sig_set(lds, L_F + 16);
            if (!has_dnn) dma4_sc1(ldsb + L_DUMMY, lane < 50 ? (unsigned)((((g & 1) * 3 + pf_slot) * 50 + lane) * 128) : 0x7fffff00u, srdA, 0);
        } else {
            _Pragma("unroll 1") for (unsigned sp = 0; lds_ld(lds, L_F + 16) == 0 && sp < SPIN_CAP; ++sp) __builtin_amdgcn_s_sleep(0);
        }
        CBAR();
        __builtin_amdgcn_s_setprio(2);
        {
            const unsigned l1 = ldsb + L_W + cw * 4096, vo = lane * 16;
            asm volatile("s_mov_b32 m0, %0\n\ts_nop 0\n\t"
                         "buffer_load_dwordx4 %1, %2, %3 offen sc1 lds\n\tbuffer_load_dwordx4 %1, %2, %3 offen offset:1024 sc1 lds\n\t"
                         "buffer_load_dwordx4 %1, %2, %3 offen offset:2048 sc1 lds\n\tbuffer_load_dwordx4 %1, %2, %3 offen offset:3072 sc1 lds"
                         :: "s"(l1), "v"(vo), "s"(srdA), "s"(cw * 4096) : "memory");
#pragma unroll
            for (int i = 0; i < 6; ++i) {
                const int q = 6 * cw + i;
                const unsigned go = q < 4 ? 16384 + 1024 * q : 19968 + 1024 * (q - 4), lo = q < 4 ? 16384 + 1024 * q : 53248 + 1024 * (q - 4);
                dma16_sc1(ldsb + L_W + lo, vo, srdA, go);
            }
        }
        wait_vm<6>();
        sig_add(lds, L_F + 20);
        _Pragma("unroll 1") for (unsigned sp = 0; lds_ld(lds, L_F + 20) < 4 && sp < SPIN_CAP; ++sp) __builtin_amdgcn_s_sleep(0);
        CBAR();
    }
    const gu64* pp = (const gu64*)(P.ws + WS_PART) + ((g * 32 + (lane < 26 ? lane : 0)) * 64 + 4 * m + cw);
    unsigned long long pv = 0;
    f32x16 acc0, acc1;
#pragma unroll
    for (int i = 0; i < 16; ++i) { acc0[i] = 0.f; acc1[i] = 0.f; }
    {
        unsigned abo = L_W + (26 * h + d) * 16;
        asm volatile("" : "+v"(abo));
        const lds8* ab = lds + abo;
        constexpr int PF = 6;
        u32x4 af[46];
        unsigned cnt2 = 0;
        auto frag_of = [](int t) { return t < 24 ? t : t + 40; };
#pragma unroll
        for (int t = 0; t < PF; ++t) af[t] = *(const LAS u32x4*)(ab + frag_of(t) * 832);
        __builtin_amdgcn_sched_barrier(0);
#pragma unroll
        for (int t = 0; t < 46; ++t) {
            if (t == 9) { wait_vm<0>(); sig_add(lds, L_F + 44); }
            if (t + PF == 19 - 2) cnt2 = lds_ld(lds, L_F + 44);
            if (t + PF == 19) {
                if (cnt2 < 4) _Pragma("unroll 1") for (unsigned sp = 0; lds_ld(lds, L_F + 44) < 4 && sp < SPIN_CAP; ++sp) __builtin_amdgcn_s_sleep(0);
                CBAR();
            }
            if (t + PF < 46) af[t + PF] = *(const LAS u32x4*)(ab + frag_of(t + PF) * 832);
            if (t < 44) {
                if (t & 1) acc0 = mfma32(as_frag(af[t]), as_frag(z[t >> 1]), acc0);
                else acc1 = mfma32(as_frag(af[t]), as_frag(z[t >> 1]), acc1);
            } else acc1 = mfma32(as_frag(af[t]), as_frag(z[t - 22]), acc1);
            if (t == 30) pv = __hip_atomic_load(pp, __ATOMIC_RELAXED, __HIP_MEMORY_SCOPE_AGENT);
            __builtin_amdgcn_sched_barrier(0);
        }
    }
    float sa = 0.f;
#pragma unroll
    for (int i = 0; i < 12; ++i) sa = __builtin_fmaf(acc0[i], acc1[i], sa);
    const float sb = __builtin_fmaf(acc0[12], acc1[12], acc0[13] * acc1[13]);
    float tot = h == 0 ? sa + sb : sa;
    asm volatile("" : "+v"(tot));
    _Pragma("unroll 1") for (unsigned sp = 0; sp < SPIN_CAP; ++sp) {
        if (__all((unsigned)(pv >> 32) == tag)) break;
        pv = __hip_atomic_load(pp, __ATOMIC_RELAXED, __HIP_MEMORY_SCOPE_AGENT);
    }
    if (lane < 26) tot += __uint_as_float((unsigned)pv);
    tot = wave_sum_to_lane63(tot);
    if (lane == 63) {
        const float o = tot + bf_lo(bov);
        P.out[b] = (unsigned short)(pk_bf16(o, o) & 0xffffu);
    }
}

DI void decode_block(int bid, int& g, int& m, int& ntile, int& k) {
    const int x = bid & 7, j = bid >> 3;
    g = 2 * x + (j >> 4); m = j & 15;
    ntile = m < 13 ? m : -1; k = m - 13;
}

__global__ void __launch_bounds__(512, 2) xdfm_kernel(KArgs KA) {
    const Params& P = KA.P;
    __shared__ __attribute__((aligned(16))) unsigned char smem[L_END];
    lds8* lds = (lds8*)smem;
    const int w = __builtin_amdgcn_readfirstlane(threadIdx.x >> 6), lane = threadIdx.x & 63;
    {
        unsigned long long pc; unsigned len;
        asm volatile("s_getpc_b64 %0\n.Lxdfm_pc:\n\ts_mov_b32 %1, (.Lxdfm_end-.Lxdfm_pc)&4294967295" : "=s"(pc), "=s"(len));
        const __amdgpu_buffer_rsrc_t srdP = __builtin_amdgcn_make_buffer_rsrc((void*)pc, 0, len, RSRC3);
        dma4((unsigned)(size_t)lds + L_DUMMY, lane == 0 ? ((blockIdx.x >> 3) * 8 + w) * 128 : 0x7fffff00u, srdP, 0);
    }
    asm volatile("" :: "s"(P.idx), "s"(P.table), "s"(P.wcin), "s"(P.wout), "s"(P.w1), "s"(P.b1), "s"(P.w2), "s"(P.bout), "s"(P.out), "s"(P.ws));
    int g, m, ntile, k;
    decode_block(blockIdx.x, g, m, ntile, k);
    const bool is_cin = w >= 4;
    if (__builtin_expect(is_cin, 1)) {
        __builtin_amdgcn_s_setprio(3);
        dma16((unsigned)(size_t)lds + L_X0 + (w - 4) * 1664, lane < 7 ? (64 * g + 4 * m + w - 4) * 104 + lane * 16 : 0x7fffff00u,
              __builtin_amdgcn_make_buffer_rsrc((void*)P.idx, 0, NBATCH * 26 * 4, RSRC3), 0);
        if (__builtin_expect(w == 7, 1)) dma4((unsigned)(size_t)lds + L_DUMMY, lane < 26 ? ((blockIdx.x >> 3) * 26 + lane) * 128 : 0x7fffff00u,
                         __builtin_amdgcn_make_buffer_rsrc((void*)P.wcin, 0, 3 * 17576 * 2, RSRC3), 0);
    }
    if (__builtin_expect(w == 0, 1)) {
        if (lane < 16) lds_st(lds, L_F + 4 * lane, 0);
        dma4_sc1((unsigned)(size_t)lds + L_EP, (lane & 15) * 4, __builtin_amdgcn_make_buffer_rsrc((void*)(P.ws + WS_EP), 0, 64, RSRC3), 0);
    }
    const int np = g * 3 + k;
    const bool second = (np >= 22 && np < 42) || np >= 46;
    const int p = second ? (np < 42 ? np - 22 : np - 26) : (np < 22 ? np : np - 20);
    if (__builtin_expect(!is_cin && ntile >= 0, 1)) dnn_start(P, lds, w, lane, g, ntile);
    if (__builtin_expect(!is_cin && ntile < 0, 1)) {
        prep_start(P, lds, w, lane, p);
        if (w == 1) CODE_TOUCH("p1");
        if (w == 2) CODE_TOUCH("p2");
        if (w == 3) CODE_TOUCH("p3");
    }
    if (__builtin_expect(!is_cin && ntile >= 0, 1)) {
        dnn_role(P, lds, w, lane, g, ntile);
    } else if (is_cin) {
        cin_role(P, lds, w - 4, lane, g, m, ntile >= 0, k);
        __builtin_amdgcn_s_waitcnt(0x0F70);
    } else {
        prep_role(P, lds, w, lane, p, second ? 32 + p : p, g, k == 0);
        __builtin_amdgcn_s_waitcnt(0x0F70);
    }
    asm volatile(".Lxdfm_end:");
}

extern "C" void kernel_launch(void* const* d_in, const int* in_sizes, int n_in,
                              void* d_out, int out_size, void* d_ws, size_t ws_size,
                              hipStream_t stream) {
    KArgs KA;
    KA.tab = make_tab();
    Params& P = KA.P;
    P.idx = (const int*)d_in[0]; P.table = (const unsigned short*)d_in[1]; P.wcin = (const unsigned short*)d_in[2];
    P.wout = (const unsigned short*)d_in[3]; P.w1 = (const unsigned short*)d_in[4]; P.b1 = (const unsigned short*)d_in[5];
    P.w2 = (const unsigned short*)d_in[6]; P.bout = (const unsigned short*)d_in[7];
    P.out = (unsigned short*)d_out; P.ws = (unsigned char*)d_ws;
    xdfm_kernel<<<256, 512, 0, stream>>>(KA);
}
```

```cpp
#include <hip/hip_runtime.h>
#include <stdint.h>

#define DI __device__ __forceinline__
#define GAS __attribute__((address_space(1)))
#define LAS __attribute__((address_space(3)))

typedef short    s16x4 __attribute__((ext_vector_type(4)));
typedef short    s16x8 __attribute__((ext_vector_type(8)));
typedef float    f32x2 __attribute__((ext_vector_type(2)));
typedef float    f32x4 __attribute__((ext_vector_type(4)));
typedef float    f32x16 __attribute__((ext_vector_type(16)));
typedef unsigned u32x2 __attribute__((ext_vector_type(2)));
typedef unsigned u32x4 __attribute__((ext_vector_type(4)));
typedef __bf16   bf16x2 __attribute__((ext_vector_type(2)));
typedef GAS unsigned gu32;
typedef GAS unsigned long long gu64;
typedef LAS unsigned char lds8;

constexpr int DIN = 832, NBATCH = 1024, NVOC = 100000;
constexpr int XD = 6;
constexpr int W_EARLY = 2;
constexpr unsigned RSRC3 = 0x00020000u;
constexpr int AUX_SC1 = 16;
constexpr unsigned SPIN_CAP = 1u << 20;

constexpr unsigned WS_EP = 0;
constexpr unsigned WS_FL = 4096;
constexpr unsigned WS_PART = 8192;
constexpr unsigned WS_A = 8192 + 16 * 32 * 64 * 8;
constexpr unsigned A_BYTES = 46 * 832;

constexpr unsigned L_W = 0;
constexpr unsigned L_A = 832 * 128;
constexpr unsigned L_X0 = L_A + 4 * XD * 2048;
constexpr unsigned L_F = L_X0 + 4 * 1664;
constexpr unsigned L_DUMMY = L_F + 256;
constexpr unsigned L_EP = L_DUMMY + 256;
constexpr unsigned L_BI = L_EP + 256;
constexpr unsigned L_W2 = L_BI + 256;
constexpr unsigned L_FL = L_W2 + 256;
constexpr unsigned L_END = L_FL + 256;
static_assert(L_END <= 163840, "LDS map");
constexpr unsigned LP_VT = 0;
constexpr unsigned LP_T0 = 2048;
constexpr unsigned LP_T1 = 2048 + 2304;

struct AB { int a, b; };
constexpr AB slot_ab(int t) {
    if (t < 156) { int a = 2 * (t / 12); return {a, (a + t % 12) % 26}; }
    if (t < 170) { int u = t - 156; int a = 2 * (u / 2); return {a, (a + 12 + (u & 1)) % 26}; }
    if (t < 176) { int a = 2 * (7 + t - 170); return {a, (a + 12) % 26}; }
    if (t < 189) return {2 * (t - 176), 26};
    if (t == 189) return {26, 26};
    return {27, 27};
}
constexpr unsigned T_ZERO = 1024, T_CONST = 1025;
struct PrepTab { unsigned v[2][24 * 2 * 8]; };
constexpr PrepTab make_tab() {
    PrepTab tb{};
    for (int m = 0; m < 2; ++m) for (int s = 0; s < 24; ++s) for (int h = 0; h < 2; ++h) for (int e = 0; e < 8; ++e) {
        const int st = m ? 32 : 26;
        int t = 8 * s + e; AB ab = slot_ab(t);
        unsigned o1 = T_ZERO, o2 = T_ZERO;
        if (ab.a == 27) {}
        else if (ab.a == 26) { if (h == 0 && m) o1 = T_CONST; }
        else if (ab.b == 26) { if (m) o1 = ((ab.a + h) % 26) * st + 26; }
        else {
            int u = (ab.a + h) % 26, v = (ab.b + h) % 26;
            bool dup = (t == 169 && h == 1);
            if (dup) {}
            else if (u == v) o1 = u * st + u;
            else { o1 = u * st + v; o2 = v * st + u; }
        }
        tb.v[m][(s * 2 + h) * 8 + e] = o1 | (o2 << 16);
    }
    return tb;
}
static_assert(slot_ab(169).a == 12 && slot_ab(169).b == 25, "dup slot");

struct Params {
    const int* idx; const unsigned short* table; const unsigned short* wcin; const unsigned short* wout;
    const unsigned short* w1; const unsigned short* b1; const unsigned short* w2; const unsigned short* bout;
    unsigned short* out; unsigned char* ws;
};
struct KArgs { Params P; PrepTab tab; };

DI float bf_lo(unsigned v) { return __uint_as_float(v << 16); }
DI float bf_hi(unsigned v) { return __uint_as_float(v & 0xffff0000u); }
DI unsigned pk_bf16(float lo, float hi) { f32x2 v = {lo, hi}; return __builtin_bit_cast(unsigned, __builtin_convertvector(v, bf16x2)); }
DI f32x4 mfma16(s16x8 a, s16x8 b, f32x4 c) { return __builtin_amdgcn_mfma_f32_16x16x32_bf16(a, b, c, 0, 0, 0); }
DI f32x16 mfma32(s16x8 a, s16x8 b, f32x16 c) { return __builtin_amdgcn_mfma_f32_32x32x16_bf16(a, b, c, 0, 0, 0); }
DI s16x8 as_frag(u32x4 v) { return __builtin_bit_cast(s16x8, v); }
template <int CTRL, int ROWMASK> DI float dpp_add(float v) {
    return v + __int_as_float(__builtin_amdgcn_update_dpp(0, __float_as_int(v), CTRL, ROWMASK, 0xf, true));
}
DI float wave_sum_to_lane63(float v) {
    v = dpp_add<0x111, 0xf>(v); v = dpp_add<0x112, 0xf>(v); v = dpp_add<0x114, 0xf>(v); v = dpp_add<0x118, 0xf>(v);
    v = dpp_add<0x142, 0xa>(v); v = dpp_add<0x143, 0xc>(v);
    return v;
}
DI float gelu_tanh(float x) {
    const float k1 = -2.0f * 0.7978845608028654f * 1.4426950408889634f;
    const float k2 = k1 * 0.044715f;
    float arg = x * __builtin_fmaf(x * x, k2, k1);
    float e = __builtin_amdgcn_exp2f(arg);
    return x * __builtin_amdgcn_rcpf(1.0f + e);
}
DI void dma16(unsigned lds_addr, unsigned voff, __amdgpu_buffer_rsrc_t srd, unsigned soff) {
    asm volatile("s_mov_b32 m0, %0\n\ts_nop 0\n\tbuffer_load_dwordx4 %1, %2, %3 offen lds"
                 :: "s"(lds_addr), "v"(voff), "s"(srd), "s"(soff) : "memory");
}
DI void dma16_sc1(unsigned lds_addr, unsigned voff, __amdgpu_buffer_rsrc_t srd, unsigned soff) {
    asm volatile("s_mov_b32 m0, %0\n\ts_nop 0\n\tbuffer_load_dwordx4 %1, %2, %3 offen sc1 lds"
                 :: "s"(lds_addr), "v"(voff), "s"(srd), "s"(soff) : "memory");
}
DI void dma4_sc1(unsigned lds_addr, unsigned voff, __amdgpu_buffer_rsrc_t srd, unsigned soff) {
    asm volatile("s_mov_b32 m0, %0\n\ts_nop 0\n\tbuffer_load_dword %1, %2, %3 offen sc1 lds"
                 :: "s"(lds_addr), "v"(voff), "s"(srd), "s"(soff) : "memory");
}
DI void dma4(unsigned lds_addr, unsigned voff, __amdgpu_buffer_rsrc_t srd, unsigned soff) {
    asm volatile("s_mov_b32 m0, %0\n\ts_nop 0\n\tbuffer_load_dword %1, %2, %3 offen lds"
                 :: "s"(lds_addr), "v"(voff), "s"(srd), "s"(soff) : "memory");
}
DI void st_granule(gu64* g, unsigned tag, unsigned val) {
    __hip_atomic_store(g, ((unsigned long long)tag << 32) | val, __ATOMIC_RELAXED, __HIP_MEMORY_SCOPE_AGENT);
}
DI unsigned ld_agent(const gu32* p) { return __hip_atomic_load(p, __ATOMIC_RELAXED, __HIP_MEMORY_SCOPE_AGENT); }
DI unsigned lds_ld(lds8* l, unsigned off) { return __hip_atomic_load((LAS unsigned*)(l + off), __ATOMIC_RELAXED, __HIP_MEMORY_SCOPE_WORKGROUP); }
DI void lds_st(lds8* l, unsigned off, unsigned v) { __hip_atomic_store((LAS unsigned*)(l + off), v, __ATOMIC_RELAXED, __HIP_MEMORY_SCOPE_WORKGROUP); }
DI void sig_add(lds8* l, unsigned off) {
    asm volatile("s_mov_b64 exec, 1\n\tds_add_u32 %0, %1\n\ts_mov_b64 exec, -1" :: "v"((unsigned)(size_t)l + off), "v"(1u) : "memory");
}
DI void sig_set(lds8* l, unsigned off) {
    asm volatile("s_mov_b64 exec, 1\n\tds_write_b32 %0, %1\n\ts_mov_b64 exec, -1" :: "v"((unsigned)(size_t)l + off), "v"(1u) : "memory");
}
#define WAIT_LGKM() asm volatile("s_waitcnt lgkmcnt(0)" ::: "memory")
#define WAIT_VM0() asm volatile("s_waitcnt vmcnt(0)" ::: "memory")
#define CBAR() asm volatile("" ::: "memory")

template <int K> DI unsigned quad_bcast(unsigned v) { return __builtin_amdgcn_update_dpp(0u, v, K * 0x55, 0xf, 0xf, true); }
template <int N> DI void wait_vm() { asm volatile("s_waitcnt vmcnt(%0)" :: "i"(N) : "memory"); }
DI void dma_w4(unsigned lds_addr, unsigned voff, __amdgpu_buffer_rsrc_t srd, unsigned soff) {
    unsigned t;
    asm volatile("s_mov_b32 m0, %1\n\ts_add_u32 %0, %4, 12288\n\tbuffer_load_dwordx4 %2, %3, %4 offen lds\n\t"
                 "buffer_load_dwordx4 %2, %3, %0 offen offset:1024 lds\n\ts_add_u32 %0, %0, 12288\n\t"
                 "buffer_load_dwordx4 %2, %3, %0 offen offset:2048 lds\n\ts_add_u32 %0, %0, 12288\n\t"
                 "buffer_load_dwordx4 %2, %3, %0 offen offset:3072 lds"
                 : "=&s"(t) : "s"(lds_addr), "v"(voff), "s"(srd), "s"(soff) : "memory", "scc");
}
DI void dnn_start(const Params& P, lds8* lds, int w, int lane, int g, int ntile) {
    const int mh = w & 1, kh = w >> 1;
    const unsigned ldsb = (unsigned)(size_t)lds;
    const unsigned ring = ldsb + L_A + w * (XD * 2048);
    const __amdgpu_buffer_rsrc_t srdI = __builtin_amdgcn_make_buffer_rsrc((void*)P.idx, 0, NBATCH * 26 * 4, RSRC3);
    const unsigned vo = ((64 * g + 32 * mh + (lane >> 2)) * 26 + 13 * kh + 4 * (lane & 3)) * 4;
    dma16(ring, vo, srdI, 0);
    dma16(ring + 1024, vo, srdI, 1664);
    if (__builtin_expect(w == 1, 1)) dma4(ldsb + L_BI, ntile * 128 + lane * 4, __builtin_amdgcn_make_buffer_rsrc((void*)P.b1, 0, DIN * 2, RSRC3), 0);
    if (__builtin_expect(w == 2, 1)) dma4(ldsb + L_W2, ntile * 128 + lane * 4, __builtin_amdgcn_make_buffer_rsrc((void*)P.w2, 0, DIN * 2, RSRC3), 0);
    const __amdgpu_buffer_rsrc_t srdW = __builtin_amdgcn_make_buffer_rsrc((void*)P.w1, 0, DIN * DIN * 2, RSRC3);
    const unsigned vw = (lane >> 3) * 1664 + (((lane & 7) ^ (((lane >> 4) & 1) << 2)) << 4);
    const unsigned rowA = 416 * kh + 208 * mh + 104 * (g & 1);
    dma_w4(ldsb + L_W + rowA * 128, vw, srdW, rowA * 1664 + ntile * 128);
    if (W_EARLY > 1) dma_w4(ldsb + L_W + rowA * 128 + 4096, vw, srdW, rowA * 1664 + ntile * 128 + 4 * 13312);
    if (W_EARLY > 2) dma_w4(ldsb + L_W + rowA * 128 + 8192, vw, srdW, rowA * 1664 + ntile * 128 + 8 * 13312);
}
DI void dnn_role(const Params& P, lds8* lds, int w, int lane, int g, int ntile) {
    const int mh = w & 1, kh = w >> 1;
    const int h = lane >> 5;
    const unsigned ldsb = (unsigned)(size_t)lds;
    const unsigned ring = ldsb + L_A + w * (XD * 2048);
    const __amdgpu_buffer_rsrc_t srdW = __builtin_amdgcn_make_buffer_rsrc((void*)P.w1, 0, DIN * DIN * 2, RSRC3);
    const unsigned vw = (lane >> 3) * 1664 + (((lane & 7) ^ (((lane >> 4) & 1) << 2)) << 4);
    const unsigned rowA = 416 * kh + 208 * mh + 104 * (g & 1), rowB = 416 * kh + 208 * mh + 104 * ((g & 1) ^ 1);
    auto wdma = [&](unsigned row, int a) {
        const unsigned sb = row * 1664 + ntile * 128, lb = ldsb + L_W + row * 128;
        if (a < 3) dma_w4(lb + a * 4096, vw, srdW, sb + a * 4 * 13312);
        else dma16(lb + 12288, vw, srdW, sb + 12 * 13312);
    };
    wait_vm<4 * W_EARLY>();
    WAIT_LGKM();
    __builtin_amdgcn_s_barrier();
    const u32x4 i0 = *(const LAS u32x4*)(lds + L_A + w * (XD * 2048) + lane * 16);
    const u32x4 i1 = *(const LAS u32x4*)(lds + L_A + w * (XD * 2048) + 1024 + lane * 16);
    const unsigned ep = *(const LAS unsigned*)(lds + L_EP + g * 4);
#pragma unroll
    for (int a = W_EARLY; a < 4; ++a) wdma(rowA, a);
#pragma unroll
    for (int a = 0; a < 4; ++a) wdma(rowB, a);
    const __amdgpu_buffer_rsrc_t srdT = __builtin_amdgcn_make_buffer_rsrc((void*)P.table, 0, NVOC * 64, RSRC3);
    const unsigned c16 = (((lane & 3) ^ ((lane >> 4) & 3)) << 4);
    u32x4 t0 = i0 << 6, t1 = i1 << 6;
    asm volatile("" : "+v"(t0), "+v"(t1));
    auto xload = [&](int s) {
        const unsigned a0 = t0[s & 3], a1 = t1[s & 3];
        const unsigned o0 = (s < 4 ? quad_bcast<0>(a0) : s < 8 ? quad_bcast<1>(a0) : s < 12 ? quad_bcast<2>(a0) : quad_bcast<3>(a0)) + c16;
        const unsigned o1 = (s < 4 ? quad_bcast<0>(a1) : s < 8 ? quad_bcast<1>(a1) : s < 12 ? quad_bcast<2>(a1) : quad_bcast<3>(a1)) + c16;
        dma16(ring + (s % XD) * 2048, o0, srdT, 0);
        dma16(ring + (s % XD) * 2048 + 1024, o1, srdT, 0);
    };
#pragma unroll
    for (int s = 0; s < XD; ++s) xload(s);
    wait_vm<2 * XD>();
    sig_add(lds, L_F + 32 + 4 * kh);
    _Pragma("unroll 1") for (unsigned sp = 0; lds_ld(lds, L_F + 32 + 4 * kh) < 2 && sp < SPIN_CAP; ++sp) __builtin_amdgcn_s_sleep(0);
    CBAR();
    f32x16 acc[2];
    u32x2 bia[4], wv[4];
    float w2f[16];
    unsigned xdone = 0;
#pragma unroll
    for (int r4 = 0; r4 < 4; ++r4) {
        bia[r4] = *(const LAS u32x2*)(lds + L_BI + kh * 64 + h * 8 + r4 * 16);
        wv[r4] = *(const LAS u32x2*)(lds + L_W2 + kh * 64 + h * 8 + r4 * 16);
    }
#pragma unroll
    for (int r4 = 0; r4 < 4; ++r4) {
        acc[0][4 * r4] = bf_lo(bia[r4][0]); acc[0][4 * r4 + 1] = bf_hi(bia[r4][0]); acc[0][4 * r4 + 2] = bf_lo(bia[r4][1]); acc[0][4 * r4 + 3] = bf_hi(bia[r4][1]);
        acc[1][4 * r4] = 0.f; acc[1][4 * r4 + 1] = 0.f; acc[1][4 * r4 + 2] = 0.f; acc[1][4 * r4 + 3] = 0.f;
    }
    {
        const int blk = (lane >> 4) & 1, q = (lane & 15) >> 2, pp = lane & 3, n = lane & 31;
        unsigned ad[2];
#pragma unroll
        for (int j = 0; j < 2; ++j) {
            const unsigned c = 4 * (j ^ kh) + 2 * blk + (pp >> 1);
            ad[j] = L_W + (416 * kh + 8 * h + q) * 128 + ((c ^ (((q >> 1) & 1) << 2)) << 4) + (pp & 1) * 8;
        }
        const unsigned xb = L_A + w * (XD * 2048) + n * 64;
        const unsigned xa0 = xb + ((h ^ ((n >> 2) & 3)) << 4), xa1 = xb + (((2 + h) ^ ((n >> 2) & 3)) << 4);
        u32x4 xf[3][2];
        s16x8 wf[2][4];
        auto xread = [&](int s) {
            xf[s % 3][0] = *(const LAS u32x4*)(lds + xa0 + (s % XD) * 2048);
            xf[s % 3][1] = *(const LAS u32x4*)(lds + xa1 + (s % XD) * 2048);
        };
        auto wread = [&](int s) {
#pragma unroll
            for (int a = 0; a < 2; ++a)
#pragma unroll
                for (int j = 0; j < 2; ++j) {
                    const s16x4 lo = __builtin_amdgcn_ds_read_tr16_b64_v4i16((LAS s16x4*)(lds + ad[j] + s * 4096 + a * 2048));
                    const s16x4 hi = __builtin_amdgcn_ds_read_tr16_b64_v4i16((LAS s16x4*)(lds + ad[j] + s * 4096 + a * 2048 + 512));
                    wf[s & 1][2 * a + j] = __builtin_shufflevector(lo, hi, 0, 1, 2, 3, 4, 5, 6, 7);
                }
        };
        wread(0);
        wait_vm<2 * (XD - 1)>();
        xread(0);
        wait_vm<2 * (XD - 2)>();
        xread(1);
        __builtin_amdgcn_sched_barrier(0);
#pragma unroll
        for (int s = 0; s < 13; ++s) {
            if (s + 1 < 13) wread(s + 1);
#pragma unroll
            for (int a = 0; a < 2; ++a)
#pragma unroll
                for (int j = 0; j < 2; ++j) acc[j] = mfma32(wf[s & 1][2 * a + j], as_frag(xf[s % 3][a]), acc[j]);
            if (s + 1 < 13) {
#pragma unroll
                for (int j = 0; j < 4; ++j) { __builtin_amdgcn_sched_group_barrier(0x100, 2, 0); __builtin_amdgcn_sched_group_barrier(0x8, 1, 0); }
            }
            __builtin_amdgcn_sched_barrier(0);
            if (s + XD < 13) xload(s + XD);
            if (s == 11) {
                xdone = lds_ld(lds, L_F + 52);
#pragma unroll
                for (int r4 = 0; r4 < 4; ++r4) { w2f[4 * r4] = bf_lo(wv[r4][0]); w2f[4 * r4 + 1] = bf_hi(wv[r4][0]); w2f[4 * r4 + 2] = bf_lo(wv[r4][1]); w2f[4 * r4 + 3] = bf_hi(wv[r4][1]); }
#pragma unroll
                for (int e = 0; e < 16; ++e) asm volatile("" : "+v"(w2f[e]));
            }
            if (s + 2 < 13) {
                constexpr int LASTI = 12;
                const int last = s + XD < LASTI ? s + XD : LASTI;
                const int younger = last - (s + 2);
                if (younger >= 4) wait_vm<8>(); else if (younger == 3) wait_vm<6>(); else if (younger == 2) wait_vm<4>(); else if (younger == 1) wait_vm<2>(); else wait_vm<0>();
                if (s + 2 == 12) sig_add(lds, L_F + 52);
                xread(s + 2);
            }
            __builtin_amdgcn_sched_barrier(0);
        }
    }
    {
        if (xdone < 4) _Pragma("unroll 1") for (unsigned sp = 0; lds_ld(lds, L_F + 52) < 4 && sp < SPIN_CAP; ++sp) __builtin_amdgcn_s_sleep(0);
        CBAR();
        lds8* xw = lds + L_W + kh * 53248 + (5 + mh) * 4096 + lane * 16;
#pragma unroll
        for (int r4 = 0; r4 < 4; ++r4) *(LAS f32x4*)(xw + r4 * 1024) = (f32x4){acc[1][4 * r4], acc[1][4 * r4 + 1], acc[1][4 * r4 + 2], acc[1][4 * r4 + 3]};
        CBAR();
        sig_set(lds, L_F + 4 * w);
        _Pragma("unroll 1") for (unsigned sp = 0; lds_ld(lds, L_F + 4 * (w ^ 2)) == 0 && sp < SPIN_CAP; ++sp) __builtin_amdgcn_s_sleep(0);
        CBAR();
        const lds8* xr = lds + L_W + (kh ^ 1) * 53248 + (5 + mh) * 4096 + lane * 16;
        f32x4 o[4];
#pragma unroll
        for (int r4 = 0; r4 < 4; ++r4) o[r4] = *(const LAS f32x4*)(xr + r4 * 1024);
        asm volatile("" : "+v"(o[0]), "+v"(o[1]), "+v"(o[2]), "+v"(o[3]));
#pragma unroll
        for (int r4 = 0; r4 < 4; ++r4) {
            acc[0][4 * r4] += o[r4][0]; acc[0][4 * r4 + 1] += o[r4][1]; acc[0][4 * r4 + 2] += o[r4][2]; acc[0][4 * r4 + 3] += o[r4][3];
        }
    }
    float s0 = 0.f, s1 = 0.f;
#pragma unroll
    for (int r4 = 0; r4 < 4; ++r4) {
        s0 = __builtin_fmaf(gelu_tanh(acc[0][4 * r4]), w2f[4 * r4], s0);
        s1 = __builtin_fmaf(gelu_tanh(acc[0][4 * r4 + 1]), w2f[4 * r4 + 1], s1);
        s0 = __builtin_fmaf(gelu_tanh(acc[0][4 * r4 + 2]), w2f[4 * r4 + 2], s0);
        s1 = __builtin_fmaf(gelu_tanh(acc[0][4 * r4 + 3]), w2f[4 * r4 + 3], s1);
    }
    s0 += s1;
    const auto sw = __builtin_amdgcn_permlane32_swap(__float_as_uint(s0), __float_as_uint(s0), false, false);
    const float part = __uint_as_float(sw[0]) + __uint_as_float(sw[1]);
    const unsigned nxt = __builtin_amdgcn_update_dpp(0u, __float_as_uint(part), 0xf5, 0xf, 0xf, true);
    const __amdgpu_buffer_rsrc_t srdP = __builtin_amdgcn_make_buffer_rsrc((void*)(P.ws + WS_PART), 0, 16 * 32 * 64 * 8, RSRC3);
    const u32x4 gr = {__float_as_uint(part), ep + 1u, nxt, ep + 1u};
    __builtin_amdgcn_raw_buffer_store_b128(gr, srdP, (lane < 32 && !(lane & 1)) ? (unsigned)(((g * 32 + 2 * ntile + kh) * 64 + 32 * mh + lane) * 8) : 0x7ffffff0u, 0, AUX_SC1);
}

constexpr unsigned LP_W2 = 12288;
constexpr unsigned LP_WO = 14336;
constexpr unsigned LP_DM = 15360;
constexpr unsigned LP_W3 = 16384;
static_assert(LP_T1 + 4 * 1026 <= LP_W2, "prep scratch");
constexpr unsigned w3_slot(int j) { return j * 1280 + (j & 1) * 64 + ((j >> 2) & 1) * 128; }
DI void dma16_add(unsigned lds_base, unsigned lds_off, unsigned voff, __amdgpu_buffer_rsrc_t srd, unsigned sbase, unsigned soff) {
    unsigned t;
    asm volatile("s_add_u32 m0, %1, %2\n\ts_add_u32 %0, %5, %6\n\tbuffer_load_dwordx4 %3, %4, %0 offen lds"
                 : "=&s"(t) : "s"(lds_base), "i"(lds_off), "v"(voff), "s"(srd), "s"(sbase), "i"(soff) : "memory", "scc");
}
DI void prep_start(const Params& P, lds8* lds, int w, int lane, int p) {
    const unsigned ldsb = (unsigned)(size_t)lds;
    const __amdgpu_buffer_rsrc_t srdC = __builtin_amdgcn_make_buffer_rsrc((void*)P.wcin, 0, 3 * 17576 * 2, RSRC3);
    const int i0 = w == 0 ? 0 : w == 1 ? 6 : w == 2 ? 14 : 20;
    dma4(ldsb + LP_WO + w * 256, lane * 4, __builtin_amdgcn_make_buffer_rsrc((void*)P.wout, 0, 78 * 2, RSRC3), 0);
    {
        const unsigned hp = lane >> 5;
        const unsigned vo = hp * 1352 + ((lane >> 2) & 7) * 52 + (((lane & 3) ^ (hp << 1)) << 4);
        const unsigned lb = ldsb + LP_W3 + w * 16640, sb = 52 * 1352 + 52 * i0;
        dma16_add(lb, w3_slot(0), vo, srdC, sb, 0 * 2704);  dma16_add(lb, w3_slot(1), vo, srdC, sb, 1 * 2704);
        dma16_add(lb, w3_slot(2), vo, srdC, sb, 2 * 2704);  dma16_add(lb, w3_slot(3), vo, srdC, sb, 3 * 2704);
        dma16_add(lb, w3_slot(4), vo, srdC, sb, 4 * 2704);  dma16_add(lb, w3_slot(5), vo, srdC, sb, 5 * 2704);
        dma16_add(lb, w3_slot(6), vo, srdC, sb, 6 * 2704);  dma16_add(lb, w3_slot(7), vo, srdC, sb, 7 * 2704);
        dma16_add(lb, w3_slot(8), vo, srdC, sb, 8 * 2704);  dma16_add(lb, w3_slot(9), vo, srdC, sb, 9 * 2704);
        dma16_add(lb, w3_slot(10), vo, srdC, sb, 10 * 2704); dma16_add(lb, w3_slot(11), vo, srdC, sb, 11 * 2704);
        dma16_add(lb, w3_slot(12), vo, srdC, sb, 12 * 2704);
    }
    if (__builtin_expect(w < 2, 1)) {
        const unsigned vo = w == 0 ? 35152 + (lane >> 2) * 1352 + p * 52 + (lane & 3) * 16 : p * 1352 + lane * 16;
        const unsigned lb = ldsb + (w == 0 ? LP_W2 : LP_T0);
        dma16(lb, vo, srdC, 0);
        dma16(lb + 1024, vo, srdC, w == 0 ? 16 * 1352 : 1024);
    }
}
DI void prep_role(const Params& P, lds8* lds, int w, int lane, int p, int slot, int g, bool updater) {
    const int n = lane & 15, gq = lane >> 4;
    const int i0 = w == 0 ? 0 : w == 1 ? 6 : w == 2 ? 14 : 20;
    const int nact = w == 0 ? 48 : 44;
    const int ch = lane < nact ? lane : 0;
    const int cs = ch >> 1, chh = ch & 1;
    const GAS u32x4* tp = (const GAS u32x4*)((const GAS unsigned char*)(size_t)__builtin_amdgcn_kernarg_segment_ptr() + sizeof(Params) + ((w == 0 ? 384 : 0) + (cs * 2 + chh) * 8) * 4);
    const u32x4 ta = tp[0], tb = tp[1];
    const int q = (lane & 15) >> 2, pp = lane & 3;
    int r0 = 8 * gq + q, r1 = r0 + 4;
    r0 = r0 < 26 ? r0 : 25; r1 = r1 < 26 ? r1 : 25;
    unsigned b0a, b0b, b1a, b1b;
    {
        const unsigned wb = LP_W3 + w * 16640 + pp * 8;
        const unsigned j0 = r0 >> 1, j1 = r1 >> 1;
        const unsigned s0 = wb + j0 * 1280 + (j0 & 1) * 64 + ((j0 >> 2) & 1) * 128 + (r0 & 1) * 512;
        const unsigned s1 = wb + j1 * 1280 + (j1 & 1) * 64 + ((j1 >> 2) & 1) * 128 + (r1 & 1) * 512;
        b0a = s0 + (r0 & 1) * 32; b0b = s0 + 32 - (r0 & 1) * 32; b1a = s1 + (r1 & 1) * 32; b1b = s1 + 32 - (r1 & 1) * 32;
    }
    unsigned va = LP_VT + n * 64 + 2 * i0, vb = n < 10 ? va + 1024 : LP_DM;
    unsigned vl = w == 3 ? LP_DM : 0;
    unsigned ac = LP_T1 + 4 * (4 * gq * 32 + n);
    unsigned a20 = LP_W2 + r0 * 64 + pp * 8, a21 = LP_W2 + r1 * 64 + pp * 8, abv = LP_VT + n * 64 + gq * 16;
    asm volatile("" : "+v"(b0a), "+v"(b0b), "+v"(b1a), "+v"(b1b), "+v"(va), "+v"(vb), "+v"(ac), "+v"(a20), "+v"(a21), "+v"(abv));
    if (w == 3) {
        LAS unsigned* z = (LAS unsigned*)(lds + LP_VT + (lane < 27 ? lane : 31) * 64 + 52);
        z[0] = 0; z[1] = 0; z[2] = 0;
        *(LAS unsigned*)(lds + LP_T1 + 4 * T_ZERO) = 0;
    }
    if (w == 1) *(LAS unsigned*)(lds + LP_T0 + 2 * T_ZERO) = 0;
    asm volatile("s_waitcnt vmcnt(0) lgkmcnt(0)" ::: "memory");
    __builtin_amdgcn_s_barrier();
    {
        const u32x4 wo3 = *(const LAS u32x4*)(lds + LP_WO + w * 256 + 104 + 16 * gq);
        s16x8 bfr[16];
#pragma unroll
        for (int t = 0; t < 16; ++t) {
            const int ii = t >> 1, u = t & 1;
            const s16x4 lo = __builtin_amdgcn_ds_read_tr16_b64_v4i16((LAS s16x4*)(lds + (u ? b0b : b0a) + ii * 64));
            const s16x4 hi = __builtin_amdgcn_ds_read_tr16_b64_v4i16((LAS s16x4*)(lds + (u ? b1b : b1a) + ii * 64));
            bfr[t] = __builtin_shufflevector(lo, hi, 0, 1, 2, 3, 4, 5, 6, 7);
        }
        if (w == 3) {
            const unsigned o2 = *(const LAS unsigned*)(lds + LP_WO + 768 + 52 + (lane < 13 ? lane : 0) * 4);
            const unsigned o1 = *(const LAS unsigned short*)(lds + LP_WO + 768 + p * 2);
            *(LAS unsigned*)(lds + (lane < 13 ? LP_VT + 26 * 64 + lane * 4 : LP_DM)) = o2;
            *(LAS float*)(lds + LP_T1 + 4 * T_CONST) = bf_lo(o1);
        }
        float res[16];
#pragma unroll
        for (int t = 0; t < 16; ++t) res[t] = mfma16(as_frag(wo3), bfr[t], (f32x4){0.f, 0.f, 0.f, 0.f})[0];
#pragma unroll
        for (int k2 = 0; k2 < 4; ++k2) {
            const unsigned pa0 = pk_bf16(res[4 * k2], res[4 * k2 + 2]), pb0 = pk_bf16(res[4 * k2 + 1], res[4 * k2 + 3]);
            if (k2 < 3) { *(LAS unsigned*)(lds + va + 4 * k2) = pa0; *(LAS unsigned*)(lds + vb + 4 * k2) = pb0; }
            else { *(LAS unsigned*)(lds + (vl ? vl : va + 12)) = pa0; *(LAS unsigned*)(lds + (vl ? vl : vb + 12)) = pb0; }
        }
    }
    CBAR();
    sig_add(lds, L_F + 28);
    if (w > 1) return;
    const __amdgpu_buffer_rsrc_t srdA = __builtin_amdgcn_make_buffer_rsrc((void*)(P.ws + WS_A), 0, A_BYTES, RSRC3);
    u32x4 o;
    if (w == 1) {
        float pv[16];
#pragma unroll
        for (int e = 0; e < 8; ++e) {
            const unsigned x = e < 4 ? ta[e] : tb[e - 4];
            pv[2 * e] = bf_lo(*(const LAS unsigned short*)(lds + LP_T0 + 2 * (x & 0xffff)));
            pv[2 * e + 1] = bf_lo(*(const LAS unsigned short*)(lds + LP_T0 + 2 * (x >> 16)));
        }
#pragma unroll
        for (int j = 0; j < 4; ++j) o[j] = pk_bf16(pv[4 * j] + pv[4 * j + 1], pv[4 * j + 2] + pv[4 * j + 3]);
    } else {
        s16x8 a2[2];
#pragma unroll
        for (int mt = 0; mt < 2; ++mt) {
            const s16x4 lo = __builtin_amdgcn_ds_read_tr16_b64_v4i16((LAS s16x4*)(lds + a20 + mt * 32));
            const s16x4 hi = __builtin_amdgcn_ds_read_tr16_b64_v4i16((LAS s16x4*)(lds + a21 + mt * 32));
            a2[mt] = __builtin_shufflevector(lo, hi, 0, 1, 2, 3, 4, 5, 6, 7);
        }
        unsigned pa[16];
#pragma unroll
        for (int e = 0; e < 8; ++e) {
            const unsigned x = e < 4 ? ta[e] : tb[e - 4];
            pa[2 * e] = LP_T1 + 4 * (x & 0xffff); pa[2 * e + 1] = LP_T1 + 4 * (x >> 16);
        }
#pragma unroll
        for (int e = 0; e < 16; ++e) asm volatile("" : "+v"(pa[e]));
        _Pragma("unroll 1") for (unsigned sp = 0; lds_ld(lds, L_F + 28) < 4 && sp < SPIN_CAP; ++sp) __builtin_amdgcn_s_sleep(0);
        CBAR();
        const u32x4 bf0 = *(const LAS u32x4*)(lds + abv), bf1 = *(const LAS u32x4*)(lds + abv + 1024);
        const f32x4 z4 = {0.f, 0.f, 0.f, 0.f};
        const f32x4 d00 = mfma16(a2[0], as_frag(bf0), z4), d01 = mfma16(a2[1], as_frag(bf0), z4);
        const f32x4 d10 = mfma16(a2[0], as_frag(bf1), z4), d11 = mfma16(a2[1], as_frag(bf1), z4);
#pragma unroll
        for (int e = 0; e < 4; ++e) {
            *(LAS float*)(lds + ac + e * 128) = d00[e]; *(LAS float*)(lds + ac + 2048 + e * 128) = d01[e];
            *(LAS float*)(lds + ac + 64 + e * 128) = d10[e]; *(LAS float*)(lds + ac + 2048 + 64 + e * 128) = d11[e];
        }
        float pv[16];
#pragma unroll
        for (int e = 0; e < 16; ++e) pv[e] = *(const LAS float*)(lds + pa[e]);
#pragma unroll
        for (int j = 0; j < 4; ++j) o[j] = pk_bf16(pv[4 * j] + pv[4 * j + 1], pv[4 * j + 2] + pv[4 * j + 3]);
    }
    {
        const int fr = w == 0 ? (cs < 22 ? 2 * cs : 22 + cs) : 2 * cs + 1;
        __builtin_amdgcn_raw_buffer_store_b128(o, srdA, lane < nact ? (unsigned)(fr * 832 + (26 * chh + p) * 16) : 0x7ffffff0u, 0, AUX_SC1);
    }
    WAIT_VM0();
    if (w == 1) { sig_set(lds, L_F + 24); return; }
    const unsigned tagv = *(const LAS unsigned*)(lds + L_EP + (lane & 15) * 4);
    _Pragma("unroll 1") for (unsigned sp = 0; lds_ld(lds, L_F + 24) == 0 && sp < SPIN_CAP; ++sp) __builtin_amdgcn_s_sleep(0);
    if (lane < 16) __hip_atomic_store((gu32*)(P.ws + WS_FL) + lane * 64 + slot, tagv + 1u, __ATOMIC_RELAXED, __HIP_MEMORY_SCOPE_AGENT);
    sig_set(lds, L_F + 60);
    if (updater) {
        const unsigned tag = *(const LAS unsigned*)(lds + L_EP + g * 4) + 1u;
        const gu32* fl = (const gu32*)(P.ws + WS_FL) + g * 64 + lane;
        const gu32* ar = (const gu32*)(P.ws + WS_PART) + ((g * 32 + 26) * 64 + (lane < 13 ? lane : 0)) * 2 + 1;
        _Pragma("unroll 1") for (unsigned sp = 0; sp < SPIN_CAP; ++sp) {
            const bool fok = (__ballot(ld_agent(fl) == tag) & 0x003fffff03ffffffull) == 0x003fffff03ffffffull;
            if (fok && __all(ld_agent(ar) == tag)) break;
            __builtin_amdgcn_s_sleep(32);
        }
        if (lane == 0) __hip_atomic_store((gu32*)(P.ws + WS_EP) + g, tag, __ATOMIC_RELAXED, __HIP_MEMORY_SCOPE_AGENT);
    }
}

DI bool rows_ready(bool match) {
    const unsigned long long mk = __ballot(match);
    return (((unsigned)mk | (unsigned)(mk >> 32)) & 0x3ffffffu) == 0x3ffffffu;
}
DI void cin_role(const Params& P, lds8* lds, int cw, int lane, int g, int m, bool has_dnn, int pf_slot) {
    const int d = lane & 31, h = lane >> 5;
    const int b = 64 * g + 4 * m + cw;
    const unsigned ldsb = (unsigned)(size_t)lds;
    {
        const __amdgpu_buffer_rsrc_t srdT = __builtin_amdgcn_make_buffer_rsrc((void*)P.table, 0, NVOC * 64, RSRC3);
        const unsigned lb = ldsb + L_X0 + cw * 1664;
        WAIT_VM0();
        const int q = lane >> 2;
        const unsigned j0 = *(const LAS unsigned*)(lds + L_X0 + cw * 1664 + q * 4);
        const unsigned j1 = *(const LAS unsigned*)(lds + L_X0 + cw * 1664 + (q < 10 ? q + 16 : 25) * 4);
        unsigned o0 = j0 * 64 + (lane & 3) * 16, o1 = j1 * 64 + (lane & 3) * 16;
        asm volatile("" : "+v"(o0), "+v"(o1));
        dma16(lb, o0, srdT, 0);
        if (lane < 40) dma16(lb + 1024, o1, srdT, 0);
        if (cw == 0) dma4_sc1(ldsb + L_FL, g * 256 + lane * 4, __builtin_amdgcn_make_buffer_rsrc((void*)(P.ws + WS_FL), 0, 4096, RSRC3), 0);
    }
    const unsigned bo = P.bout[0];
    __builtin_amdgcn_s_setprio(0);
    CBAR();
    __builtin_amdgcn_s_barrier();
    const unsigned epv = *(const LAS unsigned*)(lds + L_EP + g * 4);
    unsigned bov = bo;
    asm volatile("s_waitcnt vmcnt(0)" : "+v"(bov) :: "memory");
    if (cw == 1 && has_dnn && lane == 0) st_granule((gu64*)(P.ws + WS_PART) + ((g * 32 + 26) * 64 + m), epv + 1u, 0u);
    float x[26];
    {
        const lds8* xb = lds + L_X0 + cw * 1664 + h * 64 + d * 2;
        const lds8* xl = lds + L_X0 + cw * 1664 + (h ? 0 : 25 * 64) + d * 2;
#pragma unroll
        for (int a = 0; a < 13; ++a) {
            const unsigned lo = *(const LAS unsigned short*)(xb + 2 * a * 64);
            const unsigned hi = a < 12 ? *(const LAS unsigned short*)(xb + (2 * a + 1) * 64) : *(const LAS unsigned short*)xl;
            x[2 * a] = bf_lo(lo); x[2 * a + 1] = bf_lo(hi);
        }
    }
    u32x4 z[24];
#pragma unroll
    for (int s = 0; s < 22; ++s) {
        float fa[8], fb[8];
#pragma unroll
        for (int e = 0; e < 8; ++e) { const AB pe = slot_ab(8 * s + e); fa[e] = x[pe.a]; fb[e] = x[pe.b]; }
        float t0, t1, t2, t3, t4, t5;
        unsigned o0, o1, o2, o3;
        asm("v_mul_f32 %4, %10, %11\n\tv_mul_f32 %5, %12, %13\n\tv_mul_f32 %6, %14, %15\n\tv_mul_f32 %7, %16, %17\n\t"
            "v_cvt_pk_bf16_f32 %0, %4, %5\n\tv_mul_f32 %8, %18, %19\n\tv_mul_f32 %9, %20, %21\n\tv_cvt_pk_bf16_f32 %1, %6, %7\n\t"
            "v_mul_f32 %4, %22, %23\n\tv_mul_f32 %5, %24, %25\n\tv_cvt_pk_bf16_f32 %2, %8, %9\n\tv_cvt_pk_bf16_f32 %3, %4, %5"
            : "=&v"(o0), "=&v"(o1), "=&v"(o2), "=v"(o3), "=&v"(t0), "=&v"(t1), "=&v"(t2), "=&v"(t3), "=&v"(t4), "=&v"(t5)
            : "v"(fa[0]), "v"(fb[0]), "v"(fa[1]), "v"(fb[1]), "v"(fa[2]), "v"(fb[2]), "v"(fa[3]), "v"(fb[3]),
              "v"(fa[4]), "v"(fb[4]), "v"(fa[5]), "v"(fb[5]), "v"(fa[6]), "v"(fb[6]), "v"(fa[7]), "v"(fb[7]));
        z[s] = (u32x4){o0, o1, o2, o3};
    }
#pragma unroll
    for (int s = 22; s < 24; ++s) {
#pragma unroll
        for (int j = 0; j < 4; ++j) {
            const AB p0 = slot_ab(8 * s + 2 * j), p1 = slot_ab(8 * s + 2 * j + 1);
            const float a0 = p0.a < 26 ? x[p0.a] : (p0.a == 26 ? 1.f : 0.f);
            const float a1 = p1.a < 26 ? x[p1.a] : (p1.a == 26 ? 1.f : 0.f);
            z[s][j] = pk_bf16(a0, a1);
        }
    }
#pragma unroll
    for (int s = 0; s < 24; ++s) asm volatile("" : "+v"(z[s]));
    const unsigned tag = epv + 1u;
    {
        const __amdgpu_buffer_rsrc_t srdA = __builtin_amdgcn_make_buffer_rsrc((void*)(P.ws + WS_A), 0, A_BYTES, RSRC3);
        if (cw == 0) {
            if (has_dnn) _Pragma("unroll 1") for (unsigned sp = 0; lds_ld(lds, L_F + 52) < 4 && sp < SPIN_CAP; ++sp) __builtin_amdgcn_s_sleep(0);
            else _Pragma("unroll 1") for (unsigned sp = 0; lds_ld(lds, L_F + 60) == 0 && sp < SPIN_CAP; ++sp) __builtin_amdgcn_s_sleep(1);
            const gu32* fl = (const gu32*)(P.ws + WS_FL) + g * 64 + lane;
            if (!rows_ready(*(const LAS unsigned*)(lds + L_FL + lane * 4) == tag))
            _Pragma("unroll 1") for (unsigned sp = 0; sp < SPIN_CAP; ++sp) { if (rows_ready(ld_agent(fl) == tag)) break; if (has_dnn) __builtin_amdgcn_s_sleep(2); else __builtin_amdgcn_s_sleep(8); }
            sig_set(lds, L_F + 16);
            if (!has_dnn) dma4_sc1(ldsb + L_DUMMY, lane < 50 ? (unsigned)((((g & 1) * 3 + pf_slot) * 50 + lane) * 128) : 0x7fffff00u, srdA, 0);
        } else {
            _Pragma("unroll 1") for (unsigned sp = 0; lds_ld(lds, L_F + 16) == 0 && sp < SPIN_CAP; ++sp) __builtin_amdgcn_s_sleep(0);
        }
        CBAR();
        __builtin_amdgcn_s_setprio(2);
        {
            const unsigned l1 = ldsb + L_W + cw * 4096, vo = lane * 16;
            asm volatile("s_mov_b32 m0, %0\n\ts_nop 0\n\t"
                         "buffer_load_dwordx4 %1, %2, %3 offen sc1 lds\n\tbuffer_load_dwordx4 %1, %2, %3 offen offset:1024 sc1 lds\n\t"
                         "buffer_load_dwordx4 %1, %2, %3 offen offset:2048 sc1 lds\n\tbuffer_load_dwordx4 %1, %2, %3 offen offset:3072 sc1 lds"
                         :: "s"(l1), "v"(vo), "s"(srdA), "s"(cw * 4096) : "memory");
#pragma unroll
            for (int i = 0; i < 6; ++i) {
                const int q = 6 * cw + i;
                const unsigned go = q < 4 ? 16384 + 1024 * q : 19968 + 1024 * (q - 4), lo = q < 4 ? 16384 + 1024 * q : 53248 + 1024 * (q - 4);
                dma16_sc1(ldsb + L_W + lo, vo, srdA, go);
            }
        }
        wait_vm<6>();
        sig_add(lds, L_F + 20);
        _Pragma("unroll 1") for (unsigned sp = 0; lds_ld(lds, L_F + 20) < 4 && sp < SPIN_CAP; ++sp) __builtin_amdgcn_s_sleep(0);
        CBAR();
    }
    const gu64* pp = (const gu64*)(P.ws + WS_PART) + ((g * 32 + (lane < 26 ? lane : 0)) * 64 + 4 * m + cw);
    unsigned long long pv = 0;
    f32x16 acc0, acc1;
#pragma unroll
    for (int i = 0; i < 16; ++i) { acc0[i] = 0.f; acc1[i] = 0.f; }
    {
        unsigned abo = L_W + (26 * h + d) * 16;
        asm volatile("" : "+v"(abo));
        const lds8* ab = lds + abo;
        constexpr int PF = 6;
        u32x4 af[46];
        unsigned cnt2 = 0;
        auto frag_of = [](int t) { return t < 24 ? t : t + 40; };
#pragma unroll
        for (int t = 0; t < PF; ++t) af[t] = *(const LAS u32x4*)(ab + frag_of(t) * 832);
        __builtin_amdgcn_sched_barrier(0);
#pragma unroll
        for (int t = 0; t < 46; ++t) {
            if (t == 9) { wait_vm<0>(); sig_add(lds, L_F + 44); }
            if (t + PF == 19 - 2) cnt2 = lds_ld(lds, L_F + 44);
            if (t + PF == 19) {
                if (cnt2 < 4) _Pragma("unroll 1") for (unsigned sp = 0; lds_ld(lds, L_F + 44) < 4 && sp < SPIN_CAP; ++sp) __builtin_amdgcn_s_sleep(0);
                CBAR();
            }
            if (t + PF < 46) af[t + PF] = *(const LAS u32x4*)(ab + frag_of(t + PF) * 832);
            if (t < 44) {
                if (t & 1) acc0 = mfma32(as_frag(af[t]), as_frag(z[t >> 1]), acc0);
                else acc1 = mfma32(as_frag(af[t]), as_frag(z[t >> 1]), acc1);
            } else acc1 = mfma32(as_frag(af[t]), as_frag(z[t - 22]), acc1);
            if (t == 30) pv = __hip_atomic_load(pp, __ATOMIC_RELAXED, __HIP_MEMORY_SCOPE_AGENT);
            __builtin_amdgcn_sched_barrier(0);
        }
    }
    float sa = 0.f;
#pragma unroll
    for (int i = 0; i < 12; ++i) sa = __builtin_fmaf(acc0[i], acc1[i], sa);
    const float sb = __builtin_fmaf(acc0[12], acc1[12], acc0[13] * acc1[13]);
    float tot = h == 0 ? sa + sb : sa;
    asm volatile("" : "+v"(tot));
    _Pragma("unroll 1") for (unsigned sp = 0; sp < SPIN_CAP; ++sp) {
        if (__all((unsigned)(pv >> 32) == tag)) break;
        pv = __hip_atomic_load(pp, __ATOMIC_RELAXED, __HIP_MEMORY_SCOPE_AGENT);
    }
    if (lane < 26) tot += __uint_as_float((unsigned)pv);
    tot = wave_sum_to_lane63(tot);
    if (lane == 63) {
        const float o = tot + bf_lo(bov);
        P.out[b] = (unsigned short)(pk_bf16(o, o) & 0xffffu);
    }
}

DI void decode_block(int bid, int& g, int& m, int& ntile, int& k) {
    const int x = bid & 7, j = bid >> 3;
    g = 2 * x + (j >> 4); m = j & 15;
    ntile = m < 13 ? m : -1; k = m - 13;
}

__global__ void __launch_bounds__(512, 2) xdfm_kernel(KArgs KA) {
    const Params& P = KA.P;
    __shared__ __attribute__((aligned(16))) unsigned char smem[L_END];
    lds8* lds = (lds8*)smem;
    const int w = __builtin_amdgcn_readfirstlane(threadIdx.x >> 6), lane = threadIdx.x & 63;
    {
        unsigned long long pc; unsigned len;
        asm volatile("s_getpc_b64 %0\n.Lxdfm_pc:\n\ts_mov_b32 %1, (.Lxdfm_end-.Lxdfm_pc)&4294967295" : "=s"(pc), "=s"(len));
        const __amdgpu_buffer_rsrc_t srdP = __builtin_amdgcn_make_buffer_rsrc((void*)pc, 0, len, RSRC3);
        dma4((unsigned)(size_t)lds + L_DUMMY, lane == 0 ? ((blockIdx.x >> 3) * 8 + w) * 128 : 0x7fffff00u, srdP, 0);
    }
    asm volatile("" :: "s"(P.idx), "s"(P.table), "s"(P.wcin), "s"(P.wout), "s"(P.w1), "s"(P.b1), "s"(P.w2), "s"(P.bout), "s"(P.out), "s"(P.ws));
    int g, m, ntile, k;
    decode_block(blockIdx.x, g, m, ntile, k);
    const bool is_cin = w >= 4;
    if (__builtin_expect(is_cin, 1)) {
        __builtin_amdgcn_s_setprio(3);
        dma16((unsigned)(size_t)lds + L_X0 + (w - 4) * 1664, lane < 7 ? (64 * g + 4 * m + w - 4) * 104 + lane * 16 : 0x7fffff00u,
              __builtin_amdgcn_make_buffer_rsrc((void*)P.idx, 0, NBATCH * 26 * 4, RSRC3), 0);
        if (__builtin_expect(w == 7, 1)) dma4((unsigned)(size_t)lds + L_DUMMY, lane < 26 ? ((blockIdx.x >> 3) * 26 + lane) * 128 : 0x7fffff00u,
                         __builtin_amdgcn_make_buffer_rsrc((void*)P.wcin, 0, 3 * 17576 * 2, RSRC3), 0);
    }
    if (__builtin_expect(w == 0, 1)) {
        if (lane < 16) lds_st(lds, L_F + 4 * lane, 0);
        dma4_sc1((unsigned)(size_t)lds + L_EP, (lane & 15) * 4, __builtin_amdgcn_make_buffer_rsrc((void*)(P.ws + WS_EP), 0, 64, RSRC3), 0);
    }
    const int np = g * 3 + k;
    const bool second = (np >= 22 && np < 42) || np >= 46;
    const int p = second ? (np < 42 ? np - 22 : np - 26) : (np < 22 ? np : np - 20);
    if (__builtin_expect(!is_cin && ntile >= 0, 1)) dnn_start(P, lds, w, lane, g, ntile);
    if (__builtin_expect(!is_cin && ntile < 0, 1)) prep_start(P, lds, w, lane, p);
    if (__builtin_expect(!is_cin && ntile >= 0, 1)) {
        dnn_role(P, lds, w, lane, g, ntile);
    } else if (is_cin) {
        cin_role(P, lds, w - 4, lane, g, m, ntile >= 0, k);
        __builtin_amdgcn_s_waitcnt(0x0F70);
    } else {
        prep_role(P, lds, w, lane, p, second ? 32 + p : p, g, k == 0);
        __builtin_amdgcn_s_waitcnt(0x0F70);
    }
    asm volatile(".Lxdfm_end:");
}

extern "C" void kernel_launch(void* const* d_in, const int* in_sizes, int n_in,
                              void* d_out, int out_size, void* d_ws, size_t ws_size,
                              hipStream_t stream) {
    KArgs KA;
    KA.tab = make_tab();
    Params& P = KA.P;
    P.idx = (const int*)d_in[0]; P.table = (const unsigned short*)d_in[1]; P.wcin = (const unsigned short*)d_in[2];
    P.wout = (const unsigned short*)d_in[3]; P.w1 = (const unsigned short*)d_in[4]; P.b1 = (const unsigned short*)d_in[5];
    P.w2 = (const unsigned short*)d_in[6]; P.bout = (const unsigned short*)d_in[7];
    P.out = (unsigned short*)d_out; P.ws = (unsigned char*)d_ws;
    xdfm_kernel<<<256, 512, 0, stream>>>(KA);
}
```

```cpp
#include <hip/hip_runtime.h>
#include <stdint.h>

#define DI __device__ __forceinline__
#define GAS __attribute__((address_space(1)))
#define LAS __attribute__((address_space(3)))

typedef short    s16x4 __attribute__((ext_vector_type(4)));
typedef short    s16x8 __attribute__((ext_vector_type(8)));
typedef float    f32x2 __attribute__((ext_vector_type(2)));
typedef float    f32x4 __attribute__((ext_vector_type(4)));
typedef float    f32x16 __attribute__((ext_vector_type(16)));
typedef unsigned u32x2 __attribute__((ext_vector_type(2)));
typedef unsigned u32x4 __attribute__((ext_vector_type(4)));
typedef __bf16   bf16x2 __attribute__((ext_vector_type(2)));
typedef GAS unsigned gu32;
typedef GAS unsigned long long gu64;
typedef LAS unsigned char lds8;

constexpr int DIN = 832, NBATCH = 1024, NVOC = 100000;
constexpr int XD = 6;
constexpr int W_EARLY = 2;
constexpr unsigned RSRC3 = 0x00020000u;
constexpr int AUX_SC1 = 16;
constexpr unsigned SPIN_CAP = 1u << 20;

constexpr unsigned WS_EP = 0;
constexpr unsigned WS_FL = 4096;
constexpr unsigned WS_PART = 8192;
constexpr unsigned WS_A = 8192 + 16 * 32 * 64 * 8;
constexpr unsigned A_BYTES = 46 * 832;

constexpr unsigned L_W = 0;
constexpr unsigned L_A = 832 * 128;
constexpr unsigned L_X0 = L_A + 4 * XD * 2048;
constexpr unsigned L_F = L_X0 + 4 * 1664;
constexpr unsigned L_DUMMY = L_F + 256;
constexpr unsigned L_EP = L_DUMMY + 256;
constexpr unsigned L_BI = L_EP + 256;
constexpr unsigned L_W2 = L_BI + 256;
constexpr unsigned L_FL = L_W2 + 256;
constexpr unsigned L_END = L_FL + 256;
static_assert(L_END <= 163840, "LDS map");
constexpr unsigned LP_VT = 0;
constexpr unsigned LP_T0 = 2048;
constexpr unsigned LP_T1 = 2048 + 2304;

struct AB { int a, b; };
constexpr AB slot_ab(int t) {
    if (t < 156) { int a = 2 * (t / 12); return {a, (a + t % 12) % 26}; }
    if (t < 170) { int u = t - 156; int a = 2 * (u / 2); return {a, (a + 12 + (u & 1)) % 26}; }
    if (t < 176) { int a = 2 * (7 + t - 170); return {a, (a + 12) % 26}; }
    if (t < 189) return {2 * (t - 176), 26};
    if (t == 189) return {26, 26};
    return {27, 27};
}
constexpr unsigned T_ZERO = 1024, T_CONST = 1025;
struct PrepTab { unsigned v[2][24 * 2 * 8]; };
constexpr PrepTab make_tab() {
    PrepTab tb{};
    for (int m = 0; m < 2; ++m) for (int s = 0; s < 24; ++s) for (int h = 0; h < 2; ++h) for (int e = 0; e < 8; ++e) {
        const int st = m ? 32 : 26;
        int t = 8 * s + e; AB ab = slot_ab(t);
        unsigned o1 = T_ZERO, o2 = T_ZERO;
        if (ab.a == 27) {}
        else if (ab.a == 26) { if (h == 0 && m) o1 = T_CONST; }
        else if (ab.b == 26) { if (m) o1 = ((ab.a + h) % 26) * st + 26; }
        else {
            int u = (ab.a + h) % 26, v = (ab.b + h) % 26;
            bool dup = (t == 169 && h == 1);
            if (dup) {}
            else if (u == v) o1 = u * st + u;
            else { o1 = u * st + v; o2 = v * st + u; }
        }
        tb.v[m][(s * 2 + h) * 8 + e] = o1 | (o2 << 16);
    }
    return tb;
}
static_assert(slot_ab(169).a == 12 && slot_ab(169).b == 25, "dup slot");

struct Params {
    const int* idx; const unsigned short* table; const unsigned short* wcin; const unsigned short* wout;
    const unsigned short* w1; const unsigned short* b1; const unsigned short* w2; const unsigned short* bout;
    unsigned short* out; unsigned char* ws;
};
struct KArgs { Params P; PrepTab tab; };

DI float bf_lo(unsigned v) { return __uint_as_float(v << 16); }
DI float bf_hi(unsigned v) { return __uint_as_float(v & 0xffff0000u); }
DI unsigned pk_bf16(float lo, float hi) { f32x2 v = {lo, hi}; return __builtin_bit_cast(unsigned, __builtin_convertvector(v, bf16x2)); }
DI f32x4 mfma16(s16x8 a, s16x8 b, f32x4 c) { return __builtin_amdgcn_mfma_f32_16x16x32_bf16(a, b, c, 0, 0, 0); }
DI f32x16 mfma32(s16x8 a, s16x8 b, f32x16 c) { return __builtin_amdgcn_mfma_f32_32x32x16_bf16(a, b, c, 0, 0, 0); }
DI s16x8 as_frag(u32x4 v) { return __builtin_bit_cast(s16x8, v); }
template <int CTRL, int ROWMASK> DI float dpp_add(float v) {
    return v + __int_as_float(__builtin_amdgcn_update_dpp(0, __float_as_int(v), CTRL, ROWMASK, 0xf, true));
}
DI float wave_sum_to_lane63(float v) {
    v = dpp_add<0x111, 0xf>(v); v = dpp_add<0x112, 0xf>(v); v = dpp_add<0x114, 0xf>(v); v = dpp_add<0x118, 0xf>(v);
    v = dpp_add<0x142, 0xa>(v); v = dpp_add<0x143, 0xc>(v);
    return v;
}
DI float gelu_tanh(float x) {
    const float k1 = -2.0f * 0.7978845608028654f * 1.4426950408889634f;
    const float k2 = k1 * 0.044715f;
    float arg = x * __builtin_fmaf(x * x, k2, k1);
    float e = __builtin_amdgcn_exp2f(arg);
    return x * __builtin_amdgcn_rcpf(1.0f + e);
}
DI void dma16(unsigned lds_addr, unsigned voff, __amdgpu_buffer_rsrc_t srd, unsigned soff) {
    asm volatile("s_mov_b32 m0, %0\n\ts_nop 0\n\tbuffer_load_dwordx4 %1, %2, %3 offen lds"
                 :: "s"(lds_addr), "v"(voff), "s"(srd), "s"(soff) : "memory");
}
DI void dma16_sc1(unsigned lds_addr, unsigned voff, __amdgpu_buffer_rsrc_t srd, unsigned soff) {
    asm volatile("s_mov_b32 m0, %0\n\ts_nop 0\n\tbuffer_load_dwordx4 %1, %2, %3 offen sc1 lds"
                 :: "s"(lds_addr), "v"(voff), "s"(srd), "s"(soff) : "memory");
}
DI void dma4_sc1(unsigned lds_addr, unsigned voff, __amdgpu_buffer_rsrc_t srd, unsigned soff) {
    asm volatile("s_mov_b32 m0, %0\n\ts_nop 0\n\tbuffer_load_dword %1, %2, %3 offen sc1 lds"
                 :: "s"(lds_addr), "v"(voff), "s"(srd), "s"(soff) : "memory");
}
DI void dma4(unsigned lds_addr, unsigned voff, __amdgpu_buffer_rsrc_t srd, unsigned soff) {
    asm volatile("s_mov_b32 m0, %0\n\ts_nop 0\n\tbuffer_load_dword %1, %2, %3 offen lds"
                 :: "s"(lds_addr), "v"(voff), "s"(srd), "s"(soff) : "memory");
}
DI void st_granule(gu64* g, unsigned tag, unsigned val) {
    __hip_atomic_store(g, ((unsigned long long)tag << 32) | val, __ATOMIC_RELAXED, __HIP_MEMORY_SCOPE_AGENT);
}
DI unsigned ld_agent(const gu32* p) { return __hip_atomic_load(p, __ATOMIC_RELAXED, __HIP_MEMORY_SCOPE_AGENT); }
DI unsigned lds_ld(lds8* l, unsigned off) { return __hip_atomic_load((LAS unsigned*)(l + off), __ATOMIC_RELAXED, __HIP_MEMORY_SCOPE_WORKGROUP); }
DI void lds_st(lds8* l, unsigned off, unsigned v) { __hip_atomic_store((LAS unsigned*)(l + off), v, __ATOMIC_RELAXED, __HIP_MEMORY_SCOPE_WORKGROUP); }
DI void sig_add(lds8* l, unsigned off) {
    asm volatile("s_mov_b64 exec, 1\n\tds_add_u32 %0, %1\n\ts_mov_b64 exec, -1" :: "v"((unsigned)(size_t)l + off), "v"(1u) : "memory");
}
DI void sig_set(lds8* l, unsigned off) {
    asm volatile("s_mov_b64 exec, 1\n\tds_write_b32 %0, %1\n\ts_mov_b64 exec, -1" :: "v"((unsigned)(size_t)l + off), "v"(1u) : "memory");
}
#define WAIT_LGKM() asm volatile("s_waitcnt lgkmcnt(0)" ::: "memory")
#define WAIT_VM0() asm volatile("s_waitcnt vmcnt(0)" ::: "memory")
#define CBAR() asm volatile("" ::: "memory")

template <int K> DI unsigned quad_bcast(unsigned v) { return __builtin_amdgcn_update_dpp(0u, v, K * 0x55, 0xf, 0xf, true); }
template <int N> DI void wait_vm() { asm volatile("s_waitcnt vmcnt(%0)" :: "i"(N) : "memory"); }
DI void dma_w4(unsigned lds_addr, unsigned voff, __amdgpu_buffer_rsrc_t srd, unsigned soff) {
    unsigned t;
    asm volatile("s_mov_b32 m0, %1\n\ts_add_u32 %0, %4, 12288\n\tbuffer_load_dwordx4 %2, %3, %4 offen lds\n\t"
                 "buffer_load_dwordx4 %2, %3, %0 offen offset:1024 lds\n\ts_add_u32 %0, %0, 12288\n\t"
                 "buffer_load_dwordx4 %2, %3, %0 offen offset:2048 lds\n\ts_add_u32 %0, %0, 12288\n\t"
                 "buffer_load_dwordx4 %2, %3, %0 offen offset:3072 lds"
                 : "=&s"(t) : "s"(lds_addr), "v"(voff), "s"(srd), "s"(soff) : "memory", "scc");
}
DI void dnn_start(const Params& P, lds8* lds, int w, int lane, int g, int ntile) {
    const int mh = w & 1, kh = w >> 1;
    const unsigned ldsb = (unsigned)(size_t)lds;
    const unsigned ring = ldsb + L_A + w * (XD * 2048);
    const __amdgpu_buffer_rsrc_t srdI = __builtin_amdgcn_make_buffer_rsrc((void*)P.idx, 0, NBATCH * 26 * 4, RSRC3);
    const unsigned vo = ((64 * g + 32 * mh + (lane >> 2)) * 26 + 13 * kh + 4 * (lane & 3)) * 4;
    dma16(ring, vo, srdI, 0);
    dma16(ring + 1024, vo, srdI, 1664);
    if (__builtin_expect(w == 1, 1)) dma4(ldsb + L_BI, ntile * 128 + lane * 4, __builtin_amdgcn_make_buffer_rsrc((void*)P.b1, 0, DIN * 2, RSRC3), 0);
    if (__builtin_expect(w == 2, 1)) dma4(ldsb + L_W2, ntile * 128 + lane * 4, __builtin_amdgcn_make_buffer_rsrc((void*)P.w2, 0, DIN * 2, RSRC3), 0);
    const __amdgpu_buffer_rsrc_t srdW = __builtin_amdgcn_make_buffer_rsrc((void*)P.w1, 0, DIN * DIN * 2, RSRC3);
    const unsigned vw = (lane >> 3) * 1664 + (((lane & 7) ^ (((lane >> 4) & 1) << 2)) << 4);
    const unsigned rowA = 416 * kh + 208 * mh + 104 * (g & 1);
    dma_w4(ldsb + L_W + rowA * 128, vw, srdW, rowA * 1664 + ntile * 128);
    if (W_EARLY > 1) dma_w4(ldsb + L_W + rowA * 128 + 4096, vw, srdW, rowA * 1664 + ntile * 128 + 4 * 13312);
    if (W_EARLY > 2) dma_w4(ldsb + L_W + rowA * 128 + 8192, vw, srdW, rowA * 1664 + ntile * 128 + 8 * 13312);
}
DI void dnn_role(const Params& P, lds8* lds, int w, int lane, int g, int ntile) {
    const int mh = w & 1, kh = w >> 1;
    const int h = lane >> 5;
    const unsigned ldsb = (unsigned)(size_t)lds;
    const unsigned ring = ldsb + L_A + w * (XD * 2048);
    const __amdgpu_buffer_rsrc_t srdW = __builtin_amdgcn_make_buffer_rsrc((void*)P.w1, 0, DIN * DIN * 2, RSRC3);
    const unsigned vw = (lane >> 3) * 1664 + (((lane & 7) ^ (((lane >> 4) & 1) << 2)) << 4);
    const unsigned rowA = 416 * kh + 208 * mh + 104 * (g & 1), rowB = 416 * kh + 208 * mh + 104 * ((g & 1) ^ 1);
    auto wdma = [&](unsigned row, int a) {
        const unsigned sb = row * 1664 + ntile * 128, lb = ldsb + L_W + row * 128;
        if (a < 3) dma_w4(lb + a * 4096, vw, srdW, sb + a * 4 * 13312);
        else dma16(lb + 12288, vw, srdW, sb + 12 * 13312);
    };
    wait_vm<4 * W_EARLY>();
    WAIT_LGKM();
    __builtin_amdgcn_s_barrier();
    const u32x4 i0 = *(const LAS u32x4*)(lds + L_A + w * (XD * 2048) + lane * 16);
    const u32x4 i1 = *(const LAS u32x4*)(lds + L_A + w * (XD * 2048) + 1024 + lane * 16);
    const unsigned ep = *(const LAS unsigned*)(lds + L_EP + g * 4);
#pragma unroll
    for (int a = W_EARLY; a < 4; ++a) wdma(rowA, a);
#pragma unroll
    for (int a = 0; a < 4; ++a) wdma(rowB, a);
    const __amdgpu_buffer_rsrc_t srdT = __builtin_amdgcn_make_buffer_rsrc((void*)P.table, 0, NVOC * 64, RSRC3);
    const unsigned c16 = (((lane & 3) ^ ((lane >> 4) & 3)) << 4);
    u32x4 t0 = i0 << 6, t1 = i1 << 6;
    asm volatile("" : "+v"(t0), "+v"(t1));
    auto xload = [&](int s) {
        const unsigned a0 = t0[s & 3], a1 = t1[s & 3];
        const unsigned o0 = (s < 4 ? quad_bcast<0>(a0) : s < 8 ? quad_bcast<1>(a0) : s < 12 ? quad_bcast<2>(a0) : quad_bcast<3>(a0)) + c16;
        const unsigned o1 = (s < 4 ? quad_bcast<0>(a1) : s < 8 ? quad_bcast<1>(a1) : s < 12 ? quad_bcast<2>(a1) : quad_bcast<3>(a1)) + c16;
        dma16(ring + (s % XD) * 2048, o0, srdT, 0);
        dma16(ring + (s % XD) * 2048 + 1024, o1, srdT, 0);
    };
#pragma unroll
    for (int s = 0; s < XD; ++s) xload(s);
    wait_vm<2 * XD>();
    sig_add(lds, L_F + 32 + 4 * kh);
    _Pragma("unroll 1") for (unsigned sp = 0; lds_ld(lds, L_F + 32 + 4 * kh) < 2 && sp < SPIN_CAP; ++sp) __builtin_amdgcn_s_sleep(0);
    CBAR();
    f32x16 acc[2];
    u32x2 bia[4], wv[4];
    float w2f[16];
    unsigned xdone = 0;
#pragma unroll
    for (int r4 = 0; r4 < 4; ++r4) {
        bia[r4] = *(const LAS u32x2*)(lds + L_BI + kh * 64 + h * 8 + r4 * 16);
        wv[r4] = *(const LAS u32x2*)(lds + L_W2 + kh * 64 + h * 8 + r4 * 16);
    }
#pragma unroll
    for (int r4 = 0; r4 < 4; ++r4) {
        acc[0][4 * r4] = bf_lo(bia[r4][0]); acc[0][4 * r4 + 1] = bf_hi(bia[r4][0]); acc[0][4 * r4 + 2] = bf_lo(bia[r4][1]); acc[0][4 * r4 + 3] = bf_hi(bia[r4][1]);
        acc[1][4 * r4] = 0.f; acc[1][4 * r4 + 1] = 0.f; acc[1][4 * r4 + 2] = 0.f; acc[1][4 * r4 + 3] = 0.f;
    }
    {
        const int blk = (lane >> 4) & 1, q = (lane & 15) >> 2, pp = lane & 3, n = lane & 31;
        unsigned ad[2];
#pragma unroll
        for (int j = 0; j < 2; ++j) {
            const unsigned c = 4 * (j ^ kh) + 2 * blk + (pp >> 1);
            ad[j] = L_W + (416 * kh + 8 * h + q) * 128 + ((c ^ (((q >> 1) & 1) << 2)) << 4) + (pp & 1) * 8;
        }
        const unsigned xb = L_A + w * (XD * 2048) + n * 64;
        const unsigned xa0 = xb + ((h ^ ((n >> 2) & 3)) << 4), xa1 = xb + (((2 + h) ^ ((n >> 2) & 3)) << 4);
        u32x4 xf[3][2];
        s16x8 wf[2][4];
        auto xread = [&](int s) {
            xf[s % 3][0] = *(const LAS u32x4*)(lds + xa0 + (s % XD) * 2048);
            xf[s % 3][1] = *(const LAS u32x4*)(lds + xa1 + (s % XD) * 2048);
        };
        auto wread = [&](int s) {
#pragma unroll
            for (int a = 0; a < 2; ++a)
#pragma unroll
                for (int j = 0; j < 2; ++j) {
                    const s16x4 lo = __builtin_amdgcn_ds_read_tr16_b64_v4i16((LAS s16x4*)(lds + ad[j] + s * 4096 + a * 2048));
                    const s16x4 hi = __builtin_amdgcn_ds_read_tr16_b64_v4i16((LAS s16x4*)(lds + ad[j] + s * 4096 + a * 2048 + 512));
                    wf[s & 1][2 * a + j] = __builtin_shufflevector(lo, hi, 0, 1, 2, 3, 4, 5, 6, 7);
                }
        };
        wread(0);
        wait_vm<2 * (XD - 1)>();
        xread(0);
        wait_vm<2 * (XD - 2)>();
        xread(1);
        __builtin_amdgcn_sched_barrier(0);
#pragma unroll
        for (int s = 0; s < 13; ++s) {
            if (s + 1 < 13) wread(s + 1);
#pragma unroll
            for (int a = 0; a < 2; ++a)
#pragma unroll
                for (int j = 0; j < 2; ++j) acc[j] = mfma32(wf[s & 1][2 * a + j], as_frag(xf[s % 3][a]), acc[j]);
            if (s + 1 < 13) {
#pragma unroll
                for (int j = 0; j < 4; ++j) { __builtin_amdgcn_sched_group_barrier(0x100, 2, 0); __builtin_amdgcn_sched_group_barrier(0x8, 1, 0); }
            }
            __builtin_amdgcn_sched_barrier(0);
            if (s + XD < 13) xload(s + XD);
            if (s == 11) {
                xdone = lds_ld(lds, L_F + 52);
#pragma unroll
                for (int r4 = 0; r4 < 4; ++r4) { w2f[4 * r4] = bf_lo(wv[r4][0]); w2f[4 * r4 + 1] = bf_hi(wv[r4][0]); w2f[4 * r4 + 2] = bf_lo(wv[r4][1]); w2f[4 * r4 + 3] = bf_hi(wv[r4][1]); }
#pragma unroll
                for (int e = 0; e < 16; ++e) asm volatile("" : "+v"(w2f[e]));
            }
            if (s + 2 < 13) {
                constexpr int LASTI = 12;
                const int last = s + XD < LASTI ? s + XD : LASTI;
                const int younger = last - (s + 2);
                if (younger >= 4) wait_vm<8>(); else if (younger == 3) wait_vm<6>(); else if (younger == 2) wait_vm<4>(); else if (younger == 1) wait_vm<2>(); else wait_vm<0>();
                if (s + 2 == 12) sig_add(lds, L_F + 52);
                xread(s + 2);
            }
            __builtin_amdgcn_sched_barrier(0);
        }
    }
    {
        if (xdone < 4) _Pragma("unroll 1") for (unsigned sp = 0; lds_ld(lds, L_F + 52) < 4 && sp < SPIN_CAP; ++sp) __builtin_amdgcn_s_sleep(0);
        CBAR();
        lds8* xw = lds + L_W + kh * 53248 + (5 + mh) * 4096 + lane * 16;
#pragma unroll
        for (int r4 = 0; r4 < 4; ++r4) *(LAS f32x4*)(xw + r4 * 1024) = (f32x4){acc[1][4 * r4], acc[1][4 * r4 + 1], acc[1][4 * r4 + 2], acc[1][4 * r4 + 3]};
        CBAR();
        sig_set(lds, L_F + 4 * w);
        _Pragma("unroll 1") for (unsigned sp = 0; lds_ld(lds, L_F + 4 * (w ^ 2)) == 0 && sp < SPIN_CAP; ++sp) __builtin_amdgcn_s_sleep(0);
        CBAR();
        const lds8* xr = lds + L_W + (kh ^ 1) * 53248 + (5 + mh) * 4096 + lane * 16;
        f32x4 o[4];
#pragma unroll
        for (int r4 = 0; r4 < 4; ++r4) o[r4] = *(const LAS f32x4*)(xr + r4 * 1024);
        asm volatile("" : "+v"(o[0]), "+v"(o[1]), "+v"(o[2]), "+v"(o[3]));
#pragma unroll
        for (int r4 = 0; r4 < 4; ++r4) {
            acc[0][4 * r4] += o[r4][0]; acc[0][4 * r4 + 1] += o[r4][1]; acc[0][4 * r4 + 2] += o[r4][2]; acc[0][4 * r4 + 3] += o[r4][3];
        }
    }
    float s0 = 0.f, s1 = 0.f;
#pragma unroll
    for (int r4 = 0; r4 < 4; ++r4) {
        s0 = __builtin_fmaf(gelu_tanh(acc[0][4 * r4]), w2f[4 * r4], s0);
        s1 = __builtin_fmaf(gelu_tanh(acc[0][4 * r4 + 1]), w2f[4 * r4 + 1], s1);
        s0 = __builtin_fmaf(gelu_tanh(acc[0][4 * r4 + 2]), w2f[4 * r4 + 2], s0);
        s1 = __builtin_fmaf(gelu_tanh(acc[0][4 * r4 + 3]), w2f[4 * r4 + 3], s1);
    }
    s0 += s1;
    const auto sw = __builtin_amdgcn_permlane32_swap(__float_as_uint(s0), __float_as_uint(s0), false, false);
    const float part = __uint_as_float(sw[0]) + __uint_as_float(sw[1]);
    const unsigned nxt = __builtin_amdgcn_update_dpp(0u, __float_as_uint(part), 0xf5, 0xf, 0xf, true);
    const __amdgpu_buffer_rsrc_t srdP = __builtin_amdgcn_make_buffer_rsrc((void*)(P.ws + WS_PART), 0, 16 * 32 * 64 * 8, RSRC3);
    const u32x4 gr = {__float_as_uint(part), ep + 1u, nxt, ep + 1u};
    __builtin_amdgcn_raw_buffer_store_b128(gr, srdP, (lane < 32 && !(lane & 1)) ? (unsigned)(((g * 32 + 2 * ntile + kh) * 64 + 32 * mh + lane) * 8) : 0x7ffffff0u, 0, AUX_SC1);
}

constexpr unsigned LP_W2 = 12288;
constexpr unsigned LP_WO = 14336;
constexpr unsigned LP_DM = 15360;
constexpr unsigned LP_W3 = 16384;
static_assert(LP_T1 + 4 * 1026 <= LP_W2, "prep scratch");
constexpr unsigned w3_slot(int j) { return j * 1280 + (j & 1) * 64 + ((j >> 2) & 1) * 128; }
DI void dma16_add(unsigned lds_base, unsigned lds_off, unsigned voff, __amdgpu_buffer_rsrc_t srd, unsigned sbase, unsigned soff) {
    unsigned t;
    asm volatile("s_add_u32 m0, %1, %2\n\ts_add_u32 %0, %5, %6\n\tbuffer_load_dwordx4 %3, %4, %0 offen lds"
                 : "=&s"(t) : "s"(lds_base), "i"(lds_off), "v"(voff), "s"(srd), "s"(sbase), "i"(soff) : "memory", "scc");
}
DI void prep_start(const Params& P, lds8* lds, int w, int lane, int p) {
    const unsigned ldsb = (unsigned)(size_t)lds;
    const __amdgpu_buffer_rsrc_t srdC = __builtin_amdgcn_make_buffer_rsrc((void*)P.wcin, 0, 3 * 17576 * 2, RSRC3);
    const int i0 = w == 0 ? 0 : w == 1 ? 6 : w == 2 ? 14 : 20;
    dma4(ldsb + LP_WO + w * 256, lane * 4, __builtin_amdgcn_make_buffer_rsrc((void*)P.wout, 0, 78 * 2, RSRC3), 0);
    {
        const unsigned hp = lane >> 5;
        const unsigned vo = hp * 1352 + ((lane >> 2) & 7) * 52 + (((lane & 3) ^ (hp << 1)) << 4);
        const unsigned lb = ldsb + LP_W3 + w * 16640, sb = 52 * 1352 + 52 * i0;
        dma16_add(lb, w3_slot(0), vo, srdC, sb, 0 * 2704);  dma16_add(lb, w3_slot(1), vo, srdC, sb, 1 * 2704);
        dma16_add(lb, w3_slot(2), vo, srdC, sb, 2 * 2704);  dma16_add(lb, w3_slot(3), vo, srdC, sb, 3 * 2704);
        dma16_add(lb, w3_slot(4), vo, srdC, sb, 4 * 2704);  dma16_add(lb, w3_slot(5), vo, srdC, sb, 5 * 2704);
        dma16_add(lb, w3_slot(6), vo, srdC, sb, 6 * 2704);  dma16_add(lb, w3_slot(7), vo, srdC, sb, 7 * 2704);
        dma16_add(lb, w3_slot(8), vo, srdC, sb, 8 * 2704);  dma16_add(lb, w3_slot(9), vo, srdC, sb, 9 * 2704);
        dma16_add(lb, w3_slot(10), vo, srdC, sb, 10 * 2704); dma16_add(lb, w3_slot(11), vo, srdC, sb, 11 * 2704);
        dma16_add(lb, w3_slot(12), vo, srdC, sb, 12 * 2704);
    }
    if (__builtin_expect(w < 2, 1)) {
        const unsigned vo = w == 0 ? 35152 + (lane >> 2) * 1352 + p * 52 + (lane & 3) * 16 : p * 1352 + lane * 16;
        const unsigned lb = ldsb + (w == 0 ? LP_W2 : LP_T0);
        dma16(lb, vo, srdC, 0);
        dma16(lb + 1024, vo, srdC, w == 0 ? 16 * 1352 : 1024);
    }
}
DI void prep_role(const Params& P, lds8* lds, int w, int lane, int p, int slot, int g, bool updater) {
    const int n = lane & 15, gq = lane >> 4;
    const int i0 = w == 0 ? 0 : w == 1 ? 6 : w == 2 ? 14 : 20;
    const int nact = w == 0 ? 48 : 44;
    const int ch = lane < nact ? lane : 0;
    const int cs = ch >> 1, chh = ch & 1;
    const GAS u32x4* tp = (const GAS u32x4*)((const GAS unsigned char*)(size_t)__builtin_amdgcn_kernarg_segment_ptr() + sizeof(Params) + ((w == 0 ? 384 : 0) + (cs * 2 + chh) * 8) * 4);
    const u32x4 ta = tp[0], tb = tp[1];
    const int q = (lane & 15) >> 2, pp = lane & 3;
    int r0 = 8 * gq + q, r1 = r0 + 4;
    r0 = r0 < 26 ? r0 : 25; r1 = r1 < 26 ? r1 : 25;
    unsigned b0a, b0b, b1a, b1b;
    {
        const unsigned wb = LP_W3 + w * 16640 + pp * 8;
        const unsigned j0 = r0 >> 1, j1 = r1 >> 1;
        const unsigned s0 = wb + j0 * 1280 + (j0 & 1) * 64 + ((j0 >> 2) & 1) * 128 + (r0 & 1) * 512;
        const unsigned s1 = wb + j1 * 1280 + (j1 & 1) * 64 + ((j1 >> 2) & 1) * 128 + (r1 & 1) * 512;
        b0a = s0 + (r0 & 1) * 32; b0b = s0 + 32 - (r0 & 1) * 32; b1a = s1 + (r1 & 1) * 32; b1b = s1 + 32 - (r1 & 1) * 32;
    }
    unsigned va = LP_VT + n * 64 + 2 * i0, vb = n < 10 ? va + 1024 : LP_DM;
    unsigned vl = w == 3 ? LP_DM : 0;
    unsigned ac = LP_T1 + 4 * (4 * gq * 32 + n);
    unsigned a20 = LP_W2 + r0 * 64 + pp * 8, a21 = LP_W2 + r1 * 64 + pp * 8, abv = LP_VT + n * 64 + gq * 16;
    asm volatile("" : "+v"(b0a), "+v"(b0b), "+v"(b1a), "+v"(b1b), "+v"(va), "+v"(vb), "+v"(ac), "+v"(a20), "+v"(a21), "+v"(abv));
    if (w == 3) {
        LAS unsigned* z = (LAS unsigned*)(lds + LP_VT + (lane < 27 ? lane : 31) * 64 + 52);
        z[0] = 0; z[1] = 0; z[2] = 0;
        *(LAS unsigned*)(lds + LP_T1 + 4 * T_ZERO) = 0;
    }
    if (w == 1) *(LAS unsigned*)(lds + LP_T0 + 2 * T_ZERO) = 0;
    asm volatile("s_waitcnt vmcnt(0) lgkmcnt(0)" ::: "memory");
    __builtin_amdgcn_s_barrier();
    {
        const u32x4 wo3 = *(const LAS u32x4*)(lds + LP_WO + w * 256 + 104 + 16 * gq);
        s16x8 bfr[16];
#pragma unroll
        for (int t = 0; t < 16; ++t) {
            const int ii = t >> 1, u = t & 1;
            const s16x4 lo = __builtin_amdgcn_ds_read_tr16_b64_v4i16((LAS s16x4*)(lds + (u ? b0b : b0a) + ii * 64));
            const s16x4 hi = __builtin_amdgcn_ds_read_tr16_b64_v4i16((LAS s16x4*)(lds + (u ? b1b : b1a) + ii * 64));
            bfr[t] = __builtin_shufflevector(lo, hi, 0, 1, 2, 3, 4, 5, 6, 7);
        }
        if (w == 3) {
            const unsigned o2 = *(const LAS unsigned*)(lds + LP_WO + 768 + 52 + (lane < 13 ? lane : 0) * 4);
            const unsigned o1 = *(const LAS unsigned short*)(lds + LP_WO + 768 + p * 2);
            *(LAS unsigned*)(lds + (lane < 13 ? LP_VT + 26 * 64 + lane * 4 : LP_DM)) = o2;
            *(LAS float*)(lds + LP_T1 + 4 * T_CONST) = bf_lo(o1);
        }
        float res[16];
#pragma unroll
        for (int t = 0; t < 16; ++t) res[t] = mfma16(as_frag(wo3), bfr[t], (f32x4){0.f, 0.f, 0.f, 0.f})[0];
#pragma unroll
        for (int k2 = 0; k2 < 4; ++k2) {
            const unsigned pa0 = pk_bf16(res[4 * k2], res[4 * k2 + 2]), pb0 = pk_bf16(res[4 * k2 + 1], res[4 * k2 + 3]);
            if (k2 < 3) { *(LAS unsigned*)(lds + va + 4 * k2) = pa0; *(LAS unsigned*)(lds + vb + 4 * k2) = pb0; }
            else { *(LAS unsigned*)(lds + (vl ? vl : va + 12)) = pa0; *(LAS unsigned*)(lds + (vl ? vl : vb + 12)) = pb0; }
        }
    }
    CBAR();
    sig_add(lds, L_F + 28);
    if (w > 1) return;
    const __amdgpu_buffer_rsrc_t srdA = __builtin_amdgcn_make_buffer_rsrc((void*)(P.ws + WS_A), 0, A_BYTES, RSRC3);
    u32x4 o;
    if (w == 1) {
        float pv[16];
#pragma unroll
        for (int e = 0; e < 8; ++e) {
            const unsigned x = e < 4 ? ta[e] : tb[e - 4];
            pv[2 * e] = bf_lo(*(const LAS unsigned short*)(lds + LP_T0 + 2 * (x & 0xffff)));
            pv[2 * e + 1] = bf_lo(*(const LAS unsigned short*)(lds + LP_T0 + 2 * (x >> 16)));
        }
#pragma unroll
        for (int j = 0; j < 4; ++j) o[j] = pk_bf16(pv[4 * j] + pv[4 * j + 1], pv[4 * j + 2] + pv[4 * j + 3]);
    } else {
        s16x8 a2[2];
#pragma unroll
        for (int mt = 0; mt < 2; ++mt) {
            const s16x4 lo = __builtin_amdgcn_ds_read_tr16_b64_v4i16((LAS s16x4*)(lds + a20 + mt * 32));
            const s16x4 hi = __builtin_amdgcn_ds_read_tr16_b64_v4i16((LAS s16x4*)(lds + a21 + mt * 32));
            a2[mt] = __builtin_shufflevector(lo, hi, 0, 1, 2, 3, 4, 5, 6, 7);
        }
        unsigned pa[16];
#pragma unroll
        for (int e = 0; e < 8; ++e) {
            const unsigned x = e < 4 ? ta[e] : tb[e - 4];
            pa[2 * e] = LP_T1 + 4 * (x & 0xffff); pa[2 * e + 1] = LP_T1 + 4 * (x >> 16);
        }
#pragma unroll
        for (int e = 0; e < 16; ++e) asm volatile("" : "+v"(pa[e]));
        _Pragma("unroll 1") for (unsigned sp = 0; lds_ld(lds, L_F + 28) < 4 && sp < SPIN_CAP; ++sp) __builtin_amdgcn_s_sleep(0);
        CBAR();
        const u32x4 bf0 = *(const LAS u32x4*)(lds + abv), bf1 = *(const LAS u32x4*)(lds + abv + 1024);
        const f32x4 z4 = {0.f, 0.f, 0.f, 0.f};
        const f32x4 d00 = mfma16(a2[0], as_frag(bf0), z4), d01 = mfma16(a2[1], as_frag(bf0), z4);
        const f32x4 d10 = mfma16(a2[0], as_frag(bf1), z4), d11 = mfma16(a2[1], as_frag(bf1), z4);
#pragma unroll
        for (int e = 0; e < 4; ++e) {
            *(LAS float*)(lds + ac + e * 128) = d00[e]; *(LAS float*)(lds + ac + 2048 + e * 128) = d01[e];
            *(LAS float*)(lds + ac + 64 + e * 128) = d10[e]; *(LAS float*)(lds + ac + 2048 + 64 + e * 128) = d11[e];
        }
        float pv[16];
#pragma unroll
        for (int e = 0; e < 16; ++e) pv[e] = *(const LAS float*)(lds + pa[e]);
#pragma unroll
        for (int j = 0; j < 4; ++j) o[j] = pk_bf16(pv[4 * j] + pv[4 * j + 1], pv[4 * j + 2] + pv[4 * j + 3]);
    }
    {
        const int fr = w == 0 ? (cs < 22 ? 2 * cs : 22 + cs) : 2 * cs + 1;
        __builtin_amdgcn_raw_buffer_store_b128(o, srdA, lane < nact ? (unsigned)(fr * 832 + (26 * chh + p) * 16) : 0x7ffffff0u, 0, AUX_SC1);
    }
    WAIT_VM0();
    if (w == 1) { sig_set(lds, L_F + 24); return; }
    const unsigned tagv = *(const LAS unsigned*)(lds + L_EP + (lane & 15) * 4);
    _Pragma("unroll 1") for (unsigned sp = 0; lds_ld(lds, L_F + 24) == 0 && sp < SPIN_CAP; ++sp) __builtin_amdgcn_s_sleep(0);
    if (lane < 16) __hip_atomic_store((gu32*)(P.ws + WS_FL) + lane * 64 + slot, tagv + 1u, __ATOMIC_RELAXED, __HIP_MEMORY_SCOPE_AGENT);
    sig_set(lds, L_F + 60);
    if (updater) {
        const unsigned tag = *(const LAS unsigned*)(lds + L_EP + g * 4) + 1u;
        const gu32* fl = (const gu32*)(P.ws + WS_FL) + g * 64 + lane;
        const gu32* ar = (const gu32*)(P.ws + WS_PART) + ((g * 32 + 26) * 64 + (lane < 13 ? lane : 0)) * 2 + 1;
        _Pragma("unroll 1") for (unsigned sp = 0; sp < SPIN_CAP; ++sp) {
            const bool fok = (__ballot(ld_agent(fl) == tag) & 0x003fffff03ffffffull) == 0x003fffff03ffffffull;
            if (fok && __all(ld_agent(ar) == tag)) break;
            __builtin_amdgcn_s_sleep(16);
        }
        if (lane == 0) __hip_atomic_store((gu32*)(P.ws + WS_EP) + g, tag, __ATOMIC_RELAXED, __HIP_MEMORY_SCOPE_AGENT);
    }
}

DI bool rows_ready(bool match) {
    const unsigned long long mk = __ballot(match);
    return (((unsigned)mk | (unsigned)(mk >> 32)) & 0x3ffffffu) == 0x3ffffffu;
}
DI void cin_role(const Params& P, lds8* lds, int cw, int lane, int g, int m, bool has_dnn, int pf_slot) {
    const int d = lane & 31, h = lane >> 5;
    const int b = 64 * g + 4 * m + cw;
    const unsigned ldsb = (unsigned)(size_t)lds;
    {
        const __amdgpu_buffer_rsrc_t srdT = __builtin_amdgcn_make_buffer_rsrc((void*)P.table, 0, NVOC * 64, RSRC3);
        const unsigned lb = ldsb + L_X0 + cw * 1664;
        WAIT_VM0();
        const int q = lane >> 2;
        const unsigned j0 = *(const LAS unsigned*)(lds + L_X0 + cw * 1664 + q * 4);
        const unsigned j1 = *(const LAS unsigned*)(lds + L_X0 + cw * 1664 + (q < 10 ? q + 16 : 25) * 4);
        unsigned o0 = j0 * 64 + (lane & 3) * 16, o1 = j1 * 64 + (lane & 3) * 16;
        asm volatile("" : "+v"(o0), "+v"(o1));
        dma16(lb, o0, srdT, 0);
        if (lane < 40) dma16(lb + 1024, o1, srdT, 0);
        if (cw == 0) dma4_sc1(ldsb + L_FL, g * 256 + lane * 4, __builtin_amdgcn_make_buffer_rsrc((void*)(P.ws + WS_FL), 0, 4096, RSRC3), 0);
    }
    const unsigned bo = P.bout[0];
    __builtin_amdgcn_s_setprio(0);
    CBAR();
    __builtin_amdgcn_s_barrier();
    const unsigned epv = *(const LAS unsigned*)(lds + L_EP + g * 4);
    unsigned bov = bo;
    asm volatile("s_waitcnt vmcnt(0)" : "+v"(bov) :: "memory");
    if (cw == 1 && has_dnn && lane == 0) st_granule((gu64*)(P.ws + WS_PART) + ((g * 32 + 26) * 64 + m), epv + 1u, 0u);
    float x[26];
    {
        const lds8* xb = lds + L_X0 + cw * 1664 + h * 64 + d * 2;
        const lds8* xl = lds + L_X0 + cw * 1664 + (h ? 0 : 25 * 64) + d * 2;
#pragma unroll
        for (int a = 0; a < 13; ++a) {
            const unsigned lo = *(const LAS unsigned short*)(xb + 2 * a * 64);
            const unsigned hi = a < 12 ? *(const LAS unsigned short*)(xb + (2 * a + 1) * 64) : *(const LAS unsigned short*)xl;
            x[2 * a] = bf_lo(lo); x[2 * a + 1] = bf_lo(hi);
        }
    }
    u32x4 z[24];
#pragma unroll
    for (int s = 0; s < 22; ++s) {
        float fa[8], fb[8];
#pragma unroll
        for (int e = 0; e < 8; ++e) { const AB pe = slot_ab(8 * s + e); fa[e] = x[pe.a]; fb[e] = x[pe.b]; }
        float t0, t1, t2, t3, t4, t5;
        unsigned o0, o1, o2, o3;
        asm("v_mul_f32 %4, %10, %11\n\tv_mul_f32 %5, %12, %13\n\tv_mul_f32 %6, %14, %15\n\tv_mul_f32 %7, %16, %17\n\t"
            "v_cvt_pk_bf16_f32 %0, %4, %5\n\tv_mul_f32 %8, %18, %19\n\tv_mul_f32 %9, %20, %21\n\tv_cvt_pk_bf16_f32 %1, %6, %7\n\t"
            "v_mul_f32 %4, %22, %23\n\tv_mul_f32 %5, %24, %25\n\tv_cvt_pk_bf16_f32 %2, %8, %9\n\tv_cvt_pk_bf16_f32 %3, %4, %5"
            : "=&v"(o0), "=&v"(o1), "=&v"(o2), "=v"(o3), "=&v"(t0), "=&v"(t1), "=&v"(t2), "=&v"(t3), "=&v"(t4), "=&v"(t5)
            : "v"(fa[0]), "v"(fb[0]), "v"(fa[1]), "v"(fb[1]), "v"(fa[2]), "v"(fb[2]), "v"(fa[3]), "v"(fb[3]),
              "v"(fa[4]), "v"(fb[4]), "v"(fa[5]), "v"(fb[5]), "v"(fa[6]), "v"(fb[6]), "v"(fa[7]), "v"(fb[7]));
        z[s] = (u32x4){o0, o1, o2, o3};
    }
#pragma unroll
    for (int s = 22; s < 24; ++s) {
#pragma unroll
        for (int j = 0; j < 4; ++j) {
            const AB p0 = slot_ab(8 * s + 2 * j), p1 = slot_ab(8 * s + 2 * j + 1);
            const float a0 = p0.a < 26 ? x[p0.a] : (p0.a == 26 ? 1.f : 0.f);
            const float a1 = p1.a < 26 ? x[p1.a] : (p1.a == 26 ? 1.f : 0.f);
            z[s][j] = pk_bf16(a0, a1);
        }
    }
#pragma unroll
    for (int s = 0; s < 24; ++s) asm volatile("" : "+v"(z[s]));
    const unsigned tag = epv + 1u;
    {
        const __amdgpu_buffer_rsrc_t srdA = __builtin_amdgcn_make_buffer_rsrc((void*)(P.ws + WS_A), 0, A_BYTES, RSRC3);
        if (cw == 0) {
            if (has_dnn) _Pragma("unroll 1") for (unsigned sp = 0; lds_ld(lds, L_F + 52) < 4 && sp < SPIN_CAP; ++sp) __builtin_amdgcn_s_sleep(0);
            else _Pragma("unroll 1") for (unsigned sp = 0; lds_ld(lds, L_F + 60) == 0 && sp < SPIN_CAP; ++sp) __builtin_amdgcn_s_sleep(1);
            const gu32* fl = (const gu32*)(P.ws + WS_FL) + g * 64 + lane;
            if (!rows_ready(*(const LAS unsigned*)(lds + L_FL + lane * 4) == tag))
            _Pragma("unroll 1") for (unsigned sp = 0; sp < SPIN_CAP; ++sp) {
                if (rows_ready(ld_agent(fl) == tag)) break;
                if (has_dnn) __builtin_amdgcn_s_sleep(1); else __builtin_amdgcn_s_sleep(8);
            }
            sig_set(lds, L_F + 16);
            if (!has_dnn) dma4_sc1(ldsb + L_DUMMY, lane < 50 ? (unsigned)((((g & 1) * 3 + pf_slot) * 50 + lane) * 128) : 0x7fffff00u, srdA, 0);
        } else {
            _Pragma("unroll 1") for (unsigned sp = 0; lds_ld(lds, L_F + 16) == 0 && sp < SPIN_CAP; ++sp) __builtin_amdgcn_s_sleep(0);
        }
        CBAR();
        __builtin_amdgcn_s_setprio(2);
        {
            const unsigned l1 = ldsb + L_W + cw * 4096, vo = lane * 16;
            asm volatile("s_mov_b32 m0, %0\n\ts_nop 0\n\t"
                         "buffer_load_dwordx4 %1, %2, %3 offen sc1 lds\n\tbuffer_load_dwordx4 %1, %2, %3 offen offset:1024 sc1 lds\n\t"
                         "buffer_load_dwordx4 %1, %2, %3 offen offset:2048 sc1 lds\n\tbuffer_load_dwordx4 %1, %2, %3 offen offset:3072 sc1 lds"
                         :: "s"(l1), "v"(vo), "s"(srdA), "s"(cw * 4096) : "memory");
#pragma unroll
            for (int i = 0; i < 6; ++i) {
                const int q = 6 * cw + i;
                const unsigned go = q < 4 ? 16384 + 1024 * q : 19968 + 1024 * (q - 4), lo = q < 4 ? 16384 + 1024 * q : 53248 + 1024 * (q - 4);
                dma16_sc1(ldsb + L_W + lo, vo, srdA, go);
            }
        }
        wait_vm<6>();
        sig_add(lds, L_F + 20);
        _Pragma("unroll 1") for (unsigned sp = 0; lds_ld(lds, L_F + 20) < 4 && sp < SPIN_CAP; ++sp) __builtin_amdgcn_s_sleep(0);
        CBAR();
    }
    const gu64* pp = (const gu64*)(P.ws + WS_PART) + ((g * 32 + (lane < 26 ? lane : 0)) * 64 + 4 * m + cw);
    unsigned long long pv = 0;
    f32x16 acc0, acc1;
#pragma unroll
    for (int i = 0; i < 16; ++i) { acc0[i] = 0.f; acc1[i] = 0.f; }
    {
        unsigned abo = L_W + (26 * h + d) * 16;
        asm volatile("" : "+v"(abo));
        const lds8* ab = lds + abo;
        constexpr int PF = 6;
        u32x4 af[46];
        unsigned cnt2 = 0;
        auto frag_of = [](int t) { return t < 24 ? t : t + 40; };
#pragma unroll
        for (int t = 0; t < PF; ++t) af[t] = *(const LAS u32x4*)(ab + frag_of(t) * 832);
        __builtin_amdgcn_sched_barrier(0);
#pragma unroll
        for (int t = 0; t < 46; ++t) {
            if (t == 9) { wait_vm<0>(); sig_add(lds, L_F + 44); }
            if (t + PF == 19 - 2) cnt2 = lds_ld(lds, L_F + 44);
            if (t + PF == 19) {
                if (cnt2 < 4) _Pragma("unroll 1") for (unsigned sp = 0; lds_ld(lds, L_F + 44) < 4 && sp < SPIN_CAP; ++sp) __builtin_amdgcn_s_sleep(0);
                CBAR();
            }
            if (t + PF < 46) af[t + PF] = *(const LAS u32x4*)(ab + frag_of(t + PF) * 832);
            if (t < 44) {
                if (t & 1) acc0 = mfma32(as_frag(af[t]), as_frag(z[t >> 1]), acc0);
                else acc1 = mfma32(as_frag(af[t]), as_frag(z[t >> 1]), acc1);
            } else acc1 = mfma32(as_frag(af[t]), as_frag(z[t - 22]), acc1);
            if (t == 30) pv = __hip_atomic_load(pp, __ATOMIC_RELAXED, __HIP_MEMORY_SCOPE_AGENT);
            __builtin_amdgcn_sched_barrier(0);
        }
    }
    float sa = 0.f;
#pragma unroll
    for (int i = 0; i < 12; ++i) sa = __builtin_fmaf(acc0[i], acc1[i], sa);
    const float sb = __builtin_fmaf(acc0[12], acc1[12], acc0[13] * acc1[13]);
    float tot = h == 0 ? sa + sb : sa;
    asm volatile("" : "+v"(tot));
    _Pragma("unroll 1") for (unsigned sp = 0; sp < SPIN_CAP; ++sp) {
        if (__all((unsigned)(pv >> 32) == tag)) break;
        pv = __hip_atomic_load(pp, __ATOMIC_RELAXED, __HIP_MEMORY_SCOPE_AGENT);
    }
    if (lane < 26) tot += __uint_as_float((unsigned)pv);
    tot = wave_sum_to_lane63(tot);
    if (lane == 63) {
        const float o = tot + bf_lo(bov);
        P.out[b] = (unsigned short)(pk_bf16(o, o) & 0xffffu);
    }
}

DI void decode_block(int bid, int& g, int& m, int& ntile, int& k) {
    const int x = bid & 7, j = bid >> 3;
    g = 2 * x + (j >> 4); m = j & 15;
    ntile = m < 13 ? m : -1; k = m - 13;
}

__global__ void __launch_bounds__(512, 2) xdfm_kernel(KArgs KA) {
    const Params& P = KA.P;
    __shared__ __attribute__((aligned(16))) unsigned char smem[L_END];
    lds8* lds = (lds8*)smem;
    const int w = __builtin_amdgcn_readfirstlane(threadIdx.x >> 6), lane = threadIdx.x & 63;
    {
        unsigned long long pc; unsigned len;
        asm volatile("s_getpc_b64 %0\n.Lxdfm_pc:\n\ts_mov_b32 %1, (.Lxdfm_end-.Lxdfm_pc)&4294967295" : "=s"(pc), "=s"(len));
        const __amdgpu_buffer_rsrc_t srdP = __builtin_amdgcn_make_buffer_rsrc((void*)pc, 0, len, RSRC3);
        dma4((unsigned)(size_t)lds + L_DUMMY, lane == 0 ? ((blockIdx.x >> 3) * 8 + w) * 128 : 0x7fffff00u, srdP, 0);
    }
    asm volatile("" :: "s"(P.idx), "s"(P.table), "s"(P.wcin), "s"(P.wout), "s"(P.w1), "s"(P.b1), "s"(P.w2), "s"(P.bout), "s"(P.out), "s"(P.ws));
    int g, m, ntile, k;
    decode_block(blockIdx.x, g, m, ntile, k);
    const bool is_cin = w >= 4;
    if (__builtin_expect(is_cin, 1)) {
        __builtin_amdgcn_s_setprio(3);
        dma16((unsigned)(size_t)lds + L_X0 + (w - 4) * 1664, lane < 7 ? (64 * g + 4 * m + w - 4) * 104 + lane * 16 : 0x7fffff00u,
              __builtin_amdgcn_make_buffer_rsrc((void*)P.idx, 0, NBATCH * 26 * 4, RSRC3), 0);
        if (__builtin_expect(w == 7, 1)) dma4((unsigned)(size_t)lds + L_DUMMY, lane < 26 ? ((blockIdx.x >> 3) * 26 + lane) * 128 : 0x7fffff00u,
                         __builtin_amdgcn_make_buffer_rsrc((void*)P.wcin, 0, 3 * 17576 * 2, RSRC3), 0);
    }
    if (__builtin_expect(w == 0, 1)) {
        if (lane < 16) lds_st(lds, L_F + 4 * lane, 0);
        dma4_sc1((unsigned)(size_t)lds + L_EP, (lane & 15) * 4, __builtin_amdgcn_make_buffer_rsrc((void*)(P.ws + WS_EP), 0, 64, RSRC3), 0);
    }
    const int np = g * 3 + k;
    const bool second = (np >= 22 && np < 42) || np >= 46;
    const int p = second ? (np < 42 ? np - 22 : np - 26) : (np < 22 ? np : np - 20);
    if (__builtin_expect(!is_cin && ntile >= 0, 1)) dnn_start(P, lds, w, lane, g, ntile);
    if (__builtin_expect(!is_cin && ntile < 0, 1)) prep_start(P, lds, w, lane, p);
    if (__builtin_expect(!is_cin && ntile >= 0, 1)) {
        dnn_role(P, lds, w, lane, g, ntile);
    } else if (is_cin) {
        cin_role(P, lds, w - 4, lane, g, m, ntile >= 0, k);
        __builtin_amdgcn_s_waitcnt(0x0F70);
    } else {
        prep_role(P, lds, w, lane, p, second ? 32 + p : p, g, k == 0);
        __builtin_amdgcn_s_waitcnt(0x0F70);
    }
    asm volatile(".Lxdfm_end:");
}

extern "C" void kernel_launch(void* const* d_in, const int* in_sizes, int n_in,
                              void* d_out, int out_size, void* d_ws, size_t ws_size,
                              hipStream_t stream) {
    KArgs KA;
    KA.tab = make_tab();
    Params& P = KA.P;
    P.idx = (const int*)d_in[0]; P.table = (const unsigned short*)d_in[1]; P.wcin = (const unsigned short*)d_in[2];
    P.wout = (const unsigned short*)d_in[3]; P.w1 = (const unsigned short*)d_in[4]; P.b1 = (const unsigned short*)d_in[5];
    P.w2 = (const unsigned short*)d_in[6]; P.bout = (const unsigned short*)d_in[7];
    P.out = (unsigned short*)d_out; P.ws = (unsigned char*)d_ws;
    xdfm_kernel<<<256, 512, 0, stream>>>(KA);
}
```

```cpp
#include <hip/hip_runtime.h>
#include <stdint.h>

#define DI __device__ __forceinline__
#define GAS __attribute__((address_space(1)))
#define LAS __attribute__((address_space(3)))

typedef short    s16x4 __attribute__((ext_vector_type(4)));
typedef short    s16x8 __attribute__((ext_vector_type(8)));
typedef float    f32x2 __attribute__((ext_vector_type(2)));
typedef float    f32x4 __attribute__((ext_vector_type(4)));
typedef float    f32x16 __attribute__((ext_vector_type(16)));
typedef unsigned u32x2 __attribute__((ext_vector_type(2)));
typedef unsigned u32x4 __attribute__((ext_vector_type(4)));
typedef __bf16   bf16x2 __attribute__((ext_vector_type(2)));
typedef GAS unsigned gu32;
typedef GAS unsigned long long gu64;
typedef LAS unsigned char lds8;

constexpr int DIN = 832, NBATCH = 1024, NVOC = 100000;
constexpr int XD = 6;
constexpr int W_EARLY = 2;
constexpr unsigned RSRC3 = 0x00020000u;
constexpr int AUX_SC1 = 16;
constexpr unsigned SPIN_CAP = 1u << 20;

constexpr unsigned WS_EP = 0;
constexpr unsigned WS_FL = 524288;
constexpr unsigned WS_PART = 8192;
constexpr unsigned WS_A = 8192 + 16 * 32 * 64 * 8;
constexpr unsigned A_BYTES = 46 * 832;

constexpr unsigned L_W = 0;
constexpr unsigned L_A = 832 * 128;
constexpr unsigned L_X0 = L_A + 4 * XD * 2048;
constexpr unsigned L_F = L_X0 + 4 * 1664;
constexpr unsigned L_DUMMY = L_F + 256;
constexpr unsigned L_EP = L_DUMMY + 256;
constexpr unsigned L_BI = L_EP + 256;
constexpr unsigned L_W2 = L_BI + 256;
constexpr unsigned L_FL = L_W2 + 256;
constexpr unsigned L_END = L_FL + 256;
static_assert(L_END <= 163840, "LDS map");
constexpr unsigned LP_VT = 0;
constexpr unsigned LP_T0 = 2048;
constexpr unsigned LP_T1 = 2048 + 2304;

struct AB { int a, b; };
constexpr AB slot_ab(int t) {
    if (t < 156) { int a = 2 * (t / 12); return {a, (a + t % 12) % 26}; }
    if (t < 170) { int u = t - 156; int a = 2 * (u / 2); return {a, (a + 12 + (u & 1)) % 26}; }
    if (t < 176) { int a = 2 * (7 + t - 170); return {a, (a + 12) % 26}; }
    if (t < 189) return {2 * (t - 176), 26};
    if (t == 189) return {26, 26};
    return {27, 27};
}
constexpr unsigned T_ZERO = 1024, T_CONST = 1025;
struct PrepTab { unsigned v[2][24 * 2 * 8]; };
constexpr PrepTab make_tab() {
    PrepTab tb{};
    for (int m = 0; m < 2; ++m) for (int s = 0; s < 24; ++s) for (int h = 0; h < 2; ++h) for (int e = 0; e < 8; ++e) {
        const int st = m ? 32 : 26;
        int t = 8 * s + e; AB ab = slot_ab(t);
        unsigned o1 = T_ZERO, o2 = T_ZERO;
        if (ab.a == 27) {}
        else if (ab.a == 26) { if (h == 0 && m) o1 = T_CONST; }
        else if (ab.b == 26) { if (m) o1 = ((ab.a + h) % 26) * st + 26; }
        else {
            int u = (ab.a + h) % 26, v = (ab.b + h) % 26;
            bool dup = (t == 169 && h == 1);
            if (dup) {}
            else if (u == v) o1 = u * st + u;
            else { o1 = u * st + v; o2 = v * st + u; }
        }
        tb.v[m][(s * 2 + h) * 8 + e] = o1 | (o2 << 16);
    }
    return tb;
}
static_assert(slot_ab(169).a == 12 && slot_ab(169).b == 25, "dup slot");

struct Params {
    const int* idx; const unsigned short* table; const unsigned short* wcin; const unsigned short* wout;
    const unsigned short* w1; const unsigned short* b1; const unsigned short* w2; const unsigned short* bout;
    unsigned short* out; unsigned char* ws;
};
struct KArgs { Params P; PrepTab tab; };

DI float bf_lo(unsigned v) { return __uint_as_float(v << 16); }
DI float bf_hi(unsigned v) { return __uint_as_float(v & 0xffff0000u); }
DI unsigned pk_bf16(float lo, float hi) { f32x2 v = {lo, hi}; return __builtin_bit_cast(unsigned, __builtin_convertvector(v, bf16x2)); }
DI f32x4 mfma16(s16x8 a, s16x8 b, f32x4 c) { return __builtin_amdgcn_mfma_f32_16x16x32_bf16(a, b, c, 0, 0, 0); }
DI f32x16 mfma32(s16x8 a, s16x8 b, f32x16 c) { return __builtin_amdgcn_mfma_f32_32x32x16_bf16(a, b, c, 0, 0, 0); }
DI s16x8 as_frag(u32x4 v) { return __builtin_bit_cast(s16x8, v); }
template <int CTRL, int ROWMASK> DI float dpp_add(float v) {
    return v + __int_as_float(__builtin_amdgcn_update_dpp(0, __float_as_int(v), CTRL, ROWMASK, 0xf, true));
}
DI float wave_sum_to_lane63(float v) {
    v = dpp_add<0x111, 0xf>(v); v = dpp_add<0x112, 0xf>(v); v = dpp_add<0x114, 0xf>(v); v = dpp_add<0x118, 0xf>(v);
    v = dpp_add<0x142, 0xa>(v); v = dpp_add<0x143, 0xc>(v);
    return v;
}
DI float gelu_tanh(float x) {
    const float k1 = -2.0f * 0.7978845608028654f * 1.4426950408889634f;
    const float k2 = k1 * 0.044715f;
    float arg = x * __builtin_fmaf(x * x, k2, k1);
    float e = __builtin_amdgcn_exp2f(arg);
    return x * __builtin_amdgcn_rcpf(1.0f + e);
}
DI void dma16(unsigned lds_addr, unsigned voff, __amdgpu_buffer_rsrc_t srd, unsigned soff) {
    asm volatile("s_mov_b32 m0, %0\n\ts_nop 0\n\tbuffer_load_dwordx4 %1, %2, %3 offen lds"
                 :: "s"(lds_addr), "v"(voff), "s"(srd), "s"(soff) : "memory");
}
DI void dma16_sc1(unsigned lds_addr, unsigned voff, __amdgpu_buffer_rsrc_t srd, unsigned soff) {
    asm volatile("s_mov_b32 m0, %0\n\ts_nop 0\n\tbuffer_load_dwordx4 %1, %2, %3 offen sc1 lds"
                 :: "s"(lds_addr), "v"(voff), "s"(srd), "s"(soff) : "memory");
}
DI void dma4_sc1(unsigned lds_addr, unsigned voff, __amdgpu_buffer_rsrc_t srd, unsigned soff) {
    asm volatile("s_mov_b32 m0, %0\n\ts_nop 0\n\tbuffer_load_dword %1, %2, %3 offen sc1 lds"
                 :: "s"(lds_addr), "v"(voff), "s"(srd), "s"(soff) : "memory");
}
DI void dma4(unsigned lds_addr, unsigned voff, __amdgpu_buffer_rsrc_t srd, unsigned soff) {
    asm volatile("s_mov_b32 m0, %0\n\ts_nop 0\n\tbuffer_load_dword %1, %2, %3 offen lds"
                 :: "s"(lds_addr), "v"(voff), "s"(srd), "s"(soff) : "memory");
}
DI void st_granule(gu64* g, unsigned tag, unsigned val) {
    __hip_atomic_store(g, ((unsigned long long)tag << 32) | val, __ATOMIC_RELAXED, __HIP_MEMORY_SCOPE_AGENT);
}
DI unsigned ld_agent(const gu32* p) { return __hip_atomic_load(p, __ATOMIC_RELAXED, __HIP_MEMORY_SCOPE_AGENT); }
DI unsigned lds_ld(lds8* l, unsigned off) { return __hip_atomic_load((LAS unsigned*)(l + off), __ATOMIC_RELAXED, __HIP_MEMORY_SCOPE_WORKGROUP); }
DI void lds_st(lds8* l, unsigned off, unsigned v) { __hip_atomic_store((LAS unsigned*)(l + off), v, __ATOMIC_RELAXED, __HIP_MEMORY_SCOPE_WORKGROUP); }
DI void sig_add(lds8* l, unsigned off) {
    asm volatile("s_mov_b64 exec, 1\n\tds_add_u32 %0, %1\n\ts_mov_b64 exec, -1" :: "v"((unsigned)(size_t)l + off), "v"(1u) : "memory");
}
DI void sig_set(lds8* l, unsigned off) {
    asm volatile("s_mov_b64 exec, 1\n\tds_write_b32 %0, %1\n\ts_mov_b64 exec, -1" :: "v"((unsigned)(size_t)l + off), "v"(1u) : "memory");
}
#define WAIT_LGKM() asm volatile("s_waitcnt lgkmcnt(0)" ::: "memory")
#define WAIT_VM0() asm volatile("s_waitcnt vmcnt(0)" ::: "memory")
#define CBAR() asm volatile("" ::: "memory")

template <int K> DI unsigned quad_bcast(unsigned v) { return __builtin_amdgcn_update_dpp(0u, v, K * 0x55, 0xf, 0xf, true); }
template <int N> DI void wait_vm() { asm volatile("s_waitcnt vmcnt(%0)" :: "i"(N) : "memory"); }
DI void dma_w4(unsigned lds_addr, unsigned voff, __amdgpu_buffer_rsrc_t srd, unsigned soff) {
    unsigned t;
    asm volatile("s_mov_b32 m0, %1\n\ts_add_u32 %0, %4, 12288\n\tbuffer_load_dwordx4 %2, %3, %4 offen lds\n\t"
                 "buffer_load_dwordx4 %2, %3, %0 offen offset:1024 lds\n\ts_add_u32 %0, %0, 12288\n\t"
                 "buffer_load_dwordx4 %2, %3, %0 offen offset:2048 lds\n\ts_add_u32 %0, %0, 12288\n\t"
                 "buffer_load_dwordx4 %2, %3, %0 offen offset:3072 lds"
                 : "=&s"(t) : "s"(lds_addr), "v"(voff), "s"(srd), "s"(soff) : "memory", "scc");
}
DI void dnn_start(const Params& P, lds8* lds, int w, int lane, int g, int ntile) {
    const int mh = w & 1, kh = w >> 1;
    const unsigned ldsb = (unsigned)(size_t)lds;
    const unsigned ring = ldsb + L_A + w * (XD * 2048);
    const __amdgpu_buffer_rsrc_t srdI = __builtin_amdgcn_make_buffer_rsrc((void*)P.idx, 0, NBATCH * 26 * 4, RSRC3);
    const unsigned vo = ((64 * g + 32 * mh + (lane >> 2)) * 26 + 13 * kh + 4 * (lane & 3)) * 4;
    dma16(ring, vo, srdI, 0);
    dma16(ring + 1024, vo, srdI, 1664);
    if (__builtin_expect(w == 1, 1)) dma4(ldsb + L_BI, ntile * 128 + lane * 4, __builtin_amdgcn_make_buffer_rsrc((void*)P.b1, 0, DIN * 2, RSRC3), 0);
    if (__builtin_expect(w == 2, 1)) dma4(ldsb + L_W2, ntile * 128 + lane * 4, __builtin_amdgcn_make_buffer_rsrc((void*)P.w2, 0, DIN * 2, RSRC3), 0);
    const __amdgpu_buffer_rsrc_t srdW = __builtin_amdgcn_make_buffer_rsrc((void*)P.w1, 0, DIN * DIN * 2, RSRC3);
    const unsigned vw = (lane >> 3) * 1664 + (((lane & 7) ^ (((lane >> 4) & 1) << 2)) << 4);
    const unsigned rowA = 416 * kh + 208 * mh + 104 * (g & 1);
    dma_w4(ldsb + L_W + rowA * 128, vw, srdW, rowA * 1664 + ntile * 128);
    if (W_EARLY > 1) dma_w4(ldsb + L_W + rowA * 128 + 4096, vw, srdW, rowA * 1664 + ntile * 128 + 4 * 13312);
    if (W_EARLY > 2) dma_w4(ldsb + L_W + rowA * 128 + 8192, vw, srdW, rowA * 1664 + ntile * 128 + 8 * 13312);
}
DI void dnn_role(const Params& P, lds8* lds, int w, int lane, int g, int ntile) {
    const int mh = w & 1, kh = w >> 1;
    const int h = lane >> 5;
    const unsigned ldsb = (unsigned)(size_t)lds;
    const unsigned ring = ldsb + L_A + w * (XD * 2048);
    const __amdgpu_buffer_rsrc_t srdW = __builtin_amdgcn_make_buffer_rsrc((void*)P.w1, 0, DIN * DIN * 2, RSRC3);
    const unsigned vw = (lane >> 3) * 1664 + (((lane & 7) ^ (((lane >> 4) & 1) << 2)) << 4);
    const unsigned rowA = 416 * kh + 208 * mh + 104 * (g & 1), rowB = 416 * kh + 208 * mh + 104 * ((g & 1) ^ 1);
    auto wdma = [&](unsigned row, int a) {
        const unsigned sb = row * 1664 + ntile * 128, lb = ldsb + L_W + row * 128;
        if (a < 3) dma_w4(lb + a * 4096, vw, srdW, sb + a * 4 * 13312);
        else dma16(lb + 12288, vw, srdW, sb + 12 * 13312);
    };
    wait_vm<4 * W_EARLY>();
    WAIT_LGKM();
    __builtin_amdgcn_s_barrier();
    const u32x4 i0 = *(const LAS u32x4*)(lds + L_A + w * (XD * 2048) + lane * 16);
    const u32x4 i1 = *(const LAS u32x4*)(lds + L_A + w * (XD * 2048) + 1024 + lane * 16);
    const unsigned ep = *(const LAS unsigned*)(lds + L_EP + g * 4);
#pragma unroll
    for (int a = W_EARLY; a < 4; ++a) wdma(rowA, a);
#pragma unroll
    for (int a = 0; a < 4; ++a) wdma(rowB, a);
    const __amdgpu_buffer_rsrc_t srdT = __builtin_amdgcn_make_buffer_rsrc((void*)P.table, 0, NVOC * 64, RSRC3);
    const unsigned c16 = (((lane & 3) ^ ((lane >> 4) & 3)) << 4);
    u32x4 t0 = i0 << 6, t1 = i1 << 6;
    asm volatile("" : "+v"(t0), "+v"(t1));
    auto xload = [&](int s) {
        const unsigned a0 = t0[s & 3], a1 = t1[s & 3];
        const unsigned o0 = (s < 4 ? quad_bcast<0>(a0) : s < 8 ? quad_bcast<1>(a0) : s < 12 ? quad_bcast<2>(a0) : quad_bcast<3>(a0)) + c16;
        const unsigned o1 = (s < 4 ? quad_bcast<0>(a1) : s < 8 ? quad_bcast<1>(a1) : s < 12 ? quad_bcast<2>(a1) : quad_bcast<3>(a1)) + c16;
        dma16(ring + (s % XD) * 2048, o0, srdT, 0);
        dma16(ring + (s % XD) * 2048 + 1024, o1, srdT, 0);
    };
#pragma unroll
    for (int s = 0; s < XD; ++s) xload(s);
    wait_vm<2 * XD>();
    sig_add(lds, L_F + 32 + 4 * kh);
    _Pragma("unroll 1") for (unsigned sp = 0; lds_ld(lds, L_F + 32 + 4 * kh) < 2 && sp < SPIN_CAP; ++sp) __builtin_amdgcn_s_sleep(0);
    CBAR();
    f32x16 acc[2];
    u32x2 bia[4], wv[4];
    float w2f[16];
    unsigned xdone = 0;
#pragma unroll
    for (int r4 = 0; r4 < 4; ++r4) {
        bia[r4] = *(const LAS u32x2*)(lds + L_BI + kh * 64 + h * 8 + r4 * 16);
        wv[r4] = *(const LAS u32x2*)(lds + L_W2 + kh * 64 + h * 8 + r4 * 16);
    }
#pragma unroll
    for (int r4 = 0; r4 < 4; ++r4) {
        acc[0][4 * r4] = bf_lo(bia[r4][0]); acc[0][4 * r4 + 1] = bf_hi(bia[r4][0]); acc[0][4 * r4 + 2] = bf_lo(bia[r4][1]); acc[0][4 * r4 + 3] = bf_hi(bia[r4][1]);
        acc[1][4 * r4] = 0.f; acc[1][4 * r4 + 1] = 0.f; acc[1][4 * r4 + 2] = 0.f; acc[1][4 * r4 + 3] = 0.f;
    }
    {
        const int blk = (lane >> 4) & 1, q = (lane & 15) >> 2, pp = lane & 3, n = lane & 31;
        unsigned ad[2];
#pragma unroll
        for (int j = 0; j < 2; ++j) {
            const unsigned c = 4 * (j ^ kh) + 2 * blk + (pp >> 1);
            ad[j] = L_W + (416 * kh + 8 * h + q) * 128 + ((c ^ (((q >> 1) & 1) << 2)) << 4) + (pp & 1) * 8;
        }
        const unsigned xb = L_A + w * (XD * 2048) + n * 64;
        const unsigned xa0 = xb + ((h ^ ((n >> 2) & 3)) << 4), xa1 = xb + (((2 + h) ^ ((n >> 2) & 3)) << 4);
        u32x4 xf[3][2];
        s16x8 wf[2][4];
        auto xread = [&](int s) {
            xf[s % 3][0] = *(const LAS u32x4*)(lds + xa0 + (s % XD) * 2048);
            xf[s % 3][1] = *(const LAS u32x4*)(lds + xa1 + (s % XD) * 2048);
        };
        auto wread = [&](int s) {
#pragma unroll
            for (int a = 0; a < 2; ++a)
#pragma unroll
                for (int j = 0; j < 2; ++j) {
                    const s16x4 lo = __builtin_amdgcn_ds_read_tr16_b64_v4i16((LAS s16x4*)(lds + ad[j] + s * 4096 + a * 2048));
                    const s16x4 hi = __builtin_amdgcn_ds_read_tr16_b64_v4i16((LAS s16x4*)(lds + ad[j] + s * 4096 + a * 2048 + 512));
                    wf[s & 1][2 * a + j] = __builtin_shufflevector(lo, hi, 0, 1, 2, 3, 4, 5, 6, 7);
                }
        };
        wread(0);
        wait_vm<2 * (XD - 1)>();
        xread(0);
        wait_vm<2 * (XD - 2)>();
        xread(1);
        __builtin_amdgcn_sched_barrier(0);
#pragma unroll
        for (int s = 0; s < 13; ++s) {
            if (s + 1 < 13) wread(s + 1);
#pragma unroll
            for (int a = 0; a < 2; ++a)
#pragma unroll
                for (int j = 0; j < 2; ++j) acc[j] = mfma32(wf[s & 1][2 * a + j], as_frag(xf[s % 3][a]), acc[j]);
            if (s + 1 < 13) {
#pragma unroll
                for (int j = 0; j < 4; ++j) { __builtin_amdgcn_sched_group_barrier(0x100, 2, 0); __builtin_amdgcn_sched_group_barrier(0x8, 1, 0); }
            }
            __builtin_amdgcn_sched_barrier(0);
            if (s + XD < 13) xload(s + XD);
            if (s == 11) {
                xdone = lds_ld(lds, L_F + 52);
#pragma unroll
                for (int r4 = 0; r4 < 4; ++r4) { w2f[4 * r4] = bf_lo(wv[r4][0]); w2f[4 * r4 + 1] = bf_hi(wv[r4][0]); w2f[4 * r4 + 2] = bf_lo(wv[r4][1]); w2f[4 * r4 + 3] = bf_hi(wv[r4][1]); }
#pragma unroll
                for (int e = 0; e < 16; ++e) asm volatile("" : "+v"(w2f[e]));
            }
            if (s + 2 < 13) {
                constexpr int LASTI = 12;
                const int last = s + XD < LASTI ? s + XD : LASTI;
                const int younger = last - (s + 2);
                if (younger >= 4) wait_vm<8>(); else if (younger == 3) wait_vm<6>(); else if (younger == 2) wait_vm<4>(); else if (younger == 1) wait_vm<2>(); else wait_vm<0>();
                if (s + 2 == 12) sig_add(lds, L_F + 52);
                xread(s + 2);
            }
            __builtin_amdgcn_sched_barrier(0);
        }
    }
    {
        if (xdone < 4) _Pragma("unroll 1") for (unsigned sp = 0; lds_ld(lds, L_F + 52) < 4 && sp < SPIN_CAP; ++sp) __builtin_amdgcn_s_sleep(0);
        CBAR();
        lds8* xw = lds + L_W + kh * 53248 + (5 + mh) * 4096 + lane * 16;
#pragma unroll
        for (int r4 = 0; r4 < 4; ++r4) *(LAS f32x4*)(xw + r4 * 1024) = (f32x4){acc[1][4 * r4], acc[1][4 * r4 + 1], acc[1][4 * r4 + 2], acc[1][4 * r4 + 3]};
        CBAR();
        sig_set(lds, L_F + 4 * w);
        _Pragma("unroll 1") for (unsigned sp = 0; lds_ld(lds, L_F + 4 * (w ^ 2)) == 0 && sp < SPIN_CAP; ++sp) __builtin_amdgcn_s_sleep(0);
        CBAR();
        const lds8* xr = lds + L_W + (kh ^ 1) * 53248 + (5 + mh) * 4096 + lane * 16;
        f32x4 o[4];
#pragma unroll
        for (int r4 = 0; r4 < 4; ++r4) o[r4] = *(const LAS f32x4*)(xr + r4 * 1024);
        asm volatile("" : "+v"(o[0]), "+v"(o[1]), "+v"(o[2]), "+v"(o[3]));
#pragma unroll
        for (int r4 = 0; r4 < 4; ++r4) {
            acc[0][4 * r4] += o[r4][0]; acc[0][4 * r4 + 1] += o[r4][1]; acc[0][4 * r4 + 2] += o[r4][2]; acc[0][4 * r4 + 3] += o[r4][3];
        }
    }
    float s0 = 0.f, s1 = 0.f;
#pragma unroll
    for (int r4 = 0; r4 < 4; ++r4) {
        s0 = __builtin_fmaf(gelu_tanh(acc[0][4 * r4]), w2f[4 * r4], s0);
        s1 = __builtin_fmaf(gelu_tanh(acc[0][4 * r4 + 1]), w2f[4 * r4 + 1], s1);
        s0 = __builtin_fmaf(gelu_tanh(acc[0][4 * r4 + 2]), w2f[4 * r4 + 2], s0);
        s1 = __builtin_fmaf(gelu_tanh(acc[0][4 * r4 + 3]), w2f[4 * r4 + 3], s1);
    }
    s0 += s1;
    const auto sw = __builtin_amdgcn_permlane32_swap(__float_as_uint(s0), __float_as_uint(s0), false, false);
    const float part = __uint_as_float(sw[0]) + __uint_as_float(sw[1]);
    const unsigned nxt = __builtin_amdgcn_update_dpp(0u, __float_as_uint(part), 0xf5, 0xf, 0xf, true);
    const __amdgpu_buffer_rsrc_t srdP = __builtin_amdgcn_make_buffer_rsrc((void*)(P.ws + WS_PART), 0, 16 * 32 * 64 * 8, RSRC3);
    const u32x4 gr = {__float_as_uint(part), ep + 1u, nxt, ep + 1u};
    __builtin_amdgcn_raw_buffer_store_b128(gr, srdP, (lane < 32 && !(lane & 1)) ? (unsigned)(((g * 32 + 2 * ntile + kh) * 64 + 32 * mh + lane) * 8) : 0x7ffffff0u, 0, AUX_SC1);
}

constexpr unsigned LP_W2 = 12288;
constexpr unsigned LP_WO = 14336;
constexpr unsigned LP_DM = 15360;
constexpr unsigned LP_W3 = 16384;
static_assert(LP_T1 + 4 * 1026 <= LP_W2, "prep scratch");
constexpr unsigned w3_slot(int j) { return j * 1280 + (j & 1) * 64 + ((j >> 2) & 1) * 128; }
DI void dma16_add(unsigned lds_base, unsigned lds_off, unsigned voff, __amdgpu_buffer_rsrc_t srd, unsigned sbase, unsigned soff) {
    unsigned t;
    asm volatile("s_add_u32 m0, %1, %2\n\ts_add_u32 %0, %5, %6\n\tbuffer_load_dwordx4 %3, %4, %0 offen lds"
                 : "=&s"(t) : "s"(lds_base), "i"(lds_off), "v"(voff), "s"(srd), "s"(sbase), "i"(soff) : "memory", "scc");
}
DI void prep_start(const Params& P, lds8* lds, int w, int lane, int p) {
    const unsigned ldsb = (unsigned)(size_t)lds;
    const __amdgpu_buffer_rsrc_t srdC = __builtin_amdgcn_make_buffer_rsrc((void*)P.wcin, 0, 3 * 17576 * 2, RSRC3);
    const int i0 = w == 0 ? 0 : w == 1 ? 6 : w == 2 ? 14 : 20;
    dma4(ldsb + LP_WO + w * 256, lane * 4, __builtin_amdgcn_make_buffer_rsrc((void*)P.wout, 0, 78 * 2, RSRC3), 0);
    {
        const unsigned hp = lane >> 5;
        const unsigned vo = hp * 1352 + ((lane >> 2) & 7) * 52 + (((lane & 3) ^ (hp << 1)) << 4);
        const unsigned lb = ldsb + LP_W3 + w * 16640, sb = 52 * 1352 + 52 * i0;
        dma16_add(lb, w3_slot(0), vo, srdC, sb, 0 * 2704);  dma16_add(lb, w3_slot(1), vo, srdC, sb, 1 * 2704);
        dma16_add(lb, w3_slot(2), vo, srdC, sb, 2 * 2704);  dma16_add(lb, w3_slot(3), vo, srdC, sb, 3 * 2704);
        dma16_add(lb, w3_slot(4), vo, srdC, sb, 4 * 2704);  dma16_add(lb, w3_slot(5), vo, srdC, sb, 5 * 2704);
        dma16_add(lb, w3_slot(6), vo, srdC, sb, 6 * 2704);  dma16_add(lb, w3_slot(7), vo, srdC, sb, 7 * 2704);
        dma16_add(lb, w3_slot(8), vo, srdC, sb, 8 * 2704);  dma16_add(lb, w3_slot(9), vo, srdC, sb, 9 * 2704);
        dma16_add(lb, w3_slot(10), vo, srdC, sb, 10 * 2704); dma16_add(lb, w3_slot(11), vo, srdC, sb, 11 * 2704);
        dma16_add(lb, w3_slot(12), vo, srdC, sb, 12 * 2704);
    }
    if (__builtin_expect(w < 2, 1)) {
        const unsigned vo = w == 0 ? 35152 + (lane >> 2) * 1352 + p * 52 + (lane & 3) * 16 : p * 1352 + lane * 16;
        const unsigned lb = ldsb + (w == 0 ? LP_W2 : LP_T0);
        dma16(lb, vo, srdC, 0);
        dma16(lb + 1024, vo, srdC, w == 0 ? 16 * 1352 : 1024);
    }
}
DI void prep_role(const Params& P, lds8* lds, int w, int lane, int p, int slot, int g, bool updater) {
    const int n = lane & 15, gq = lane >> 4;
    const int i0 = w == 0 ? 0 : w == 1 ? 6 : w == 2 ? 14 : 20;
    const int nact = w == 0 ? 48 : 44;
    const int ch = lane < nact ? lane : 0;
    const int cs = ch >> 1, chh = ch & 1;
    const GAS u32x4* tp = (const GAS u32x4*)((const GAS unsigned char*)(size_t)__builtin_amdgcn_kernarg_segment_ptr() + sizeof(Params) + ((w == 0 ? 384 : 0) + (cs * 2 + chh) * 8) * 4);
    const u32x4 ta = tp[0], tb = tp[1];
    const int q = (lane & 15) >> 2, pp = lane & 3;
    int r0 = 8 * gq + q, r1 = r0 + 4;
    r0 = r0 < 26 ? r0 : 25; r1 = r1 < 26 ? r1 : 25;
    unsigned b0a, b0b, b1a, b1b;
    {
        const unsigned wb = LP_W3 + w * 16640 + pp * 8;
        const unsigned j0 = r0 >> 1, j1 = r1 >> 1;
        const unsigned s0 = wb + j0 * 1280 + (j0 & 1) * 64 + ((j0 >> 2) & 1) * 128 + (r0 & 1) * 512;
        const unsigned s1 = wb + j1 * 1280 + (j1 & 1) * 64 + ((j1 >> 2) & 1) * 128 + (r1 & 1) * 512;
        b0a = s0 + (r0 & 1) * 32; b0b = s0 + 32 - (r0 & 1) * 32; b1a = s1 + (r1 & 1) * 32; b1b = s1 + 32 - (r1 & 1) * 32;
    }
    unsigned va = LP_VT + n * 64 + 2 * i0, vb = n < 10 ? va + 1024 : LP_DM;
    unsigned vl = w == 3 ? LP_DM : 0;
    unsigned ac = LP_T1 + 4 * (4 * gq * 32 + n);
    unsigned a20 = LP_W2 + r0 * 64 + pp * 8, a21 = LP_W2 + r1 * 64 + pp * 8, abv = LP_VT + n * 64 + gq * 16;
    asm volatile("" : "+v"(b0a), "+v"(b0b), "+v"(b1a), "+v"(b1b), "+v"(va), "+v"(vb), "+v"(ac), "+v"(a20), "+v"(a21), "+v"(abv));
    if (w == 3) {
        LAS unsigned* z = (LAS unsigned*)(lds + LP_VT + (lane < 27 ? lane : 31) * 64 + 52);
        z[0] = 0; z[1] = 0; z[2] = 0;
        *(LAS unsigned*)(lds + LP_T1 + 4 * T_ZERO) = 0;
    }
    if (w == 1) *(LAS unsigned*)(lds + LP_T0 + 2 * T_ZERO) = 0;
    asm volatile("s_waitcnt vmcnt(0) lgkmcnt(0)" ::: "memory");
    __builtin_amdgcn_s_barrier();
    {
        const u32x4 wo3 = *(const LAS u32x4*)(lds + LP_WO + w * 256 + 104 + 16 * gq);
        s16x8 bfr[16];
#pragma unroll
        for (int t = 0; t < 16; ++t) {
            const int ii = t >> 1, u = t & 1;
            const s16x4 lo = __builtin_amdgcn_ds_read_tr16_b64_v4i16((LAS s16x4*)(lds + (u ? b0b : b0a) + ii * 64));
            const s16x4 hi = __builtin_amdgcn_ds_read_tr16_b64_v4i16((LAS s16x4*)(lds + (u ? b1b : b1a) + ii * 64));
            bfr[t] = __builtin_shufflevector(lo, hi, 0, 1, 2, 3, 4, 5, 6, 7);
        }
        if (w == 3) {
            const unsigned o2 = *(const LAS unsigned*)(lds + LP_WO + 768 + 52 + (lane < 13 ? lane : 0) * 4);
            const unsigned o1 = *(const LAS unsigned short*)(lds + LP_WO + 768 + p * 2);
            *(LAS unsigned*)(lds + (lane < 13 ? LP_VT + 26 * 64 + lane * 4 : LP_DM)) = o2;
            *(LAS float*)(lds + LP_T1 + 4 * T_CONST) = bf_lo(o1);
        }
        float res[16];
#pragma unroll
        for (int t = 0; t < 16; ++t) res[t] = mfma16(as_frag(wo3), bfr[t], (f32x4){0.f, 0.f, 0.f, 0.f})[0];
#pragma unroll
        for (int k2 = 0; k2 < 4; ++k2) {
            const unsigned pa0 = pk_bf16(res[4 * k2], res[4 * k2 + 2]), pb0 = pk_bf16(res[4 * k2 + 1], res[4 * k2 + 3]);
            if (k2 < 3) { *(LAS unsigned*)(lds + va + 4 * k2) = pa0; *(LAS unsigned*)(lds + vb + 4 * k2) = pb0; }
            else { *(LAS unsigned*)(lds + (vl ? vl : va + 12)) = pa0; *(LAS unsigned*)(lds + (vl ? vl : vb + 12)) = pb0; }
        }
    }
    CBAR();
    sig_add(lds, L_F + 28);
    if (w > 1) return;
    const __amdgpu_buffer_rsrc_t srdA = __builtin_amdgcn_make_buffer_rsrc((void*)(P.ws + WS_A), 0, A_BYTES, RSRC3);
    u32x4 o;
    if (w == 1) {
        float pv[16];
#pragma unroll
        for (int e = 0; e < 8; ++e) {
            const unsigned x = e < 4 ? ta[e] : tb[e - 4];
            pv[2 * e] = bf_lo(*(const LAS unsigned short*)(lds + LP_T0 + 2 * (x & 0xffff)));
            pv[2 * e + 1] = bf_lo(*(const LAS unsigned short*)(lds + LP_T0 + 2 * (x >> 16)));
        }
#pragma unroll
        for (int j = 0; j < 4; ++j) o[j] = pk_bf16(pv[4 * j] + pv[4 * j + 1], pv[4 * j + 2] + pv[4 * j + 3]);
    } else {
        s16x8 a2[2];
#pragma unroll
        for (int mt = 0; mt < 2; ++mt) {
            const s16x4 lo = __builtin_amdgcn_ds_read_tr16_b64_v4i16((LAS s16x4*)(lds + a20 + mt * 32));
            const s16x4 hi = __builtin_amdgcn_ds_read_tr16_b64_v4i16((LAS s16x4*)(lds + a21 + mt * 32));
            a2[mt] = __builtin_shufflevector(lo, hi, 0, 1, 2, 3, 4, 5, 6, 7);
        }
        unsigned pa[16];
#pragma unroll
        for (int e = 0; e < 8; ++e) {
            const unsigned x = e < 4 ? ta[e] : tb[e - 4];
            pa[2 * e] = LP_T1 + 4 * (x & 0xffff); pa[2 * e + 1] = LP_T1 + 4 * (x >> 16);
        }
#pragma unroll
        for (int e = 0; e < 16; ++e) asm volatile("" : "+v"(pa[e]));
        _Pragma("unroll 1") for (unsigned sp = 0; lds_ld(lds, L_F + 28) < 4 && sp < SPIN_CAP; ++sp) __builtin_amdgcn_s_sleep(0);
        CBAR();
        const u32x4 bf0 = *(const LAS u32x4*)(lds + abv), bf1 = *(const LAS u32x4*)(lds + abv + 1024);
        const f32x4 z4 = {0.f, 0.f, 0.f, 0.f};
        const f32x4 d00 = mfma16(a2[0], as_frag(bf0), z4), d01 = mfma16(a2[1], as_frag(bf0), z4);
        const f32x4 d10 = mfma16(a2[0], as_frag(bf1), z4), d11 = mfma16(a2[1], as_frag(bf1), z4);
#pragma unroll
        for (int e = 0; e < 4; ++e) {
            *(LAS float*)(lds + ac + e * 128) = d00[e]; *(LAS float*)(lds + ac + 2048 + e * 128) = d01[e];
            *(LAS float*)(lds + ac + 64 + e * 128) = d10[e]; *(LAS float*)(lds + ac + 2048 + 64 + e * 128) = d11[e];
        }
        float pv[16];
#pragma unroll
        for (int e = 0; e < 16; ++e) pv[e] = *(const LAS float*)(lds + pa[e]);
#pragma unroll
        for (int j = 0; j < 4; ++j) o[j] = pk_bf16(pv[4 * j] + pv[4 * j + 1], pv[4 * j + 2] + pv[4 * j + 3]);
    }
    {
        const int fr = w == 0 ? (cs < 22 ? 2 * cs : 22 + cs) : 2 * cs + 1;
        __builtin_amdgcn_raw_buffer_store_b128(o, srdA, lane < nact ? (unsigned)(fr * 832 + (26 * chh + p) * 16) : 0x7ffffff0u, 0, AUX_SC1);
    }
    WAIT_VM0();
    if (w == 1) { sig_set(lds, L_F + 24); return; }
    const unsigned tagv = *(const LAS unsigned*)(lds + L_EP + (lane & 15) * 4);
    _Pragma("unroll 1") for (unsigned sp = 0; lds_ld(lds, L_F + 24) == 0 && sp < SPIN_CAP; ++sp) __builtin_amdgcn_s_sleep(0);
    if (lane < 16) __hip_atomic_store((gu32*)(P.ws + WS_FL) + (lane * 64 + slot) * 8, tagv + 1u, __ATOMIC_RELAXED, __HIP_MEMORY_SCOPE_AGENT);
    sig_set(lds, L_F + 60);
    if (updater) {
        const unsigned tag = *(const LAS unsigned*)(lds + L_EP + g * 4) + 1u;
        const gu32* fl = (const gu32*)(P.ws + WS_FL) + (g * 64 + lane) * 8;
        const gu32* ar = (const gu32*)(P.ws + WS_PART) + ((g * 32 + 26) * 64 + (lane < 13 ? lane : 0)) * 2 + 1;
        _Pragma("unroll 1") for (unsigned sp = 0; sp < SPIN_CAP; ++sp) {
            const bool fok = (__ballot(ld_agent(fl) == tag) & 0x003fffff03ffffffull) == 0x003fffff03ffffffull;
            if (fok && __all(ld_agent(ar) == tag)) break;
            __builtin_amdgcn_s_sleep(16);
        }
        if (lane == 0) __hip_atomic_store((gu32*)(P.ws + WS_EP) + g, tag, __ATOMIC_RELAXED, __HIP_MEMORY_SCOPE_AGENT);
    }
}

DI bool rows_ready(bool match) {
    const unsigned long long mk = __ballot(match);
    return (((unsigned)mk | (unsigned)(mk >> 32)) & 0x3ffffffu) == 0x3ffffffu;
}
DI void cin_role(const Params& P, lds8* lds, int cw, int lane, int g, int m, bool has_dnn, int pf_slot) {
    const int d = lane & 31, h = lane >> 5;
    const int b = 64 * g + 4 * m + cw;
    const unsigned ldsb = (unsigned)(size_t)lds;
    {
        const __amdgpu_buffer_rsrc_t srdT = __builtin_amdgcn_make_buffer_rsrc((void*)P.table, 0, NVOC * 64, RSRC3);
        const unsigned lb = ldsb + L_X0 + cw * 1664;
        WAIT_VM0();
        const int q = lane >> 2;
        const unsigned j0 = *(const LAS unsigned*)(lds + L_X0 + cw * 1664 + q * 4);
        const unsigned j1 = *(const LAS unsigned*)(lds + L_X0 + cw * 1664 + (q < 10 ? q + 16 : 25) * 4);
        unsigned o0 = j0 * 64 + (lane & 3) * 16, o1 = j1 * 64 + (lane & 3) * 16;
        asm volatile("" : "+v"(o0), "+v"(o1));
        dma16(lb, o0, srdT, 0);
        if (lane < 40) dma16(lb + 1024, o1, srdT, 0);
        if (cw == 0) dma4_sc1(ldsb + L_FL, (g * 64 + lane) * 32, __builtin_amdgcn_make_buffer_rsrc((void*)(P.ws + WS_FL), 0, 32768, RSRC3), 0);
    }
    const unsigned bo = P.bout[0];
    __builtin_amdgcn_s_setprio(0);
    CBAR();
    __builtin_amdgcn_s_barrier();
    const unsigned epv = *(const LAS unsigned*)(lds + L_EP + g * 4);
    unsigned bov = bo;
    asm volatile("s_waitcnt vmcnt(0)" : "+v"(bov) :: "memory");
    if (cw == 1 && has_dnn && lane == 0) st_granule((gu64*)(P.ws + WS_PART) + ((g * 32 + 26) * 64 + m), epv + 1u, 0u);
    float x[26];
    {
        const lds8* xb = lds + L_X0 + cw * 1664 + h * 64 + d * 2;
        const lds8* xl = lds + L_X0 + cw * 1664 + (h ? 0 : 25 * 64) + d * 2;
#pragma unroll
        for (int a = 0; a < 13; ++a) {
            const unsigned lo = *(const LAS unsigned short*)(xb + 2 * a * 64);
            const unsigned hi = a < 12 ? *(const LAS unsigned short*)(xb + (2 * a + 1) * 64) : *(const LAS unsigned short*)xl;
            x[2 * a] = bf_lo(lo); x[2 * a + 1] = bf_lo(hi);
        }
    }
    u32x4 z[24];
#pragma unroll
    for (int s = 0; s < 22; ++s) {
        float fa[8], fb[8];
#pragma unroll
        for (int e = 0; e < 8; ++e) { const AB pe = slot_ab(8 * s + e); fa[e] = x[pe.a]; fb[e] = x[pe.b]; }
        float t0, t1, t2, t3, t4, t5;
        unsigned o0, o1, o2, o3;
        asm("v_mul_f32 %4, %10, %11\n\tv_mul_f32 %5, %12, %13\n\tv_mul_f32 %6, %14, %15\n\tv_mul_f32 %7, %16, %17\n\t"
            "v_cvt_pk_bf16_f32 %0, %4, %5\n\tv_mul_f32 %8, %18, %19\n\tv_mul_f32 %9, %20, %21\n\tv_cvt_pk_bf16_f32 %1, %6, %7\n\t"
            "v_mul_f32 %4, %22, %23\n\tv_mul_f32 %5, %24, %25\n\tv_cvt_pk_bf16_f32 %2, %8, %9\n\tv_cvt_pk_bf16_f32 %3, %4, %5"
            : "=&v"(o0), "=&v"(o1), "=&v"(o2), "=v"(o3), "=&v"(t0), "=&v"(t1), "=&v"(t2), "=&v"(t3), "=&v"(t4), "=&v"(t5)
            : "v"(fa[0]), "v"(fb[0]), "v"(fa[1]), "v"(fb[1]), "v"(fa[2]), "v"(fb[2]), "v"(fa[3]), "v"(fb[3]),
              "v"(fa[4]), "v"(fb[4]), "v"(fa[5]), "v"(fb[5]), "v"(fa[6]), "v"(fb[6]), "v"(fa[7]), "v"(fb[7]));
        z[s] = (u32x4){o0, o1, o2, o3};
    }
#pragma unroll
    for (int s = 22; s < 24; ++s) {
#pragma unroll
        for (int j = 0; j < 4; ++j) {
            const AB p0 = slot_ab(8 * s + 2 * j), p1 = slot_ab(8 * s + 2 * j + 1);
            const float a0 = p0.a < 26 ? x[p0.a] : (p0.a == 26 ? 1.f : 0.f);
            const float a1 = p1.a < 26 ? x[p1.a] : (p1.a == 26 ? 1.f : 0.f);
            z[s][j] = pk_bf16(a0, a1);
        }
    }
#pragma unroll
    for (int s = 0; s < 24; ++s) asm volatile("" : "+v"(z[s]));
    const unsigned tag = epv + 1u;
    {
        const __amdgpu_buffer_rsrc_t srdA = __builtin_amdgcn_make_buffer_rsrc((void*)(P.ws + WS_A), 0, A_BYTES, RSRC3);
        if (cw == 0) {
            if (has_dnn) _Pragma("unroll 1") for (unsigned sp = 0; lds_ld(lds, L_F + 52) < 4 && sp < SPIN_CAP; ++sp) __builtin_amdgcn_s_sleep(0);
            else _Pragma("unroll 1") for (unsigned sp = 0; lds_ld(lds, L_F + 60) == 0 && sp < SPIN_CAP; ++sp) __builtin_amdgcn_s_sleep(1);
            const gu32* fl = (const gu32*)(P.ws + WS_FL) + (g * 64 + lane) * 8;
            if (!rows_ready(*(const LAS unsigned*)(lds + L_FL + lane * 4) == tag))
            _Pragma("unroll 1") for (unsigned sp = 0; sp < SPIN_CAP; ++sp) {
                if (rows_ready(ld_agent(fl) == tag)) break;
                if (has_dnn) __builtin_amdgcn_s_sleep(1); else __builtin_amdgcn_s_sleep(8);
            }
            sig_set(lds, L_F + 16);
            if (!has_dnn) dma4_sc1(ldsb + L_DUMMY, lane < 50 ? (unsigned)((((g & 1) * 3 + pf_slot) * 50 + lane) * 128) : 0x7fffff00u, srdA, 0);
        } else {
            _Pragma("unroll 1") for (unsigned sp = 0; lds_ld(lds, L_F + 16) == 0 && sp < SPIN_CAP; ++sp) __builtin_amdgcn_s_sleep(0);
        }
        CBAR();
        __builtin_amdgcn_s_setprio(2);
        {
            const unsigned l1 = ldsb + L_W + cw * 4096, vo = lane * 16;
            asm volatile("s_mov_b32 m0, %0\n\ts_nop 0\n\t"
                         "buffer_load_dwordx4 %1, %2, %3 offen sc1 lds\n\tbuffer_load_dwordx4 %1, %2, %3 offen offset:1024 sc1 lds\n\t"
                         "buffer_load_dwordx4 %1, %2, %3 offen offset:2048 sc1 lds\n\tbuffer_load_dwordx4 %1, %2, %3 offen offset:3072 sc1 lds"
                         :: "s"(l1), "v"(vo), "s"(srdA), "s"(cw * 4096) : "memory");
#pragma unroll
            for (int i = 0; i < 6; ++i) {
                const int q = 6 * cw + i;
                const unsigned go = q < 4 ? 16384 + 1024 * q : 19968 + 1024 * (q - 4), lo = q < 4 ? 16384 + 1024 * q : 53248 + 1024 * (q - 4);
                dma16_sc1(ldsb + L_W + lo, vo, srdA, go);
            }
        }
        wait_vm<6>();
        sig_add(lds, L_F + 20);
        _Pragma("unroll 1") for (unsigned sp = 0; lds_ld(lds, L_F + 20) < 4 && sp < SPIN_CAP; ++sp) __builtin_amdgcn_s_sleep(0);
        CBAR();
    }
    const gu64* pp = (const gu64*)(P.ws + WS_PART) + ((g * 32 + (lane < 26 ? lane : 0)) * 64 + 4 * m + cw);
    unsigned long long pv = 0;
    f32x16 acc0, acc1;
#pragma unroll
    for (int i = 0; i < 16; ++i) { acc0[i] = 0.f; acc1[i] = 0.f; }
    {
        unsigned abo = L_W + (26 * h + d) * 16;
        asm volatile("" : "+v"(abo));
        const lds8* ab = lds + abo;
        constexpr int PF = 6;
        u32x4 af[46];
        unsigned cnt2 = 0;
        auto frag_of = [](int t) { return t < 24 ? t : t + 40; };
#pragma unroll
        for (int t = 0; t < PF; ++t) af[t] = *(const LAS u32x4*)(ab + frag_of(t) * 832);
        __builtin_amdgcn_sched_barrier(0);
#pragma unroll
        for (int t = 0; t < 46; ++t) {
            if (t == 9) { wait_vm<0>(); sig_add(lds, L_F + 44); }
            if (t + PF == 19 - 2) cnt2 = lds_ld(lds, L_F + 44);
            if (t + PF == 19) {
                if (cnt2 < 4) _Pragma("unroll 1") for (unsigned sp = 0; lds_ld(lds, L_F + 44) < 4 && sp < SPIN_CAP; ++sp) __builtin_amdgcn_s_sleep(0);
                CBAR();
            }
            if (t + PF < 46) af[t + PF] = *(const LAS u32x4*)(ab + frag_of(t + PF) * 832);
            if (t < 44) {
                if (t & 1) acc0 = mfma32(as_frag(af[t]), as_frag(z[t >> 1]), acc0);
                else acc1 = mfma32(as_frag(af[t]), as_frag(z[t >> 1]), acc1);
            } else acc1 = mfma32(as_frag(af[t]), as_frag(z[t - 22]), acc1);
            if (t == 30) pv = __hip_atomic_load(pp, __ATOMIC_RELAXED, __HIP_MEMORY_SCOPE_AGENT);
            __builtin_amdgcn_sched_barrier(0);
        }
    }
    float sa = 0.f;
#pragma unroll
    for (int i = 0; i < 12; ++i) sa = __builtin_fmaf(acc0[i], acc1[i], sa);
    const float sb = __builtin_fmaf(acc0[12], acc1[12], acc0[13] * acc1[13]);
    float tot = h == 0 ? sa + sb : sa;
    asm volatile("" : "+v"(tot));
    _Pragma("unroll 1") for (unsigned sp = 0; sp < SPIN_CAP; ++sp) {
        if (__all((unsigned)(pv >> 32) == tag)) break;
        pv = __hip_atomic_load(pp, __ATOMIC_RELAXED, __HIP_MEMORY_SCOPE_AGENT);
    }
    if (lane < 26) tot += __uint_as_float((unsigned)pv);
    tot = wave_sum_to_lane63(tot);
    if (lane == 63) {
        const float o = tot + bf_lo(bov);
        P.out[b] = (unsigned short)(pk_bf16(o, o) & 0xffffu);
    }
}

DI void decode_block(int bid, int& g, int& m, int& ntile, int& k) {
    const int x = bid & 7, j = bid >> 3;
    g = 2 * x + (j >> 4); m = j & 15;
    ntile = m < 13 ? m : -1; k = m - 13;
}

__global__ void __launch_bounds__(512, 2) xdfm_kernel(KArgs KA) {
    const Params& P = KA.P;
    __shared__ __attribute__((aligned(16))) unsigned char smem[L_END];
    lds8* lds = (lds8*)smem;
    const int w = __builtin_amdgcn_readfirstlane(threadIdx.x >> 6), lane = threadIdx.x & 63;
    {
        unsigned long long pc; unsigned len;
        asm volatile("s_getpc_b64 %0\n.Lxdfm_pc:\n\ts_mov_b32 %1, (.Lxdfm_end-.Lxdfm_pc)&4294967295" : "=s"(pc), "=s"(len));
        const __amdgpu_buffer_rsrc_t srdP = __builtin_amdgcn_make_buffer_rsrc((void*)pc, 0, len, RSRC3);
        dma4((unsigned)(size_t)lds + L_DUMMY, lane == 0 ? ((blockIdx.x >> 3) * 8 + w) * 128 : 0x7fffff00u, srdP, 0);
    }
    asm volatile("" :: "s"(P.idx), "s"(P.table), "s"(P.wcin), "s"(P.wout), "s"(P.w1), "s"(P.b1), "s"(P.w2), "s"(P.bout), "s"(P.out), "s"(P.ws));
    int g, m, ntile, k;
    decode_block(blockIdx.x, g, m, ntile, k);
    const bool is_cin = w >= 4;
    if (__builtin_expect(is_cin, 1)) {
        __builtin_amdgcn_s_setprio(3);
        dma16((unsigned)(size_t)lds + L_X0 + (w - 4) * 1664, lane < 7 ? (64 * g + 4 * m + w - 4) * 104 + lane * 16 : 0x7fffff00u,
              __builtin_amdgcn_make_buffer_rsrc((void*)P.idx, 0, NBATCH * 26 * 4, RSRC3), 0);
        if (__builtin_expect(w == 7, 1)) dma4((unsigned)(size_t)lds + L_DUMMY, lane < 26 ? ((blockIdx.x >> 3) * 26 + lane) * 128 : 0x7fffff00u,
                         __builtin_amdgcn_make_buffer_rsrc((void*)P.wcin, 0, 3 * 17576 * 2, RSRC3), 0);
    }
    if (__builtin_expect(w == 0, 1)) {
        if (lane < 16) lds_st(lds, L_F + 4 * lane, 0);
        dma4_sc1((unsigned)(size_t)lds + L_EP, (lane & 15) * 4, __builtin_amdgcn_make_buffer_rsrc((void*)(P.ws + WS_EP), 0, 64, RSRC3), 0);
    }
    const int np = g * 3 + k;
    const bool second = (np >= 22 && np < 42) || np >= 46;
    const int p = second ? (np < 42 ? np - 22 : np - 26) : (np < 22 ? np : np - 20);
    if (__builtin_expect(!is_cin && ntile >= 0, 1)) dnn_start(P, lds, w, lane, g, ntile);
    if (__builtin_expect(!is_cin && ntile < 0, 1)) prep_start(P, lds, w, lane, p);
    if (__builtin_expect(!is_cin && ntile >= 0, 1)) {
        dnn_role(P, lds, w, lane, g, ntile);
    } else if (is_cin) {
        cin_role(P, lds, w - 4, lane, g, m, ntile >= 0, k);
        __builtin_amdgcn_s_waitcnt(0x0F70);
    } else {
        prep_role(P, lds, w, lane, p, second ? 32 + p : p, g, k == 0);
        __builtin_amdgcn_s_waitcnt(0x0F70);
    }
    asm volatile(".Lxdfm_end:");
}

extern "C" void kernel_launch(void* const* d_in, const int* in_sizes, int n_in,
                              void* d_out, int out_size, void* d_ws, size_t ws_size,
                              hipStream_t stream) {
    KArgs KA;
    KA.tab = make_tab();
    Params& P = KA.P;
    P.idx = (const int*)d_in[0]; P.table = (const unsigned short*)d_in[1]; P.wcin = (const unsigned short*)d_in[2];
    P.wout = (const unsigned short*)d_in[3]; P.w1 = (const unsigned short*)d_in[4]; P.b1 = (const unsigned short*)d_in[5];
    P.w2 = (const unsigned short*)d_in[6]; P.bout = (const unsigned short*)d_in[7];
    P.out = (unsigned short*)d_out; P.ws = (unsigned char*)d_ws;
    xdfm_kernel<<<256, 512, 0, stream>>>(KA);
}
```
